```python
import torch

def _softmax(x, axis=-1):
    return torch.softmax(x, dim=axis)
def _gelu(x):
    return torch.nn.functional.gelu(x, approximate="tanh")
def _silu(x):
    return torch.nn.functional.silu(x)
def _sigmoid(x):
    return torch.sigmoid(x)
def _layernorm(x, axis=-1, eps=1e-5):
    m = x.mean(dim=axis, keepdim=True)
    v = x.var(dim=axis, keepdim=True, unbiased=False)
    return (x - m) / torch.sqrt(v + eps)
def _scatter_add(tgt, idx, src, axis=0):
    return tgt.clone().index_add_(axis, idx, src)
def _segment_sum(src, seg, n):
    out = torch.zeros((n,) + src.shape[1:], dtype=src.dtype)
    return out.index_add_(0, seg, src)
def _pad(x, pw):
    flat = tuple(v for pair in reversed(pw) for v in pair)
    return torch.nn.functional.pad(x, flat)

kernel_name = 'concat_heads__ssm_conv_h3__sliding_block__prompt'

def setup_inputs():

    _w = (lambda a: (lambda t: t.to(torch.bfloat16) if t.is_floating_point() else t)(torch.as_tensor(a)))
    def _A_setup():
        B, S, D = 1, 2048, 640
        ks = 4
        return {
            "q": _w((lambda *s: torch.randn(*s, dtype=torch.bfloat16) * 0.05)(B, S, D)),
            "k": _w((lambda *s: torch.randn(*s, dtype=torch.bfloat16) * 0.8)(B, S, D)),
            "v": _w((lambda *s: torch.randn(*s, dtype=torch.bfloat16) * 0.8)(B, S, D)),
            "shift_k": _w((lambda *s: torch.randn(*s, dtype=torch.bfloat16) * 0.8)(D, ks) * 0.5),
            "ssm_k": _w((lambda *s: torch.randn(*s, dtype=torch.bfloat16) * 3.2)(D, S) * (1.0 / S ** 0.5)),
            "ssm_D": _w((lambda *s: torch.randn(*s, dtype=torch.bfloat16) * 0.8)(D)),
        }
    _A_in = _A_setup()

    def _B_setup():
        B, S, D = 1, 2048, 640
        return {
            "x": _w((lambda *s: torch.randn(*s, dtype=torch.bfloat16) * 0.2)(B, S, D)),
            "wqkv": _w((lambda *s: torch.randn(*s, dtype=torch.bfloat16) * 0.2)(D, 3 * D)),
            "wo": _w((lambda *s: torch.randn(*s, dtype=torch.bfloat16) * 0.05)(D, D)),
        }
    _B_in = _B_setup()

    return {
        "A_q": _A_in["q"],
        "A_k": _A_in["k"],
        "A_v": _A_in["v"],
        "A_shift_k": _A_in["shift_k"],
        "A_ssm_k": _A_in["ssm_k"],
        "A_ssm_D": _A_in["ssm_D"],
        "B_x": _B_in["x"],
        "B_wqkv": _B_in["wqkv"],
        "B_wo": _B_in["wo"],
    }

def reference(A_q, A_k, A_v, A_shift_k, A_ssm_k, A_ssm_D, B_x, B_wqkv, B_wo):
    def _A_ref(q, k, v, shift_k, ssm_k, ssm_D):
        B, S, D = q.shape
        ks = shift_k.shape[-1]
        k_pad = _pad(k, ((0, 0), (ks - 1, 0), (0, 0)))
        k_shift = torch.zeros_like(k)
        for t in range(ks):
            k_shift = k_shift + k_pad[:, t:t + S, :] * shift_k[:, ks - 1 - t][None, None, :]
        kv = k_shift * v
        L = 2 * S
        kv_pad = _pad(kv, ((0, 0), (0, S), (0, 0)))
        kf_pad = _pad(ssm_k, ((0, 0), (0, S)))
        kv_f = (lambda x, axes=(-2,-1): torch.fft.rfft2(x.float(), dim=axes))(kv_pad[:, None, :, :], axes=(1, 2))
        kf_f = (lambda x, axes=(-2,-1): torch.fft.rfft2(x.float(), dim=axes))(kf_pad[:, None, :], axes=(1, 2))
        kf_f = (lambda x, *d: x.permute(*d))(kf_f, 1, 2, 0)
        y_f = kv_f * kf_f[None, :, :, :]
        y = (lambda x, s, axes=(-2,-1): torch.fft.irfft2(x, s=s, dim=axes).to(torch.bfloat16))(y_f, (1, L), axes=(1, 2))[:, 0, :S, :]
        y = y + kv * ssm_D[None, None, :]
        return q * y

    def _B_ref(x, wqkv, wo):
        B, S, D = x.shape
        H = 32; Dh = D // H; W = 256
        qkv = (lambda x, *d: x.permute(*d))((x @ wqkv).reshape(B, S, 3, H, Dh), 2, 0, 3, 1, 4)
        q, k, v = qkv[0], qkv[1], qkv[2]
        scores = torch.einsum("bhqd,bhkd->bhqk", q, k) * (Dh ** -0.5)
        idx = torch.arange(S)
        mask = (idx[:, None] - idx[None, :] >= 0) & (idx[:, None] - idx[None, :] < W)
        scores = torch.where(mask[None, None], scores, -1e9)
        p = _softmax(scores, axis=-1)
        o = torch.einsum("bhqk,bhkd->bhqd", p, v)
        return (lambda x, *d: x.permute(*d))(o, 0, 2, 1, 3).reshape(B, S, D) @ wo

    a = _A_ref(A_q, A_k, A_v, A_shift_k, A_ssm_k, A_ssm_D)
    b = _B_ref(B_x, B_wqkv, B_wo)
    return torch.concatenate([a, b], axis=-1)
```

```cpp
#include <hip/hip_runtime.h>
#include <hip/hip_bf16.h>
#include <stdint.h>

typedef unsigned short u16;
typedef short s16x4 __attribute__((ext_vector_type(4)));
typedef short s16x8 __attribute__((ext_vector_type(8)));
typedef float f32x2 __attribute__((ext_vector_type(2)));
typedef float f32x4 __attribute__((ext_vector_type(4)));
typedef float f32x16 __attribute__((ext_vector_type(16)));
typedef unsigned u32x2 __attribute__((ext_vector_type(2)));
typedef unsigned u32x4 __attribute__((ext_vector_type(4)));
typedef __bf16 bf16x2 __attribute__((ext_vector_type(2)));
#define LAS __attribute__((address_space(3)))
#define DI __device__ __forceinline__

constexpr int SEQ = 2048, DM = 640, NQKV = 1920, NH = 32;
constexpr size_t QF_HEAD = 64 * 1280;
constexpr size_t VF_HEAD = 128 * 672;
constexpr size_t WS_QF = 0;
constexpr size_t WS_KF = WS_QF + NH * QF_HEAD;
constexpr size_t WS_VF = WS_KF + NH * QF_HEAD;
constexpr size_t WS_UT = WS_VF + NH * VF_HEAD + 4096;
constexpr size_t WS_ZT = WS_UT + (size_t)DM * SEQ * 2;
constexpr size_t WS_OW = WS_ZT + (size_t)DM * SEQ * 2;

DI unsigned pk_bf16(float lo, float hi) {
  f32x2 f = {lo, hi};
  return __builtin_bit_cast(unsigned, __builtin_convertvector(f, bf16x2));
}
DI float bf_lo(unsigned u) { return __uint_as_float(u << 16); }
DI float bf_hi(unsigned u) { return __uint_as_float(u & 0xffff0000u); }
DI float rbf(float x) { return bf_lo(pk_bf16(x, 0.f)); }

DI unsigned lds_addr(const void* p) { return (unsigned)(uintptr_t)(LAS const char*)p; }

DI void glds16(unsigned voff, const void* sbase, unsigned lds_dst) {
  unsigned keep;
  asm volatile("s_mov_b32 %0, m0\n\ts_mov_b32 m0, %3\n\ts_nop 0\n\tglobal_load_lds_dwordx4 %1, %2\n\ts_mov_b32 m0, %0"
               : "=&s"(keep) : "v"(voff), "s"(sbase), "s"(lds_dst) : "memory");
}
DI u32x4 gload16_asm(const void* p) {
  u32x4 r; asm volatile("global_load_dwordx4 %0, %1, off" : "=v"(r) : "v"(p) : "memory"); return r;
}
DI u32x2 gload8_asm(const void* p) {
  u32x2 r; asm volatile("global_load_dwordx2 %0, %1, off" : "=v"(r) : "v"(p) : "memory"); return r;
}
DI void gstore16_asm(void* p, u32x4 v) {
  asm volatile("global_store_dwordx4 %0, %1, off\n\ts_nop 1" :: "v"(p), "v"(v) : "memory");
}
DI void gstore8_asm(void* p, u32x2 v) {
  asm volatile("global_store_dwordx2 %0, %1, off\n\ts_nop 1" :: "v"(p), "v"(v) : "memory");
}
template <int N> DI void wait_vm() { asm volatile("s_waitcnt vmcnt(%0)" :: "n"(N) : "memory"); }
DI void bar() { asm volatile("s_waitcnt lgkmcnt(0)\n\ts_barrier" ::: "memory"); }
DI s16x4 tr_read(unsigned a) {
  return __builtin_amdgcn_ds_read_tr16_b64_v4i16((LAS s16x4*)(uintptr_t)a);
}
DI s16x8 lds_read16(unsigned a) { return *(LAS const s16x8*)(uintptr_t)a; }
DI s16x4 lds_read8(unsigned a) { return *(LAS const s16x4*)(uintptr_t)a; }
DI void lds_write16(unsigned a, u32x4 v) { *(LAS u32x4*)(uintptr_t)a = v; }
DI s16x8 cat8(s16x4 a, s16x4 b) { return __builtin_shufflevector(a, b, 0, 1, 2, 3, 4, 5, 6, 7); }
#define MFMA16(a, b, c) __builtin_amdgcn_mfma_f32_16x16x32_bf16((a), (b), (c), 0, 0, 0)
#define MFMA32(a, b, c) __builtin_amdgcn_mfma_f32_32x32x16_bf16((a), (b), (c), 0, 0, 0)
#define MFMA32K8(a, b, c) __builtin_amdgcn_mfma_f32_32x32x8bf16_1k((a), (b), (c), 0, 0, 0)

constexpr int K1_STAGE = 32768;
constexpr int K1_LDS_A1 = 4 * K1_STAGE;
constexpr int K1_LDS = K1_LDS_A1 + 4 * 2048;
constexpr int A1_NST = 2;

struct A1Regs { u32x4 k[5], v[2], w[4]; };

DI void a1_issue(A1Regs& r, int tile, int lane, const u16* ak, const u16* av, const u16* shk) {
  const int ts = tile / 20, td = tile - ts * 20;
  const int c = lane & 3, rg = lane >> 2;
  const int d = td * 32 + c * 8, s = ts * 32 + rg * 2;
#pragma unroll
  for (int i = 0; i < 5; ++i) {
    int sr = s - 3 + i; sr = sr < 0 ? 0 : sr;
    r.k[i] = gload16_asm(ak + (size_t)sr * DM + d);
  }
#pragma unroll
  for (int i = 0; i < 2; ++i) r.v[i] = gload16_asm(av + (size_t)(s + i) * DM + d);
#pragma unroll
  for (int i = 0; i < 4; ++i) r.w[i] = gload16_asm(shk + (size_t)d * 4 + i * 8);
}
template <int WAITN>
DI void a1_finish(A1Regs& r, int tile, int lane, unsigned lds, u16* ut) {
  asm volatile("s_waitcnt vmcnt(%11)"
               : "+v"(r.k[0]), "+v"(r.k[1]), "+v"(r.k[2]), "+v"(r.k[3]), "+v"(r.k[4]), "+v"(r.v[0]), "+v"(r.v[1]),
                 "+v"(r.w[0]), "+v"(r.w[1]), "+v"(r.w[2]), "+v"(r.w[3])
               : "n"(WAITN) : "memory");
  const int ts = tile / 20, td = tile - ts * 20;
  const int c = lane & 3, rg = lane >> 2;
  const int s = ts * 32 + rg * 2;
#pragma unroll
  for (int i = 0; i < 3; ++i)
    if (s - 3 + i < 0) r.k[i] = (u32x4){0u, 0u, 0u, 0u};
#pragma unroll
  for (int row = 0; row < 2; ++row) {
    u32x4 o;
#pragma unroll
    for (int e2 = 0; e2 < 4; ++e2) {
      float res[2];
#pragma unroll
      for (int hlf = 0; hlf < 2; ++hlf) {
        const unsigned w01 = r.w[e2][hlf * 2], w23 = r.w[e2][hlf * 2 + 1];
        const float w0 = bf_lo(w01), w1 = bf_hi(w01), w2 = bf_lo(w23), w3 = bf_hi(w23);
        float k3 = hlf ? bf_hi(r.k[row + 0][e2]) : bf_lo(r.k[row + 0][e2]);
        float k2 = hlf ? bf_hi(r.k[row + 1][e2]) : bf_lo(r.k[row + 1][e2]);
        float k1 = hlf ? bf_hi(r.k[row + 2][e2]) : bf_lo(r.k[row + 2][e2]);
        float k0 = hlf ? bf_hi(r.k[row + 3][e2]) : bf_lo(r.k[row + 3][e2]);
        float vv = hlf ? bf_hi(r.v[row][e2]) : bf_lo(r.v[row][e2]);
        float a = rbf(k3 * w3);
        a = rbf(a + rbf(k2 * w2));
        a = rbf(a + rbf(k1 * w1));
        a = rbf(a + rbf(k0 * w0));
        res[hlf] = a * vv;
      }
      o[e2] = pk_bf16(res[0], res[1]);
    }
    lds_write16(lds + (31 - (rg * 2 + row)) * 64 + c * 16, o);
  }
  asm volatile("s_waitcnt lgkmcnt(0)" ::: "memory");
  const int gg = lane >> 4, q = (lane & 15) >> 2, p = lane & 3;
  const int dh = gg & 1, xh = gg >> 1;
  s16x4 t[4];
#pragma unroll
  for (int i = 0; i < 4; ++i) t[i] = tr_read(lds + (xh * 16 + i * 4 + q) * 64 + dh * 32 + p * 8);
  const int d = td * 32 + dh * 16 + (lane & 15);
  const int x0 = (SEQ - 32 - ts * 32) + xh * 16;
  u16* dst = ut + (size_t)d * SEQ + x0;
  gstore16_asm(dst, __builtin_bit_cast(u32x4, cat8(t[0], t[1])));
  gstore16_asm(dst + 8, __builtin_bit_cast(u32x4, cat8(t[2], t[3])));
  asm volatile("s_waitcnt lgkmcnt(0)" ::: "memory");
}

template <bool VT>
DI void k1_gemm(unsigned lds0, int m0, int n0, int tid, const u16* x, const u16* wqkv, char* ws,
                unsigned xoff, unsigned woff) {
  const int lane = tid & 63, wave = __builtin_amdgcn_readfirstlane(tid >> 6);
  const int wm = wave >> 1, wn = wave & 1;
  const int r16 = lane & 15, g = lane >> 4, q = r16 >> 2, p = lane & 3;
  f32x4 acc[4][4];
#pragma unroll
  for (int i = 0; i < 4; ++i)
#pragma unroll
    for (int j = 0; j < 4; ++j) acc[i][j] = (f32x4){0.f, 0.f, 0.f, 0.f};
  const unsigned xsw = (unsigned)(r16 >> 1);
  const unsigned xb0 = lds0 + (wm * 64 + r16) * 128 + ((g ^ xsw) * 16);
  const unsigned xb1 = lds0 + (wm * 64 + r16) * 128 + (((4 + g) ^ xsw) * 16);
  const unsigned wf = (unsigned)(q | ((g & 1) << 2));
  unsigned wb[4];
#pragma unroll
  for (int nt = 0; nt < 4; ++nt)
    wb[nt] = lds0 + 16384 + (8 * g + q) * 256 + (((unsigned)(wn * 8 + 2 * nt + (p >> 1)) ^ (wf << 1)) * 16) + (p & 1) * 8;
  const char* xg = (const char*)(x + (size_t)m0 * DM);
  const char* wg = (const char*)(wqkv + n0);
  auto stage = [&](int t) {
    const unsigned buf = lds0 + (t & 3) * K1_STAGE + wave * 1024;
#pragma unroll
    for (int i = 0; i < 4; ++i) glds16(xoff, xg + (size_t)i * 32 * DM * 2 + t * 128, buf + i * 4096);
#pragma unroll
    for (int i = 0; i < 4; ++i) glds16(woff, wg + (size_t)(t * 64 + i * 16) * NQKV * 2, buf + 16384 + i * 4096);
  };
#pragma unroll
  for (int t = 0; t < 10; ++t) {
    if (t == 0) wait_vm<24 + A1_NST>();
    else if (t <= 3) wait_vm<16 + A1_NST>();
    else if (t <= 7) wait_vm<16>();
    else if (t == 8) wait_vm<8>();
    else wait_vm<0>();
    bar();
    if (t >= 1 && t + 3 <= 9) stage(t + 3);
    const unsigned bo = (t & 3) * K1_STAGE;
#pragma unroll
    for (int kk = 0; kk < 2; ++kk) {
      s16x8 xf[4], wfr[4];
#pragma unroll
      for (int mt = 0; mt < 4; ++mt) xf[mt] = lds_read16((kk ? xb1 : xb0) + bo + mt * 2048);
#pragma unroll
      for (int nt = 0; nt < 4; ++nt)
        wfr[nt] = cat8(tr_read(wb[nt] + bo + kk * 8192), tr_read(wb[nt] + bo + kk * 8192 + 1024));
#pragma unroll
      for (int mt = 0; mt < 4; ++mt)
#pragma unroll
        for (int nt = 0; nt < 4; ++nt)
          acc[mt][nt] = VT ? MFMA16(xf[mt], wfr[nt], acc[mt][nt]) : MFMA16(wfr[nt], xf[mt], acc[mt][nt]);
    }
  }
  if (!VT) {
    const int part = n0 >= DM ? 1 : 0;
    const float sc = part == 0 ? 0.22360679774997896f * 1.4426950408889634f : 1.0f;
    char* base = ws + (part ? WS_KF : WS_QF);
#pragma unroll
    for (int nt = 0; nt < 4; ++nt) {
      const int nn4 = (n0 - part * DM + wn * 64 + nt * 16) / 4 + g;
      const int head = nn4 / 5, dg = nn4 - head * 5;
      const int coff = dg < 4 ? (dg >> 1) * 512 + (dg & 1) * 8 : 1024;
      const int cstr = dg < 4 ? 16 : 8;
#pragma unroll
      for (int mt = 0; mt < 4; ++mt) {
        const int s = m0 + wm * 64 + mt * 16 + r16;
        u32x2 o;
        o[0] = pk_bf16(acc[mt][nt][0] * sc, acc[mt][nt][1] * sc);
        o[1] = pk_bf16(acc[mt][nt][2] * sc, acc[mt][nt][3] * sc);
        *(u32x2*)(base + (size_t)head * QF_HEAD + (s >> 5) * 1280 + coff + (s & 31) * cstr) = o;
      }
    }
  } else {
    char* base = ws + WS_VF;
#pragma unroll
    for (int nt = 0; nt < 4; ++nt) {
      const int nn = n0 - 2 * DM + wn * 64 + nt * 16 + r16;
      const int head = nn / 20, d = nn - head * 20;
#pragma unroll
      for (int mt = 0; mt < 4; ++mt) {
        const int sg = (m0 + wm * 64) / 16 + mt;
        u32x2 o;
        o[0] = pk_bf16(acc[mt][nt][0], acc[mt][nt][1]);
        o[1] = pk_bf16(acc[mt][nt][2], acc[mt][nt][3]);
        char* dst = base + (size_t)head * VF_HEAD + sg * 672 + (g & 1) * 336 + d * 16 + (g >> 1) * 8;
        *(u32x2*)dst = o;
        if (d == 19) *(u32x2*)(dst + 16) = (u32x2){0x3f803f80u, 0x3f803f80u};
      }
    }
  }
}

__global__ __launch_bounds__(256, 1) void k1(const u16* __restrict__ x, const u16* __restrict__ wqkv,
                                             const u16* __restrict__ ak, const u16* __restrict__ av,
                                             const u16* __restrict__ shk, char* __restrict__ ws) {
  extern __shared__ __attribute__((aligned(16))) char smem[];
  const unsigned lds0 = lds_addr(smem);
  const int tid = threadIdx.x, lane = tid & 63, wave = __builtin_amdgcn_readfirstlane(tid >> 6);
  const int xcd = blockIdx.x & 7, idx = blockIdx.x >> 3;
  u16* ut = (u16*)(ws + WS_UT);
  const unsigned a1lds = lds0 + K1_LDS_A1 + wave * 2048;
  if (idx >= 30) {
    const int fw = ((idx - 30) * 8 + xcd) * 4 + wave;
    for (int i = 0; i < 5; ++i) {
      A1Regs r;
      const int tile = 960 + fw * 5 + i;
      a1_issue(r, tile, lane, ak, av, shk);
      a1_finish<0>(r, tile, lane, a1lds, ut);
    }
    return;
  }
  const int gt = (xcd & 1) * 30 + idx;
  const int tm = (xcd >> 1) * 4 + (gt & 3), tn = gt >> 2;
  const int m0 = tm * 128, n0 = tn * 128;
  const int a1tile = (xcd * 30 + idx) * 4 + wave;
  A1Regs r;
  a1_issue(r, a1tile, lane, ak, av, shk);
  const unsigned xoff = (unsigned)((tid >> 3) * DM * 2 + (((tid & 7) ^ ((tid >> 4) & 7)) * 16));
  const unsigned wsw = (unsigned)(((tid >> 4) & 3) | (((tid >> 7) & 1) << 2));
  const unsigned woff = (unsigned)((tid >> 4) * NQKV * 2 + (((unsigned)(tid & 15) ^ (wsw << 1)) * 16));
  {
    const char* xg = (const char*)(x + (size_t)m0 * DM);
    const char* wg = (const char*)(wqkv + n0);
#pragma unroll
    for (int t = 0; t < 4; ++t) {
      const unsigned buf = lds0 + t * K1_STAGE + wave * 1024;
#pragma unroll
      for (int i = 0; i < 4; ++i) glds16(xoff, xg + (size_t)i * 32 * DM * 2 + t * 128, buf + i * 4096);
#pragma unroll
      for (int i = 0; i < 4; ++i) glds16(woff, wg + (size_t)(t * 64 + i * 16) * NQKV * 2, buf + 16384 + i * 4096);
    }
  }
  a1_finish<32>(r, a1tile, lane, a1lds, ut);
  if (tn >= 10) k1_gemm<true>(lds0, m0, n0, tid, x, wqkv, ws, xoff, woff);
  else k1_gemm<false>(lds0, m0, n0, tid, x, wqkv, ws, xoff, woff);
}

constexpr int CV_COPY = 4160;
constexpr int CV_U = 4 * CV_COPY;
constexpr int CV_CH = CV_U + 4608;
constexpr int AT_V = 15360;
constexpr int K2_LDS = 2 * CV_CH;
constexpr int K2_NCONV = 320, K2_NATT = 512;

DI void k2_conv(unsigned lds0, int cb, int tid, const u16* __restrict__ ssmk, const u16* __restrict__ ssmd,
                char* __restrict__ ws) {
  const int lane = tid & 63, wave = __builtin_amdgcn_readfirstlane(tid >> 6);
  const int chl = wave >> 1, wh = wave & 1;
  const int ch = cb * 2 + chl;
  const unsigned reg = lds0 + chl * CV_CH;
  const int t128 = tid & 127;
  const u16* hrow = ssmk + (size_t)ch * SEQ;
  const u16* urow = (const u16*)(ws + WS_UT) + (size_t)ch * SEQ;
  u32x4 hc[2], uc[2]; u32x2 hn[2];
#pragma unroll
  for (int i = 0; i < 2; ++i) {
    const int tt = t128 + i * 128;
    hc[i] = *(const u32x4*)(hrow + tt * 8);
    hn[i] = (u32x2){0u, 0u};
    if (tt < 255) hn[i] = *(const u32x2*)(hrow + tt * 8 + 8);
    uc[i] = *(const u32x4*)(urow + tt * 8);
  }
  const float dco = bf_lo((unsigned)ssmd[ch]);
#pragma unroll
  for (int i = 0; i < 2; ++i) {
    const int tt = t128 + i * 128;
    const unsigned d0 = hc[i][0], d1 = hc[i][1], d2 = hc[i][2], d3 = hc[i][3], d4 = hn[i][0], d5 = hn[i][1];
    const unsigned a0 = __builtin_amdgcn_alignbit(d1, d0, 16), a1 = __builtin_amdgcn_alignbit(d2, d1, 16),
                   a2 = __builtin_amdgcn_alignbit(d3, d2, 16), a3 = __builtin_amdgcn_alignbit(d4, d3, 16),
                   a4 = __builtin_amdgcn_alignbit(d5, d4, 16);
    const unsigned y = reg + (tt + 2) * 16;
    lds_write16(y, (u32x4){d0, d1, d2, d3});
    lds_write16(y + CV_COPY, (u32x4){a0, a1, a2, a3});
    lds_write16(y + 2 * CV_COPY, (u32x4){d1, d2, d3, d4});
    lds_write16(y + 3 * CV_COPY, (u32x4){a1, a2, a3, a4});
    lds_write16(reg + CV_U + tt * 16, uc[i]);
    if (tt == 0) {
      const u32x4 z = {0u, 0u, 0u, 0u};
      lds_write16(reg, z); lds_write16(reg + 16, z);
      lds_write16(reg + CV_COPY, z); lds_write16(reg + CV_COPY + 16, (u32x4){0u, 0u, 0u, d0 << 16});
      lds_write16(reg + 2 * CV_COPY, z); lds_write16(reg + 2 * CV_COPY + 16, (u32x4){0u, 0u, 0u, d0});
      lds_write16(reg + 3 * CV_COPY, z); lds_write16(reg + 3 * CV_COPY + 16, (u32x4){0u, 0u, d0 << 16, a0});
    }
  }
  if (t128 < 32) lds_write16(reg + CV_U + 4096 + t128 * 16, (u32x4){0u, 0u, 0u, 0u});
  __syncthreads();
  const int r16 = lane & 15, g = lane >> 4, q = r16 >> 2, p = lane & 3;
  const int cpy = (q + 1) & 3;
  const unsigned abase = reg + cpy * CV_COPY + 2 * (8 * g + 4 * p + (q == 3 ? 4 : 0));
  const unsigned bbase = reg + CV_U + 2 * (2032 - 16 * r16 + 8 * g);
  f32x4 acc[8];
#pragma unroll
  for (int i = 0; i < 8; ++i) acc[i] = (f32x4){0.f, 0.f, 0.f, 0.f};
#pragma unroll
  for (int bi = 0; bi < 4; ++bi) {
    const int b = wh * 4 + bi;
    const unsigned ab = abase + b * 64, bb = bbase + b * 64;
    s16x8 A[8], B[8];
#pragma unroll
    for (int a = 0; a < 8; ++a) {
      A[a] = cat8(tr_read(ab + a * 512), tr_read(ab + a * 512 + 8));
      B[a] = lds_read16(bb - a * 512);
    }
#pragma unroll
    for (int nt = 0; nt < 8; ++nt)
#pragma unroll
      for (int a = 0; a <= nt; ++a) acc[nt] = MFMA16(A[a], B[nt - a], acc[nt]);
  }
  __syncthreads();
  const unsigned xw = reg + wh * 4096 + lane * 16, xr = reg + (wh ^ 1) * 4096 + lane * 16;
  f32x4 keep[4];
#pragma unroll
  for (int i = 0; i < 4; ++i) {
    const f32x4 give = wh ? acc[i] : acc[4 + i];
    keep[i] = wh ? acc[4 + i] : acc[i];
    lds_write16(xw + i * 1024, __builtin_bit_cast(u32x4, give));
  }
  __syncthreads();
  u16* zrow = (u16*)(ws + WS_ZT) + (size_t)ch * SEQ;
#pragma unroll
  for (int i = 0; i < 4; ++i) {
    const f32x4 o = __builtin_bit_cast(f32x4, lds_read16(xr + i * 1024));
    const int s = (wh * 4 + i) * 256 + 16 * r16 + 4 * g;
    const u32x2 uu = __builtin_bit_cast(u32x2, lds_read8(reg + CV_U + 2 * (SEQ - 4 - s)));
    const float kv0 = bf_hi(uu[1]), kv1 = bf_lo(uu[1]), kv2 = bf_hi(uu[0]), kv3 = bf_lo(uu[0]);
    const float y0 = rbf(keep[i][0] + o[0]), y1 = rbf(keep[i][1] + o[1]), y2 = rbf(keep[i][2] + o[2]), y3 = rbf(keep[i][3] + o[3]);
    u32x2 z;
    z[0] = pk_bf16(y0 + rbf(kv0 * dco), y1 + rbf(kv1 * dco));
    z[1] = pk_bf16(y2 + rbf(kv2 * dco), y3 + rbf(kv3 * dco));
    *(u32x2*)(zrow + s) = z;
  }
}

DI s16x8 pack8(const f32x16& v, int o) {
  u32x4 r;
  r[0] = pk_bf16(v[o + 0], v[o + 1]); r[1] = pk_bf16(v[o + 2], v[o + 3]);
  r[2] = pk_bf16(v[o + 4], v[o + 5]); r[3] = pk_bf16(v[o + 6], v[o + 7]);
  return __builtin_bit_cast(s16x8, r);
}

DI void k2_attn(unsigned lds0, char* smem, int ab, int tid, char* __restrict__ ws) {
  const int lane = tid & 63, wave = __builtin_amdgcn_readfirstlane(tid >> 6);
  const int h = ab & 31, qblk = ab >> 5;
  const int kt0 = qblk * 4 - 8;
  const int skip = kt0 < 0 ? -kt0 : 0;
  {
    const char* kg = ws + WS_KF + (size_t)h * QF_HEAD + (ptrdiff_t)kt0 * 1280;
    const char* vg = ws + WS_VF + (size_t)h * VF_HEAD + (ptrdiff_t)kt0 * 1344;
    const int kb0 = skip * 1280, vb0 = skip * 1344;
    for (int o = kb0 + wave * 1024; o < 15360; o += 4096)
      if (o + lane * 16 < 15360)
        __builtin_amdgcn_global_load_lds((const __attribute__((address_space(1))) void*)(kg + o + lane * 16),
                                         (LAS void*)(smem + o), 16, 0, 0);
    for (int o = vb0 + wave * 1024; o < 16128; o += 4096)
      if (o + lane * 16 < 16128)
        __builtin_amdgcn_global_load_lds((const __attribute__((address_space(1))) void*)(vg + o + lane * 16),
                                         (LAS void*)(smem + AT_V + o), 16, 0, 0);
  }
  const int qt = qblk * 4 + wave;
  const int l32 = lane & 31, hh = lane >> 5;
  const char* qg = ws + WS_QF + (size_t)h * QF_HEAD + (size_t)qt * 1280;
  const s16x8 qlo = *(const s16x8*)(qg + lane * 16);
  s16x4 qhi = *(const s16x4*)(qg + 1024 + l32 * 8);
  if (hh) qhi = (s16x4){0, 0, 0, 0};
  asm volatile("s_waitcnt vmcnt(0)" ::: "memory");
  __syncthreads();
  const unsigned kb = lds0 + wave * 1280;
  f32x16 st[9];
  const f32x16 zero16 = {0.f, 0.f, 0.f, 0.f, 0.f, 0.f, 0.f, 0.f, 0.f, 0.f, 0.f, 0.f, 0.f, 0.f, 0.f, 0.f};
  const int tfirst = qt >= 8 ? 0 : 8 - qt;
#pragma unroll
  for (int t = 0; t < 9; ++t) {
    if (t >= tfirst) {
      const s16x8 klo = lds_read16(kb + t * 1280 + lane * 16);
      const s16x4 khi = lds_read8(kb + t * 1280 + 1024 + l32 * 8);
      st[t] = MFMA32(klo, qlo, zero16);
      st[t] = MFMA32K8(khi, qhi, st[t]);
    } else {
#pragma unroll
      for (int r = 0; r < 16; ++r) st[t][r] = -INFINITY;
    }
  }
#pragma unroll
  for (int r = 0; r < 16; ++r) {
    const int kk = (r & 3) + 8 * (r >> 2) + 4 * hh;
    st[8][r] = kk > l32 ? st[0][r] : st[8][r];
  }
  float m = st[8][0];
#pragma unroll
  for (int t = 1; t < 9; ++t)
#pragma unroll
    for (int r = (t == 8 ? 1 : 0); r < 16; ++r) m = __builtin_fmaxf(m, st[t][r]);
  {
    auto sw = __builtin_amdgcn_permlane32_swap(__float_as_uint(m), __float_as_uint(m), false, false);
    m = __builtin_fmaxf(__uint_as_float(sw[0]), __uint_as_float(sw[1]));
  }
  const int vrow = l32 <= 20 ? l32 : (l32 == 24 ? 20 : l32 - 16);
  const unsigned vb = lds0 + AT_V + wave * 1344 + hh * 336 + vrow * 16;
  f32x16 oacc = zero16;
#pragma unroll
  for (int t = 1; t < 8; ++t) {
    if (t >= tfirst) {
#pragma unroll
      for (int r = 0; r < 16; ++r) st[t][r] = __builtin_amdgcn_exp2f(st[t][r] - m);
      oacc = MFMA32(lds_read16(vb + t * 1344), pack8(st[t], 0), oacc);
      oacc = MFMA32(lds_read16(vb + t * 1344 + 672), pack8(st[t], 8), oacc);
    }
  }
  {
    f32x16 p0, p8;
#pragma unroll
    for (int r = 0; r < 16; ++r) {
      const int kk = (r & 3) + 8 * (r >> 2) + 4 * hh;
      const float e = __builtin_amdgcn_exp2f(st[8][r] - m);
      p0[r] = kk > l32 ? e : 0.f;
      p8[r] = kk > l32 ? 0.f : e;
    }
    oacc = MFMA32(lds_read16(vb + 8 * 1344), pack8(p8, 0), oacc);
    oacc = MFMA32(lds_read16(vb + 8 * 1344 + 672), pack8(p8, 8), oacc);
    if (tfirst == 0) {
      oacc = MFMA32(lds_read16(vb), pack8(p0, 0), oacc);
      oacc = MFMA32(lds_read16(vb + 672), pack8(p0, 8), oacc);
    }
  }
  const float l = hh ? oacc[8] : oacc[12];
  const float inv = __builtin_amdgcn_rcpf(l);
  u16* orow = (u16*)(ws + WS_OW) + (size_t)(qt * 32 + l32) * DM;
#pragma unroll
  for (int j = 0; j < 3; ++j) {
    if (j < 2 || !hh) {
      const int k4 = 5 * h + 2 * j + hh;
      const int pos = (k4 >> 3) * 32 + (k4 & 3) * 8 + ((k4 >> 2) & 1) * 4;
      u32x2 o;
      o[0] = pk_bf16(oacc[4 * j] * inv, oacc[4 * j + 1] * inv);
      o[1] = pk_bf16(oacc[4 * j + 2] * inv, oacc[4 * j + 3] * inv);
      *(u32x2*)(orow + pos) = o;
    }
  }
}

__global__ __launch_bounds__(256, 2) void k2(const u16* __restrict__ ssmk, const u16* __restrict__ ssmd,
                                             char* __restrict__ ws) {
  extern __shared__ __attribute__((aligned(16))) char smem[];
  const unsigned lds0 = lds_addr(smem);
  const int bid = blockIdx.x;
  const int row = bid >> 3, xcd = bid & 7;
  const int grp = row / 13, rr = row - grp * 13;
  if (rr < 5) k2_conv(lds0, (grp * 5 + rr) * 8 + xcd, threadIdx.x, ssmk, ssmd, ws);
  else k2_attn(lds0, smem, (grp * 8 + rr - 5) * 8 + xcd, threadIdx.x, ws);
}

constexpr int K3_STAGE = 16384 + 20480;
constexpr int K3_LDS_A3 = 4 * K3_STAGE;
constexpr int K3_LDS = K3_LDS_A3 + 4 * 2560;
constexpr int A3_NST = 5;

__global__ __launch_bounds__(256, 1) void k3(const u16* __restrict__ wo, const u16* __restrict__ aq,
                                             const char* __restrict__ ws, u16* __restrict__ out) {
  extern __shared__ __attribute__((aligned(16))) char smem[];
  const unsigned lds0 = lds_addr(smem);
  const int tid = threadIdx.x, lane = tid & 63, wave = __builtin_amdgcn_readfirstlane(tid >> 6);
  const int xcd = blockIdx.x & 7, idx = blockIdx.x >> 3;
  const int tm = xcd * 4 + (idx >> 3), tn = idx & 7;
  const int m0 = tm * 64, n0 = tn * 80;
  const int r16 = lane & 15, g = lane >> 4, q = r16 >> 2, p = lane & 3;
  const int tile = (xcd * 32 + idx) * 4 + wave;
  const int ts = tile >> 4, td = tile & 15;
  const int s0 = ts * 32, d0 = td * 40;
  const u16* zt = (const u16*)(ws + WS_ZT);
  u32x4 zc[3]; u32x2 qc[5];
#pragma unroll
  for (int i = 0; i < 3; ++i) {
    int it = i * 64 + lane; it = it < 160 ? it : 159;
    zc[i] = gload16_asm(zt + (size_t)(d0 + (it >> 2)) * SEQ + s0 + (it & 3) * 8);
  }
#pragma unroll
  for (int j = 0; j < 5; ++j) {
    const int bb = j * 4 + g;
    qc[j] = gload8_asm(aq + (size_t)(s0 + (bb & 1) * 16 + r16) * DM + d0 + (bb >> 1) * 4);
  }
  const unsigned ooff = (unsigned)((tid >> 4) * DM * 2 + (((tid & 15) ^ ((tid >> 4) & 15)) * 16));
  unsigned woff[5];
#pragma unroll
  for (int i = 0; i < 5; ++i) {
    const int L = i * 256 + tid, row = L / 10, chn = L - row * 10;
    woff[i] = (unsigned)(row * DM * 2 + chn * 16);
  }
  const char* og = ws + WS_OW + (size_t)m0 * DM * 2;
  const char* wg = (const char*)(wo + n0);
  auto stage = [&](int t) {
    const unsigned buf = lds0 + (t & 3) * K3_STAGE + wave * 1024;
#pragma unroll
    for (int i = 0; i < 4; ++i) glds16(ooff, og + (size_t)i * 16 * DM * 2 + t * 256, buf + i * 4096);
#pragma unroll
    for (int i = 0; i < 5; ++i) glds16(woff[i], wg + (size_t)t * 128 * DM * 2, buf + 16384 + i * 4096);
  };
  stage(0); stage(1); stage(2); stage(3);
  {
    asm volatile("s_waitcnt vmcnt(36)"
                 : "+v"(zc[0]), "+v"(zc[1]), "+v"(zc[2]), "+v"(qc[0]), "+v"(qc[1]), "+v"(qc[2]), "+v"(qc[3]), "+v"(qc[4])
                 :: "memory");
    const unsigned al = lds0 + K3_LDS_A3 + wave * 2560;
    lds_write16(al + lane * 16, zc[0]);
    lds_write16(al + 1024 + lane * 16, zc[1]);
    if (lane < 32) lds_write16(al + 2048 + lane * 16, zc[2]);
    asm volatile("s_waitcnt lgkmcnt(0)" ::: "memory");
#pragma unroll
    for (int j = 0; j < 5; ++j) {
      const int bb = j * 4 + g, sh = bb & 1, dq = bb >> 1;
      const u32x2 z = __builtin_bit_cast(u32x2, tr_read(al + (dq * 4 + q) * 64 + sh * 32 + p * 8));
      u32x2 o;
      o[0] = pk_bf16(bf_lo(qc[j][0]) * bf_lo(z[0]), bf_hi(qc[j][0]) * bf_hi(z[0]));
      o[1] = pk_bf16(bf_lo(qc[j][1]) * bf_lo(z[1]), bf_hi(qc[j][1]) * bf_hi(z[1]));
      gstore8_asm(out + (size_t)(s0 + sh * 16 + r16) * (2 * DM) + d0 + dq * 4, o);
    }
  }
  f32x4 acc[5];
#pragma unroll
  for (int i = 0; i < 5; ++i) acc[i] = (f32x4){0.f, 0.f, 0.f, 0.f};
  const unsigned orow = lds0 + (wave * 16 + r16) * 256;
  const unsigned wbase = lds0 + 16384 + (4 * g + q) * 160 + p * 8;
#pragma unroll
  for (int t = 0; t < 5; ++t) {
    if (t == 0) wait_vm<27 + A3_NST>();
    else if (t == 1) wait_vm<18 + A3_NST>();
    else if (t == 2) wait_vm<18 + A3_NST>();
    else if (t == 3) wait_vm<9 + A3_NST>();
    else wait_vm<0>();
    bar();
    if (t == 1) stage(4);
    const unsigned bo = (t & 3) * K3_STAGE;
#pragma unroll
    for (int kk = 0; kk < 4; ++kk) {
      const s16x8 of = lds_read16(orow + bo + (((unsigned)(kk * 4 + g) ^ (unsigned)r16) * 16));
#pragma unroll
      for (int nt = 0; nt < 5; ++nt) {
        const s16x8 wf = cat8(tr_read(wbase + bo + kk * 32 * 160 + nt * 32), tr_read(wbase + bo + (kk * 32 + 16) * 160 + nt * 32));
        acc[nt] = MFMA16(wf, of, acc[nt]);
      }
    }
  }
  u16* orow_g = out + (size_t)(m0 + wave * 16 + r16) * (2 * DM) + DM + n0 + 4 * g;
#pragma unroll
  for (int nt = 0; nt < 5; ++nt) {
    u32x2 o;
    o[0] = pk_bf16(acc[nt][0], acc[nt][1]);
    o[1] = pk_bf16(acc[nt][2], acc[nt][3]);
    *(u32x2*)(orow_g + nt * 16) = o;
  }
}

extern "C" void kernel_launch(void* const* d_in, const int* in_sizes, int n_in, void* d_out, int out_size,
                              void* d_ws, size_t ws_size, hipStream_t stream) {
  const u16* aq = (const u16*)d_in[0];
  const u16* ak = (const u16*)d_in[1];
  const u16* av = (const u16*)d_in[2];
  const u16* shk = (const u16*)d_in[3];
  const u16* ssmk = (const u16*)d_in[4];
  const u16* ssmd = (const u16*)d_in[5];
  const u16* x = (const u16*)d_in[6];
  const u16* wqkv = (const u16*)d_in[7];
  const u16* wo = (const u16*)d_in[8];
  char* ws = (char*)d_ws;
  (void)hipFuncSetAttribute((const void*)k1, hipFuncAttributeMaxDynamicSharedMemorySize, K1_LDS);
  (void)hipFuncSetAttribute((const void*)k3, hipFuncAttributeMaxDynamicSharedMemorySize, K3_LDS);
  k1<<<256, 256, K1_LDS, stream>>>(x, wqkv, ak, av, shk, ws);
  k2<<<K2_NCONV + K2_NATT, 256, K2_LDS, stream>>>(ssmk, ssmd, ws);
  k3<<<256, 256, K3_LDS, stream>>>(wo, aq, ws, (u16*)d_out);
}
```

```cpp
#include <hip/hip_runtime.h>
#include <hip/hip_bf16.h>
#include <stdint.h>

typedef unsigned short u16;
typedef short s16x4 __attribute__((ext_vector_type(4)));
typedef short s16x8 __attribute__((ext_vector_type(8)));
typedef float f32x2 __attribute__((ext_vector_type(2)));
typedef float f32x4 __attribute__((ext_vector_type(4)));
typedef float f32x16 __attribute__((ext_vector_type(16)));
typedef unsigned u32x2 __attribute__((ext_vector_type(2)));
typedef unsigned u32x4 __attribute__((ext_vector_type(4)));
typedef __bf16 bf16x2 __attribute__((ext_vector_type(2)));
#define LAS __attribute__((address_space(3)))
#define DI __device__ __forceinline__

constexpr int SEQ = 2048, DM = 640, NQKV = 1920, NH = 32;
constexpr size_t QF_HEAD = 64 * 1280;
constexpr size_t VF_HEAD = 128 * 672;
constexpr size_t WS_QF = 0;
constexpr size_t WS_KF = WS_QF + NH * QF_HEAD;
constexpr size_t WS_VF = WS_KF + NH * QF_HEAD;
constexpr size_t WS_UT = WS_VF + NH * VF_HEAD + 4096;
constexpr size_t WS_ZT = WS_UT + (size_t)DM * SEQ * 2;
constexpr size_t WS_OW = WS_ZT + (size_t)DM * SEQ * 2;
constexpr size_t WS_WP = WS_OW + (size_t)DM * SEQ * 2;

DI unsigned pk_bf16(float lo, float hi) {
  f32x2 f = {lo, hi};
  return __builtin_bit_cast(unsigned, __builtin_convertvector(f, bf16x2));
}
DI float bf_lo(unsigned u) { return __uint_as_float(u << 16); }
DI float bf_hi(unsigned u) { return __uint_as_float(u & 0xffff0000u); }
DI float rbf(float x) { return bf_lo(pk_bf16(x, 0.f)); }

DI unsigned lds_addr(const void* p) { return (unsigned)(uintptr_t)(LAS const char*)p; }

DI void glds16(unsigned voff, const void* sbase, unsigned lds_dst) {
  unsigned keep;
  asm volatile("s_mov_b32 %0, m0\n\ts_mov_b32 m0, %3\n\ts_nop 0\n\tglobal_load_lds_dwordx4 %1, %2\n\ts_mov_b32 m0, %0"
               : "=&s"(keep) : "v"(voff), "s"(sbase), "s"(lds_dst) : "memory");
}
DI void glds16nt(unsigned voff, const void* sbase, unsigned lds_dst) {
  unsigned keep;
  asm volatile("s_mov_b32 %0, m0\n\ts_mov_b32 m0, %3\n\ts_nop 0\n\tglobal_load_lds_dwordx4 %1, %2 nt\n\ts_mov_b32 m0, %0"
               : "=&s"(keep) : "v"(voff), "s"(sbase), "s"(lds_dst) : "memory");
}
#define glds16g glds16
DI void gtouch(unsigned voff, const void* sbase, unsigned lds_dst) {
  unsigned keep;
  asm volatile("s_mov_b32 %0, m0\n\ts_mov_b32 m0, %3\n\ts_nop 0\n\tglobal_load_lds_dword %1, %2\n\ts_mov_b32 m0, %0"
               : "=&s"(keep) : "v"(voff), "s"(sbase), "s"(lds_dst) : "memory");
}
DI float dot2bf(unsigned a, unsigned b, float c) {
  return __builtin_amdgcn_fdot2_f32_bf16(__builtin_bit_cast(bf16x2, a), __builtin_bit_cast(bf16x2, b), c, false);
}
DI u32x4 gload16_asm(const void* p) {
  u32x4 r; asm volatile("global_load_dwordx4 %0, %1, off" : "=v"(r) : "v"(p) : "memory"); return r;
}
DI u32x2 gload8_asm(const void* p) {
  u32x2 r; asm volatile("global_load_dwordx2 %0, %1, off" : "=v"(r) : "v"(p) : "memory"); return r;
}
#define STPOL ""
DI void gstore16_asm(void* p, u32x4 v) {
  asm volatile("global_store_dwordx4 %0, %1, off " STPOL "\n\ts_nop 1" :: "v"(p), "v"(v) : "memory");
}
DI void gstore8_asm(void* p, u32x2 v) {
  asm volatile("global_store_dwordx2 %0, %1, off " STPOL :: "v"(p), "v"(v) : "memory");
}
template <int N> DI void wait_vm() { asm volatile("s_waitcnt vmcnt(%0)" :: "n"(N) : "memory"); }
DI void bar() { asm volatile("s_waitcnt lgkmcnt(0)\n\ts_barrier" ::: "memory"); }
DI s16x4 tr_read(unsigned a) {
  return __builtin_amdgcn_ds_read_tr16_b64_v4i16((LAS s16x4*)(uintptr_t)a);
}
DI s16x8 lds_read16(unsigned a) { return *(LAS const s16x8*)(uintptr_t)a; }
DI s16x4 lds_read8(unsigned a) { return *(LAS const s16x4*)(uintptr_t)a; }
DI void lds_write16(unsigned a, u32x4 v) { *(LAS u32x4*)(uintptr_t)a = v; }
DI s16x8 cat8(s16x4 a, s16x4 b) { return __builtin_shufflevector(a, b, 0, 1, 2, 3, 4, 5, 6, 7); }
#define MFMA16(a, b, c) __builtin_amdgcn_mfma_f32_16x16x32_bf16((a), (b), (c), 0, 0, 0)
#define MFMA32(a, b, c) __builtin_amdgcn_mfma_f32_32x32x16_bf16((a), (b), (c), 0, 0, 0)

struct Diag {};
#define STAMP(i) do {} while (0)
#define DIAG_INIT() Diag dg
#define DIAG_DUMP(kid) do {} while (0)

#define SBAR() __builtin_amdgcn_sched_barrier(0)
DI unsigned lds_ld32(unsigned a) { return *(volatile LAS unsigned*)(uintptr_t)a; }
DI void lds_st32(unsigned a, unsigned v) { *(volatile LAS unsigned*)(uintptr_t)a = v; }
DI void spin_ge(unsigned a, unsigned want) {
  while ((unsigned)__builtin_amdgcn_readfirstlane(lds_ld32(a)) < want) __builtin_amdgcn_s_sleep(1);
  asm volatile("" ::: "memory");
}
DI void spin_ge4(unsigned a, unsigned want) {
  for (;;) {
    const u32x4 v = *(volatile LAS u32x4*)(uintptr_t)a;
    const unsigned m = min(min(v[0], v[1]), min(v[2], v[3]));
    if ((unsigned)__builtin_amdgcn_readfirstlane(m) >= want) break;
    __builtin_amdgcn_s_sleep(1);
  }
  asm volatile("" ::: "memory");
}
constexpr int CT_S0 = 0, CT_FULL = 16, CT_FREE = 16 + 8 * 16, CT_BYTES = 16 + 16 * 16;

constexpr int K1_STAGE = 32768;
constexpr int A1_K = 0, A1_V = 3072, A1_W = 5120, A1_O = 5376, A1_BYTES = 7424;
constexpr int K1_A1 = 4 * K1_STAGE;
constexpr int K1_CT = K1_A1 + 4 * A1_BYTES;
constexpr int K1_LDS = K1_CT + 512;
constexpr int A1_NLD = 6;

DI void a1_issue(unsigned lds, int qtile, int lane, const u16* ak, const u16* av, const u16* shk) {
  const int ts = qtile / 20, td = qtile - ts * 20;
  const int s0 = ts * 32, d0 = td * 32;
  const int lr = lane >> 2, lc = (lane & 3) * 16;
#pragma unroll
  for (int i = 0; i < 3; ++i) {
    int r = s0 - 3 + i * 16 + lr; r = r < 0 ? 0 : (r > SEQ - 1 ? SEQ - 1 : r);
    if (i < 2 || lane < 12) glds16nt((unsigned)(r * DM * 2 + lc), (const char*)ak + d0 * 2, lds + A1_K + i * 1024);
  }
#pragma unroll
  for (int i = 0; i < 2; ++i)
    glds16nt((unsigned)(lr * DM * 2 + lc), (const char*)av + ((size_t)(s0 + i * 16) * DM + d0) * 2, lds + A1_V + i * 1024);
  if (lane < 16) glds16((unsigned)(lane * 16), (const char*)shk + d0 * 8, lds + A1_W);
}
DI void a1_finish(unsigned lds, int qtile, int lane, u16* ut) {
  const int ts = qtile / 20, td = qtile - ts * 20;
  const int c = lane & 3;
  u32x4 wt[4];
#pragma unroll
  for (int i = 0; i < 4; ++i) wt[i] = __builtin_bit_cast(u32x4, lds_read16(lds + A1_W + c * 64 + i * 16));
#pragma unroll
  for (int j = 0; j < 2; ++j) {
    const int r = j * 16 + (lane >> 2);
    const unsigned ka = lds + A1_K + (r + 3) * 64 + c * 16;
    u32x4 k0 = __builtin_bit_cast(u32x4, lds_read16(ka)), k1 = __builtin_bit_cast(u32x4, lds_read16(ka - 64));
    u32x4 k2 = __builtin_bit_cast(u32x4, lds_read16(ka - 128)), k3 = __builtin_bit_cast(u32x4, lds_read16(ka - 192));
    const u32x4 vv = __builtin_bit_cast(u32x4, lds_read16(lds + A1_V + r * 64 + c * 16));
    if (j == 0 && ts == 0) {
      const u32x4 z = {0u, 0u, 0u, 0u};
      if (r < 1) k1 = z;
      if (r < 2) k2 = z;
      if (r < 3) k3 = z;
    }
    u32x4 o;
#pragma unroll
    for (int i = 0; i < 4; ++i) {
      const unsigned pa0 = __builtin_amdgcn_perm(k1[i], k0[i], 0x05040100u), pb0 = __builtin_amdgcn_perm(k3[i], k2[i], 0x05040100u);
      const unsigned pa1 = __builtin_amdgcn_perm(k1[i], k0[i], 0x07060302u), pb1 = __builtin_amdgcn_perm(k3[i], k2[i], 0x07060302u);
      const float e0 = dot2bf(pb0, wt[i][1], dot2bf(pa0, wt[i][0], 0.f));
      const float e1 = dot2bf(pb1, wt[i][3], dot2bf(pa1, wt[i][2], 0.f));
      o[i] = pk_bf16(e0 * bf_lo(vv[i]), e1 * bf_hi(vv[i]));
    }
    lds_write16(lds + A1_O + (31 - r) * 64 + c * 16, o);
  }
  asm volatile("s_waitcnt lgkmcnt(0)" ::: "memory");
  const int gg = lane >> 4, q = (lane & 15) >> 2, p = lane & 3;
#pragma unroll
  for (int j = 0; j < 2; ++j) {
    const unsigned ra = lds + A1_O + (gg * 8 + q) * 64 + j * 32 + p * 8;
    const s16x4 t0 = tr_read(ra), t1 = tr_read(ra + 256);
    const int d = td * 32 + j * 16 + (lane & 15);
    gstore16_asm(ut + (size_t)d * SEQ + (SEQ - 32 - ts * 32) + gg * 8, __builtin_bit_cast(u32x4, cat8(t0, t1)));
  }
  asm volatile("s_waitcnt lgkmcnt(0)" ::: "memory");
}

template <bool VT>
DI void k1_gemm(unsigned lds0, int m0, int n0, int tid, char* ws, Diag& dg) {
  const int lane = tid & 63, wave = __builtin_amdgcn_readfirstlane(tid >> 6);
  const int wm = wave >> 1, wn = wave & 1;
  const int l32 = lane & 31, hh = lane >> 5, gg = lane >> 4, q = (lane & 15) >> 2, p = lane & 3;
  const unsigned ct = lds0 + K1_CT;
  f32x16 acc[2][2];
#pragma unroll
  for (int i = 0; i < 2; ++i)
#pragma unroll
    for (int j = 0; j < 2; ++j)
#pragma unroll
      for (int r = 0; r < 16; ++r) acc[i][j][r] = 0.f;
  unsigned xb[4];
#pragma unroll
  for (int ks = 0; ks < 4; ++ks) xb[ks] = lds0 + (wm * 64 + l32) * 128 + (((unsigned)(2 * ks + hh) ^ (unsigned)((l32 >> 1) & 7)) * 16);
  unsigned wb[2];
#pragma unroll
  for (int nt = 0; nt < 2; ++nt)
    wb[nt] = lds0 + 16384 + (8 * hh + q) * 256 + (((unsigned)(wn * 4 + 2 * nt + (gg & 1)) ^ (unsigned)(q << 1)) * 32) + p * 8;
  s16x8 xa[2], wa[2], xc[2], wc[2];
#define K1_LOADF(XF, WF, bo_, ks) do { \
    _Pragma("unroll") for (int i_ = 0; i_ < 2; ++i_) { \
      WF[i_] = cat8(tr_read(wb[i_] + (bo_) + (ks) * 4096), tr_read(wb[i_] + (bo_) + (ks) * 4096 + 1024)); \
      XF[i_] = lds_read16(xb[ks] + (bo_) + i_ * 4096); } } while (0)
#define K1_MM(XF, WF) do { _Pragma("unroll") for (int mt = 0; mt < 2; ++mt) _Pragma("unroll") for (int nt = 0; nt < 2; ++nt) \
      acc[mt][nt] = VT ? MFMA32(XF[mt], WF[nt], acc[mt][nt]) : MFMA32(WF[nt], XF[mt], acc[mt][nt]); } while (0)
#define K1_ILV() do { _Pragma("unroll") for (int i_ = 0; i_ < 3; ++i_) { \
      __builtin_amdgcn_sched_group_barrier(0x008, 1, 0); __builtin_amdgcn_sched_group_barrier(0x100, 3, 0); } \
      __builtin_amdgcn_sched_group_barrier(0x008, 1, 0); } while (0)
  STAMP(2);
  K1_LOADF(xa, wa, 0, 0);
#pragma nounroll
  for (int t = 0; t < 10; ++t) {
    const unsigned bo = (t & 3) * K1_STAGE;
    SBAR();
    K1_LOADF(xc, wc, bo, 1); K1_MM(xa, wa); K1_ILV();
    SBAR();
    K1_LOADF(xa, wa, bo, 2); K1_MM(xc, wc); K1_ILV();
    SBAR();
    const u32x4 fl4 = *(volatile LAS u32x4*)(uintptr_t)(ct + CT_FULL + ((t + 1) & 3) * 16);
    K1_LOADF(xc, wc, bo, 3);
    K1_MM(xa, wa); K1_ILV();
    SBAR();
    asm volatile("s_waitcnt lgkmcnt(0)" ::: "memory");
    const unsigned flu = (unsigned)__builtin_amdgcn_readfirstlane(min(min(fl4[0], fl4[1]), min(fl4[2], fl4[3])));
    SBAR();
    if (lane == 0) lds_st32(ct + CT_FREE + (t & 3) * 16 + wave * 4, (unsigned)(t + 1));
    if (t == 4) STAMP(4);
    if (t < 9 && flu < (unsigned)(t + 2)) spin_ge4(ct + CT_FULL + ((t + 1) & 3) * 16, (unsigned)(t + 2));
    asm volatile("" ::: "memory");
    SBAR();
    K1_LOADF(xa, wa, ((t + 1) & 3) * K1_STAGE, 0);
    K1_MM(xc, wc); K1_ILV();
  }
  SBAR();
  STAMP(5);
  if (!VT) {
    const int part = n0 >= DM ? 1 : 0;
    const float sc = part == 0 ? 0.22360679774997896f * 1.4426950408889634f : 1.0f;
    char* base = ws + (part ? WS_KF : WS_QF) + (size_t)((m0 + wm * 64) >> 5) * 1280;
#pragma unroll
    for (int nt = 0; nt < 2; ++nt)
#pragma unroll
      for (int u = 0; u < 4; ++u) {
        const int nn4 = (n0 - part * DM + wn * 64 + nt * 32) / 4 + 2 * u + hh;
        const int head = nn4 / 5, dgr = nn4 - head * 5;
        const int coff = head * (int)QF_HEAD + (dgr < 4 ? (dgr >> 1) * 512 + (dgr & 1) * 8 + l32 * 16 : 1024 + l32 * 8);
#pragma unroll
        for (int mt = 0; mt < 2; ++mt) {
          u32x2 o;
          o[0] = pk_bf16(acc[mt][nt][4 * u] * sc, acc[mt][nt][4 * u + 1] * sc);
          o[1] = pk_bf16(acc[mt][nt][4 * u + 2] * sc, acc[mt][nt][4 * u + 3] * sc);
          gstore8_asm(base + coff + mt * 1280, o);
        }
      }
  } else {
    char* base = ws + WS_VF + (size_t)((m0 + wm * 64) >> 4) * 672 + hh * 336;
#pragma unroll
    for (int nt = 0; nt < 2; ++nt) {
      const int nn = n0 - 2 * DM + wn * 64 + nt * 32 + l32;
      const int head = nn / 20, d = nn - head * 20;
      char* dst = base + head * (int)VF_HEAD + d * 16;
#pragma unroll
      for (int mt = 0; mt < 2; ++mt)
#pragma unroll
        for (int kg = 0; kg < 2; ++kg) {
          u32x4 o;
#pragma unroll
          for (int i = 0; i < 4; ++i) o[i] = pk_bf16(acc[mt][nt][8 * kg + 2 * i], acc[mt][nt][8 * kg + 2 * i + 1]);
          gstore16_asm(dst + (mt * 2 + kg) * 672, o);
        }
      if (d == 19) {
#pragma unroll
        for (int i = 0; i < 4; ++i) gstore16_asm(dst + i * 672 + 16, (u32x4){0x3f803f80u, 0x3f803f80u, 0x3f803f80u, 0x3f803f80u});
      }
    }
  }
}

#define K1_A1_DELAY 8
__global__ __launch_bounds__(768, 3) void k1(const u16* __restrict__ x, const u16* __restrict__ wqkv,
                                             const u16* __restrict__ ak, const u16* __restrict__ av,
                                             const u16* __restrict__ shk, const u16* __restrict__ wo,
                                             char* __restrict__ ws) {
  extern __shared__ __attribute__((aligned(16))) char smem[];
  const unsigned lds0 = lds_addr(smem);
  const int tid = threadIdx.x, lane = tid & 63, wave = __builtin_amdgcn_readfirstlane(tid >> 6);
  const int xcd = blockIdx.x & 7, idx = blockIdx.x >> 3;
  u16* ut = (u16*)(ws + WS_UT);
  DIAG_INIT();
  if (idx >= 30) {
    const int q0 = 960 + ((idx - 30) * 8 + xcd) * 20;
    const unsigned al = lds0 + wave * A1_BYTES, al2 = lds0 + (12 + wave) * A1_BYTES;
    a1_issue(al, q0 + wave, lane, ak, av, shk);
    if (wave < 8) {
      a1_issue(al2, q0 + 12 + wave, lane, ak, av, shk);
      wait_vm<A1_NLD>();
      a1_finish(al, q0 + wave, lane, ut);
      wait_vm<2>();
      a1_finish(al2, q0 + 12 + wave, lane, ut);
    } else {
      wait_vm<0>();
      a1_finish(al, q0 + wave, lane, ut);
      return;
    }
    DIAG_DUMP(0);
    return;
  }
  const int gt = (xcd & 1) * 30 + idx;
  const int tm = (xcd >> 1) * 4 + (gt & 3), tn = gt >> 2;
  const int m0 = tm * 128, n0 = tn * 128;
#define ROTG 5
  const int rot = ROTG >= 10 ? (((gt & 3) * 5 + 1) / 2 + 3 * tn) % 10 : ((((gt & 3) + tn) % ROTG) * 10) / ROTG;
  const unsigned ct = lds0 + K1_CT;
  const unsigned xo0 = (unsigned)((lane >> 3) * DM * 2 + (((lane & 7) ^ (lane >> 4)) * 16));
  const unsigned xo1 = (unsigned)((lane >> 3) * DM * 2 + (((lane & 7) ^ (4 + (lane >> 4))) * 16));
  const unsigned wof = (unsigned)((lane >> 4) * NQKV * 2 + (((lane & 15) ^ ((lane >> 4) << 2)) * 16));
  const char* xg = (const char*)(x + (size_t)m0 * DM);
  const char* wg = (const char*)(wqkv + n0);
#define K1_PIECE_X(t, kt, j) glds16g(((j) & 1) ? xo1 : xo0, xg + (size_t)(j) * 8 * DM * 2 + (kt) * 128, lds0 + ((t) & 3) * K1_STAGE + (j) * 1024)
#define K1_PIECE_W(t, kt, j) glds16g(wof, wg + (size_t)((kt) * 64 + (j) * 4) * NQKV * 2, lds0 + ((t) & 3) * K1_STAGE + 16384 + (j) * 1024)
  if (wave >= 4) {
    if (wave >= 8) {
      const int w = wave - 8;
      const unsigned al = lds0 + K1_A1 + w * A1_BYTES;
      const int a1q = (xcd * 30 + idx) * 4 + w;
      bar();
      __builtin_amdgcn_s_sleep(K1_A1_DELAY);
      a1_issue(al, a1q, lane, ak, av, shk);
      const int ci = (xcd * 30 + idx) * 256 + (tid - 512);
      if (ci < DM * 80) {
        const int kr = ci / 80, j = ci - kr * 80, tnn = j / 10, c = j - tnn * 10;
        const u32x4 wv = __builtin_nontemporal_load((const u32x4*)(wo + (size_t)ci * 8));
        gstore16_asm(ws + WS_WP + (size_t)tnn * (DM * 160) + kr * 160 + c * 16, wv);
      }
      wait_vm<0>();
      a1_finish(al, a1q, lane, ut);
      return;
    }
    const int w = wave - 4;
    __builtin_amdgcn_s_setprio(3);
    if (tid - 256 < CT_BYTES / 4) lds_st32(ct + (tid - 256) * 4, 0u);
#define K1_ST(t) do { const int kt_ = (t) + rot >= 10 ? (t) + rot - 10 : (t) + rot; \
      if ((t) >= 4) spin_ge4(ct + CT_FREE + ((t) & 3) * 16, (unsigned)((t) - 3)); \
      _Pragma("unroll") for (int i_ = 0; i_ < 4; ++i_) { K1_PIECE_X(t, kt_, w * 4 + i_); K1_PIECE_W(t, kt_, w * 4 + i_); } } while (0)
#define K1_SIG(t) do { if (lane == 0) lds_st32(ct + CT_FULL + ((t) & 3) * 16 + w * 4, (unsigned)((t) + 1)); } while (0)
    bar();
    K1_ST(1); K1_ST(2); K1_ST(3); wait_vm<16>(); K1_SIG(1);
    K1_ST(4); wait_vm<16>(); K1_SIG(2);
    K1_ST(5); wait_vm<16>(); K1_SIG(3);
    K1_ST(6); wait_vm<16>(); K1_SIG(4);
    K1_ST(7); wait_vm<16>(); K1_SIG(5);
    K1_ST(8); wait_vm<16>(); K1_SIG(6);
    K1_ST(9); wait_vm<16>(); K1_SIG(7);
    wait_vm<8>(); K1_SIG(8);
    wait_vm<0>(); K1_SIG(9);
    return;
  }
#pragma unroll
  for (int i = 0; i < 4; ++i) { K1_PIECE_X(0, rot, wave * 4 + i); K1_PIECE_W(0, rot, wave * 4 + i); }
  bar();
  wait_vm<0>();
  if (lane == 0) lds_st32(ct + CT_S0 + wave * 4, 1u);
  spin_ge4(ct + CT_S0, 1u);
  if (tn >= 10) k1_gemm<true>(lds0, m0, n0, tid, ws, dg);
  else k1_gemm<false>(lds0, m0, n0, tid, ws, dg);
  STAMP(6);
  DIAG_DUMP(0);
}

constexpr int CV_COPY = 2112;
constexpr int CV_U = 4 * CV_COPY;
constexpr int CV_CH = CV_U + 4608;
constexpr int AT_V = 15360;
constexpr int K2_LDS = AT_V + 16128;
constexpr int K2_NCONV = 320, K2_NATT = 512;

DI void cv_put(unsigned y, const u32x4& c, const u32x2& n) {
  const unsigned d0 = c[0], d1 = c[1], d2 = c[2], d3 = c[3], d4 = n[0], d5 = n[1];
  const unsigned a0 = __builtin_amdgcn_alignbit(d1, d0, 16), a1 = __builtin_amdgcn_alignbit(d2, d1, 16),
                 a2 = __builtin_amdgcn_alignbit(d3, d2, 16), a3 = __builtin_amdgcn_alignbit(d4, d3, 16),
                 a4 = __builtin_amdgcn_alignbit(d5, d4, 16);
  lds_write16(y, (u32x4){d0, d1, d2, d3});
  lds_write16(y + CV_COPY, (u32x4){a0, a1, a2, a3});
  lds_write16(y + 2 * CV_COPY, (u32x4){d1, d2, d3, d4});
  lds_write16(y + 3 * CV_COPY, (u32x4){a1, a2, a3, a4});
}

DI void k2_conv(unsigned lds0, int cb, int tid, const u16* __restrict__ ssmk, const u16* __restrict__ ssmd,
                char* __restrict__ ws, Diag& dg) {
  const int lane = tid & 63, wave = __builtin_amdgcn_readfirstlane(tid >> 6);
  const int wh = wave & 1, ch = cb * 2 + (wave >> 1), t128 = tid & 127;
  const unsigned reg = lds0 + (wave >> 1) * CV_CH;
  const u16* hrow = ssmk + (size_t)ch * SEQ;
  const char* urow = ws + WS_UT + (size_t)ch * SEQ * 2;
  u32x4 hc[2]; u32x2 hn[2];
#pragma unroll
  for (int i = 0; i < 2; ++i) {
    const int tt = t128 + i * 128;
    hc[i] = __builtin_nontemporal_load((const u32x4*)(hrow + tt * 8));
    hn[i] = *(const u32x2*)(hrow + (tt < 255 ? tt * 8 + 8 : 0));
  }
#pragma unroll
  for (int i = 0; i < 2; ++i) glds16((unsigned)(lane * 16), urow + (wh * 2 + i) * 1024, reg + CV_U + (wh * 2 + i) * 1024);
  const float dco = bf_lo((unsigned)ssmd[ch]);
  if (t128 < 32) lds_write16(reg + CV_U + 4096 + t128 * 16, (u32x4){0u, 0u, 0u, 0u});
  cv_put(reg + (t128 + 2) * 16, hc[0], hn[0]);
  if (t128 < 2) {
    const u32x4 z = {0u, 0u, 0u, 0u};
    u32x2 nx = {0u, 0u};
    const unsigned h0 = __builtin_amdgcn_readfirstlane(hc[0][0]), h1 = __builtin_amdgcn_readfirstlane(hc[0][1]);
    if (t128 == 1) nx = (u32x2){h0, h1};
    cv_put(reg + t128 * 16, z, nx);
  }
  STAMP(1);
  wait_vm<0>(); bar();
  STAMP(2);
  const int r16 = lane & 15, g = lane >> 4, q = r16 >> 2, p = lane & 3;
  const int cpy = (q + 1) & 3;
  const unsigned ab0 = reg + cpy * CV_COPY + 2 * (8 * g + 4 * p + (q == 3 ? 4 : 0)) + wh * 256;
  const unsigned bb0 = reg + CV_U + 2 * (2032 - 16 * r16 + 8 * g) + wh * 256 - 7 * 512;
  f32x4 acc[8];
#pragma unroll
  for (int i = 0; i < 8; ++i) acc[i] = (f32x4){0.f, 0.f, 0.f, 0.f};
#define CV_LDA(a_, ad_) cat8(tr_read((ad_) + (a_) * 512), tr_read((ad_) + (a_) * 512 + 8))
#define CV_LDB(c_, bd_) lds_read16((bd_) + (7 - (c_)) * 512)
  __builtin_amdgcn_s_setprio(0);
  {
    s16x8 A[4], B[8], An[4];
#pragma unroll
    for (int a = 0; a < 4; ++a) A[a] = CV_LDA(a, ab0);
#pragma unroll
    for (int c = 0; c < 8; ++c) B[c] = CV_LDB(c, bb0);
#pragma unroll
    for (int bi = 0; bi < 4; ++bi) {
      const unsigned an = ab0 + (bi + 1) * 64, bn = bb0 + (bi + 1) * 64;
#pragma unroll
      for (int c = 0; c < 8; ++c) {
        SBAR();
#pragma unroll
        for (int a = 0; a < 4; ++a)
          if (a + c < 8) acc[a + c] = MFMA16(A[a], B[c], acc[a + c]);
        if (bi < 3) {
          B[c] = CV_LDB(c, bn);
          if (c < 4) An[c] = CV_LDA(c, an);
        } else if (c < 4) B[c] = CV_LDB(c, bb0);
      }
#pragma unroll
      for (int a = 0; a < 4; ++a) A[a] = An[a];
    }
    SBAR();
    bar();
    if (t128 >= 126) {
#pragma unroll
      for (int c = 0; c < 4; ++c)
        lds_write16(reg + c * CV_COPY + (t128 - 126) * 16, __builtin_bit_cast(u32x4, lds_read16(reg + c * CV_COPY + (t128 + 2) * 16)));
    }
    asm volatile("s_waitcnt lgkmcnt(0)" ::: "memory");
    cv_put(reg + (t128 + 2) * 16, hc[1], hn[1]);
    bar();
#pragma unroll
    for (int a = 0; a < 4; ++a) A[a] = CV_LDA(a, ab0);
#pragma unroll
    for (int bi = 0; bi < 4; ++bi) {
      const unsigned an = ab0 + (bi + 1) * 64, bn = bb0 + (bi + 1) * 64;
      if (bi < 3) {
#pragma unroll
        for (int a = 0; a < 4; ++a) { An[a] = CV_LDA(a, an); B[4 + a] = CV_LDB(a, bn); }
      }
      SBAR();
#pragma unroll
      for (int c = 0; c < 4; ++c)
#pragma unroll
        for (int a = 0; a < 4; ++a)
          if (a + c < 4) acc[4 + a + c] = MFMA16(A[a], B[c], acc[4 + a + c]);
      SBAR();
#pragma unroll
      for (int a = 0; a < 4; ++a) { A[a] = An[a]; B[a] = B[4 + a]; }
    }
  }
  STAMP(3);
  bar();
  const unsigned xw = reg + wh * 4096 + lane * 16, xr = reg + (wh ^ 1) * 4096 + lane * 16;
  f32x4 keep[4];
  if (wh) {
#pragma unroll
    for (int i = 0; i < 4; ++i) { lds_write16(xw + i * 1024, __builtin_bit_cast(u32x4, acc[i])); keep[i] = acc[4 + i]; }
  } else {
#pragma unroll
    for (int i = 0; i < 4; ++i) { lds_write16(xw + i * 1024, __builtin_bit_cast(u32x4, acc[4 + i])); keep[i] = acc[i]; }
  }
  bar();
  STAMP(4);
  u16* zrow = (u16*)(ws + WS_ZT) + (size_t)ch * SEQ;
#pragma unroll
  for (int i = 0; i < 4; ++i) {
    const f32x4 o = __builtin_bit_cast(f32x4, lds_read16(xr + i * 1024));
    const int s = (wh * 4 + i) * 256 + 16 * r16 + 4 * g;
    const u32x2 uu = __builtin_bit_cast(u32x2, lds_read8(reg + CV_U + 2 * (SEQ - 4 - s)));
    const float kv0 = bf_hi(uu[1]), kv1 = bf_lo(uu[1]), kv2 = bf_hi(uu[0]), kv3 = bf_lo(uu[0]);
    const unsigned y01 = pk_bf16(keep[i][0] + o[0], keep[i][1] + o[1]), y23 = pk_bf16(keep[i][2] + o[2], keep[i][3] + o[3]);
    const unsigned e01 = pk_bf16(kv0 * dco, kv1 * dco), e23 = pk_bf16(kv2 * dco, kv3 * dco);
    u32x2 z;
    z[0] = pk_bf16(bf_lo(y01) + bf_lo(e01), bf_hi(y01) + bf_hi(e01));
    z[1] = pk_bf16(bf_lo(y23) + bf_lo(e23), bf_hi(y23) + bf_hi(e23));
    gstore8_asm(zrow + s, z);
  }
}

DI s16x8 pack8(const f32x16& v, int o) {
  u32x4 r;
  r[0] = pk_bf16(v[o + 0], v[o + 1]); r[1] = pk_bf16(v[o + 2], v[o + 3]);
  r[2] = pk_bf16(v[o + 4], v[o + 5]); r[3] = pk_bf16(v[o + 6], v[o + 7]);
  return __builtin_bit_cast(s16x8, r);
}
DI float max3f(float a, float b, float c) { return __builtin_fmaxf(__builtin_fmaxf(a, b), c); }
DI float max16(const f32x16& v, float m) {
  float a = max3f(v[0], v[1], m), b = max3f(v[2], v[3], v[4]);
  a = max3f(a, v[5], v[6]); b = max3f(b, v[7], v[8]);
  a = max3f(a, v[9], v[10]); b = max3f(b, v[11], v[12]);
  a = max3f(a, v[13], v[14]);
  return max3f(a, b, v[15]);
}
DI float xhalf_max(float m) {
  auto sw = __builtin_amdgcn_permlane32_swap(__float_as_uint(m), __float_as_uint(m), false, false);
  return __builtin_fmaxf(__uint_as_float(sw[0]), __uint_as_float(sw[1]));
}

#define AT_LMAX 65536.0f
template <bool FULL>
DI void k2_attn(unsigned lds0, int h, int qb, int tid, char* __restrict__ ws, Diag& dg) {
  const int lane = tid & 63, wave = __builtin_amdgcn_readfirstlane(tid >> 6);
  const int l32 = lane & 31, hh = lane >> 5;
  const int qt = qb * 4 + wave;
  const int tfirst = FULL ? 0 : 8 - qt;
  const char* qg = ws + WS_QF + (size_t)h * QF_HEAD + (size_t)qt * 1280;
  const int kt0 = qb * 4 - 8;
  const char* kg = ws + WS_KF + (size_t)h * QF_HEAD + (ptrdiff_t)kt0 * 1280;
  const char* vg = ws + WS_VF + (size_t)h * VF_HEAD + (ptrdiff_t)kt0 * 1344;
  const unsigned l16 = (unsigned)(lane * 16);
  u32x4 qlo_r = gload16_asm(qg + lane * 16);
  u32x2 qhi_r = gload8_asm(qg + 1024 + l32 * 8);
  if (FULL) {
#pragma unroll
    for (int i = 0; i < 4; ++i)
      if (i < 3 || wave < 3) glds16(l16, kg + (i * 4 + wave) * 1024, lds0 + (i * 4 + wave) * 1024);
#pragma unroll
    for (int i = 0; i < 4; ++i)
      if (i < 3 || wave < 3 || lane < 48) glds16(l16, vg + (i * 4 + wave) * 1024, lds0 + AT_V + (i * 4 + wave) * 1024);
  } else {
    for (int o = -kt0 * 1280 + wave * 1024; o < 15360; o += 4096)
      if (o + lane * 16 < 15360) glds16(l16, kg + o, lds0 + o);
    for (int o = -kt0 * 1344 + wave * 1024; o < 16128; o += 4096)
      if (o + lane * 16 < 16128) glds16(l16, vg + o, lds0 + AT_V + o);
  }
  const s16x4 z4 = {0, 0, 0, 0};
  STAMP(1);
  asm volatile("s_waitcnt vmcnt(%2)" : "+v"(qlo_r), "+v"(qhi_r) : "n"(FULL ? 4 : 0) : "memory");
  bar();
  s16x8 qlo = __builtin_bit_cast(s16x8, qlo_r);
  s16x4 qhi = __builtin_bit_cast(s16x4, qhi_r);
  if (hh) qhi = z4;
  s16x8 qhi8 = cat8(qhi, z4);
  STAMP(2);
  const f32x16 zero16 = {0.f, 0.f, 0.f, 0.f, 0.f, 0.f, 0.f, 0.f, 0.f, 0.f, 0.f, 0.f, 0.f, 0.f, 0.f, 0.f};
  const float ninf = -INFINITY;
  const f32x16 ninf16 = {ninf, ninf, ninf, ninf, ninf, ninf, ninf, ninf, ninf, ninf, ninf, ninf, ninf, ninf, ninf, ninf};
  const unsigned kb = lds0 + wave * 1280 + l16, kh = lds0 + wave * 1280 + 1024 + l32 * 8;
#define AT_QK(dst, t, cin) do { if (FULL || (t) >= tfirst) { dst = MFMA32(lds_read16(kb + (t) * 1280), qlo, cin); \
    dst = MFMA32(cat8(lds_read8(kh + (t) * 1280), z4), qhi8, dst); } else dst = ninf16; } while (0)
  const int vrow = l32 <= 20 ? l32 : (l32 == 24 ? 20 : l32 - 16);
  const unsigned vb = lds0 + AT_V + wave * 1344 + hh * 336 + vrow * 16;
#define AT_EXP(sv) do { _Pragma("unroll") for (int r = 0; r < 16; ++r) sv[r] = __builtin_amdgcn_exp2f(sv[r] - m); } while (0)
#define AT_PV(sv, t) do { oacc = MFMA32(lds_read16(vb + (t) * 1344), pack8(sv, 0), oacc); oacc = MFMA32(lds_read16(vb + (t) * 1344 + 672), pack8(sv, 8), oacc); } while (0)
  f32x16 oacc;
  bool exact = !FULL;
  if (FULL) {
    f32x16 sm, sa, sb;
    AT_QK(sm, 8, zero16); AT_QK(sa, 0, zero16);
    const int tq = l32 - 4 * hh;
    const unsigned wm = tq < 0 ? 0xffffffffu : (0xfffffffeu << tq);
    unsigned mk[16];
#pragma unroll
    for (int r = 0; r < 16; ++r) {
      mk[r] = (unsigned)__builtin_amdgcn_sbfe((int)wm, (r & 3) + 8 * (r >> 2), 1);
      sm[r] = __uint_as_float((mk[r] & __float_as_uint(sa[r])) | (~mk[r] & __float_as_uint(sm[r])));
    }
    const float m = xhalf_max(max16(sm, ninf));
    const float nm = -m;
    const f32x16 cneg = {nm, nm, nm, nm, nm, nm, nm, nm, nm, nm, nm, nm, nm, nm, nm, nm};
    AT_QK(sa, 1, cneg);
    AT_EXP(sm);
    u32x4 pl = __builtin_bit_cast(u32x4, pack8(sm, 0)), ph = __builtin_bit_cast(u32x4, pack8(sm, 8)), ql, qh;
#pragma unroll
    for (int i = 0; i < 4; ++i) {
      ql[i] = pl[i] & ((mk[2 * i] & 0xffffu) | (mk[2 * i + 1] & 0xffff0000u)); pl[i] ^= ql[i];
      qh[i] = ph[i] & ((mk[8 + 2 * i] & 0xffffu) | (mk[9 + 2 * i] & 0xffff0000u)); ph[i] ^= qh[i];
    }
    wait_vm<0>(); bar();
    STAMP(3);
    oacc = MFMA32(lds_read16(vb + 8 * 1344), __builtin_bit_cast(s16x8, pl), zero16);
    oacc = MFMA32(lds_read16(vb + 8 * 1344 + 672), __builtin_bit_cast(s16x8, ph), oacc);
    oacc = MFMA32(lds_read16(vb), __builtin_bit_cast(s16x8, ql), oacc);
    oacc = MFMA32(lds_read16(vb + 672), __builtin_bit_cast(s16x8, qh), oacc);
#define AT_EX0(sv) do { _Pragma("unroll") for (int r = 0; r < 16; ++r) sv[r] = __builtin_amdgcn_exp2f(sv[r]); } while (0)
    AT_QK(sb, 2, cneg); AT_EX0(sa); AT_PV(sa, 1);
    AT_QK(sa, 3, cneg); AT_EX0(sb); AT_PV(sb, 2);
    AT_QK(sb, 4, cneg); AT_EX0(sa); AT_PV(sa, 3);
    AT_QK(sa, 5, cneg); AT_EX0(sb); AT_PV(sb, 4);
    AT_QK(sb, 6, cneg); AT_EX0(sa); AT_PV(sa, 5);
    AT_QK(sa, 7, cneg); AT_EX0(sb); AT_PV(sb, 6);
    AT_EX0(sa); AT_PV(sa, 7);
    float big = max3f(__builtin_fabsf(oacc[0]), __builtin_fabsf(oacc[1]), __builtin_fabsf(oacc[2]));
    big = max3f(big, __builtin_fabsf(oacc[3]), __builtin_fabsf(oacc[4]));
    big = max3f(big, __builtin_fabsf(oacc[5]), __builtin_fabsf(oacc[6]));
    big = max3f(big, __builtin_fabsf(oacc[7]), __builtin_fabsf(oacc[8]));
    big = max3f(big, __builtin_fabsf(oacc[9]), __builtin_fabsf(oacc[10]));
    big = __builtin_fmaxf(big, __builtin_fabsf(oacc[11]));
    const float ls = hh ? oacc[8] : oacc[12];
    float osum = 0.f;
#pragma unroll
    for (int r = 0; r < 8; ++r) osum += oacc[r];
    const bool ok = (ls < AT_LMAX) && (big < 1e37f) && (osum == osum);
    exact = __builtin_amdgcn_ballot_w64(!ok) != 0ull;
  }
  if (exact) {
    asm volatile("" : "+v"(qlo), "+v"(qhi8) :: "memory");
    f32x16 sm, s1, s2, s3;
    {
      f32x16 s0;
      AT_QK(sm, 8, zero16); AT_QK(s0, 0, zero16);
      AT_QK(s1, 1, zero16); AT_QK(s2, 2, zero16); AT_QK(s3, 3, zero16);
#pragma unroll
      for (int r = 0; r < 16; ++r) {
        const int kk = (r & 3) + 8 * (r >> 2) + 4 * hh;
        sm[r] = kk > l32 ? s0[r] : sm[r];
      }
    }
    float m = max16(sm, ninf);
    m = max16(s1, m); m = max16(s2, m); m = max16(s3, m);
    m = xhalf_max(m);
    AT_EXP(sm);
    oacc = zero16;
    {
      f32x16 p0, p8;
#pragma unroll
      for (int r = 0; r < 16; ++r) {
        const int kk = (r & 3) + 8 * (r >> 2) + 4 * hh;
        p0[r] = kk > l32 ? sm[r] : 0.f;
        p8[r] = kk > l32 ? 0.f : sm[r];
      }
      AT_PV(p8, 8);
      if (FULL) AT_PV(p0, 0);
    }
    if (FULL || 1 >= tfirst) { AT_EXP(s1); AT_PV(s1, 1); }
    AT_QK(sm, 4, zero16); AT_QK(s1, 5, zero16);
    if (FULL || 2 >= tfirst) { AT_EXP(s2); AT_PV(s2, 2); }
    AT_QK(s2, 6, zero16);
    if (FULL || 3 >= tfirst) { AT_EXP(s3); AT_PV(s3, 3); }
    AT_QK(s3, 7, zero16);
    float m2 = max16(sm, m);
    m2 = max16(s1, m2); m2 = max16(s2, m2); m2 = max16(s3, m2);
    m2 = xhalf_max(m2);
    {
      const float alpha = __builtin_amdgcn_exp2f(m - m2);
#pragma unroll
      for (int r = 0; r < 13; ++r) oacc[r] *= alpha;
      m = m2;
    }
    if (FULL || 4 >= tfirst) { AT_EXP(sm); AT_PV(sm, 4); }
    if (FULL || 5 >= tfirst) { AT_EXP(s1); AT_PV(s1, 5); }
    if (FULL || 6 >= tfirst) { AT_EXP(s2); AT_PV(s2, 6); }
    if (FULL || 7 >= tfirst) { AT_EXP(s3); AT_PV(s3, 7); }
  }
  STAMP(4);
  const float l = hh ? oacc[8] : oacc[12];
  const float inv = __builtin_amdgcn_rcpf(l);
  char* oc = ws + WS_OW + ((size_t)qt * NH + h) * 1280;
  u32x4 o;
  o[0] = pk_bf16(oacc[0] * inv, oacc[1] * inv); o[1] = pk_bf16(oacc[2] * inv, oacc[3] * inv);
  o[2] = pk_bf16(oacc[4] * inv, oacc[5] * inv); o[3] = pk_bf16(oacc[6] * inv, oacc[7] * inv);
  gstore16_asm(oc + lane * 16, o);
  if (!hh) {
    u32x2 o2;
    o2[0] = pk_bf16(oacc[8] * inv, oacc[9] * inv); o2[1] = pk_bf16(oacc[10] * inv, oacc[11] * inv);
    gstore8_asm(oc + 1024 + l32 * 8, o2);
  }
}

__global__ __launch_bounds__(256, 4) void k2(const u16* __restrict__ ssmk, const u16* __restrict__ ssmd,
                                             char* __restrict__ ws) {
  extern __shared__ __attribute__((aligned(16))) char smem[];
  const unsigned lds0 = lds_addr(smem);
  const int bid = blockIdx.x;
  DIAG_INIT();
  if (bid < K2_NCONV) {
    __builtin_amdgcn_s_setprio(3);
    k2_conv(lds0, bid, threadIdx.x, ssmk, ssmd, ws, dg); STAMP(5);
  } else {
    __builtin_amdgcn_s_setprio(2);
    const int ab = bid - K2_NCONV;
    const int h = ab & 31, qb = 15 - (ab >> 5);
    if (qb >= 2) k2_attn<true>(lds0, h, qb, threadIdx.x, ws, dg);
    else k2_attn<false>(lds0, h, qb, threadIdx.x, ws, dg);
    STAMP(6);
  }
  DIAG_DUMP(1);
}

#define ROT3 1
#define K3_A3_DELAY 8
constexpr int K3_W = 10240;
constexpr int K3_STAGE = K3_W + 12800;
constexpr int K3_NBUF = 6;
constexpr int K3_A3 = K3_NBUF * K3_STAGE;
constexpr int K3_CT = K3_A3 + 10240;
constexpr int K3_LDS = K3_CT + 512;

__global__ __launch_bounds__(768, 3) void k3(const u16* __restrict__ aq,
                                             const char* __restrict__ ws, u16* __restrict__ out) {
  extern __shared__ __attribute__((aligned(16))) char smem[];
  const unsigned lds0 = lds_addr(smem);
  const int tid = threadIdx.x, lane = tid & 63, wave = __builtin_amdgcn_readfirstlane(tid >> 6);
  const int xcd = blockIdx.x & 7, idx = blockIdx.x >> 3;
  const int tm = xcd * 4 + (idx >> 3), tn = idx & 7;
  const int m0 = tm * 64, n0 = tn * 80;
  const unsigned ct = lds0 + K3_CT;
  const int r16 = lane & 15, g = lane >> 4, q = r16 >> 2, p = lane & 3;
  DIAG_INIT();
  if (wave >= 8) {
    const int w = wave - 8;
    bar();
    __builtin_amdgcn_s_sleep(K3_A3_DELAY);
    {
      const char* zg = ws + WS_ZT + ((size_t)n0 * SEQ + m0) * 2;
      const unsigned zo = (unsigned)((lane >> 3) * SEQ * 2 + (lane & 7) * 16);
      glds16(zo, zg + (size_t)(w * 8) * SEQ * 2, lds0 + K3_A3 + w * 1024);
      glds16(zo, zg + (size_t)(32 + w * 8) * SEQ * 2, lds0 + K3_A3 + 4096 + w * 1024);
      if (lane < 32) glds16(zo, zg + (size_t)(64 + w * 4) * SEQ * 2, lds0 + K3_A3 + 8192 + w * 512);
    }
    u32x4 qv[3];
#pragma unroll
    for (int j = 0; j < 3; ++j) {
      const int dc = j < 2 ? j * 4 + g : 8 + (g & 1);
      qv[j] = __builtin_nontemporal_load((const u32x4*)(aq + (size_t)(m0 + w * 16 + r16) * DM + n0 + dc * 8));
    }
    wait_vm<3>();
    if (lane == 0) lds_st32(ct + CT_S0 + w * 4, 1u);
    spin_ge4(ct + CT_S0, 1u);
#pragma unroll
    for (int j = 0; j < 3; ++j) {
      const int dc = j < 2 ? j * 4 + g : 8 + (g & 1);
      const unsigned za = lds0 + K3_A3 + (dc * 8 + q) * 128 + w * 32 + p * 8;
      const u32x4 z = __builtin_bit_cast(u32x4, cat8(tr_read(za), tr_read(za + 512)));
      u32x4 o;
#pragma unroll
      for (int i = 0; i < 4; ++i) o[i] = pk_bf16(bf_lo(qv[j][i]) * bf_lo(z[i]), bf_hi(qv[j][i]) * bf_hi(z[i]));
      if (j < 2 || g < 2) gstore16_asm(out + (size_t)(m0 + w * 16 + r16) * (2 * DM) + n0 + dc * 8, o);
    }
    return;
  }
  if (wave >= 4) {
    const int w = wave - 4;
    __builtin_amdgcn_s_setprio(3);
    const int rot = ROT3 >= 8 ? (2 * (idx >> 3) + tn) & 7 : ((((idx >> 3) + tn) % ROT3) * 8) / ROT3;
    const unsigned l16 = (unsigned)(lane * 16);
    const char* og = ws + WS_OW + (size_t)(m0 >> 5) * (NH * 1280);
    const char* wg = ws + WS_WP + (size_t)tn * (DM * 160);
#define K3_ST(t) do { const int kt_ = ((t) + rot) & 7; const unsigned lb_ = lds0 + ((t) % K3_NBUF) * K3_STAGE; \
      const char* os_ = og + kt_ * 5120; const char* ws_ = wg + kt_ * 12800; \
      if ((t) >= K3_NBUF) spin_ge4(ct + CT_FREE + ((t) - K3_NBUF) * 16, 1u); \
      _Pragma("unroll") for (int i_ = 0; i_ < 6; ++i_) { const int j_ = i_ * 4 + w; \
        const int go_ = j_ < 10 ? (j_ >= 5 ? NH * 1280 + (j_ - 5) * 1024 : j_ * 1024) : (j_ == 23 ? 12288 + 256 : (j_ - 10) * 1024); \
        const int lo_ = j_ < 10 ? j_ * 1024 : K3_W + go_; \
        const char* sb_ = (j_ < 10 ? os_ : ws_) + go_; \
        if (i_ < 5 || j_ < 22 || lane < 16) glds16(l16, sb_, lb_ + lo_); } } while (0)
#define K3_SIG(t) do { if (lane == 0) lds_st32(ct + CT_FULL + (t) * 16 + w * 4, 1u); } while (0)
    if (tid - 256 < CT_BYTES / 4) lds_st32(ct + (tid - 256) * 4, 0u);
    K3_ST(0);
    bar();
    K3_ST(1); K3_ST(2); wait_vm<12>(); K3_SIG(0);
    K3_ST(3); wait_vm<12>(); K3_SIG(1);
    K3_ST(4); wait_vm<12>(); K3_SIG(2);
    K3_ST(5); wait_vm<12>(); K3_SIG(3);
    K3_ST(6); wait_vm<12>(); K3_SIG(4);
    K3_ST(7); wait_vm<12>(); K3_SIG(5);
    wait_vm<6>(); K3_SIG(6);
    wait_vm<0>(); K3_SIG(7);
    return;
  }
  f32x4 acc[5], acs[5];
#pragma unroll
  for (int i = 0; i < 5; ++i) { acc[i] = (f32x4){0.f, 0.f, 0.f, 0.f}; acs[i] = (f32x4){0.f, 0.f, 0.f, 0.f}; }
  const unsigned orow = (unsigned)((wave & 1) * 16 + r16);
  const unsigned ob = lds0 + (wave >> 1) * 5120 + (g >> 1) * 1280 + (g & 1) * 512 + orow * 16;
  const unsigned os = lds0 + (wave >> 1) * 5120 + g * 1280 + 1024 + orow * 8;
  const unsigned wbp = lds0 + K3_W + (20 * (g >> 1) + 4 * (g & 1) + q) * 160 + p * 8;
  const unsigned wbs = lds0 + K3_W + (20 * g + 16 + q) * 160 + p * 8;
  struct Fr { s16x8 o0, o1, w0[5], w1[5]; s16x4 os, w2[5]; };
  Fr fa, fb;
#define K3_LOADF(F, t) do { const unsigned bo_ = ((t) % K3_NBUF) * K3_STAGE; \
    F.o0 = lds_read16(ob + bo_); F.o1 = lds_read16(ob + bo_ + 2560); F.os = lds_read8(os + bo_); \
    _Pragma("unroll") for (int nt = 0; nt < 5; ++nt) { \
      F.w0[nt] = cat8(tr_read(wbp + bo_ + nt * 32), tr_read(wbp + bo_ + 8 * 160 + nt * 32)); \
      F.w1[nt] = cat8(tr_read(wbp + bo_ + 40 * 160 + nt * 32), tr_read(wbp + bo_ + 48 * 160 + nt * 32)); \
      F.w2[nt] = tr_read(wbs + bo_ + nt * 32); } } while (0)
#define K3_MM(F) do { \
    _Pragma("unroll") for (int nt = 0; nt < 5; ++nt) acc[nt] = MFMA16(F.w0[nt], F.o0, acc[nt]); \
    _Pragma("unroll") for (int nt = 0; nt < 5; ++nt) acc[nt] = MFMA16(F.w1[nt], F.o1, acc[nt]); \
    _Pragma("unroll") for (int nt = 0; nt < 5; ++nt) acs[nt] = __builtin_amdgcn_mfma_f32_16x16x16bf16_1k(F.w2[nt], F.os, acs[nt], 0, 0, 0); } while (0)
#define K3_ILV() do { _Pragma("unroll") for (int i_ = 0; i_ < 14; ++i_) { \
      __builtin_amdgcn_sched_group_barrier(0x100, 2, 0); __builtin_amdgcn_sched_group_barrier(0x008, 1, 0); } \
      __builtin_amdgcn_sched_group_barrier(0x008, 1, 0); } while (0)
#define K3_WAIT(t) do { SBAR(); spin_ge4(ct + CT_FULL + (t) * 16, 1u); \
    if ((t) == 1 || (t) == 2) { if (lane == 0) lds_st32(ct + CT_FREE + ((t) - 1) * 16 + wave * 4, 1u); } SBAR(); } while (0)
  bar();
  K3_WAIT(0); STAMP(2);
  K3_LOADF(fa, 0);
  K3_WAIT(1); K3_LOADF(fb, 1); K3_MM(fa); K3_ILV();
  K3_WAIT(2); K3_LOADF(fa, 2); K3_MM(fb); K3_ILV();
  K3_WAIT(3); K3_LOADF(fb, 3); K3_MM(fa); K3_ILV();
  K3_WAIT(4); STAMP(4); K3_LOADF(fa, 4); K3_MM(fb); K3_ILV();
  K3_WAIT(5); K3_LOADF(fb, 5); K3_MM(fa); K3_ILV();
  K3_WAIT(6); K3_LOADF(fa, 6); K3_MM(fb); K3_ILV();
  K3_WAIT(7); K3_LOADF(fb, 7); K3_MM(fa); K3_ILV();
  SBAR();
  K3_MM(fb);
  SBAR();
  STAMP(5);
  u16* orow_g = out + (size_t)(m0 + wave * 16 + r16) * (2 * DM) + DM + n0 + 4 * g;
#pragma unroll
  for (int nt = 0; nt < 5; ++nt) {
    u32x2 o;
    o[0] = pk_bf16(acc[nt][0] + acs[nt][0], acc[nt][1] + acs[nt][1]);
    o[1] = pk_bf16(acc[nt][2] + acs[nt][2], acc[nt][3] + acs[nt][3]);
    gstore8_asm(orow_g + nt * 16, o);
  }
  STAMP(6);
  DIAG_DUMP(2);
}

extern "C" void kernel_launch(void* const* d_in, const int* in_sizes, int n_in, void* d_out, int out_size,
                              void* d_ws, size_t ws_size, hipStream_t stream) {
  const u16* aq = (const u16*)d_in[0];
  const u16* ak = (const u16*)d_in[1];
  const u16* av = (const u16*)d_in[2];
  const u16* shk = (const u16*)d_in[3];
  const u16* ssmk = (const u16*)d_in[4];
  const u16* ssmd = (const u16*)d_in[5];
  const u16* x = (const u16*)d_in[6];
  const u16* wqkv = (const u16*)d_in[7];
  const u16* wo = (const u16*)d_in[8];
  char* ws = (char*)d_ws;
  (void)hipFuncSetAttribute((const void*)k1, hipFuncAttributeMaxDynamicSharedMemorySize, K1_LDS);
  (void)hipFuncSetAttribute((const void*)k3, hipFuncAttributeMaxDynamicSharedMemorySize, K3_LDS);
  k1<<<256, 768, K1_LDS, stream>>>(x, wqkv, ak, av, shk, wo, ws);
  k2<<<K2_NCONV + K2_NATT, 256, K2_LDS, stream>>>(ssmk, ssmd, ws);
  k3<<<256, 768, K3_LDS, stream>>>(aq, ws, (u16*)d_out);
}
```

```cpp
#include <hip/hip_runtime.h>
#include <hip/hip_bf16.h>
#include <stdint.h>

typedef unsigned short u16;
typedef short s16x4 __attribute__((ext_vector_type(4)));
typedef short s16x8 __attribute__((ext_vector_type(8)));
typedef float f32x2 __attribute__((ext_vector_type(2)));
typedef float f32x4 __attribute__((ext_vector_type(4)));
typedef float f32x16 __attribute__((ext_vector_type(16)));
typedef unsigned u32x2 __attribute__((ext_vector_type(2)));
typedef unsigned u32x4 __attribute__((ext_vector_type(4)));
typedef __bf16 bf16x2 __attribute__((ext_vector_type(2)));
#define LAS __attribute__((address_space(3)))
#define DI __device__ __forceinline__

constexpr int SEQ = 2048, DM = 640, NQKV = 1920, NH = 32;
constexpr size_t WS_QF = 0;
constexpr size_t WS_KF = WS_QF + (size_t)DM * SEQ * 2;
constexpr size_t WS_VF = WS_KF + (size_t)DM * SEQ * 2;
constexpr size_t WS_UT = WS_VF + (size_t)DM * SEQ * 2;
constexpr size_t WS_ZT = WS_UT + (size_t)DM * SEQ * 2;
constexpr size_t WS_OW = WS_ZT + (size_t)DM * SEQ * 2;
typedef unsigned long long u64;
constexpr size_t WS_FL = WS_OW + (size_t)DM * SEQ * 2;
constexpr size_t FL_U = WS_FL + 256;
constexpr size_t FL_Q = FL_U + 20 * 64 * 8;
constexpr size_t FL_Z = FL_Q + 16 * 16 * 8;
constexpr size_t FL_O = FL_Z + 640 * 8;

DI unsigned pk_bf16(float lo, float hi) {
  f32x2 f = {lo, hi};
  return __builtin_bit_cast(unsigned, __builtin_convertvector(f, bf16x2));
}
DI float bf_lo(unsigned u) { return __uint_as_float(u << 16); }
DI float bf_hi(unsigned u) { return __uint_as_float(u & 0xffff0000u); }
DI float rbf(float x) { return bf_lo(pk_bf16(x, 0.f)); }

DI unsigned lds_addr(const void* p) { return (unsigned)(uintptr_t)(LAS const char*)p; }

DI void glds16(unsigned voff, const void* sbase, unsigned lds_dst) {
  unsigned keep;
  asm volatile("s_mov_b32 %0, m0\n\ts_mov_b32 m0, %3\n\ts_nop 0\n\tglobal_load_lds_dwordx4 %1, %2\n\ts_mov_b32 m0, %0"
               : "=&s"(keep) : "v"(voff), "s"(sbase), "s"(lds_dst) : "memory");
}
DI void glds16nt(unsigned voff, const void* sbase, unsigned lds_dst) {
  unsigned keep;
  asm volatile("s_mov_b32 %0, m0\n\ts_mov_b32 m0, %3\n\ts_nop 0\n\tglobal_load_lds_dwordx4 %1, %2 nt\n\ts_mov_b32 m0, %0"
               : "=&s"(keep) : "v"(voff), "s"(sbase), "s"(lds_dst) : "memory");
}
DI void glds16c(unsigned voff, const void* sbase, unsigned lds_dst) {
  unsigned keep;
  asm volatile("s_mov_b32 %0, m0\n\ts_mov_b32 m0, %3\n\ts_nop 0\n\tglobal_load_lds_dwordx4 %1, %2 sc1\n\ts_mov_b32 m0, %0"
               : "=&s"(keep) : "v"(voff), "s"(sbase), "s"(lds_dst) : "memory");
}
DI float dot2bf(unsigned a, unsigned b, float c) {
  return __builtin_amdgcn_fdot2_f32_bf16(__builtin_bit_cast(bf16x2, a), __builtin_bit_cast(bf16x2, b), c, false);
}
DI void gstore16c(void* p, u32x4 v) {
  asm volatile("global_store_dwordx4 %0, %1, off sc1\n\ts_nop 1" :: "v"(p), "v"(v) : "memory");
}
DI void gstore8c(void* p, u32x2 v) {
  asm volatile("global_store_dwordx2 %0, %1, off sc1" :: "v"(p), "v"(v) : "memory");
}
template <int N> DI void wait_vm() { asm volatile("s_waitcnt vmcnt(%0)" :: "n"(N) : "memory"); }
DI void bar() { asm volatile("s_waitcnt lgkmcnt(0)\n\ts_barrier" ::: "memory"); }
DI s16x4 tr_read(unsigned a) {
  return __builtin_amdgcn_ds_read_tr16_b64_v4i16((LAS s16x4*)(uintptr_t)a);
}
DI s16x8 lds_read16(unsigned a) { return *(LAS const s16x8*)(uintptr_t)a; }
DI s16x4 lds_read8(unsigned a) { return *(LAS const s16x4*)(uintptr_t)a; }
DI void lds_write16(unsigned a, u32x4 v) { *(LAS u32x4*)(uintptr_t)a = v; }
DI s16x8 cat8(s16x4 a, s16x4 b) { return __builtin_shufflevector(a, b, 0, 1, 2, 3, 4, 5, 6, 7); }
#define MFMA16(a, b, c) __builtin_amdgcn_mfma_f32_16x16x32_bf16((a), (b), (c), 0, 0, 0)
#define MFMA32(a, b, c) __builtin_amdgcn_mfma_f32_32x32x16_bf16((a), (b), (c), 0, 0, 0)

struct Diag {};
#define STAMP(i) do {} while (0)
#define DIAG_INIT() Diag dg
#define DIAG_DUMP(kid) do {} while (0)

#define SBAR() __builtin_amdgcn_sched_barrier(0)
#define SPIN_MAX (1 << 21)
DI unsigned lds_ld32(unsigned a) { return *(volatile LAS unsigned*)(uintptr_t)a; }
DI void lds_st32(unsigned a, unsigned v) { *(volatile LAS unsigned*)(uintptr_t)a = v; }
DI void spin_ge(unsigned a, unsigned want) {
  for (int it = 0; it < SPIN_MAX; ++it) {
    if ((unsigned)__builtin_amdgcn_readfirstlane(lds_ld32(a)) >= want) break;
    __builtin_amdgcn_s_sleep(1);
  }
  asm volatile("" ::: "memory");
}
DI void spin_ge4(unsigned a, unsigned want) {
  for (int it = 0; it < SPIN_MAX; ++it) {
    const u32x4 v = *(volatile LAS u32x4*)(uintptr_t)a;
    const unsigned m = min(min(v[0], v[1]), min(v[2], v[3]));
    if ((unsigned)__builtin_amdgcn_readfirstlane(m) >= want) break;
    __builtin_amdgcn_s_sleep(1);
  }
  asm volatile("" ::: "memory");
}
DI void spin_ge8(unsigned a, unsigned want) { spin_ge4(a, want); spin_ge4(a + 16, want); }
constexpr int CT_S0 = 0, CT_FULL = 16, CT_FREE = 80, CT_PAIR = 144  , CT_QUAD = 192  , CT_ZCNT = 240  ,
              CT_GO = 252, CT_QCNT = 256, CT_GO3 = 260, CT_WRDY = 272, CT_WRD2 = 288, CT_ZRDY = 304, CT_A3D = 320,
              CT_CVD = 336  , CT_KR8 = 384, CT_VR8 = 416, CT_ATT = 448, CT_RED = 480, CT_BYTES = 1024;
DI void sig(unsigned a, unsigned v, int lane) { if (lane == 0) lds_st32(a, v); }

#define POLL_MAX (1 << 15)
DI void publish(char* ws, size_t off, u64 E, int lane) {
  asm volatile("s_waitcnt vmcnt(0)" ::: "memory");
  if (lane == 0) __hip_atomic_store((u64*)(ws + off), E, __ATOMIC_RELAXED, __HIP_MEMORY_SCOPE_AGENT);
}
#define POLL_SLEEP 6
DI void poll(const char* ws, size_t off, u64 E) {
  for (int it = 0; it < POLL_MAX; ++it) {
    const u64 v = __hip_atomic_load((const u64*)(ws + off), __ATOMIC_RELAXED, __HIP_MEMORY_SCOPE_AGENT);
    if (__builtin_amdgcn_ballot_w64(v != E) == 0ull) break;
    __builtin_amdgcn_s_sleep(POLL_SLEEP);
  }
  asm volatile("" ::: "memory");
}

constexpr int K1_STAGE = 32768;
constexpr int A1_K = 0, A1_V = 3072, A1_W = 5120, A1_WAVE = 5632, A1_O = 2 * A1_WAVE, A1_PAIR = A1_O + 4096;
constexpr int L_AUX = 4 * K1_STAGE;
constexpr int L_CT = L_AUX + 2 * A1_PAIR;
constexpr int L_TOTAL = L_CT + CT_BYTES;
constexpr int K1_CT = L_CT;
constexpr int CV_COPY = 4160, CV_H = 4608, CV_REG = CV_H + 24576;
constexpr int AT_K = 64000, AT_V = AT_K + 16 * 2048, AT_END = AT_V + 16 * 1344;
constexpr int p3_w(int t) { return t < 5 ? t * 12800 : (t == 5 ? AT_END : L_AUX + (t - 6) * 12800); }

#define PSYNC() do { asm volatile("s_waitcnt lgkmcnt(0)" ::: "memory"); ++ps; sig(pf + wh * 4, ps, lane); spin_ge(pf + (wh ^ 1) * 4, ps); } while (0)
DI void a1_issue(unsigned lds, int tile, int lane, int wh, const u16* ak, const u16* av, const u16* shk) {
  const int ts = tile / 10, td = tile - ts * 10;
  const int s0 = ts * 32 + wh * 16, d0 = td * 64;
  const int lr = lane >> 3, lc = (lane & 7) * 16;
  const unsigned wl = lds + wh * A1_WAVE;
#pragma unroll
  for (int i = 0; i < 3; ++i) {
    int r = s0 - 3 + i * 8 + lr; r = r < 0 ? 0 : r;
    if (i < 2 || lane < 24) glds16nt((unsigned)(r * DM * 2 + lc), (const char*)ak + d0 * 2, wl + A1_K + i * 1024);
  }
#pragma unroll
  for (int i = 0; i < 2; ++i)
    glds16nt((unsigned)(lr * DM * 2 + lc), (const char*)av + ((size_t)(s0 + i * 8) * DM + d0) * 2, wl + A1_V + i * 1024);
  if (lane < 32) glds16((unsigned)(lane * 16), (const char*)shk + d0 * 8, wl + A1_W);
}
DI void a1_finish(unsigned lds, unsigned pf, unsigned& ps, int tile, int lane, int wh, char* ws, u64 E) {
  const int ts = tile / 10, td = tile - ts * 10;
  const int c = lane & 7;
  const unsigned wl = lds + wh * A1_WAVE;
  u32x4 wt[4];
#pragma unroll
  for (int i = 0; i < 4; ++i) wt[i] = __builtin_bit_cast(u32x4, lds_read16(wl + A1_W + c * 64 + i * 16));
#pragma unroll
  for (int j = 0; j < 2; ++j) {
    const int r = j * 8 + (lane >> 3);
    const unsigned ka = wl + A1_K + (r + 3) * 128 + c * 16;
    u32x4 k0 = __builtin_bit_cast(u32x4, lds_read16(ka)), k1 = __builtin_bit_cast(u32x4, lds_read16(ka - 128));
    u32x4 k2 = __builtin_bit_cast(u32x4, lds_read16(ka - 256)), k3 = __builtin_bit_cast(u32x4, lds_read16(ka - 384));
    const u32x4 vv = __builtin_bit_cast(u32x4, lds_read16(wl + A1_V + r * 128 + c * 16));
    if (j == 0 && ts == 0 && wh == 0) {
      const u32x4 z = {0u, 0u, 0u, 0u};
      if (r < 1) k1 = z;
      if (r < 2) k2 = z;
      if (r < 3) k3 = z;
    }
    u32x4 o;
#pragma unroll
    for (int i = 0; i < 4; ++i) {
      const unsigned pa0 = __builtin_amdgcn_perm(k1[i], k0[i], 0x05040100u), pb0 = __builtin_amdgcn_perm(k3[i], k2[i], 0x05040100u);
      const unsigned pa1 = __builtin_amdgcn_perm(k1[i], k0[i], 0x07060302u), pb1 = __builtin_amdgcn_perm(k3[i], k2[i], 0x07060302u);
      const float e0 = dot2bf(pb0, wt[i][1], dot2bf(pa0, wt[i][0], 0.f));
      const float e1 = dot2bf(pb1, wt[i][3], dot2bf(pa1, wt[i][2], 0.f));
      o[i] = pk_bf16(e0 * bf_lo(vv[i]), e1 * bf_hi(vv[i]));
    }
    lds_write16(lds + A1_O + (31 - wh * 16 - r) * 128 + c * 16, o);
  }
  PSYNC();
  const int gg = lane >> 4, q = (lane & 15) >> 2, p = lane & 3;
  u16* ut = (u16*)(ws + WS_UT);
#pragma unroll
  for (int j = 0; j < 2; ++j) {
    const unsigned ra = lds + A1_O + (gg * 8 + q) * 128 + wh * 64 + j * 32 + p * 8;
    const s16x4 t0 = tr_read(ra), t1 = tr_read(ra + 512);
    const int d = td * 64 + wh * 32 + j * 16 + (lane & 15);
    gstore16c(ut + (size_t)d * SEQ + (SEQ - 32 - ts * 32) + gg * 8, __builtin_bit_cast(u32x4, cat8(t0, t1)));
  }
  publish(ws, FL_U + (size_t)((td * 2 + wh) * 64 + ts) * 8, E, lane);
}

template <bool VT>
DI void k1_gemm(unsigned lds0, int m0, int n0, int tid, char* ws, Diag& dg) {
  const int lane = tid & 63, wave = __builtin_amdgcn_readfirstlane(tid >> 6);
  const int wm = wave >> 1, wn = wave & 1;
  const int l32 = lane & 31, hh = lane >> 5, gg = lane >> 4, q = (lane & 15) >> 2, p = lane & 3;
  const unsigned ct = lds0 + K1_CT;
  f32x16 acc[2][2];
#pragma unroll
  for (int i = 0; i < 2; ++i)
#pragma unroll
    for (int j = 0; j < 2; ++j)
#pragma unroll
      for (int r = 0; r < 16; ++r) acc[i][j][r] = 0.f;
  unsigned xb[4];
#pragma unroll
  for (int ks = 0; ks < 4; ++ks) xb[ks] = lds0 + (wm * 64 + l32) * 128 + (((unsigned)(2 * ks + hh) ^ (unsigned)((l32 >> 1) & 7)) * 16);
  unsigned wb[2];
#pragma unroll
  for (int nt = 0; nt < 2; ++nt)
    wb[nt] = lds0 + 16384 + (8 * hh + q) * 256 + (((unsigned)(wn * 4 + 2 * nt + (gg & 1)) ^ (unsigned)(q << 1)) * 32) + p * 8;
  s16x8 xa[2], wa[2], xc[2], wc[2];
#define K1_LOADF(XF, WF, bo_, ks) do { \
    _Pragma("unroll") for (int i_ = 0; i_ < 2; ++i_) { \
      WF[i_] = cat8(tr_read(wb[i_] + (bo_) + (ks) * 4096), tr_read(wb[i_] + (bo_) + (ks) * 4096 + 1024)); \
      XF[i_] = lds_read16(xb[ks] + (bo_) + i_ * 4096); } } while (0)
#define K1_MM(XF, WF) do { _Pragma("unroll") for (int mt = 0; mt < 2; ++mt) _Pragma("unroll") for (int nt = 0; nt < 2; ++nt) \
      acc[mt][nt] = VT ? MFMA32(XF[mt], WF[nt], acc[mt][nt]) : MFMA32(WF[nt], XF[mt], acc[mt][nt]); } while (0)
#define K1_ILV() do { _Pragma("unroll") for (int i_ = 0; i_ < 3; ++i_) { \
      __builtin_amdgcn_sched_group_barrier(0x008, 1, 0); __builtin_amdgcn_sched_group_barrier(0x100, 3, 0); } \
      __builtin_amdgcn_sched_group_barrier(0x008, 1, 0); } while (0)
  STAMP(1);
  K1_LOADF(xa, wa, 0, 0);
#pragma nounroll
  for (int t = 0; t < 10; ++t) {
    const unsigned bo = (t & 3) * K1_STAGE;
    SBAR();
    K1_LOADF(xc, wc, bo, 1); K1_MM(xa, wa); K1_ILV();
    SBAR();
    K1_LOADF(xa, wa, bo, 2); K1_MM(xc, wc); K1_ILV();
    SBAR();
    const u32x4 fl4 = *(volatile LAS u32x4*)(uintptr_t)(ct + CT_FULL + ((t + 1) & 3) * 16);
    K1_LOADF(xc, wc, bo, 3);
    K1_MM(xa, wa); K1_ILV();
    SBAR();
    asm volatile("s_waitcnt lgkmcnt(0)" ::: "memory");
    const unsigned flu = (unsigned)__builtin_amdgcn_readfirstlane(min(min(fl4[0], fl4[1]), min(fl4[2], fl4[3])));
    SBAR();
    if (lane == 0) lds_st32(ct + CT_FREE + (t & 3) * 16 + wave * 4, (unsigned)(t + 1));
    if (t < 9 && flu < (unsigned)(t + 2)) spin_ge4(ct + CT_FULL + ((t + 1) & 3) * 16, (unsigned)(t + 2));
    asm volatile("" ::: "memory");
    SBAR();
    K1_LOADF(xa, wa, ((t + 1) & 3) * K1_STAGE, 0);
    K1_MM(xc, wc); K1_ILV();
  }
  SBAR();
  STAMP(2);
  if (!VT) {
    const int part = n0 >= DM ? 1 : 0;
    const float sc = part == 0 ? 0.22360679774997896f * 1.4426950408889634f : 1.0f;
    char* base = ws + (part ? WS_KF : WS_QF) + ((size_t)((m0 + wm * 64) >> 5) * 40 + ((n0 - part * DM + wn * 64) >> 4)) * 1024 + lane * 16;
#pragma unroll
    for (int mt = 0; mt < 2; ++mt)
#pragma unroll
      for (int nt = 0; nt < 2; ++nt)
#pragma unroll
        for (int uh = 0; uh < 2; ++uh) {
          u32x4 o;
#pragma unroll
          for (int i = 0; i < 4; ++i) o[i] = pk_bf16(acc[mt][nt][8 * uh + 2 * i] * sc, acc[mt][nt][8 * uh + 2 * i + 1] * sc);
          gstore16c(base + (mt * 40 + nt * 2 + uh) * 1024, o);
        }
  } else {
    char* base = ws + WS_VF + ((size_t)((m0 + wm * 64) >> 4) * 20 + ((n0 - 2 * DM + wn * 64) >> 5)) * 1024 + lane * 16;
#pragma unroll
    for (int mt = 0; mt < 2; ++mt)
#pragma unroll
      for (int nt = 0; nt < 2; ++nt)
#pragma unroll
        for (int kg = 0; kg < 2; ++kg) {
          u32x4 o;
#pragma unroll
          for (int i = 0; i < 4; ++i) o[i] = pk_bf16(acc[mt][nt][8 * kg + 2 * i], acc[mt][nt][8 * kg + 2 * i + 1]);
          gstore16c(base + ((mt * 2 + kg) * 20 + nt) * 1024, o);
        }
  }
}

DI void cv_put(unsigned y, const u32x4& c, const u32x2& n) {
  const unsigned d0 = c[0], d1 = c[1], d2 = c[2], d3 = c[3], d4 = n[0], d5 = n[1];
  const unsigned a0 = __builtin_amdgcn_alignbit(d1, d0, 16), a1 = __builtin_amdgcn_alignbit(d2, d1, 16),
                 a2 = __builtin_amdgcn_alignbit(d3, d2, 16), a3 = __builtin_amdgcn_alignbit(d4, d3, 16),
                 a4 = __builtin_amdgcn_alignbit(d5, d4, 16);
  lds_write16(y, (u32x4){d0, d1, d2, d3});
  lds_write16(y + CV_COPY, (u32x4){a0, a1, a2, a3});
  lds_write16(y + 2 * CV_COPY, (u32x4){d1, d2, d3, d4});
  lds_write16(y + 3 * CV_COPY, (u32x4){a1, a2, a3, a4});
}

#define QSYNC() do { asm volatile("s_waitcnt lgkmcnt(0)" ::: "memory"); ++qs; sig(qf + wq * 4, qs, lane); spin_ge4(qf, qs); } while (0)
struct HRegs { u32x4 c; u32x2 n; unsigned d; };
DI HRegs conv_load_h(int ch, int t256, const u16* __restrict__ ssmk, const u16* __restrict__ ssmd) {
  HRegs r;
  const u16* hrow = ssmk + (size_t)ch * SEQ;
  r.c = __builtin_nontemporal_load((const u32x4*)(hrow + t256 * 8));
  r.n = *(const u32x2*)(hrow + (t256 < 255 ? t256 * 8 + 8 : 0));
  r.d = ssmd[ch];
  return r;
}
DI void conv_quad(unsigned reg, unsigned qf, unsigned& qs, unsigned zc, int ch, int lane, int wq, const HRegs& hr,
                  char* __restrict__ ws, u64 E, Diag& dg) {
  const int t256 = wq * 64 + lane;
  const unsigned hb = reg + CV_H;
  QSYNC();
  if (t256 < 32) lds_write16(reg + 4096 + t256 * 16, (u32x4){0u, 0u, 0u, 0u});
  cv_put(hb + (t256 + 2) * 16, hr.c, hr.n);
  if (t256 < 2) {
    const u32x4 z = {0u, 0u, 0u, 0u};
    u32x2 nx = {0u, 0u};
    const unsigned h0 = __builtin_amdgcn_readfirstlane(hr.c[0]), h1 = __builtin_amdgcn_readfirstlane(hr.c[1]);
    if (t256 == 1) nx = (u32x2){h0, h1};
    cv_put(hb + t256 * 16, z, nx);
  }
  STAMP(5);
  poll(ws, FL_U + (size_t)((ch >> 5) * 64 + 48 - 16 * wq + (lane & 15)) * 8, E);
  glds16c((unsigned)(lane * 16), ws + WS_UT + (size_t)ch * SEQ * 2 + wq * 1024, reg + wq * 1024);
  wait_vm<0>();
  QSYNC();
  STAMP(6);
  const int r16 = lane & 15, g = lane >> 4, q = r16 >> 2, p = lane & 3;
  const unsigned ab0 = hb + ((q + 1) & 3) * CV_COPY + 2 * (8 * g + 4 * p + (q == 3 ? 4 : 0)) + wq * 128;
  const unsigned bb0 = reg + 2 * (2032 - 16 * r16 + 8 * g) + wq * 128 - 7 * 512;
  f32x4 acc[8];
#pragma unroll
  for (int i = 0; i < 8; ++i) acc[i] = (f32x4){0.f, 0.f, 0.f, 0.f};
#define CV_LDA(a_, ad_) cat8(tr_read((ad_) + (a_) * 512), tr_read((ad_) + (a_) * 512 + 8))
#define CV_LDB(c_, bd_) lds_read16((bd_) + (7 - (c_)) * 512)
  {
    s16x8 A[8], B[8];
    B[0] = CV_LDB(0, bb0);
#pragma unroll
    for (int a = 0; a < 8; ++a) A[a] = CV_LDA(a, ab0);
#pragma unroll
    for (int c = 1; c < 8; ++c) B[c] = CV_LDB(c, bb0);
#pragma unroll
    for (int c = 0; c < 8; ++c) {
      SBAR();
#pragma unroll
      for (int a = 0; a + c < 8; ++a) acc[a + c] = MFMA16(A[a], B[c], acc[a + c]);
      B[c] = CV_LDB(c, bb0 + 64);
      A[7 - c] = CV_LDA(7 - c, ab0 + 64);
    }
    SBAR();
#pragma unroll
    for (int a = 7; a >= 0; --a)
#pragma unroll
      for (int c = 0; a + c < 8; ++c) acc[a + c] = MFMA16(A[a], B[c], acc[a + c]);
  }
  QSYNC();
#pragma unroll
  for (int nt = 0; nt < 8; ++nt) {
    const int dw = nt >> 1;
    if (dw != wq) lds_write16(hb + (nt * 3 + (wq > dw ? wq - 1 : wq)) * 1024 + lane * 16, __builtin_bit_cast(u32x4, acc[nt]));
  }
  f32x4 k0 = wq == 0 ? acc[0] : (wq == 1 ? acc[2] : (wq == 2 ? acc[4] : acc[6]));
  f32x4 k1 = wq == 0 ? acc[1] : (wq == 1 ? acc[3] : (wq == 2 ? acc[5] : acc[7]));
  QSYNC();
  const float dco = bf_lo(hr.d);
  u32x2 zz[2];
#pragma unroll
  for (int i = 0; i < 2; ++i) {
    f32x4 o = i ? k1 : k0;
#pragma unroll
    for (int j = 0; j < 3; ++j) o += __builtin_bit_cast(f32x4, lds_read16(hb + ((wq * 2 + i) * 3 + j) * 1024 + lane * 16));
    const int s = (wq * 2 + i) * 256 + 16 * r16 + 4 * g;
    const u32x2 uu = __builtin_bit_cast(u32x2, lds_read8(reg + 2 * (SEQ - 4 - s)));
    const float kv0 = bf_hi(uu[1]), kv1 = bf_lo(uu[1]), kv2 = bf_hi(uu[0]), kv3 = bf_lo(uu[0]);
    const unsigned y01 = pk_bf16(o[0], o[1]), y23 = pk_bf16(o[2], o[3]);
    const unsigned e01 = pk_bf16(kv0 * dco, kv1 * dco), e23 = pk_bf16(kv2 * dco, kv3 * dco);
    zz[i][0] = pk_bf16(bf_lo(y01) + bf_lo(e01), bf_hi(y01) + bf_hi(e01));
    zz[i][1] = pk_bf16(bf_lo(y23) + bf_lo(e23), bf_hi(y23) + bf_hi(e23));
  }
  const auto sx = __builtin_amdgcn_permlane16_swap(zz[0][0], zz[1][0], false, false);
  const auto sy = __builtin_amdgcn_permlane16_swap(zz[0][1], zz[1][1], false, false);
  u16* zrow = (u16*)(ws + WS_ZT) + (size_t)ch * SEQ;
  gstore16c(zrow + (wq * 2 + (g & 1)) * 256 + 16 * r16 + 8 * (g >> 1), (u32x4){sx[0], sy[0], sx[1], sy[1]});
  asm volatile("s_waitcnt vmcnt(0) lgkmcnt(0)" ::: "memory");
  if (lane == 0) {
    const unsigned old = __hip_atomic_fetch_add((LAS unsigned*)(uintptr_t)zc, 1u, __ATOMIC_RELAXED, __HIP_MEMORY_SCOPE_WORKGROUP);
    if ((old & 3u) == 3u) __hip_atomic_store((u64*)(ws + FL_Z + (size_t)ch * 8), E, __ATOMIC_RELAXED, __HIP_MEMORY_SCOPE_AGENT);
  }
}

DI s16x8 pack8(const f32x16& v, int o) {
  u32x4 r;
  r[0] = pk_bf16(v[o + 0], v[o + 1]); r[1] = pk_bf16(v[o + 2], v[o + 3]);
  r[2] = pk_bf16(v[o + 4], v[o + 5]); r[3] = pk_bf16(v[o + 6], v[o + 7]);
  return __builtin_bit_cast(s16x8, r);
}
DI float max3f(float a, float b, float c) { return __builtin_fmaxf(__builtin_fmaxf(a, b), c); }
DI float max16(const f32x16& v, float m) {
  float a = max3f(v[0], v[1], m), b = max3f(v[2], v[3], v[4]);
  a = max3f(a, v[5], v[6]); b = max3f(b, v[7], v[8]);
  a = max3f(a, v[9], v[10]); b = max3f(b, v[11], v[12]);
  a = max3f(a, v[13], v[14]);
  return max3f(a, b, v[15]);
}
DI float xhalf_max(float m) {
  auto sw = __builtin_amdgcn_permlane32_swap(__float_as_uint(m), __float_as_uint(m), false, false);
  return __builtin_fmaxf(__uint_as_float(sw[0]), __uint_as_float(sw[1]));
}

#define AT_LMAX 65536.0f
template <bool FULL>
DI void attn_task(unsigned lds0, int h, int qb8, int j, int lane, char* __restrict__ ws, u64 E, Diag& dg) {
  const int l32 = lane & 31, hh = lane >> 5;
  const int qt = qb8 * 8 + j;
  const int tfirst = FULL ? 0 : 8 - qt;
  const unsigned l16 = (unsigned)(lane * 16), ct = lds0 + L_CT;
  const int qa = (5 * h) >> 2;
  spin_ge(ct + CT_GO, 1u);
  STAMP(7);
  u32x4 q0r, q1r;
  {
    const char* qg = ws + WS_QF + ((size_t)qt * 40 + qa) * 1024 + lane * 16;
    asm volatile("global_load_dwordx4 %0, %2, off sc1\n\tglobal_load_dwordx4 %1, %2, off offset:1024 sc1"
                 : "=&v"(q0r), "=&v"(q1r) : "v"(qg) : "memory");
    const int kt0 = qb8 * 8 - 8;
    if (FULL || j >= 4) {
#pragma unroll
      for (int i = 0; i < 4; ++i)
        glds16c(l16, ws + WS_KF + ((size_t)(kt0 + 2 * j + (i >> 1)) * 40 + qa + (i & 1)) * 1024, lds0 + AT_K + (4 * j + i) * 1024);
    }
#pragma unroll
    for (int i = 0; i < 3; ++i) {
      const int pc = j + i * 8;
      const int c = pc * 64 + lane;
      const int kgp = (c * 1561) >> 16, rem = c - kgp * 42, vh = rem >= 21 ? 1 : 0, d = rem - vh * 21;
      const int n = h * 20 + d;
      const bool in = pc < 21 && (FULL || kgp >= 16);
      if (in && d == 20) lds_write16(lds0 + AT_V + c * 16, (u32x4){0x3f803f80u, 0x3f803f80u, 0x3f803f80u, 0x3f803f80u});
      const unsigned vo = (unsigned)(((kgp + 16) * 20 + (n >> 5)) * 1024 + (vh * 32 + (n & 31)) * 16);
      if (i < 2 || pc < 21) {
        if (in && d < 20) glds16c(vo, ws + WS_VF + ((ptrdiff_t)(kt0 * 2 - 16) * 20) * 1024, lds0 + AT_V + pc * 1024);
      }
    }
  }
  if (FULL) { if (j < 5) wait_vm<3>(); else wait_vm<2>(); }
  else asm volatile("s_waitcnt vmcnt(0) lgkmcnt(0)" ::: "memory");
  asm volatile("" : "+v"(q0r), "+v"(q1r) :: "memory");
  sig(ct + CT_KR8 + j * 4, 1u, lane);
  {
    const int g0 = 4 * qa + hh - 5 * h;
    const bool k0 = g0 >= 0, k1 = g0 + 2 >= 0 && g0 + 2 < 5, k2 = g0 + 4 < 5, k3 = g0 + 6 < 5;
    q0r[0] = k0 ? q0r[0] : 0u; q0r[1] = k0 ? q0r[1] : 0u; q0r[2] = k1 ? q0r[2] : 0u; q0r[3] = k1 ? q0r[3] : 0u;
    q1r[0] = k2 ? q1r[0] : 0u; q1r[1] = k2 ? q1r[1] : 0u; q1r[2] = k3 ? q1r[2] : 0u; q1r[3] = k3 ? q1r[3] : 0u;
  }
  s16x8 qlo = __builtin_bit_cast(s16x8, q0r), qhi8 = __builtin_bit_cast(s16x8, q1r);
  spin_ge8(ct + CT_KR8, 1u);
  STAMP(8);
  const f32x16 zero16 = {0.f, 0.f, 0.f, 0.f, 0.f, 0.f, 0.f, 0.f, 0.f, 0.f, 0.f, 0.f, 0.f, 0.f, 0.f, 0.f};
  const float ninf = -INFINITY;
  const f32x16 ninf16 = {ninf, ninf, ninf, ninf, ninf, ninf, ninf, ninf, ninf, ninf, ninf, ninf, ninf, ninf, ninf, ninf};
  const unsigned kb = lds0 + AT_K + j * 2048 + l16;
#define AT_QK(dst, t, cin) do { if (FULL || (t) >= tfirst) { dst = MFMA32(lds_read16(kb + (t) * 2048), qlo, cin); \
    dst = MFMA32(lds_read16(kb + (t) * 2048 + 1024), qhi8, dst); } else dst = ninf16; } while (0)
  const int vrow = l32 <= 20 ? l32 : (l32 == 24 ? 20 : l32 - 16);
  const unsigned vb = lds0 + AT_V + j * 1344 + hh * 336 + vrow * 16;
#define AT_EXP(sv) do { _Pragma("unroll") for (int r = 0; r < 16; ++r) sv[r] = __builtin_amdgcn_exp2f(sv[r] - m); } while (0)
#define AT_PV(sv, t) do { oacc = MFMA32(lds_read16(vb + (t) * 1344), pack8(sv, 0), oacc); oacc = MFMA32(lds_read16(vb + (t) * 1344 + 672), pack8(sv, 8), oacc); } while (0)
  f32x16 oacc;
  bool exact = !FULL;
  if (FULL) {
    f32x16 sm, sa, sb;
    AT_QK(sm, 8, zero16); AT_QK(sa, 0, zero16);
    const int tq = l32 - 4 * hh;
    const unsigned wm = tq < 0 ? 0xffffffffu : (0xfffffffeu << tq);
    unsigned mk[16];
#pragma unroll
    for (int r = 0; r < 16; ++r) {
      mk[r] = (unsigned)__builtin_amdgcn_sbfe((int)wm, (r & 3) + 8 * (r >> 2), 1);
      sm[r] = __uint_as_float((mk[r] & __float_as_uint(sa[r])) | (~mk[r] & __float_as_uint(sm[r])));
    }
    const float m = xhalf_max(max16(sm, ninf));
    const float nm = -m;
    const f32x16 cneg = {nm, nm, nm, nm, nm, nm, nm, nm, nm, nm, nm, nm, nm, nm, nm, nm};
    AT_QK(sa, 1, cneg);
    AT_EXP(sm);
    u32x4 pl = __builtin_bit_cast(u32x4, pack8(sm, 0)), ph = __builtin_bit_cast(u32x4, pack8(sm, 8)), ql, qh;
#pragma unroll
    for (int i = 0; i < 4; ++i) {
      ql[i] = pl[i] & ((mk[2 * i] & 0xffffu) | (mk[2 * i + 1] & 0xffff0000u)); pl[i] ^= ql[i];
      qh[i] = ph[i] & ((mk[8 + 2 * i] & 0xffffu) | (mk[9 + 2 * i] & 0xffff0000u)); ph[i] ^= qh[i];
    }
    asm volatile("s_waitcnt vmcnt(0) lgkmcnt(0)" ::: "memory");
    sig(ct + CT_VR8 + j * 4, 1u, lane);
    spin_ge8(ct + CT_VR8, 1u);
    STAMP(9);
    oacc = MFMA32(lds_read16(vb + 8 * 1344), __builtin_bit_cast(s16x8, pl), zero16);
    oacc = MFMA32(lds_read16(vb + 8 * 1344 + 672), __builtin_bit_cast(s16x8, ph), oacc);
    oacc = MFMA32(lds_read16(vb), __builtin_bit_cast(s16x8, ql), oacc);
    oacc = MFMA32(lds_read16(vb + 672), __builtin_bit_cast(s16x8, qh), oacc);
#define AT_EX0(sv) do { _Pragma("unroll") for (int r = 0; r < 16; ++r) sv[r] = __builtin_amdgcn_exp2f(sv[r]); } while (0)
    AT_QK(sb, 2, cneg); AT_EX0(sa); AT_PV(sa, 1);
    AT_QK(sa, 3, cneg); AT_EX0(sb); AT_PV(sb, 2);
    AT_QK(sb, 4, cneg); AT_EX0(sa); AT_PV(sa, 3);
    AT_QK(sa, 5, cneg); AT_EX0(sb); AT_PV(sb, 4);
    AT_QK(sb, 6, cneg); AT_EX0(sa); AT_PV(sa, 5);
    AT_QK(sa, 7, cneg); AT_EX0(sb); AT_PV(sb, 6);
    AT_EX0(sa); AT_PV(sa, 7);
    float big = max3f(__builtin_fabsf(oacc[0]), __builtin_fabsf(oacc[1]), __builtin_fabsf(oacc[2]));
    big = max3f(big, __builtin_fabsf(oacc[3]), __builtin_fabsf(oacc[4]));
    big = max3f(big, __builtin_fabsf(oacc[5]), __builtin_fabsf(oacc[6]));
    big = max3f(big, __builtin_fabsf(oacc[7]), __builtin_fabsf(oacc[8]));
    big = max3f(big, __builtin_fabsf(oacc[9]), __builtin_fabsf(oacc[10]));
    big = __builtin_fmaxf(big, __builtin_fabsf(oacc[11]));
    const float ls = hh ? oacc[8] : oacc[12];
    float osum = 0.f;
#pragma unroll
    for (int r = 0; r < 8; ++r) osum += oacc[r];
    const bool ok = (ls < AT_LMAX) && (big < 1e37f) && (osum == osum);
    exact = __builtin_amdgcn_ballot_w64(!ok) != 0ull;
  }
  if (exact) {
    asm volatile("" : "+v"(qlo), "+v"(qhi8) :: "memory");
    f32x16 sm, s1, s2, s3;
    {
      f32x16 s0;
      AT_QK(sm, 8, zero16); AT_QK(s0, 0, zero16);
      AT_QK(s1, 1, zero16); AT_QK(s2, 2, zero16); AT_QK(s3, 3, zero16);
#pragma unroll
      for (int r = 0; r < 16; ++r) {
        const int kk = (r & 3) + 8 * (r >> 2) + 4 * hh;
        sm[r] = kk > l32 ? s0[r] : sm[r];
      }
    }
    float m = max16(sm, ninf);
    m = max16(s1, m); m = max16(s2, m); m = max16(s3, m);
    m = xhalf_max(m);
    AT_EXP(sm);
    oacc = zero16;
    {
      f32x16 p0, p8;
#pragma unroll
      for (int r = 0; r < 16; ++r) {
        const int kk = (r & 3) + 8 * (r >> 2) + 4 * hh;
        p0[r] = kk > l32 ? sm[r] : 0.f;
        p8[r] = kk > l32 ? 0.f : sm[r];
      }
      AT_PV(p8, 8);
      if (FULL) AT_PV(p0, 0);
    }
    if (FULL || 1 >= tfirst) { AT_EXP(s1); AT_PV(s1, 1); }
    AT_QK(sm, 4, zero16); AT_QK(s1, 5, zero16);
    if (FULL || 2 >= tfirst) { AT_EXP(s2); AT_PV(s2, 2); }
    AT_QK(s2, 6, zero16);
    if (FULL || 3 >= tfirst) { AT_EXP(s3); AT_PV(s3, 3); }
    AT_QK(s3, 7, zero16);
    float m2 = max16(sm, m);
    m2 = max16(s1, m2); m2 = max16(s2, m2); m2 = max16(s3, m2);
    m2 = xhalf_max(m2);
    {
      const float alpha = __builtin_amdgcn_exp2f(m - m2);
#pragma unroll
      for (int r = 0; r < 13; ++r) oacc[r] *= alpha;
      m = m2;
    }
    if (FULL || 4 >= tfirst) { AT_EXP(sm); AT_PV(sm, 4); }
    if (FULL || 5 >= tfirst) { AT_EXP(s1); AT_PV(s1, 5); }
    if (FULL || 6 >= tfirst) { AT_EXP(s2); AT_PV(s2, 6); }
    if (FULL || 7 >= tfirst) { AT_EXP(s3); AT_PV(s3, 7); }
  }
  asm volatile("s_waitcnt lgkmcnt(0)" ::: "memory");
  sig(ct + CT_ATT + j * 4, 1u, lane);
  STAMP(10);
  const float l = hh ? oacc[8] : oacc[12];
  const float inv = __builtin_amdgcn_rcpf(l);
  char* oc = ws + WS_OW + ((size_t)qt * NH + h) * 1280;
  u32x4 o;
  o[0] = pk_bf16(oacc[0] * inv, oacc[1] * inv); o[1] = pk_bf16(oacc[2] * inv, oacc[3] * inv);
  o[2] = pk_bf16(oacc[4] * inv, oacc[5] * inv); o[3] = pk_bf16(oacc[6] * inv, oacc[7] * inv);
  gstore16c(oc + lane * 16, o);
  if (!hh) {
    u32x2 o2;
    o2[0] = pk_bf16(oacc[8] * inv, oacc[9] * inv); o2[1] = pk_bf16(oacc[10] * inv, oacc[11] * inv);
    gstore8c(oc + 1024 + l32 * 8, o2);
  }
  publish(ws, FL_O + (size_t)(qt * NH + h) * 8, E, lane);
}

#define ROTG 5
#define A1_DELAY 8
__global__ __launch_bounds__(768, 3) void mega(const u16* __restrict__ x, const u16* __restrict__ wqkv,
                                               const u16* __restrict__ ak, const u16* __restrict__ av,
                                               const u16* __restrict__ shk, const u16* __restrict__ wo,
                                               const u16* __restrict__ ssmk, const u16* __restrict__ ssmd,
                                               const u16* __restrict__ aq, char* __restrict__ ws,
                                               u16* __restrict__ out) {
  extern __shared__ __attribute__((aligned(16))) char smem[];
  const unsigned lds0 = lds_addr(smem);
  const int tid = threadIdx.x, wave = __builtin_amdgcn_readfirstlane(tid >> 6);
  int lane = tid & 63;
#define RELANE() asm volatile("" : "+v"(lane))
  const int role = wave >> 2, w = wave & 3;
  const int bid = blockIdx.x, xcd = bid & 7, idx = bid >> 3;
  const bool has_gemm = idx < 30;
  const unsigned ct = lds0 + L_CT;
  DIAG_INIT();
  u64 E;
  asm volatile("s_load_dwordx2 %0, %1, 0x0" : "=s"(E) : "s"(ws + WS_FL) : "memory");
  const int gt = (xcd & 1) * 30 + idx;
  const int tm = (xcd >> 1) * 4 + (gt & 3), tn = gt >> 2;
  const int m0 = tm * 128, n0 = tn * 128;
  const int rot = ROTG >= 10 ? (((gt & 3) * 5 + 1) / 2 + 3 * tn) % 10 : ((((gt & 3) + tn) % ROTG) * 10) / ROTG;
  const unsigned xo0 = (unsigned)((lane >> 3) * DM * 2 + (((lane & 7) ^ (lane >> 4)) * 16));
  const unsigned xo1 = (unsigned)((lane >> 3) * DM * 2 + (((lane & 7) ^ (4 + (lane >> 4))) * 16));
  const unsigned wof = (unsigned)((lane >> 4) * NQKV * 2 + (((lane & 15) ^ ((lane >> 4) << 2)) * 16));
  const char* xg = (const char*)(x + (size_t)m0 * DM);
  const char* wg = (const char*)(wqkv + n0);
#define K1_PIECE_X(t, kt, j) glds16(((j) & 1) ? xo1 : xo0, xg + (size_t)(j) * 8 * DM * 2 + (kt) * 128, lds0 + ((t) & 3) * K1_STAGE + (j) * 1024)
#define K1_PIECE_W(t, kt, j) glds16(wof, wg + (size_t)((kt) * 64 + (j) * 4) * NQKV * 2, lds0 + ((t) & 3) * K1_STAGE + 16384 + (j) * 1024)
  if (has_gemm && role == 0) {
#pragma unroll
    for (int i = 0; i < 4; ++i) { K1_PIECE_X(0, rot, w * 4 + i); K1_PIECE_W(0, rot, w * 4 + i); }
  }
  const unsigned pf = ct + CT_PAIR + (wave >> 1) * 8;
  const int wh = wave & 1;
  unsigned ps = 0;
  const int fb = (idx - 30) * 8 + xcd;
  const int a1t = has_gemm ? (xcd * 30 + idx) * 2 + (w >> 1) : 480 + fb * 10 + (wave >> 1);
  const unsigned a1l = has_gemm ? lds0 + L_AUX + (w >> 1) * A1_PAIR : lds0 + (wave >> 1) * A1_PAIR;
  if (!has_gemm || role == 2) a1_issue(a1l, a1t, lane, wh, ak, av, shk);
  int nconv, ch0, ch1;
  if (has_gemm) {
    const int gb = xcd * 30 + idx;
    nconv = role == 0 ? 0 : ((role == 2 && gb < 64) ? 2 : 1);
    ch0 = gb * 2 + (role == 2 ? 0 : 1); ch1 = 576 + gb;
  } else { nconv = 2; ch0 = 480 + fb * 6 + role * 2; ch1 = ch0 + 1; }
  if (role == 1 && tid - 256 < CT_BYTES / 4) lds_st32(ct + (tid - 256) * 4, 0u);
  asm volatile("s_waitcnt lgkmcnt(0)\n\ts_barrier" : "+s"(E) :: "memory");
  E += 1ull;

  if (!has_gemm) {
    const unsigned a2l = lds0 + (6 + (wave >> 1)) * A1_PAIR;
    if (wave < 8) a1_issue(a2l, a1t + 6, lane, wh, ak, av, shk);
    if (wave < 8) wait_vm<6>(); else wait_vm<0>();
    a1_finish(a1l, pf, ps, a1t, lane, wh, ws, E);
    if (wave < 8) a1_finish(a2l, pf, ps, a1t + 6, lane, wh, ws, E);
    bar();
    STAMP(1);
  } else if (role == 0) {
    wait_vm<0>();
    sig(ct + CT_S0 + w * 4, 1u, lane);
    spin_ge4(ct + CT_S0, 1u);
    __builtin_amdgcn_s_setprio(2);
    if (tn >= 10) k1_gemm<true>(lds0, m0, n0, tid, ws, dg);
    else k1_gemm<false>(lds0, m0, n0, tid, ws, dg);
    STAMP(3);
    asm volatile("s_waitcnt vmcnt(0)" ::: "memory");
    if (lane == 0) {
      const unsigned old = __hip_atomic_fetch_add((LAS unsigned*)(uintptr_t)(ct + CT_QCNT), 1u, __ATOMIC_RELAXED, __HIP_MEMORY_SCOPE_WORKGROUP);
      if (old == 3u) __hip_atomic_store((u64*)(ws + FL_Q + (size_t)(tm * 16 + tn) * 8), E, __ATOMIC_RELAXED, __HIP_MEMORY_SCOPE_AGENT);
    }
    STAMP(4);
  } else if (role == 1) {
    __builtin_amdgcn_s_setprio(3);
#define K1_ST(t) do { const int kt_ = (t) + rot >= 10 ? (t) + rot - 10 : (t) + rot; \
      if ((t) >= 4) spin_ge4(ct + CT_FREE + ((t) & 3) * 16, (unsigned)((t) - 3)); \
      _Pragma("unroll") for (int i_ = 0; i_ < 4; ++i_) { K1_PIECE_X(t, kt_, w * 4 + i_); K1_PIECE_W(t, kt_, w * 4 + i_); } } while (0)
#define K1_SIG(t) sig(ct + CT_FULL + ((t) & 3) * 16 + w * 4, (unsigned)((t) + 1), lane)
    K1_ST(1); K1_ST(2); K1_ST(3); wait_vm<16>(); K1_SIG(1);
    K1_ST(4); wait_vm<16>(); K1_SIG(2);
    K1_ST(5); wait_vm<16>(); K1_SIG(3);
    K1_ST(6); wait_vm<16>(); K1_SIG(4);
    K1_ST(7); wait_vm<16>(); K1_SIG(5);
    K1_ST(8); wait_vm<16>(); K1_SIG(6);
    K1_ST(9); wait_vm<16>(); K1_SIG(7);
    wait_vm<8>(); K1_SIG(8);
    wait_vm<0>(); K1_SIG(9);
    __builtin_amdgcn_s_setprio(0);
    STAMP(1);
  } else {
    __builtin_amdgcn_s_setprio(2);
    wait_vm<0>();
    a1_finish(a1l, pf, ps, a1t, lane, wh, ws, E);
    __builtin_amdgcn_s_setprio(0);
    STAMP(1);
  }

  RELANE();
  if (nconv > 0) {
    unsigned qs = 0;
    const unsigned qf = ct + CT_QUAD + role * 16;
    const unsigned creg = lds0 + (role == 2 ? L_AUX : (role == 1 ? 29184 : 0));
    HRegs hr = conv_load_h(ch0, w * 64 + lane, ssmk, ssmd);
    if (has_gemm && role == 1) spin_ge4(ct + CT_FREE + 16, 10u);
    for (int r = 0; r < nconv; ++r) {
      HRegs hn = hr;
      if (r + 1 < nconv) hn = conv_load_h(ch1, w * 64 + lane, ssmk, ssmd);
      conv_quad(creg, qf, qs, ct + CT_ZCNT + role * 4, r ? ch1 : ch0, lane, w, hr, ws, E, dg);
      hr = hn;
    }
  }
  sig(ct + CT_CVD + wave * 4, 1u, lane);
  STAMP(2 + (role == 0 ? 3 : 0));

  const int tm3 = xcd * 4 + (idx >> 3), tn3 = idx & 7;
  const int m3 = tm3 * 64, n3 = tn3 * 80;
  RELANE();

  if (role == 2) {
    const int r16 = lane & 15, g = lane >> 4, q = r16 >> 2, p = lane & 3;
    u32x4 qv[3];
#pragma unroll
    for (int j = 0; j < 3; ++j) {
      const int dc = j < 2 ? j * 4 + g : 8 + (g & 1);
      qv[j] = __builtin_nontemporal_load((const u32x4*)(aq + (size_t)(m3 + w * 16 + r16) * DM + n3 + dc * 8));
    }
    unsigned wv[4];
#pragma unroll
    for (int i = 0; i < 4; ++i) {
      const int c = i < 3 ? (i * 4 + w) * 64 + lane : 768 + w * 8 + (lane & 7);
      const int row = c / 10;
      wv[i] = (unsigned)(row * DM * 2 + (c - row * 10) * 16);
    }
    const char* wog = (const char*)(wo + n3);
#define P3_STW(t) do { _Pragma("unroll") for (int i_ = 0; i_ < 3; ++i_) \
        glds16(wv[i_], wog + (size_t)(t) * 80 * DM * 2, lds0 + p3_w(t) + (i_ * 4 + w) * 1024); \
      if (lane < 8) glds16(wv[3], wog + (size_t)(t) * 80 * DM * 2, lds0 + p3_w(t) + 12288 + w * 128); } while (0)
    spin_ge4(ct + CT_CVD + 32, 1u);
    P3_STW(7);
    spin_ge8(ct + CT_CVD, 1u);
    P3_STW(0); P3_STW(1); P3_STW(2); P3_STW(3); P3_STW(4); P3_STW(5);
    wait_vm<0>();
    sig(ct + CT_WRDY + w * 4, 1u, lane);
    STAMP(3);
    {
      const int l5 = lane & 31;
      const int cl = l5 < 8 ? w * 8 + l5 : (l5 < 16 ? 32 + w * 8 + (l5 - 8) : 64 + w * 4 + (l5 & 3));
      poll(ws, FL_Z + (size_t)(n3 + cl) * 8, E);
    }
    {
      const char* zg = ws + WS_ZT + ((size_t)n3 * SEQ + m3) * 2;
      const unsigned zo = (unsigned)((lane >> 3) * SEQ * 2 + (lane & 7) * 16);
      glds16c(zo, zg + (size_t)(w * 8) * SEQ * 2, lds0 + L_AUX + w * 1024);
      glds16c(zo, zg + (size_t)(32 + w * 8) * SEQ * 2, lds0 + L_AUX + 4096 + w * 1024);
      if (lane < 32) glds16c(zo, zg + (size_t)(64 + w * 4) * SEQ * 2, lds0 + L_AUX + 8192 + w * 512);
    }
    wait_vm<0>();
    sig(ct + CT_ZRDY + w * 4, 1u, lane);
    spin_ge4(ct + CT_ZRDY, 1u);
    STAMP(4);
    u32x4 zz[3];
#pragma unroll
    for (int j = 0; j < 3; ++j) {
      const int dc = j < 2 ? j * 4 + g : 8 + (g & 1);
      const unsigned za = lds0 + L_AUX + (dc * 8 + q) * 128 + w * 32 + p * 8;
      zz[j] = __builtin_bit_cast(u32x4, cat8(tr_read(za), tr_read(za + 512)));
    }
    asm volatile("s_waitcnt lgkmcnt(0)" : "+v"(zz[0]), "+v"(zz[1]), "+v"(zz[2]) :: "memory");
    sig(ct + CT_A3D + w * 4, 1u, lane);
    spin_ge4(ct + CT_A3D, 1u);
    P3_STW(6);
    wait_vm<0>();
    sig(ct + CT_WRD2 + w * 4, 1u, lane);
#pragma unroll
    for (int j = 0; j < 3; ++j) {
      const int dc = j < 2 ? j * 4 + g : 8 + (g & 1);
      u32x4 o;
#pragma unroll
      for (int i = 0; i < 4; ++i) o[i] = pk_bf16(bf_lo(qv[j][i]) * bf_lo(zz[j][i]), bf_hi(qv[j][i]) * bf_hi(zz[j][i]));
      if (j < 2 || g < 2) *(u32x4*)(out + (size_t)(m3 + w * 16 + r16) * (2 * DM) + n3 + dc * 8) = o;
    }
    STAMP(7);
    if (bid == 0 && w == 0) {
#pragma unroll
      for (int i = 0; i < 4; ++i) poll(ws, FL_O + (size_t)(((i * 2 + (lane >> 5)) * 8) * NH + (lane & 31)) * 8, E);
      if (lane == 0) __hip_atomic_store((u64*)(ws + WS_FL), E, __ATOMIC_RELAXED, __HIP_MEMORY_SCOPE_AGENT);
    }
    DIAG_DUMP(2);
    return;
  }

  const int h = xcd * 4 + (idx & 3), qb8 = idx >> 2;
  if (wave == 0) {
    const int qa = (5 * h) >> 2, sel = lane >> 3, hi = lane & 1;
    int rb = sel < 2 ? 2 * qb8 - 2 + ((lane >> 1) & 3) : 2 * qb8 + ((lane >> 1) & 1);
    rb = rb < 0 ? 0 : rb;
    const int col = sel == 1 ? 2 * DM + h * 20 + hi * 19 : (sel == 0 ? DM : 0) + qa * 16 + hi * 31;
    spin_ge4(ct + CT_CVD, 1u);
    poll(ws, FL_Q + (size_t)(rb * 16 + (col >> 7)) * 8, E);
    sig(ct + CT_GO, 1u, lane);
  }
  __builtin_amdgcn_s_setprio(1);
  RELANE();
  if (qb8 > 0) attn_task<true>(lds0, h, qb8, wave, lane, ws, E, dg);
  else attn_task<false>(lds0, h, qb8, wave, lane, ws, E, dg);
  STAMP(11);

  RELANE();
  const int r16 = lane & 15, g = lane >> 4, q = r16 >> 2, p = lane & 3;
  f32x4 acc[5];
#pragma unroll
  for (int i = 0; i < 5; ++i) acc[i] = (f32x4){0.f, 0.f, 0.f, 0.f};
  const unsigned wbp = lds0 + (20 * (g >> 1) + 4 * (g & 1) + q) * 160 + p * 8;
  const unsigned wbs = lds0 + (20 * g + 16 + q) * 160 + p * 8;
  u32x4 op[8]; u32x2 osg[4];
  if (wave == 0) {
    poll(ws, FL_O + (size_t)((m3 >> 5) * NH + lane) * 8, E);
    sig(ct + CT_GO3, 1u, lane);
  } else spin_ge(ct + CT_GO3, 1u);
  STAMP(12);
  {
    const int orow = (w & 1) * 16 + r16;
    const char* ob = ws + WS_OW + ((size_t)((m3 >> 5) + (w >> 1)) * NH + role * 16) * 1280;
    const char* pp = ob + (g >> 1) * 1280 + (g & 1) * 512 + orow * 16;
    const char* sp = ob + g * 1280 + 1024 + orow * 8;
#pragma unroll
    for (int t = 0; t < 4; ++t) {
      asm volatile("global_load_dwordx4 %0, %3, off sc1\n\tglobal_load_dwordx4 %1, %3, off offset:2560 sc1\n\tglobal_load_dwordx2 %2, %4, off sc1"
                   : "=&v"(op[2 * t]), "=&v"(op[2 * t + 1]), "=&v"(osg[t]) : "v"(pp + t * 5120), "v"(sp + t * 5120) : "memory");
    }
  }
  spin_ge4(ct + CT_WRDY, 1u);
  if (role == 1) spin_ge4(ct + CT_WRD2, 1u);
  const unsigned w0 = role ? p3_w(4) : p3_w(0), w1 = role ? p3_w(5) : p3_w(1), w2 = role ? p3_w(6) : p3_w(2), w3 = role ? p3_w(7) : p3_w(3);
  s16x8 fw0[5], fw1[5]; s16x4 fs[2][5];
#define P3_LOADW(bo_, si) do { _Pragma("unroll") for (int nt = 0; nt < 5; ++nt) { \
      fw0[nt] = cat8(tr_read(wbp + (bo_) + nt * 32), tr_read(wbp + (bo_) + 8 * 160 + nt * 32)); \
      fw1[nt] = cat8(tr_read(wbp + (bo_) + 40 * 160 + nt * 32), tr_read(wbp + (bo_) + 48 * 160 + nt * 32)); \
      fs[si][nt] = tr_read(wbs + (bo_) + nt * 32); } } while (0)
#define P3_MM(t) do { \
    _Pragma("unroll") for (int nt = 0; nt < 5; ++nt) acc[nt] = MFMA16(fw0[nt], __builtin_bit_cast(s16x8, op[2 * (t)]), acc[nt]); \
    _Pragma("unroll") for (int nt = 0; nt < 5; ++nt) acc[nt] = MFMA16(fw1[nt], __builtin_bit_cast(s16x8, op[2 * (t) + 1]), acc[nt]); \
    if ((t) & 1) { const s16x8 os_ = cat8(__builtin_bit_cast(s16x4, osg[(t) - 1]), __builtin_bit_cast(s16x4, osg[t])); \
      _Pragma("unroll") for (int nt = 0; nt < 5; ++nt) acc[nt] = MFMA16(cat8(fs[0][nt], fs[1][nt]), os_, acc[nt]); } } while (0)
#define P3_WAITO(t, n) asm volatile("s_waitcnt vmcnt(" #n ")" : "+v"(op[2 * (t)]), "+v"(op[2 * (t) + 1]), "+v"(osg[t]) :: "memory")
  P3_LOADW(w0, 0); P3_WAITO(0, 9); P3_MM(0); SBAR();
  STAMP(13);
  P3_LOADW(w1, 1); P3_WAITO(1, 6); P3_MM(1); SBAR();
  P3_LOADW(w2, 0); P3_WAITO(2, 3); P3_MM(2); SBAR();
  P3_LOADW(w3, 1); P3_WAITO(3, 0); P3_MM(3); SBAR();
  const unsigned rb = lds0 + AT_K + w * 5120 + lane * 16;
  if (role == 1) {
    spin_ge8(ct + CT_ATT, 1u);
#pragma unroll
    for (int nt = 0; nt < 5; ++nt) lds_write16(rb + nt * 1024, __builtin_bit_cast(u32x4, acc[nt]));
    asm volatile("s_waitcnt lgkmcnt(0)" ::: "memory");
    sig(ct + CT_RED + w * 4, 1u, lane);
    STAMP(14);
    DIAG_DUMP(1);
    return;
  }
  spin_ge(ct + CT_RED + w * 4, 1u);
  u16* orow_g = out + (size_t)(m3 + w * 16 + r16) * (2 * DM) + DM + n3 + 4 * g;
#pragma unroll
  for (int nt = 0; nt < 5; ++nt) {
    const f32x4 o2 = acc[nt] + __builtin_bit_cast(f32x4, lds_read16(rb + nt * 1024));
    u32x2 o;
    o[0] = pk_bf16(o2[0], o2[1]);
    o[1] = pk_bf16(o2[2], o2[3]);
    *(u32x2*)(orow_g + nt * 16) = o;
  }
  STAMP(14);
  DIAG_DUMP(0);
}

extern "C" void kernel_launch(void* const* d_in, const int* in_sizes, int n_in, void* d_out, int out_size,
                              void* d_ws, size_t ws_size, hipStream_t stream) {
  const u16* aq = (const u16*)d_in[0];
  const u16* ak = (const u16*)d_in[1];
  const u16* av = (const u16*)d_in[2];
  const u16* shk = (const u16*)d_in[3];
  const u16* ssmk = (const u16*)d_in[4];
  const u16* ssmd = (const u16*)d_in[5];
  const u16* x = (const u16*)d_in[6];
  const u16* wqkv = (const u16*)d_in[7];
  const u16* wo = (const u16*)d_in[8];
  (void)hipFuncSetAttribute((const void*)mega, hipFuncAttributeMaxDynamicSharedMemorySize, L_TOTAL);
  mega<<<256, 768, L_TOTAL, stream>>>(x, wqkv, ak, av, shk, wo, ssmk, ssmd, aq, (char*)d_ws, (u16*)d_out);
}
```

```cpp
#include <hip/hip_runtime.h>
#include <hip/hip_bf16.h>
#include <stdint.h>

typedef unsigned short u16;
typedef short s16x4 __attribute__((ext_vector_type(4)));
typedef short s16x8 __attribute__((ext_vector_type(8)));
typedef float f32x2 __attribute__((ext_vector_type(2)));
typedef float f32x4 __attribute__((ext_vector_type(4)));
typedef float f32x16 __attribute__((ext_vector_type(16)));
typedef unsigned u32x2 __attribute__((ext_vector_type(2)));
typedef unsigned u32x4 __attribute__((ext_vector_type(4)));
typedef __bf16 bf16x2 __attribute__((ext_vector_type(2)));
#define LAS __attribute__((address_space(3)))
#define DI __device__ __forceinline__

constexpr int SEQ = 2048, DM = 640, NQKV = 1920, NH = 32;
constexpr size_t WS_QF = 0;
constexpr size_t WS_KF = WS_QF + (size_t)DM * SEQ * 2;
constexpr size_t WS_VF = WS_KF + (size_t)DM * SEQ * 2;
constexpr size_t WS_UT = WS_VF + (size_t)DM * SEQ * 2;
constexpr size_t WS_ZT = WS_UT + (size_t)DM * SEQ * 2;
constexpr size_t WS_OW = WS_ZT + (size_t)DM * SEQ * 2;
typedef unsigned long long u64;
constexpr size_t WS_FL = WS_OW + (size_t)DM * SEQ * 2;
constexpr size_t FL_U = WS_FL + 256;
constexpr size_t FL_Q = FL_U + 20 * 64 * 8;
constexpr size_t FL_Z = FL_Q + 16 * 16 * 8;
constexpr size_t FL_O = FL_Z + 640 * 8;

DI unsigned pk_bf16(float lo, float hi) {
  f32x2 f = {lo, hi};
  return __builtin_bit_cast(unsigned, __builtin_convertvector(f, bf16x2));
}
DI float bf_lo(unsigned u) { return __uint_as_float(u << 16); }
DI float bf_hi(unsigned u) { return __uint_as_float(u & 0xffff0000u); }
DI float rbf(float x) { return bf_lo(pk_bf16(x, 0.f)); }

DI unsigned lds_addr(const void* p) { return (unsigned)(uintptr_t)(LAS const char*)p; }

DI void glds16(unsigned voff, const void* sbase, unsigned lds_dst) {
  unsigned keep;
  asm volatile("s_mov_b32 %0, m0\n\ts_mov_b32 m0, %3\n\ts_nop 0\n\tglobal_load_lds_dwordx4 %1, %2\n\ts_mov_b32 m0, %0"
               : "=&s"(keep) : "v"(voff), "s"(sbase), "s"(lds_dst) : "memory");
}
DI void glds16nt(unsigned voff, const void* sbase, unsigned lds_dst) {
  unsigned keep;
  asm volatile("s_mov_b32 %0, m0\n\ts_mov_b32 m0, %3\n\ts_nop 0\n\tglobal_load_lds_dwordx4 %1, %2 nt\n\ts_mov_b32 m0, %0"
               : "=&s"(keep) : "v"(voff), "s"(sbase), "s"(lds_dst) : "memory");
}
#define LDPOL "sc1"
DI void glds16c(unsigned voff, const void* sbase, unsigned lds_dst) {
  unsigned keep;
  asm volatile("s_mov_b32 %0, m0\n\ts_mov_b32 m0, %3\n\ts_nop 0\n\tglobal_load_lds_dwordx4 %1, %2 " LDPOL "\n\ts_mov_b32 m0, %0"
               : "=&s"(keep) : "v"(voff), "s"(sbase), "s"(lds_dst) : "memory");
}
static __device__ const unsigned k_one[4] = {1u, 1u, 1u, 1u};
DI void glds_mark(unsigned lds_dst, int lane) {
  if (lane == 0) {
    unsigned keep;
    asm volatile("s_mov_b32 %0, m0\n\ts_mov_b32 m0, %3\n\ts_nop 0\n\tglobal_load_lds_dword %1, %2\n\ts_mov_b32 m0, %0"
                 : "=&s"(keep) : "v"(0u), "s"(k_one), "s"(lds_dst) : "memory");
  }
}
DI float dot2bf(unsigned a, unsigned b, float c) {
  return __builtin_amdgcn_fdot2_f32_bf16(__builtin_bit_cast(bf16x2, a), __builtin_bit_cast(bf16x2, b), c, false);
}
DI void gstore16c(void* p, u32x4 v) {
  asm volatile("global_store_dwordx4 %0, %1, off sc1\n\ts_nop 1" :: "v"(p), "v"(v) : "memory");
}
DI void gstore8c(void* p, u32x2 v) {
  asm volatile("global_store_dwordx2 %0, %1, off sc1" :: "v"(p), "v"(v) : "memory");
}
template <int N> DI void wait_vm() { asm volatile("s_waitcnt vmcnt(%0)" :: "n"(N) : "memory"); }
DI void bar() { asm volatile("s_waitcnt lgkmcnt(0)\n\ts_barrier" ::: "memory"); }
DI s16x4 tr_read(unsigned a) {
  return __builtin_amdgcn_ds_read_tr16_b64_v4i16((LAS s16x4*)(uintptr_t)a);
}
DI s16x8 lds_read16(unsigned a) { return *(LAS const s16x8*)(uintptr_t)a; }
DI s16x4 lds_read8(unsigned a) { return *(LAS const s16x4*)(uintptr_t)a; }
DI void lds_write16(unsigned a, u32x4 v) { *(LAS u32x4*)(uintptr_t)a = v; }
DI s16x8 cat8(s16x4 a, s16x4 b) { return __builtin_shufflevector(a, b, 0, 1, 2, 3, 4, 5, 6, 7); }
#define MFMA16(a, b, c) __builtin_amdgcn_mfma_f32_16x16x32_bf16((a), (b), (c), 0, 0, 0)
#define MFMA32(a, b, c) __builtin_amdgcn_mfma_f32_32x32x16_bf16((a), (b), (c), 0, 0, 0)

struct Diag {};
#define STAMP(i) do {} while (0)
#define DIAG_INIT() Diag dg
#define DIAG_DUMP(kid) do {} while (0)

#define SBAR() __builtin_amdgcn_sched_barrier(0)
#define SPIN_MAX (1 << 21)
DI unsigned lds_ld32(unsigned a) { return *(volatile LAS unsigned*)(uintptr_t)a; }
DI void lds_st32(unsigned a, unsigned v) { *(volatile LAS unsigned*)(uintptr_t)a = v; }
DI void spin_ge(unsigned a, unsigned want) {
#pragma nounroll
  for (int it = 0; it < SPIN_MAX; ++it) {
    if ((unsigned)__builtin_amdgcn_readfirstlane(lds_ld32(a)) >= want) break;
    __builtin_amdgcn_s_sleep(1);
  }
  asm volatile("" ::: "memory");
}
DI void spin_ge4(unsigned a, unsigned want) {
#pragma nounroll
  for (int it = 0; it < SPIN_MAX; ++it) {
    const u32x4 v = *(volatile LAS u32x4*)(uintptr_t)a;
    const unsigned m = min(min(v[0], v[1]), min(v[2], v[3]));
    if ((unsigned)__builtin_amdgcn_readfirstlane(m) >= want) break;
    __builtin_amdgcn_s_sleep(1);
  }
  asm volatile("" ::: "memory");
}
DI void spin_ge8(unsigned a, unsigned want) { spin_ge4(a, want); spin_ge4(a + 16, want); }
constexpr int CT_S0 = 0, CT_FULL = 16, CT_FREE = 80, CT_PAIR = 144  , CT_QUAD = 192  , CT_ZCNT = 240  ,
              CT_GO = 252, CT_QCNT = 256, CT_GO3 = 260, CT_WRDY = 272, CT_WRD2 = 288, CT_ZRDY = 304, CT_A3D = 320,
              CT_CVD = 336  , CT_KR8 = 384, CT_VR8 = 416, CT_ATT = 448, CT_RED = 480, CT_ZRDY2 = 496, CT_FX = 512  , CT_BYTES = 1024;
DI void sig(unsigned a, unsigned v, int lane) { if (lane == 0) lds_st32(a, v); }

#define POLL_MAX (1 << 15)
DI void publish(char* ws, size_t off, u64 E, int lane) {
  asm volatile("s_waitcnt vmcnt(0)" ::: "memory");
  if (lane == 0) __hip_atomic_store((u64*)(ws + off), E, __ATOMIC_RELAXED, __HIP_MEMORY_SCOPE_AGENT);
}
#define POLL_SLEEP 6
DI void poll(const char* ws, size_t off, u64 E) {
#pragma nounroll
  for (int it = 0; it < POLL_MAX; ++it) {
    const u64 v = __hip_atomic_load((const u64*)(ws + off), __ATOMIC_RELAXED, __HIP_MEMORY_SCOPE_AGENT);
    if (__builtin_amdgcn_ballot_w64(v != E) == 0ull) break;
    __builtin_amdgcn_s_sleep(POLL_SLEEP);
  }
  asm volatile("" ::: "memory");
}

constexpr int K1_STAGE = 32768;
constexpr int A1_K = 0, A1_V = 3072, A1_W = 5120, A1_WAVE = 5632, A1_O = 2 * A1_WAVE, A1_PAIR = A1_O + 4096;
constexpr int L_AUX = 4 * K1_STAGE;
constexpr int L_CT = L_AUX + 2 * A1_PAIR;
constexpr int L_TOTAL = L_CT + CT_BYTES;
constexpr int K1_CT = L_CT;
constexpr int CV_COPY = 4160, CV_H = 4608, CV_REG = CV_H + 24576;
constexpr int AT_K = 64000, AT_V = AT_K + 16 * 2048, AT_END = AT_V + 16 * 1344;
constexpr int p3_w(int t) { return t < 5 ? t * 12800 : (t == 5 ? AT_END : AT_K + (t - 6) * 12800); }

#define PSYNC() do { asm volatile("s_waitcnt lgkmcnt(0)" ::: "memory"); ++ps; sig(pf + wh * 4, ps, lane); spin_ge(pf + (wh ^ 1) * 4, ps); } while (0)
DI void a1_issue(unsigned lds, int tile, int lane, int wh, const u16* ak, const u16* av, const u16* shk) {
  const int ts = tile / 10, td = tile - ts * 10;
  const int s0 = ts * 32 + wh * 16, d0 = td * 64;
  const int lr = lane >> 3, lc = (lane & 7) * 16;
  const unsigned wl = lds + wh * A1_WAVE;
#pragma unroll
  for (int i = 0; i < 3; ++i) {
    int r = s0 - 3 + i * 8 + lr; r = r < 0 ? 0 : r;
    if (i < 2 || lane < 24) glds16nt((unsigned)(r * DM * 2 + lc), (const char*)ak + d0 * 2, wl + A1_K + i * 1024);
  }
#pragma unroll
  for (int i = 0; i < 2; ++i)
    glds16nt((unsigned)(lr * DM * 2 + lc), (const char*)av + ((size_t)(s0 + i * 8) * DM + d0) * 2, wl + A1_V + i * 1024);
  if (lane < 32) glds16((unsigned)(lane * 16), (const char*)shk + d0 * 8, wl + A1_W);
}
DI void a1_finish(unsigned lds, unsigned pf, unsigned& ps, int tile, int lane, int wh, char* ws, u64 E) {
  const int ts = tile / 10, td = tile - ts * 10;
  const int c = lane & 7;
  const unsigned wl = lds + wh * A1_WAVE;
  u32x4 wt[4];
#pragma unroll
  for (int i = 0; i < 4; ++i) wt[i] = __builtin_bit_cast(u32x4, lds_read16(wl + A1_W + c * 64 + i * 16));
#pragma unroll
  for (int j = 0; j < 2; ++j) {
    const int r = j * 8 + (lane >> 3);
    const unsigned ka = wl + A1_K + (r + 3) * 128 + c * 16;
    u32x4 k0 = __builtin_bit_cast(u32x4, lds_read16(ka)), k1 = __builtin_bit_cast(u32x4, lds_read16(ka - 128));
    u32x4 k2 = __builtin_bit_cast(u32x4, lds_read16(ka - 256)), k3 = __builtin_bit_cast(u32x4, lds_read16(ka - 384));
    const u32x4 vv = __builtin_bit_cast(u32x4, lds_read16(wl + A1_V + r * 128 + c * 16));
    if (j == 0 && ts == 0 && wh == 0) {
      const u32x4 z = {0u, 0u, 0u, 0u};
      if (r < 1) k1 = z;
      if (r < 2) k2 = z;
      if (r < 3) k3 = z;
    }
    u32x4 o;
#pragma unroll
    for (int i = 0; i < 4; ++i) {
      const unsigned pa0 = __builtin_amdgcn_perm(k1[i], k0[i], 0x05040100u), pb0 = __builtin_amdgcn_perm(k3[i], k2[i], 0x05040100u);
      const unsigned pa1 = __builtin_amdgcn_perm(k1[i], k0[i], 0x07060302u), pb1 = __builtin_amdgcn_perm(k3[i], k2[i], 0x07060302u);
      const float e0 = dot2bf(pb0, wt[i][1], dot2bf(pa0, wt[i][0], 0.f));
      const float e1 = dot2bf(pb1, wt[i][3], dot2bf(pa1, wt[i][2], 0.f));
      o[i] = pk_bf16(e0 * bf_lo(vv[i]), e1 * bf_hi(vv[i]));
    }
    lds_write16(lds + A1_O + (31 - wh * 16 - r) * 128 + c * 16, o);
  }
  PSYNC();
  const int gg = lane >> 4, q = (lane & 15) >> 2, p = lane & 3;
  u16* ut = (u16*)(ws + WS_UT);
#pragma unroll
  for (int j = 0; j < 2; ++j) {
    const unsigned ra = lds + A1_O + (gg * 8 + q) * 128 + wh * 64 + j * 32 + p * 8;
    const s16x4 t0 = tr_read(ra), t1 = tr_read(ra + 512);
    const int d = td * 64 + wh * 32 + j * 16 + (lane & 15);
    gstore16c(ut + (size_t)d * SEQ + (SEQ - 32 - ts * 32) + gg * 8, __builtin_bit_cast(u32x4, cat8(t0, t1)));
  }
}
DI void a1_publish(int tile, int lane, int wh, char* ws, u64 E) {
  const int ts = tile / 10, td = tile - ts * 10;
  publish(ws, FL_U + (size_t)((td * 2 + wh) * 64 + ts) * 8, E, lane);
}

template <bool VT>
DI void k1_gemm(unsigned lds0, int m0, int n0, int tid, char* ws, Diag& dg) {
  const int lane = tid & 63, wave = __builtin_amdgcn_readfirstlane(tid >> 6);
  const int wm = wave >> 1, wn = wave & 1;
  const int l32 = lane & 31, hh = lane >> 5, gg = lane >> 4, q = (lane & 15) >> 2, p = lane & 3;
  const unsigned ct = lds0 + K1_CT;
  f32x16 acc[2][2];
#pragma unroll
  for (int i = 0; i < 2; ++i)
#pragma unroll
    for (int j = 0; j < 2; ++j)
#pragma unroll
      for (int r = 0; r < 16; ++r) acc[i][j][r] = 0.f;
  unsigned xb[4];
#pragma unroll
  for (int ks = 0; ks < 4; ++ks) xb[ks] = lds0 + (wm * 64 + l32) * 128 + (((unsigned)(2 * ks + hh) ^ (unsigned)((l32 >> 1) & 7)) * 16);
  unsigned wb[2];
#pragma unroll
  for (int nt = 0; nt < 2; ++nt)
    wb[nt] = lds0 + 16384 + (8 * hh + q) * 256 + (((unsigned)(wn * 4 + 2 * nt + (gg & 1)) ^ (unsigned)(q << 1)) * 32) + p * 8;
  s16x8 x0[2], w0[2], x1[2], w1[2], x2[2], w2[2], x3[2], w3[2];
#define K1_LOADF(XF, WF, bo_, ks) do { \
    _Pragma("unroll") for (int i_ = 0; i_ < 2; ++i_) { \
      WF[i_] = cat8(tr_read(wb[i_] + (bo_) + (ks) * 4096), tr_read(wb[i_] + (bo_) + (ks) * 4096 + 1024)); \
      XF[i_] = lds_read16(xb[ks] + (bo_) + i_ * 4096); } } while (0)
#define K1_MM(XF, WF) do { _Pragma("unroll") for (int mt = 0; mt < 2; ++mt) _Pragma("unroll") for (int nt = 0; nt < 2; ++nt) \
      acc[mt][nt] = VT ? MFMA32(XF[mt], WF[nt], acc[mt][nt]) : MFMA32(WF[nt], XF[mt], acc[mt][nt]); } while (0)
#define K1_ILV() do { _Pragma("unroll") for (int i_ = 0; i_ < 3; ++i_) { \
      __builtin_amdgcn_sched_group_barrier(0x008, 1, 0); __builtin_amdgcn_sched_group_barrier(0x100, 3, 0); } \
      __builtin_amdgcn_sched_group_barrier(0x008, 1, 0); } while (0)
  STAMP(1);
  K1_LOADF(x0, w0, 0, 0);
  K1_LOADF(x1, w1, 0, 1);
#pragma nounroll
  for (int t = 0; t < 10; ++t) {
    const unsigned bo = (t & 3) * K1_STAGE, bn = ((t + 1) & 3) * K1_STAGE;
    SBAR();
    const u32x4 fl4 = *(volatile LAS u32x4*)(uintptr_t)(ct + CT_FX + (t + 1) * 16);
    K1_LOADF(x2, w2, bo, 2); K1_MM(x0, w0); K1_ILV();
    SBAR();
    K1_LOADF(x3, w3, bo, 3); K1_MM(x1, w1); K1_ILV();
    SBAR();
    const unsigned flu = (unsigned)__builtin_amdgcn_readfirstlane(min(min(fl4[0], fl4[1]), min(fl4[2], fl4[3])));
    if (t < 9 && flu == 0u) spin_ge4(ct + CT_FX + (t + 1) * 16, 1u);
    asm volatile("" ::: "memory");
    SBAR();
    K1_LOADF(x0, w0, bn, 0); K1_MM(x2, w2); K1_ILV();
    SBAR();
    asm volatile("" : "+v"(x3[0]), "+v"(x3[1]), "+v"(w3[0]), "+v"(w3[1]) :: "memory");
    if (lane == 0) lds_st32(ct + CT_FREE + (t & 3) * 16 + wave * 4, (unsigned)(t + 1));
    SBAR();
    K1_LOADF(x1, w1, bn, 1); K1_MM(x3, w3); K1_ILV();
  }
  SBAR();
  STAMP(2);
  if (!VT) {
    const int part = n0 >= DM ? 1 : 0;
    const float sc = part == 0 ? 0.22360679774997896f * 1.4426950408889634f : 1.0f;
    char* base = ws + (part ? WS_KF : WS_QF) + ((size_t)((m0 + wm * 64) >> 5) * 40 + ((n0 - part * DM + wn * 64) >> 4)) * 1024 + lane * 16;
#pragma unroll
    for (int mt = 0; mt < 2; ++mt)
#pragma unroll
      for (int nt = 0; nt < 2; ++nt)
#pragma unroll
        for (int uh = 0; uh < 2; ++uh) {
          u32x4 o;
#pragma unroll
          for (int i = 0; i < 4; ++i) o[i] = pk_bf16(acc[mt][nt][8 * uh + 2 * i] * sc, acc[mt][nt][8 * uh + 2 * i + 1] * sc);
          gstore16c(base + (mt * 40 + nt * 2 + uh) * 1024, o);
        }
  } else {
    char* base = ws + WS_VF + ((size_t)((m0 + wm * 64) >> 4) * 20 + ((n0 - 2 * DM + wn * 64) >> 5)) * 1024 + lane * 16;
#pragma unroll
    for (int mt = 0; mt < 2; ++mt)
#pragma unroll
      for (int nt = 0; nt < 2; ++nt)
#pragma unroll
        for (int kg = 0; kg < 2; ++kg) {
          u32x4 o;
#pragma unroll
          for (int i = 0; i < 4; ++i) o[i] = pk_bf16(acc[mt][nt][8 * kg + 2 * i], acc[mt][nt][8 * kg + 2 * i + 1]);
          gstore16c(base + ((mt * 2 + kg) * 20 + nt) * 1024, o);
        }
  }
}

DI void cv_put(unsigned y, const u32x4& c, const u32x2& n) {
  const unsigned d0 = c[0], d1 = c[1], d2 = c[2], d3 = c[3], d4 = n[0], d5 = n[1];
  const unsigned a0 = __builtin_amdgcn_alignbit(d1, d0, 16), a1 = __builtin_amdgcn_alignbit(d2, d1, 16),
                 a2 = __builtin_amdgcn_alignbit(d3, d2, 16), a3 = __builtin_amdgcn_alignbit(d4, d3, 16),
                 a4 = __builtin_amdgcn_alignbit(d5, d4, 16);
  lds_write16(y, (u32x4){d0, d1, d2, d3});
  lds_write16(y + CV_COPY, (u32x4){a0, a1, a2, a3});
  lds_write16(y + 2 * CV_COPY, (u32x4){d1, d2, d3, d4});
  lds_write16(y + 3 * CV_COPY, (u32x4){a1, a2, a3, a4});
}

#define QSYNC() do { asm volatile("s_waitcnt lgkmcnt(0)" ::: "memory"); ++qs; sig(qf + wq * 4, qs, lane); spin_ge4(qf, qs); } while (0)
struct HRegs { u32x4 c, u; u32x2 n; unsigned d; };
DI HRegs conv_load_h(int ch, int t256, const u16* __restrict__ ssmk, const u16* __restrict__ ssmd) {
  HRegs r;
  const u16* hrow = ssmk + (size_t)ch * SEQ;
  r.c = __builtin_nontemporal_load((const u32x4*)(hrow + t256 * 8));
  r.n = *(const u32x2*)(hrow + (t256 < 255 ? t256 * 8 + 8 : 0));
  r.d = ssmd[ch];
  return r;
}
DI void conv_load_u(HRegs& r, int ch, int lane, int wq, const char* __restrict__ ws, u64 E) {
  poll(ws, FL_U + (size_t)((ch >> 5) * 64 + 48 - 16 * wq + (lane & 15)) * 8, E);
  const u64* up = (const u64*)(ws + WS_UT + (size_t)ch * SEQ * 2 + wq * 1024 + lane * 16);
  const u64 a = __hip_atomic_load(up, __ATOMIC_RELAXED, __HIP_MEMORY_SCOPE_AGENT);
  const u64 b = __hip_atomic_load(up + 1, __ATOMIC_RELAXED, __HIP_MEMORY_SCOPE_AGENT);
  r.u = (u32x4){(unsigned)a, (unsigned)(a >> 32), (unsigned)b, (unsigned)(b >> 32)};
}
DI void conv_stage(unsigned reg, unsigned qf, unsigned& qs, int lane, int wq, const HRegs& hr) {
  const int t256 = wq * 64 + lane;
  const unsigned hb = reg + CV_H;
  QSYNC();
  if (t256 < 32) lds_write16(reg + 4096 + t256 * 16, (u32x4){0u, 0u, 0u, 0u});
  cv_put(hb + (t256 + 2) * 16, hr.c, hr.n);
  if (t256 < 2) {
    const u32x4 z = {0u, 0u, 0u, 0u};
    u32x2 nx = {0u, 0u};
    const unsigned h0 = __builtin_amdgcn_readfirstlane(hr.c[0]), h1 = __builtin_amdgcn_readfirstlane(hr.c[1]);
    if (t256 == 1) nx = (u32x2){h0, h1};
    cv_put(hb + t256 * 16, z, nx);
  }
}
DI void conv_run(unsigned reg, unsigned qf, unsigned& qs, int ch, int lane, int wq, const HRegs& hr,
                 char* __restrict__ ws, Diag& dg) {
  const unsigned hb = reg + CV_H;
  lds_write16(reg + (wq * 64 + lane) * 16, hr.u);
  QSYNC();
  STAMP(6);
  const int r16 = lane & 15, g = lane >> 4, q = r16 >> 2, p = lane & 3;
  const unsigned ab0 = hb + ((q + 1) & 3) * CV_COPY + 2 * (8 * g + 4 * p + (q == 3 ? 4 : 0)) + wq * 128;
  const unsigned bb0 = reg + 2 * (2032 - 16 * r16 + 8 * g) + wq * 128 - 7 * 512;
  f32x4 acc[8];
#pragma unroll
  for (int i = 0; i < 8; ++i) acc[i] = (f32x4){0.f, 0.f, 0.f, 0.f};
#define CV_LDA(a_, ad_) cat8(tr_read((ad_) + (a_) * 512), tr_read((ad_) + (a_) * 512 + 8))
#define CV_LDB(c_, bd_) lds_read16((bd_) + (7 - (c_)) * 512)
  {
    s16x8 A[8], B[8];
    B[0] = CV_LDB(0, bb0);
#pragma unroll
    for (int a = 0; a < 8; ++a) A[a] = CV_LDA(a, ab0);
#pragma unroll
    for (int c = 1; c < 8; ++c) B[c] = CV_LDB(c, bb0);
#pragma unroll
    for (int c = 0; c < 8; ++c) {
      SBAR();
#pragma unroll
      for (int a = 0; a + c < 8; ++a) acc[a + c] = MFMA16(A[a], B[c], acc[a + c]);
      B[c] = CV_LDB(c, bb0 + 64);
      A[7 - c] = CV_LDA(7 - c, ab0 + 64);
    }
    SBAR();
#pragma unroll
    for (int a = 7; a >= 0; --a)
#pragma unroll
      for (int c = 0; a + c < 8; ++c) acc[a + c] = MFMA16(A[a], B[c], acc[a + c]);
  }
  QSYNC();
#pragma unroll
  for (int nt = 0; nt < 8; ++nt) {
    const int dw = nt >> 1;
    if (dw != wq) lds_write16(hb + (nt * 3 + (wq > dw ? wq - 1 : wq)) * 1024 + lane * 16, __builtin_bit_cast(u32x4, acc[nt]));
  }
  f32x4 k0 = wq == 0 ? acc[0] : (wq == 1 ? acc[2] : (wq == 2 ? acc[4] : acc[6]));
  f32x4 k1 = wq == 0 ? acc[1] : (wq == 1 ? acc[3] : (wq == 2 ? acc[5] : acc[7]));
  QSYNC();
  const float dco = bf_lo(hr.d);
  u32x2 zz[2];
#pragma unroll
  for (int i = 0; i < 2; ++i) {
    f32x4 o = i ? k1 : k0;
#pragma unroll
    for (int j = 0; j < 3; ++j) o += __builtin_bit_cast(f32x4, lds_read16(hb + ((wq * 2 + i) * 3 + j) * 1024 + lane * 16));
    const int s = (wq * 2 + i) * 256 + 16 * r16 + 4 * g;
    const u32x2 uu = __builtin_bit_cast(u32x2, lds_read8(reg + 2 * (SEQ - 4 - s)));
    const float kv0 = bf_hi(uu[1]), kv1 = bf_lo(uu[1]), kv2 = bf_hi(uu[0]), kv3 = bf_lo(uu[0]);
    const unsigned y01 = pk_bf16(o[0], o[1]), y23 = pk_bf16(o[2], o[3]);
    const unsigned e01 = pk_bf16(kv0 * dco, kv1 * dco), e23 = pk_bf16(kv2 * dco, kv3 * dco);
    zz[i][0] = pk_bf16(bf_lo(y01) + bf_lo(e01), bf_hi(y01) + bf_hi(e01));
    zz[i][1] = pk_bf16(bf_lo(y23) + bf_lo(e23), bf_hi(y23) + bf_hi(e23));
  }
  const auto sx = __builtin_amdgcn_permlane16_swap(zz[0][0], zz[1][0], false, false);
  const auto sy = __builtin_amdgcn_permlane16_swap(zz[0][1], zz[1][1], false, false);
  u16* zrow = (u16*)(ws + WS_ZT) + (size_t)ch * SEQ;
  gstore16c(zrow + (wq * 2 + (g & 1)) * 256 + 16 * r16 + 8 * (g >> 1), (u32x4){sx[0], sy[0], sx[1], sy[1]});
  asm volatile("s_waitcnt lgkmcnt(0)" ::: "memory");
}
DI void conv_publish(unsigned zc, int ch, int lane, char* __restrict__ ws, u64 E) {
  asm volatile("s_waitcnt vmcnt(0)" ::: "memory");
  if (lane == 0) {
    const unsigned old = __hip_atomic_fetch_add((LAS unsigned*)(uintptr_t)zc, 1u, __ATOMIC_RELAXED, __HIP_MEMORY_SCOPE_WORKGROUP);
    if ((old & 3u) == 3u) __hip_atomic_store((u64*)(ws + FL_Z + (size_t)ch * 8), E, __ATOMIC_RELAXED, __HIP_MEMORY_SCOPE_AGENT);
  }
}

DI s16x8 pack8(const f32x16& v, int o) {
  u32x4 r;
  r[0] = pk_bf16(v[o + 0], v[o + 1]); r[1] = pk_bf16(v[o + 2], v[o + 3]);
  r[2] = pk_bf16(v[o + 4], v[o + 5]); r[3] = pk_bf16(v[o + 6], v[o + 7]);
  return __builtin_bit_cast(s16x8, r);
}
DI float max3f(float a, float b, float c) { return __builtin_fmaxf(__builtin_fmaxf(a, b), c); }
DI float max16(const f32x16& v, float m) {
  float a = max3f(v[0], v[1], m), b = max3f(v[2], v[3], v[4]);
  a = max3f(a, v[5], v[6]); b = max3f(b, v[7], v[8]);
  a = max3f(a, v[9], v[10]); b = max3f(b, v[11], v[12]);
  a = max3f(a, v[13], v[14]);
  return max3f(a, b, v[15]);
}
DI float xhalf_max(float m) {
  auto sw = __builtin_amdgcn_permlane32_swap(__float_as_uint(m), __float_as_uint(m), false, false);
  return __builtin_fmaxf(__uint_as_float(sw[0]), __uint_as_float(sw[1]));
}

#define AT_LMAX 65536.0f
#define P3_WADDR() unsigned wv[4]; _Pragma("unroll") for (int i = 0; i < 4; ++i) { \
      const int c = i < 3 ? (i * 4 + w) * 64 + lane : 768 + w * 8 + (lane & 7); const int row = c / 10; \
      wv[i] = (unsigned)(row * DM * 2 + (c - row * 10) * 16); } const char* wog = (const char*)(wo + n3)
#define P3_STW(t) do { _Pragma("unroll") for (int i_ = 0; i_ < 3; ++i_) \
        glds16(wv[i_], wog + (size_t)(t) * 80 * DM * 2, lds0 + p3_w(t) + (i_ * 4 + w) * 1024); \
      if (lane < 8) glds16(wv[3], wog + (size_t)(t) * 80 * DM * 2, lds0 + p3_w(t) + 12288 + w * 128); } while (0)
template <bool FULL>
DI void attn_task(unsigned lds0, int h, int qb8, int j, int lane, char* __restrict__ ws, u64 E, int zch, const u16* __restrict__ wo, int n3, Diag& dg) {
  const int l32 = lane & 31, hh = lane >> 5;
  const int qt = qb8 * 8 + j;
  const int tfirst = FULL ? 0 : 8 - qt;
  const unsigned l16 = (unsigned)(lane * 16), ct = lds0 + L_CT;
  const int qa = (5 * h) >> 2;
  unsigned vo[6];
  if (j < 4) {
#pragma unroll
    for (int i = 0; i < 6; ++i) {
      const int c = (i * 4 + j) * 64 + lane;
      const int kgp = (c * 1561) >> 16, rem = c - kgp * 42, vh = rem >= 21 ? 1 : 0, d = rem - vh * 21;
      const int n = h * 20 + d;
      const bool in = c < 1344 && (FULL || kgp >= 16);
      if (in && d == 20) lds_write16(lds0 + AT_V + c * 16, (u32x4){0x3f803f80u, 0x3f803f80u, 0x3f803f80u, 0x3f803f80u});
      vo[i] = (in && d < 20) ? (unsigned)(((kgp + 16) * 20 + (n >> 5)) * 1024 + (vh * 32 + (n & 31)) * 16) : 0xffffffffu;
    }
    asm volatile("s_waitcnt lgkmcnt(0)" ::: "memory");
  }
  if (j == 0) {
    const int sel = lane >> 3, hi = lane & 1;
    int rb = sel < 2 ? 2 * qb8 - 2 + ((lane >> 1) & 3) : 2 * qb8 + ((lane >> 1) & 1);
    rb = rb < 0 ? 0 : rb;
    const int col = sel == 1 ? 2 * DM + h * 20 + hi * 19 : (sel == 0 ? DM : 0) + qa * 16 + hi * 31;
    poll(ws, FL_Q + (size_t)(rb * 16 + (col >> 7)) * 8, E);
    sig(ct + CT_GO, 1u, lane);
  } else spin_ge(ct + CT_GO, 1u);
  STAMP(7);
  u32x4 q0r, q1r;
  {
    const char* qg = ws + WS_QF + ((size_t)qt * 40 + qa) * 1024 + lane * 16;
    asm volatile("global_load_dwordx4 %0, %2, off sc1\n\tglobal_load_dwordx4 %1, %2, off offset:1024 sc1"
                 : "=&v"(q0r), "=&v"(q1r) : "v"(qg) : "memory");
  }
  if (j < 4) {
    const int kt0 = qb8 * 8 - 8;
#pragma unroll
    for (int i = 0; i < 8; ++i) {
      const int pc = i * 4 + j;
      if (FULL || i >= 4)
        glds16c(l16, ws + WS_KF + ((size_t)(kt0 + (pc >> 1)) * 40 + qa + (pc & 1)) * 1024, lds0 + AT_K + pc * 1024);
    }
    glds_mark(ct + CT_KR8 + j * 4, lane);
#pragma unroll
    for (int i = 0; i < 6; ++i) {
      const int pc = i * 4 + j;
      if (i < 5 || j == 0) {
        if (vo[i] != 0xffffffffu) glds16c(vo[i], ws + WS_VF + ((ptrdiff_t)(kt0 * 2 - 16) * 20) * 1024, lds0 + AT_V + pc * 1024);
      }
    }
    glds_mark(ct + CT_VR8 + j * 4, lane);
  }
  spin_ge4(ct + CT_KR8, 1u);
  if (!FULL) spin_ge4(ct + CT_VR8, 1u);
  if (j >= 4) conv_publish(ct + CT_ZCNT + 4, zch, lane, ws, E);
  else wait_vm<7>();
  asm volatile("" : "+v"(q0r), "+v"(q1r) :: "memory");
  {
    const int g0 = 4 * qa + hh - 5 * h;
    const bool k0 = g0 >= 0, k1 = g0 + 2 >= 0 && g0 + 2 < 5, k2 = g0 + 4 < 5, k3 = g0 + 6 < 5;
    q0r[0] = k0 ? q0r[0] : 0u; q0r[1] = k0 ? q0r[1] : 0u; q0r[2] = k1 ? q0r[2] : 0u; q0r[3] = k1 ? q0r[3] : 0u;
    q1r[0] = k2 ? q1r[0] : 0u; q1r[1] = k2 ? q1r[1] : 0u; q1r[2] = k3 ? q1r[2] : 0u; q1r[3] = k3 ? q1r[3] : 0u;
  }
  s16x8 qlo = __builtin_bit_cast(s16x8, q0r), qhi8 = __builtin_bit_cast(s16x8, q1r);
  STAMP(8);
  const f32x16 zero16 = {0.f, 0.f, 0.f, 0.f, 0.f, 0.f, 0.f, 0.f, 0.f, 0.f, 0.f, 0.f, 0.f, 0.f, 0.f, 0.f};
  const float ninf = -INFINITY;
  const f32x16 ninf16 = {ninf, ninf, ninf, ninf, ninf, ninf, ninf, ninf, ninf, ninf, ninf, ninf, ninf, ninf, ninf, ninf};
  const unsigned kb = lds0 + AT_K + j * 2048 + l16;
#define AT_QK(dst, t, cin) do { if (FULL || (t) >= tfirst) { dst = MFMA32(lds_read16(kb + (t) * 2048), qlo, cin); \
    dst = MFMA32(lds_read16(kb + (t) * 2048 + 1024), qhi8, dst); } else dst = ninf16; } while (0)
  const int vrow = l32 <= 20 ? l32 : (l32 == 24 ? 20 : l32 - 16);
  const unsigned vb = lds0 + AT_V + j * 1344 + hh * 336 + vrow * 16;
#define AT_EXP(sv) do { _Pragma("unroll") for (int r = 0; r < 16; ++r) sv[r] = __builtin_amdgcn_exp2f(sv[r] - m); } while (0)
#define AT_PV(sv, t) do { oacc = MFMA32(lds_read16(vb + (t) * 1344), pack8(sv, 0), oacc); oacc = MFMA32(lds_read16(vb + (t) * 1344 + 672), pack8(sv, 8), oacc); } while (0)
  f32x16 oacc;
  bool exact = !FULL;
  if (FULL) {
    f32x16 sm, sa, sb;
    AT_QK(sm, 8, zero16); AT_QK(sa, 0, zero16);
    const int tq = l32 - 4 * hh;
    const unsigned wm = tq < 0 ? 0xffffffffu : (0xfffffffeu << tq);
    unsigned mk[16];
#pragma unroll
    for (int r = 0; r < 16; ++r) {
      mk[r] = (unsigned)__builtin_amdgcn_sbfe((int)wm, (r & 3) + 8 * (r >> 2), 1);
      sm[r] = __uint_as_float((mk[r] & __float_as_uint(sa[r])) | (~mk[r] & __float_as_uint(sm[r])));
    }
    const float m = xhalf_max(max16(sm, ninf));
    const float nm = -m;
    const f32x16 cneg = {nm, nm, nm, nm, nm, nm, nm, nm, nm, nm, nm, nm, nm, nm, nm, nm};
    AT_QK(sa, 1, cneg);
    AT_EXP(sm);
    u32x4 pl = __builtin_bit_cast(u32x4, pack8(sm, 0)), ph = __builtin_bit_cast(u32x4, pack8(sm, 8)), ql, qh;
#pragma unroll
    for (int i = 0; i < 4; ++i) {
      ql[i] = pl[i] & ((mk[2 * i] & 0xffffu) | (mk[2 * i + 1] & 0xffff0000u)); pl[i] ^= ql[i];
      qh[i] = ph[i] & ((mk[8 + 2 * i] & 0xffffu) | (mk[9 + 2 * i] & 0xffff0000u)); ph[i] ^= qh[i];
    }
    spin_ge4(ct + CT_VR8, 1u);
    STAMP(9);
    oacc = MFMA32(lds_read16(vb + 8 * 1344), __builtin_bit_cast(s16x8, pl), zero16);
    oacc = MFMA32(lds_read16(vb + 8 * 1344 + 672), __builtin_bit_cast(s16x8, ph), oacc);
    oacc = MFMA32(lds_read16(vb), __builtin_bit_cast(s16x8, ql), oacc);
    oacc = MFMA32(lds_read16(vb + 672), __builtin_bit_cast(s16x8, qh), oacc);
#define AT_EX0(sv) do { _Pragma("unroll") for (int r = 0; r < 16; ++r) sv[r] = __builtin_amdgcn_exp2f(sv[r]); } while (0)
    AT_QK(sb, 2, cneg); AT_EX0(sa); AT_PV(sa, 1);
    AT_QK(sa, 3, cneg); AT_EX0(sb); AT_PV(sb, 2);
    AT_QK(sb, 4, cneg); AT_EX0(sa); AT_PV(sa, 3);
    AT_QK(sa, 5, cneg); AT_EX0(sb); AT_PV(sb, 4);
    AT_QK(sb, 6, cneg); AT_EX0(sa); AT_PV(sa, 5);
    AT_QK(sa, 7, cneg); AT_EX0(sb); AT_PV(sb, 6);
    AT_EX0(sa); AT_PV(sa, 7);
    float big = max3f(__builtin_fabsf(oacc[0]), __builtin_fabsf(oacc[1]), __builtin_fabsf(oacc[2]));
    big = max3f(big, __builtin_fabsf(oacc[3]), __builtin_fabsf(oacc[4]));
    big = max3f(big, __builtin_fabsf(oacc[5]), __builtin_fabsf(oacc[6]));
    big = max3f(big, __builtin_fabsf(oacc[7]), __builtin_fabsf(oacc[8]));
    big = max3f(big, __builtin_fabsf(oacc[9]), __builtin_fabsf(oacc[10]));
    big = __builtin_fmaxf(big, __builtin_fabsf(oacc[11]));
    const float ls = hh ? oacc[8] : oacc[12];
    float osum = 0.f;
#pragma unroll
    for (int r = 0; r < 8; ++r) osum += oacc[r];
    const bool ok = (ls < AT_LMAX) && (big < 1e37f) && (osum == osum);
    exact = __builtin_amdgcn_ballot_w64(!ok) != 0ull;
  }
  if (exact) {
    asm volatile("" : "+v"(qlo), "+v"(qhi8) :: "memory");
    f32x16 sm, s1, s2, s3;
    {
      f32x16 s0;
      AT_QK(sm, 8, zero16); AT_QK(s0, 0, zero16);
      AT_QK(s1, 1, zero16); AT_QK(s2, 2, zero16); AT_QK(s3, 3, zero16);
#pragma unroll
      for (int r = 0; r < 16; ++r) {
        const int kk = (r & 3) + 8 * (r >> 2) + 4 * hh;
        sm[r] = kk > l32 ? s0[r] : sm[r];
      }
    }
    float m = max16(sm, ninf);
    m = max16(s1, m); m = max16(s2, m); m = max16(s3, m);
    m = xhalf_max(m);
    AT_EXP(sm);
    oacc = zero16;
    {
      f32x16 p0, p8;
#pragma unroll
      for (int r = 0; r < 16; ++r) {
        const int kk = (r & 3) + 8 * (r >> 2) + 4 * hh;
        p0[r] = kk > l32 ? sm[r] : 0.f;
        p8[r] = kk > l32 ? 0.f : sm[r];
      }
      AT_PV(p8, 8);
      if (FULL) AT_PV(p0, 0);
    }
    if (FULL || 1 >= tfirst) { AT_EXP(s1); AT_PV(s1, 1); }
    AT_QK(sm, 4, zero16); AT_QK(s1, 5, zero16);
    if (FULL || 2 >= tfirst) { AT_EXP(s2); AT_PV(s2, 2); }
    AT_QK(s2, 6, zero16);
    if (FULL || 3 >= tfirst) { AT_EXP(s3); AT_PV(s3, 3); }
    AT_QK(s3, 7, zero16);
    float m2 = max16(sm, m);
    m2 = max16(s1, m2); m2 = max16(s2, m2); m2 = max16(s3, m2);
    m2 = xhalf_max(m2);
    {
      const float alpha = __builtin_amdgcn_exp2f(m - m2);
#pragma unroll
      for (int r = 0; r < 13; ++r) oacc[r] *= alpha;
      m = m2;
    }
    if (FULL || 4 >= tfirst) { AT_EXP(sm); AT_PV(sm, 4); }
    if (FULL || 5 >= tfirst) { AT_EXP(s1); AT_PV(s1, 5); }
    if (FULL || 6 >= tfirst) { AT_EXP(s2); AT_PV(s2, 6); }
    if (FULL || 7 >= tfirst) { AT_EXP(s3); AT_PV(s3, 7); }
  }
  asm volatile("s_waitcnt lgkmcnt(0)" ::: "memory");
  sig(ct + CT_ATT + j * 4, 1u, lane);
  STAMP(10);
  const float l = hh ? oacc[8] : oacc[12];
  const float inv = __builtin_amdgcn_rcpf(l);
  char* oc = ws + WS_OW + ((size_t)qt * NH + h) * 1280;
  u32x4 o;
  o[0] = pk_bf16(oacc[0] * inv, oacc[1] * inv); o[1] = pk_bf16(oacc[2] * inv, oacc[3] * inv);
  o[2] = pk_bf16(oacc[4] * inv, oacc[5] * inv); o[3] = pk_bf16(oacc[6] * inv, oacc[7] * inv);
  gstore16c(oc + lane * 16, o);
  if (!hh) {
    u32x2 o2;
    o2[0] = pk_bf16(oacc[8] * inv, oacc[9] * inv); o2[1] = pk_bf16(oacc[10] * inv, oacc[11] * inv);
    gstore8c(oc + 1024 + l32 * 8, o2);
  }
  publish(ws, FL_O + (size_t)(qt * NH + h) * 8, E, lane);
}

#define ROTG 2
#define A1_DELAY 8
__global__ __launch_bounds__(768, 3) void mega(const u16* __restrict__ x, const u16* __restrict__ wqkv,
                                               const u16* __restrict__ ak, const u16* __restrict__ av,
                                               const u16* __restrict__ shk, const u16* __restrict__ wo,
                                               const u16* __restrict__ ssmk, const u16* __restrict__ ssmd,
                                               const u16* __restrict__ aq, char* __restrict__ ws,
                                               u16* __restrict__ out) {
  extern __shared__ __attribute__((aligned(16))) char smem[];
  const unsigned lds0 = lds_addr(smem);
  const int tid = threadIdx.x, wave = __builtin_amdgcn_readfirstlane(tid >> 6);
  int lane = tid & 63;
#define RELANE() asm volatile("" : "+v"(lane))
  const int role = wave >> 2, w = wave & 3;
  const int bid = blockIdx.x, xcd = bid & 7, idx = bid >> 3;
  const bool has_gemm = idx < 30;
  const unsigned ct = lds0 + L_CT;
  DIAG_INIT();
  u64 E;
  asm volatile("s_load_dwordx2 %0, %1, 0x0" : "=s"(E) : "s"(ws + WS_FL) : "memory");
  const int gt = (xcd & 1) * 30 + idx;
  const int tm = (xcd >> 1) * 4 + (gt & 3), tn = gt >> 2;
  const int m0 = tm * 128, n0 = tn * 128;
  const int rot = ROTG >= 10 ? (((gt & 3) * 5 + 1) / 2 + 3 * tn) % 10 : ((((gt & 3) + tn) % ROTG) * 10) / ROTG;
  const unsigned xo0 = (unsigned)((lane >> 3) * DM * 2 + (((lane & 7) ^ (lane >> 4)) * 16));
  const unsigned xo1 = (unsigned)((lane >> 3) * DM * 2 + (((lane & 7) ^ (4 + (lane >> 4))) * 16));
  const unsigned wof = (unsigned)((lane >> 4) * NQKV * 2 + (((lane & 15) ^ ((lane >> 4) << 2)) * 16));
  const char* xg = (const char*)(x + (size_t)m0 * DM);
  const char* wg = (const char*)(wqkv + n0);
#define K1_PIECE_X(t, kt, j) glds16(((j) & 1) ? xo1 : xo0, xg + (size_t)(j) * 8 * DM * 2 + (kt) * 128, lds0 + ((t) & 3) * K1_STAGE + (j) * 1024)
#define K1_PIECE_W(t, kt, j) glds16(wof, wg + (size_t)((kt) * 64 + (j) * 4) * NQKV * 2, lds0 + ((t) & 3) * K1_STAGE + 16384 + (j) * 1024)
  if (has_gemm && role == 0) {
#pragma unroll
    for (int i = 0; i < 4; ++i) { K1_PIECE_X(0, rot, w * 4 + i); K1_PIECE_W(0, rot, w * 4 + i); }
  }
  const unsigned pf = ct + CT_PAIR + (wave >> 1) * 8;
  const int wh = wave & 1;
  unsigned ps = 0;
  const int fb = (idx - 30) * 8 + xcd;
  const int a1t = has_gemm ? (xcd * 30 + idx) * 2 + (w >> 1) : 480 + fb * 10 + (wave >> 1);
  const unsigned a1l = has_gemm ? lds0 + L_AUX + (w >> 1) * A1_PAIR : lds0 + (wave >> 1) * A1_PAIR;
  if (!has_gemm || role == 2) a1_issue(a1l, a1t, lane, wh, ak, av, shk);
  const bool cv0 = role != 0 || !has_gemm, cv1 = role == 2 && bid < 112;
  const int ch0 = role == 0 ? 512 + fb : bid * 2 + (role == 1 ? 1 : 0), ch1 = 528 + bid;
  if (role == 1 && tid - 256 < CT_BYTES / 4) lds_st32(ct + (tid - 256) * 4, 0u);
  asm volatile("s_waitcnt lgkmcnt(0)\n\ts_barrier" : "+s"(E) :: "memory");
  E += 1ull;

  if (!has_gemm) {
    const unsigned a2l = lds0 + (6 + (wave >> 1)) * A1_PAIR;
    if (wave < 8) a1_issue(a2l, a1t + 6, lane, wh, ak, av, shk);
    if (wave < 8) wait_vm<6>(); else wait_vm<0>();
    a1_finish(a1l, pf, ps, a1t, lane, wh, ws, E);
    if (wave < 8) { wait_vm<2>(); a1_finish(a2l, pf, ps, a1t + 6, lane, wh, ws, E); }
    a1_publish(a1t, lane, wh, ws, E);
    if (wave < 8) a1_publish(a1t + 6, lane, wh, ws, E);
    bar();
    STAMP(1);
  } else if (role == 0) {
    wait_vm<0>();
    sig(ct + CT_S0 + w * 4, 1u, lane);
    spin_ge4(ct + CT_S0, 1u);
    __builtin_amdgcn_s_setprio(2);
    if (tn >= 10) k1_gemm<true>(lds0, m0, n0, tid, ws, dg);
    else k1_gemm<false>(lds0, m0, n0, tid, ws, dg);
    STAMP(3);
    asm volatile("s_waitcnt vmcnt(0)" ::: "memory");
    if (lane == 0) {
      const unsigned old = __hip_atomic_fetch_add((LAS unsigned*)(uintptr_t)(ct + CT_QCNT), 1u, __ATOMIC_RELAXED, __HIP_MEMORY_SCOPE_WORKGROUP);
      if (old == 3u) __hip_atomic_store((u64*)(ws + FL_Q + (size_t)(tm * 16 + tn) * 8), E, __ATOMIC_RELAXED, __HIP_MEMORY_SCOPE_AGENT);
    }
    STAMP(4);
  } else if (role == 1) {
    __builtin_amdgcn_s_setprio(3);
#define K1_ST(t) do { const int kt_ = (t) + rot >= 10 ? (t) + rot - 10 : (t) + rot; \
      if ((t) >= 4) spin_ge4(ct + CT_FREE + ((t) & 3) * 16, (unsigned)((t) - 3)); \
      _Pragma("unroll") for (int i_ = 0; i_ < 4; ++i_) { K1_PIECE_X(t, kt_, w * 4 + i_); K1_PIECE_W(t, kt_, w * 4 + i_); } } while (0)
#pragma nounroll
    for (int t = 1; t < 10; ++t) { K1_ST(t); glds_mark(ct + CT_FX + t * 16 + w * 4, lane); }
    __builtin_amdgcn_s_setprio(0);
    STAMP(1);
  } else {
    __builtin_amdgcn_s_setprio(2);
    wait_vm<0>();
    a1_finish(a1l, pf, ps, a1t, lane, wh, ws, E);
    a1_publish(a1t, lane, wh, ws, E);
    __builtin_amdgcn_s_setprio(0);
    STAMP(1);
  }

  RELANE();
  unsigned qs = 0;
  const unsigned qf = ct + CT_QUAD + role * 16;
  const unsigned creg = lds0 + (role == 2 ? L_AUX : (role == 1 ? 29184 : 0));
  if (cv0) {
    HRegs hr = conv_load_h(ch0, w * 64 + lane, ssmk, ssmd), h2 = hr;
    if (cv1) h2 = conv_load_h(ch1, w * 64 + lane, ssmk, ssmd);
    if (has_gemm && role == 1) {
      conv_load_u(hr, ch0, lane, w, ws, E);
      spin_ge4(ct + CT_FREE + 16, 10u);
      conv_stage(creg, qf, qs, lane, w, hr);
    } else {
      conv_stage(creg, qf, qs, lane, w, hr);
      conv_load_u(hr, ch0, lane, w, ws, E);
      if (cv1) conv_load_u(h2, ch1, lane, w, ws, E);
    }
    STAMP(5);
    for (int r = 0; r < (cv1 ? 2 : 1); ++r) {
      conv_run(creg, qf, qs, r ? ch1 : ch0, lane, w, hr, ws, dg);
      if (r == 0 && cv1) {
        __builtin_amdgcn_s_setprio(2);
        hr = h2;
        conv_stage(creg, qf, qs, lane, w, hr);
        conv_publish(ct + CT_ZCNT + 8 + 768, ch0, lane, ws, E);
      }
    }
    __builtin_amdgcn_s_setprio(0);
    sig(ct + CT_CVD + wave * 4, 1u, lane);
    if (role != 1) conv_publish(ct + CT_ZCNT + role * 4, cv1 ? ch1 : ch0, lane, ws, E);
  } else sig(ct + CT_CVD + wave * 4, 1u, lane);
  if (role != 0) STAMP(2);

  const int tm3 = xcd * 4 + (idx >> 3), tn3 = idx & 7;
  const int m3 = tm3 * 64, n3 = tn3 * 80;
  RELANE();

  if (role == 2) {
    const int r16 = lane & 15, g = lane >> 4, q = r16 >> 2, p = lane & 3;
    u32x4 qv[3];
#pragma unroll
    for (int j = 0; j < 3; ++j) {
      const int dc = j < 2 ? j * 4 + g : 8 + (g & 1);
      qv[j] = __builtin_nontemporal_load((const u32x4*)(aq + (size_t)(m3 + w * 16 + r16) * DM + n3 + dc * 8));
    }
    {
      P3_WADDR();
      spin_ge8(ct + CT_CVD, 1u);
      spin_ge4(ct + CT_VR8, 1u);
      P3_STW(2); P3_STW(3); P3_STW(4);
      glds_mark(ct + CT_WRDY + w * 4, lane);
      STAMP(3);
    }
    spin_ge4(ct + CT_CVD + 32, 1u);
    {
      const int l5 = lane & 31;
      const int cl = l5 < 8 ? w * 8 + l5 : (l5 < 16 ? 32 + w * 8 + (l5 - 8) : 64 + w * 4 + (l5 & 3));
      poll(ws, FL_Z + (size_t)(n3 + cl) * 8, E);
    }
    {
      const char* zg = ws + WS_ZT + ((size_t)n3 * SEQ + m3) * 2;
      const unsigned zo = (unsigned)((lane >> 3) * SEQ * 2 + (lane & 7) * 16);
      glds16c(zo, zg + (size_t)(w * 8) * SEQ * 2, lds0 + L_AUX + w * 1024);
      glds16c(zo, zg + (size_t)(32 + w * 8) * SEQ * 2, lds0 + L_AUX + 4096 + w * 1024);
      if (lane < 32) glds16c(zo, zg + (size_t)(64 + w * 4) * SEQ * 2, lds0 + L_AUX + 8192 + w * 512);
    }
    wait_vm<0>();
    sig(ct + CT_ZRDY + w * 4, 1u, lane);
    spin_ge4(ct + CT_ZRDY, 1u);
    STAMP(4);
    u32x4 zz[3];
#pragma unroll
    for (int j = 0; j < 3; ++j) {
      const int dc = j < 2 ? j * 4 + g : 8 + (g & 1);
      const unsigned za = lds0 + L_AUX + (dc * 8 + q) * 128 + w * 32 + p * 8;
      zz[j] = __builtin_bit_cast(u32x4, cat8(tr_read(za), tr_read(za + 512)));
    }
#pragma unroll
    for (int j = 0; j < 3; ++j) {
      const int dc = j < 2 ? j * 4 + g : 8 + (g & 1);
      u32x4 o;
#pragma unroll
      for (int i = 0; i < 4; ++i) o[i] = pk_bf16(bf_lo(qv[j][i]) * bf_lo(zz[j][i]), bf_hi(qv[j][i]) * bf_hi(zz[j][i]));
      if (j < 2 || g < 2) *(u32x4*)(out + (size_t)(m3 + w * 16 + r16) * (2 * DM) + n3 + dc * 8) = o;
    }
    STAMP(7);
    if (bid == 0 && w == 0) {
#pragma unroll
      for (int i = 0; i < 4; ++i) poll(ws, FL_O + (size_t)(((i * 2 + (lane >> 5)) * 8) * NH + (lane & 31)) * 8, E);
      if (lane == 0) __hip_atomic_store((u64*)(ws + WS_FL), E, __ATOMIC_RELAXED, __HIP_MEMORY_SCOPE_AGENT);
    }
    DIAG_DUMP(2);
    return;
  }

  const int h = xcd * 4 + (idx & 3), qb8 = idx >> 2;
  if (role == 0) {
    spin_ge4(ct + CT_CVD, 1u);
    if (w > 0) {
      const int t = w == 3 ? 5 : w - 1;
      const char* wsrc = (const char*)(wo + n3) + (size_t)t * 80 * DM * 2;
      const unsigned wdst = lds0 + (w == 3 ? p3_w(5) : (w - 1) * 12800);
#pragma unroll
      for (int i = 0; i < 13; ++i) {
        const int c = i * 64 + lane, row = (c * 6554) >> 16;
        if (i < 12 || lane < 32) glds16((unsigned)(row * DM * 2 + (c - row * 10) * 16), wsrc, wdst + i * 1024);
      }
    }
    glds_mark(ct + CT_ZRDY2 + w * 4, lane);
  }
  __builtin_amdgcn_s_setprio(1);
  RELANE();
  if (qb8 > 0) attn_task<true>(lds0, h, qb8, wave, lane, ws, E, ch0, wo, n3, dg);
  else attn_task<false>(lds0, h, qb8, wave, lane, ws, E, ch0, wo, n3, dg);
  STAMP(11);

  RELANE();
  const int r16 = lane & 15, g = lane >> 4, q = r16 >> 2, p = lane & 3;
  f32x4 acc[5];
#pragma unroll
  for (int i = 0; i < 5; ++i) acc[i] = (f32x4){0.f, 0.f, 0.f, 0.f};
  const unsigned wbp = lds0 + (20 * (g >> 1) + 4 * (g & 1) + q) * 160 + p * 8;
  const unsigned wbs = lds0 + (20 * g + 16 + q) * 160 + p * 8;
  u32x4 op[8]; u32x2 osg[4];
  if (role == 1) {
    P3_WADDR();
    spin_ge8(ct + CT_ATT, 1u);
    P3_STW(6); P3_STW(7);
    glds_mark(ct + CT_WRD2 + w * 4, lane);
  }
  spin_ge4(ct + CT_WRDY, 1u);
  spin_ge4(ct + CT_ZRDY2, 1u);
  const unsigned w0 = role ? p3_w(4) : p3_w(0), w1 = role ? p3_w(5) : p3_w(1), w2 = role ? p3_w(6) : p3_w(2), w3 = role ? p3_w(7) : p3_w(3);
  struct Fw { s16x8 a[5], b[5]; s16x4 s[5]; };
  Fw fwa, fwb;
#define P3_LOADW(F, bo_) do { _Pragma("unroll") for (int nt = 0; nt < 5; ++nt) { \
      F.a[nt] = cat8(tr_read(wbp + (bo_) + nt * 32), tr_read(wbp + (bo_) + 8 * 160 + nt * 32)); \
      F.b[nt] = cat8(tr_read(wbp + (bo_) + 40 * 160 + nt * 32), tr_read(wbp + (bo_) + 48 * 160 + nt * 32)); \
      F.s[nt] = tr_read(wbs + (bo_) + nt * 32); } } while (0)
#define P3_MM(F, t) do { \
    _Pragma("unroll") for (int nt = 0; nt < 5; ++nt) acc[nt] = MFMA16(F.a[nt], __builtin_bit_cast(s16x8, op[2 * (t)]), acc[nt]); \
    _Pragma("unroll") for (int nt = 0; nt < 5; ++nt) acc[nt] = MFMA16(F.b[nt], __builtin_bit_cast(s16x8, op[2 * (t) + 1]), acc[nt]); } while (0)
#define P3_MS(t) do { const s16x8 os_ = cat8(__builtin_bit_cast(s16x4, osg[t]), __builtin_bit_cast(s16x4, osg[(t) + 1])); \
    _Pragma("unroll") for (int nt = 0; nt < 5; ++nt) acc[nt] = MFMA16(cat8(fwa.s[nt], fwb.s[nt]), os_, acc[nt]); } while (0)
  P3_LOADW(fwa, w0);
  SBAR();
  if (wave == 0) {
    poll(ws, FL_O + (size_t)((m3 >> 5) * NH + lane) * 8, E);
    sig(ct + CT_GO3, 1u, lane);
  } else spin_ge(ct + CT_GO3, 1u);
  STAMP(12);
  {
    const int orow = (w & 1) * 16 + r16;
    const char* ob = ws + WS_OW + ((size_t)((m3 >> 5) + (w >> 1)) * NH + role * 16) * 1280;
    const char* pp = ob + (g >> 1) * 1280 + (g & 1) * 512 + orow * 16;
    const char* sp = ob + g * 1280 + 1024 + orow * 8;
#pragma unroll
    for (int t = 0; t < 4; ++t) {
      asm volatile("global_load_dwordx4 %0, %3, off sc1\n\tglobal_load_dwordx4 %1, %3, off offset:2560 sc1\n\tglobal_load_dwordx2 %2, %4, off sc1"
                   : "=&v"(op[2 * t]), "=&v"(op[2 * t + 1]), "=&v"(osg[t]) : "v"(pp + t * 5120), "v"(sp + t * 5120) : "memory");
    }
  }
  P3_LOADW(fwb, w1);
  if (role == 1) spin_ge4(ct + CT_WRD2, 1u);
  SBAR();
  asm volatile("s_waitcnt vmcnt(6)" : "+v"(op[0]), "+v"(op[1]), "+v"(op[2]), "+v"(op[3]), "+v"(osg[0]), "+v"(osg[1]) :: "memory");
  STAMP(13);
  P3_MS(0); P3_MM(fwa, 0);
  SBAR();
  P3_LOADW(fwa, w2); P3_MM(fwb, 1);
#pragma unroll
  for (int i = 0; i < 10; ++i) { __builtin_amdgcn_sched_group_barrier(0x008, 1, 0); __builtin_amdgcn_sched_group_barrier(0x100, 3, 0); }
  SBAR();
  asm volatile("s_waitcnt vmcnt(0)" : "+v"(op[4]), "+v"(op[5]), "+v"(op[6]), "+v"(op[7]), "+v"(osg[2]), "+v"(osg[3]) :: "memory");
  P3_LOADW(fwb, w3); P3_MM(fwa, 2);
#pragma unroll
  for (int i = 0; i < 10; ++i) { __builtin_amdgcn_sched_group_barrier(0x008, 1, 0); __builtin_amdgcn_sched_group_barrier(0x100, 3, 0); }
  SBAR();
  P3_MM(fwb, 3); P3_MS(2);
  SBAR();
  const unsigned rb = lds0 + AT_K + 25600 + w * 5120 + lane * 16;
  if (role == 1) {
#pragma unroll
    for (int nt = 0; nt < 5; ++nt) lds_write16(rb + nt * 1024, __builtin_bit_cast(u32x4, acc[nt]));
    asm volatile("s_waitcnt lgkmcnt(0)" ::: "memory");
    sig(ct + CT_RED + w * 4, 1u, lane);
    STAMP(14);
    DIAG_DUMP(1);
    return;
  }
  spin_ge(ct + CT_RED + w * 4, 1u);
  u16* orow_g = out + (size_t)(m3 + w * 16 + r16) * (2 * DM) + DM + n3 + 4 * g;
#pragma unroll
  for (int nt = 0; nt < 5; ++nt) {
    const f32x4 o2 = acc[nt] + __builtin_bit_cast(f32x4, lds_read16(rb + nt * 1024));
    u32x2 o;
    o[0] = pk_bf16(o2[0], o2[1]);
    o[1] = pk_bf16(o2[2], o2[3]);
    *(u32x2*)(orow_g + nt * 16) = o;
  }
  STAMP(14);
  DIAG_DUMP(0);
}

extern "C" void kernel_launch(void* const* d_in, const int* in_sizes, int n_in, void* d_out, int out_size,
                              void* d_ws, size_t ws_size, hipStream_t stream) {
  const u16* aq = (const u16*)d_in[0];
  const u16* ak = (const u16*)d_in[1];
  const u16* av = (const u16*)d_in[2];
  const u16* shk = (const u16*)d_in[3];
  const u16* ssmk = (const u16*)d_in[4];
  const u16* ssmd = (const u16*)d_in[5];
  const u16* x = (const u16*)d_in[6];
  const u16* wqkv = (const u16*)d_in[7];
  const u16* wo = (const u16*)d_in[8];
  (void)hipFuncSetAttribute((const void*)mega, hipFuncAttributeMaxDynamicSharedMemorySize, L_TOTAL);
  mega<<<256, 768, L_TOTAL, stream>>>(x, wqkv, ak, av, shk, wo, ssmk, ssmd, aq, (char*)d_ws, (u16*)d_out);
}
```

```cpp
#include <hip/hip_runtime.h>
#include <hip/hip_bf16.h>
#include <stdint.h>

typedef unsigned short u16;
typedef short s16x4 __attribute__((ext_vector_type(4)));
typedef short s16x8 __attribute__((ext_vector_type(8)));
typedef float f32x2 __attribute__((ext_vector_type(2)));
typedef float f32x4 __attribute__((ext_vector_type(4)));
typedef float f32x16 __attribute__((ext_vector_type(16)));
typedef unsigned u32x2 __attribute__((ext_vector_type(2)));
typedef unsigned u32x4 __attribute__((ext_vector_type(4)));
typedef __bf16 bf16x2 __attribute__((ext_vector_type(2)));
#define LAS __attribute__((address_space(3)))
#define DI __device__ __forceinline__

constexpr int SEQ = 2048, DM = 640, NQKV = 1920, NH = 32;
constexpr size_t WS_QF = 0;
constexpr size_t WS_KF = WS_QF + (size_t)DM * SEQ * 2;
constexpr size_t WS_VF = WS_KF + (size_t)DM * SEQ * 2;
constexpr size_t WS_UT = WS_VF + (size_t)DM * SEQ * 2;
constexpr size_t WS_ZT = WS_UT + (size_t)DM * SEQ * 2;
constexpr size_t WS_OW = WS_ZT + (size_t)DM * SEQ * 2;
typedef unsigned long long u64;
constexpr size_t WS_FL = WS_OW + (size_t)DM * SEQ * 2;
constexpr size_t FL_U = WS_FL + 256;
constexpr size_t FL_Q = FL_U + 20 * 64 * 8;
constexpr size_t FL_Z = FL_Q + 16 * 16 * 8;
constexpr size_t FL_O = FL_Z + 640 * 8;

DI unsigned pk_bf16(float lo, float hi) {
  f32x2 f = {lo, hi};
  return __builtin_bit_cast(unsigned, __builtin_convertvector(f, bf16x2));
}
DI float bf_lo(unsigned u) { return __uint_as_float(u << 16); }
DI float bf_hi(unsigned u) { return __uint_as_float(u & 0xffff0000u); }
DI float rbf(float x) { return bf_lo(pk_bf16(x, 0.f)); }

DI unsigned lds_addr(const void* p) { return (unsigned)(uintptr_t)(LAS const char*)p; }

DI void glds16(unsigned voff, const void* sbase, unsigned lds_dst) {
  unsigned keep;
  asm volatile("s_mov_b32 %0, m0\n\ts_mov_b32 m0, %3\n\ts_nop 0\n\tglobal_load_lds_dwordx4 %1, %2\n\ts_mov_b32 m0, %0"
               : "=&s"(keep) : "v"(voff), "s"(sbase), "s"(lds_dst) : "memory");
}
DI void glds16f(unsigned voff, const void* sbase, unsigned lds_dst) {
  asm volatile("s_mov_b32 m0, %2\n\ts_nop 0\n\tglobal_load_lds_dwordx4 %0, %1" :: "v"(voff), "s"(sbase), "s"(lds_dst) : "memory");
}
#define glds16p glds16
DI void glds16nt(unsigned voff, const void* sbase, unsigned lds_dst) {
  unsigned keep;
  asm volatile("s_mov_b32 %0, m0\n\ts_mov_b32 m0, %3\n\ts_nop 0\n\tglobal_load_lds_dwordx4 %1, %2\n\ts_mov_b32 m0, %0"
               : "=&s"(keep) : "v"(voff), "s"(sbase), "s"(lds_dst) : "memory");
}
#define LDPOL "sc1"
DI void glds16c(unsigned voff, const void* sbase, unsigned lds_dst) {
  unsigned keep;
  asm volatile("s_mov_b32 %0, m0\n\ts_mov_b32 m0, %3\n\ts_nop 0\n\tglobal_load_lds_dwordx4 %1, %2 " LDPOL "\n\ts_mov_b32 m0, %0"
               : "=&s"(keep) : "v"(voff), "s"(sbase), "s"(lds_dst) : "memory");
}
static __device__ const unsigned k_one[4] = {1u, 1u, 1u, 1u};
DI void glds_mark(unsigned lds_dst, int lane) {
  if (lane == 0) {
    unsigned keep;
    asm volatile("s_mov_b32 %0, m0\n\ts_mov_b32 m0, %3\n\ts_nop 0\n\tglobal_load_lds_dword %1, %2\n\ts_mov_b32 m0, %0"
                 : "=&s"(keep) : "v"(0u), "s"(k_one), "s"(lds_dst) : "memory");
  }
}
DI float dot2bf(unsigned a, unsigned b, float c) {
  return __builtin_amdgcn_fdot2_f32_bf16(__builtin_bit_cast(bf16x2, a), __builtin_bit_cast(bf16x2, b), c, false);
}
DI void gstore16c(void* p, u32x4 v) {
  asm volatile("global_store_dwordx4 %0, %1, off sc1\n\ts_nop 1" :: "v"(p), "v"(v) : "memory");
}
DI void gstore8c(void* p, u32x2 v) {
  asm volatile("global_store_dwordx2 %0, %1, off sc1" :: "v"(p), "v"(v) : "memory");
}
#define OUTPOL "nt"
DI void ostore16(void* p, u32x4 v) { asm volatile("global_store_dwordx4 %0, %1, off " OUTPOL "\n\ts_nop 1" :: "v"(p), "v"(v) : "memory"); }
DI void ostore8(void* p, u32x2 v) { asm volatile("global_store_dwordx2 %0, %1, off " OUTPOL :: "v"(p), "v"(v) : "memory"); }
template <int N> DI void wait_vm() { asm volatile("s_waitcnt vmcnt(%0)" :: "n"(N) : "memory"); }
DI void bar() { asm volatile("s_waitcnt lgkmcnt(0)\n\ts_barrier" ::: "memory"); }
DI s16x4 tr_read(unsigned a) {
  return __builtin_amdgcn_ds_read_tr16_b64_v4i16((LAS s16x4*)(uintptr_t)a);
}
DI s16x8 lds_read16(unsigned a) { return *(LAS const s16x8*)(uintptr_t)a; }
DI s16x4 lds_read8(unsigned a) { return *(LAS const s16x4*)(uintptr_t)a; }
DI void lds_write16(unsigned a, u32x4 v) { *(LAS u32x4*)(uintptr_t)a = v; }
DI s16x8 cat8(s16x4 a, s16x4 b) { return __builtin_shufflevector(a, b, 0, 1, 2, 3, 4, 5, 6, 7); }
#define MFMA16(a, b, c) __builtin_amdgcn_mfma_f32_16x16x32_bf16((a), (b), (c), 0, 0, 0)
#define MFMA32(a, b, c) __builtin_amdgcn_mfma_f32_32x32x16_bf16((a), (b), (c), 0, 0, 0)

struct Diag {};
#define STAMP(i) do {} while (0)
#define DIAG_INIT() Diag dg
#define DIAG_DUMP(kid) do {} while (0)

#define SBAR() __builtin_amdgcn_sched_barrier(0)
#define SPIN_SLEEP 1
#define SPIN_MAX (1 << 21)
DI unsigned lds_ld32(unsigned a) { return *(volatile LAS unsigned*)(uintptr_t)a; }
DI void lds_st32(unsigned a, unsigned v) { *(volatile LAS unsigned*)(uintptr_t)a = v; }
DI void spin_ge(unsigned a, unsigned want) {
#pragma nounroll
  for (int it = 0; it < SPIN_MAX; ++it) {
    if ((unsigned)__builtin_amdgcn_readfirstlane(lds_ld32(a)) >= want) break;
    __builtin_amdgcn_s_sleep(1);
  }
  asm volatile("" ::: "memory");
}
DI void spin_ge4(unsigned a, unsigned want) {
#pragma nounroll
  for (int it = 0; it < SPIN_MAX; ++it) {
    const u32x4 v = *(volatile LAS u32x4*)(uintptr_t)a;
    const unsigned m = min(min(v[0], v[1]), min(v[2], v[3]));
    if ((unsigned)__builtin_amdgcn_readfirstlane(m) >= want) break;
    __builtin_amdgcn_s_sleep(SPIN_SLEEP);
  }
  asm volatile("" ::: "memory");
}
DI void spin_ge8(unsigned a, unsigned want) { spin_ge4(a, want); spin_ge4(a + 16, want); }
constexpr int CT_S0 = 0, CT_FULL = 16, CT_FREE = 80, CT_PAIR = 144  , CT_QUAD = 192  , CT_ZCNT = 240  ,
              CT_GO = 252, CT_QCNT = 256, CT_GO3 = 260, CT_WRDY = 272, CT_WRD2 = 288, CT_ZRDY = 304, CT_A3D = 320,
              CT_CVD = 336  , CT_KR8 = 384, CT_VR8 = 416, CT_ATT = 448, CT_RED = 480, CT_ZRDY2 = 496, CT_FX = 512  , CT_QRD = 704  , CT_XST = 736, CT_BYTES = 1024;
DI void sig(unsigned a, unsigned v, int lane) { if (lane == 0) lds_st32(a, v); }

#define POLL_MAX (1 << 15)
DI void publish(char* ws, size_t off, u64 E, int lane) {
  asm volatile("s_waitcnt vmcnt(0)" ::: "memory");
  if (lane == 0) __hip_atomic_store((u64*)(ws + off), E, __ATOMIC_RELAXED, __HIP_MEMORY_SCOPE_AGENT);
}
#define POLL_SLEEP 6
DI void poll(const char* ws, size_t off, u64 E) {
#pragma nounroll
  for (int it = 0; it < POLL_MAX; ++it) {
    const u64 v = __hip_atomic_load((const u64*)(ws + off), __ATOMIC_RELAXED, __HIP_MEMORY_SCOPE_AGENT);
    if (__builtin_amdgcn_ballot_w64(v != E) == 0ull) break;
    __builtin_amdgcn_s_sleep(POLL_SLEEP);
  }
  asm volatile("" ::: "memory");
}

constexpr int K1_STAGE = 32768;
constexpr int A1_K = 0, A1_V = 3072, A1_W = 5120, A1_WAVE = 5632, A1_O = 2 * A1_WAVE, A1_PAIR = A1_O + 4096;
constexpr int L_AUX = 4 * K1_STAGE;
constexpr int L_CT = L_AUX + 2 * A1_PAIR;
constexpr int L_TOTAL = L_CT + CT_BYTES;
constexpr int K1_CT = L_CT;
constexpr int CV_COPY = 4160, CV_H = 4608, CV_REG = CV_H + 24576;
constexpr int AT_Q = L_AUX + 12288, AT_K = 64000, AT_V = AT_K + 16 * 2048, AT_END = AT_V + 16 * 1344;
constexpr int p3_w(int t) { return t < 5 ? t * 12800 : (t == 5 ? AT_END : AT_K + (t - 6) * 12800); }

#define PSYNC() do { asm volatile("s_waitcnt lgkmcnt(0)" ::: "memory"); ++ps; sig(pf + wh * 4, ps, lane); spin_ge(pf + (wh ^ 1) * 4, ps); } while (0)
DI void a1_issue(unsigned lds, int tile, int lane, int wh, const u16* ak, const u16* av, const u16* shk) {
  const int ts = tile / 10, td = tile - ts * 10;
  const int s0 = ts * 32 + wh * 16, d0 = td * 64;
  const int lr = lane >> 3, lc = (lane & 7) * 16;
  const unsigned wl = lds + wh * A1_WAVE;
#pragma unroll
  for (int i = 0; i < 3; ++i) {
    int r = s0 - 3 + i * 8 + lr; r = r < 0 ? 0 : r;
    if (i < 2 || lane < 24) glds16nt((unsigned)(r * DM * 2 + lc), (const char*)ak + d0 * 2, wl + A1_K + i * 1024);
  }
#pragma unroll
  for (int i = 0; i < 2; ++i)
    glds16nt((unsigned)(lr * DM * 2 + lc), (const char*)av + ((size_t)(s0 + i * 8) * DM + d0) * 2, wl + A1_V + i * 1024);
  if (lane < 32) glds16((unsigned)(lane * 16), (const char*)shk + d0 * 8, wl + A1_W);
}
DI void a1_finish(unsigned lds, unsigned pf, unsigned& ps, int tile, int lane, int wh, char* ws, u64 E) {
  const int ts = tile / 10, td = tile - ts * 10;
  const int c = lane & 7;
  const unsigned wl = lds + wh * A1_WAVE;
  u32x4 wt[4];
#pragma unroll
  for (int i = 0; i < 4; ++i) wt[i] = __builtin_bit_cast(u32x4, lds_read16(wl + A1_W + c * 64 + i * 16));
#pragma unroll
  for (int j = 0; j < 2; ++j) {
    const int r = j * 8 + (lane >> 3);
    const unsigned ka = wl + A1_K + (r + 3) * 128 + c * 16;
    u32x4 k0 = __builtin_bit_cast(u32x4, lds_read16(ka)), k1 = __builtin_bit_cast(u32x4, lds_read16(ka - 128));
    u32x4 k2 = __builtin_bit_cast(u32x4, lds_read16(ka - 256)), k3 = __builtin_bit_cast(u32x4, lds_read16(ka - 384));
    const u32x4 vv = __builtin_bit_cast(u32x4, lds_read16(wl + A1_V + r * 128 + c * 16));
    if (j == 0 && ts == 0 && wh == 0) {
      const u32x4 z = {0u, 0u, 0u, 0u};
      if (r < 1) k1 = z;
      if (r < 2) k2 = z;
      if (r < 3) k3 = z;
    }
    u32x4 o;
#pragma unroll
    for (int i = 0; i < 4; ++i) {
      const unsigned pa0 = __builtin_amdgcn_perm(k1[i], k0[i], 0x05040100u), pb0 = __builtin_amdgcn_perm(k3[i], k2[i], 0x05040100u);
      const unsigned pa1 = __builtin_amdgcn_perm(k1[i], k0[i], 0x07060302u), pb1 = __builtin_amdgcn_perm(k3[i], k2[i], 0x07060302u);
      const float e0 = dot2bf(pb0, wt[i][1], dot2bf(pa0, wt[i][0], 0.f));
      const float e1 = dot2bf(pb1, wt[i][3], dot2bf(pa1, wt[i][2], 0.f));
      o[i] = pk_bf16(e0 * bf_lo(vv[i]), e1 * bf_hi(vv[i]));
    }
    lds_write16(lds + A1_O + (31 - wh * 16 - r) * 128 + c * 16, o);
  }
  PSYNC();
  const int gg = lane >> 4, q = (lane & 15) >> 2, p = lane & 3;
  u16* ut = (u16*)(ws + WS_UT);
#pragma unroll
  for (int j = 0; j < 2; ++j) {
    const unsigned ra = lds + A1_O + (gg * 8 + q) * 128 + wh * 64 + j * 32 + p * 8;
    const s16x4 t0 = tr_read(ra), t1 = tr_read(ra + 512);
    const int d = td * 64 + wh * 32 + j * 16 + (lane & 15);
    gstore16c(ut + (size_t)d * SEQ + (SEQ - 32 - ts * 32) + gg * 8, __builtin_bit_cast(u32x4, cat8(t0, t1)));
  }
}
DI void a1_publish(int tile, int lane, int wh, char* ws, u64 E) {
  const int ts = tile / 10, td = tile - ts * 10;
  publish(ws, FL_U + (size_t)((td * 2 + wh) * 64 + ts) * 8, E, lane);
}

template <bool VT>
DI void k1_gemm(unsigned lds0, int m0, int n0, int tid, char* ws, Diag& dg) {
  const int lane = tid & 63, wave = __builtin_amdgcn_readfirstlane(tid >> 6);
  const int wm = wave >> 1, wn = wave & 1;
  const int l32 = lane & 31, hh = lane >> 5, gg = lane >> 4, q = (lane & 15) >> 2, p = lane & 3;
  const unsigned ct = lds0 + K1_CT;
  f32x16 acc[2][2];
#pragma unroll
  for (int i = 0; i < 2; ++i)
#pragma unroll
    for (int j = 0; j < 2; ++j)
#pragma unroll
      for (int r = 0; r < 16; ++r) acc[i][j][r] = 0.f;
  unsigned xb[4];
#pragma unroll
  for (int ks = 0; ks < 4; ++ks) xb[ks] = lds0 + (wm * 64 + l32) * 128 + (((unsigned)(2 * ks + hh) ^ (unsigned)((l32 >> 1) & 7)) * 16);
  unsigned wb[2];
#pragma unroll
  for (int nt = 0; nt < 2; ++nt)
    wb[nt] = lds0 + 16384 + (8 * hh + q) * 256 + (((unsigned)(wn * 4 + 2 * nt + (gg & 1)) ^ (unsigned)(q << 1)) * 32) + p * 8;
  s16x8 x0[2], w0[2], x1[2], w1[2], x2[2], w2[2], x3[2], w3[2];
#define K1_LOADF(XF, WF, bo_, ks) do { \
    _Pragma("unroll") for (int i_ = 0; i_ < 2; ++i_) { \
      WF[i_] = cat8(tr_read(wb[i_] + (bo_) + (ks) * 4096), tr_read(wb[i_] + (bo_) + (ks) * 4096 + 1024)); \
      XF[i_] = lds_read16(xb[ks] + (bo_) + i_ * 4096); } } while (0)
#define K1_MM(XF, WF) do { _Pragma("unroll") for (int mt = 0; mt < 2; ++mt) _Pragma("unroll") for (int nt = 0; nt < 2; ++nt) \
      acc[mt][nt] = VT ? MFMA32(XF[mt], WF[nt], acc[mt][nt]) : MFMA32(WF[nt], XF[mt], acc[mt][nt]); } while (0)
#define ILV_A 2
#define ILV_B 2
#define ILV_C 2
#define K1_ILV() do { \
      __builtin_amdgcn_sched_group_barrier(0x008, 1, 0); __builtin_amdgcn_sched_group_barrier(0x100, ILV_A, 0); \
      __builtin_amdgcn_sched_group_barrier(0x008, 1, 0); __builtin_amdgcn_sched_group_barrier(0x100, ILV_B, 0); \
      __builtin_amdgcn_sched_group_barrier(0x008, 1, 0); __builtin_amdgcn_sched_group_barrier(0x100, ILV_C, 0); \
      __builtin_amdgcn_sched_group_barrier(0x008, 1, 0); __builtin_amdgcn_sched_group_barrier(0x100, 2, 0); } while (0)
  STAMP(1);
  K1_LOADF(x0, w0, 0, 0);
  K1_LOADF(x1, w1, 0, 1);
#pragma unroll
  for (int t = 0; t < 10; ++t) {
    const unsigned bo = (t & 3) * K1_STAGE, bn = ((t + 1) & 3) * K1_STAGE;
    SBAR();
    const u32x4 fl4 = *(volatile LAS u32x4*)(uintptr_t)(ct + CT_FX + (t + 1) * 16);
    K1_LOADF(x2, w2, bo, 2); K1_MM(x0, w0); K1_ILV();
    SBAR();
    K1_LOADF(x3, w3, bo, 3); K1_MM(x1, w1); K1_ILV();
    SBAR();
    const unsigned flu = (unsigned)__builtin_amdgcn_readfirstlane(min(min(fl4[0], fl4[1]), min(fl4[2], fl4[3])));
    if (t < 9 && flu == 0u) spin_ge4(ct + CT_FX + (t + 1) * 16, 1u);
    asm volatile("" ::: "memory");
    SBAR();
    K1_LOADF(x0, w0, bn, 0); K1_MM(x2, w2); K1_ILV();
    SBAR();
    asm volatile("" : "+v"(x3[0]), "+v"(x3[1]), "+v"(w3[0]), "+v"(w3[1]) :: "memory");
    if (lane == 0) lds_st32(ct + CT_FREE + (t & 3) * 16 + wave * 4, (unsigned)(t + 1));
    SBAR();
    K1_LOADF(x1, w1, bn, 1); K1_MM(x3, w3); K1_ILV();
  }
  SBAR();
  STAMP(2);
  if (!VT) {
    const int part = n0 >= DM ? 1 : 0;
    const float sc = part == 0 ? 0.22360679774997896f * 1.4426950408889634f : 1.0f;
    char* base = ws + (part ? WS_KF : WS_QF) + ((size_t)((m0 + wm * 64) >> 5) * 40 + ((n0 - part * DM + wn * 64) >> 4)) * 1024 + lane * 16;
#pragma unroll
    for (int mt = 0; mt < 2; ++mt)
#pragma unroll
      for (int nt = 0; nt < 2; ++nt)
#pragma unroll
        for (int uh = 0; uh < 2; ++uh) {
          u32x4 o;
#pragma unroll
          for (int i = 0; i < 4; ++i) o[i] = pk_bf16(acc[mt][nt][8 * uh + 2 * i] * sc, acc[mt][nt][8 * uh + 2 * i + 1] * sc);
          gstore16c(base + (mt * 40 + nt * 2 + uh) * 1024, o);
        }
  } else {
    char* base = ws + WS_VF + ((size_t)((m0 + wm * 64) >> 4) * 20 + ((n0 - 2 * DM + wn * 64) >> 5)) * 1024 + lane * 16;
#pragma unroll
    for (int mt = 0; mt < 2; ++mt)
#pragma unroll
      for (int nt = 0; nt < 2; ++nt)
#pragma unroll
        for (int kg = 0; kg < 2; ++kg) {
          u32x4 o;
#pragma unroll
          for (int i = 0; i < 4; ++i) o[i] = pk_bf16(acc[mt][nt][8 * kg + 2 * i], acc[mt][nt][8 * kg + 2 * i + 1]);
          gstore16c(base + ((mt * 2 + kg) * 20 + nt) * 1024, o);
        }
  }
}

DI void cv_put(unsigned y, const u32x4& c, const u32x2& n) {
  const unsigned d0 = c[0], d1 = c[1], d2 = c[2], d3 = c[3], d4 = n[0], d5 = n[1];
  const unsigned a0 = __builtin_amdgcn_alignbit(d1, d0, 16), a1 = __builtin_amdgcn_alignbit(d2, d1, 16),
                 a2 = __builtin_amdgcn_alignbit(d3, d2, 16), a3 = __builtin_amdgcn_alignbit(d4, d3, 16),
                 a4 = __builtin_amdgcn_alignbit(d5, d4, 16);
  lds_write16(y, (u32x4){d0, d1, d2, d3});
  lds_write16(y + CV_COPY, (u32x4){a0, a1, a2, a3});
  lds_write16(y + 2 * CV_COPY, (u32x4){d1, d2, d3, d4});
  lds_write16(y + 3 * CV_COPY, (u32x4){a1, a2, a3, a4});
}

#define QSYNC() do { asm volatile("s_waitcnt lgkmcnt(0)" ::: "memory"); ++qs; sig(qf + wq * 4, qs, lane); spin_ge4(qf, qs); } while (0)
struct HRegs { u32x4 c, u; u32x2 n; unsigned d; };
DI HRegs conv_load_h(int ch, int t256, const u16* __restrict__ ssmk, const u16* __restrict__ ssmd) {
  HRegs r;
  const u16* hrow = ssmk + (size_t)ch * SEQ;
  r.c = *((const u32x4*)(hrow + t256 * 8));
  r.n = *(const u32x2*)(hrow + (t256 < 255 ? t256 * 8 + 8 : 0));
  r.d = ssmd[ch];
  return r;
}
DI void conv_load_u(HRegs& r, int ch, int lane, int wq, const char* __restrict__ ws, u64 E) {
  poll(ws, FL_U + (size_t)((ch >> 5) * 64 + 48 - 16 * wq + (lane & 15)) * 8, E);
  const u64* up = (const u64*)(ws + WS_UT + (size_t)ch * SEQ * 2 + wq * 1024 + lane * 16);
  const u64 a = __hip_atomic_load(up, __ATOMIC_RELAXED, __HIP_MEMORY_SCOPE_AGENT);
  const u64 b = __hip_atomic_load(up + 1, __ATOMIC_RELAXED, __HIP_MEMORY_SCOPE_AGENT);
  r.u = (u32x4){(unsigned)a, (unsigned)(a >> 32), (unsigned)b, (unsigned)(b >> 32)};
}
DI void conv_stage(unsigned reg, int lane, int wq, const HRegs& hr) {
  const int t256 = wq * 64 + lane;
  const unsigned hb = reg + CV_H;
  if (t256 < 32) lds_write16(reg + 4096 + t256 * 16, (u32x4){0u, 0u, 0u, 0u});
  cv_put(hb + (t256 + 2) * 16, hr.c, hr.n);
  if (t256 < 2) {
    const u32x4 z = {0u, 0u, 0u, 0u};
    u32x2 nx = {0u, 0u};
    const unsigned h0 = __builtin_amdgcn_readfirstlane(hr.c[0]), h1 = __builtin_amdgcn_readfirstlane(hr.c[1]);
    if (t256 == 1) nx = (u32x2){h0, h1};
    cv_put(hb + t256 * 16, z, nx);
  }
}
DI void conv_run(unsigned reg, unsigned qf, unsigned& qs, int ch, int lane, int wq, const HRegs& hr,
                 char* __restrict__ ws, unsigned xst, Diag& dg) {
  const unsigned hb = reg + CV_H;
  if (xst) spin_ge4(xst, 1u);
  else { lds_write16(reg + (wq * 64 + lane) * 16, hr.u); QSYNC(); }
  STAMP(6);
  const int r16 = lane & 15, g = lane >> 4, q = r16 >> 2, p = lane & 3;
  const unsigned ab0 = hb + ((q + 1) & 3) * CV_COPY + 2 * (8 * g + 4 * p + (q == 3 ? 4 : 0)) + wq * 128;
  const unsigned bb0 = reg + 2 * (2032 - 16 * r16 + 8 * g) + wq * 128 - 7 * 512;
  f32x4 acc[8];
#pragma unroll
  for (int i = 0; i < 8; ++i) acc[i] = (f32x4){0.f, 0.f, 0.f, 0.f};
#define CV_LDA(a_, ad_) cat8(tr_read((ad_) + (a_) * 512), tr_read((ad_) + (a_) * 512 + 8))
#define CV_LDB(c_, bd_) lds_read16((bd_) + (7 - (c_)) * 512)
  {
    s16x8 A[8], B[8];
    B[0] = CV_LDB(0, bb0);
#pragma unroll
    for (int a = 0; a < 8; ++a) A[a] = CV_LDA(a, ab0);
#pragma unroll
    for (int c = 1; c < 8; ++c) B[c] = CV_LDB(c, bb0);
#pragma unroll
    for (int c = 0; c < 8; ++c) {
      SBAR();
#pragma unroll
      for (int a = 0; a + c < 8; ++a) acc[a + c] = MFMA16(A[a], B[c], acc[a + c]);
      B[c] = CV_LDB(c, bb0 + 64);
      A[7 - c] = CV_LDA(7 - c, ab0 + 64);
    }
    SBAR();
#pragma unroll
    for (int a = 7; a >= 0; --a)
#pragma unroll
      for (int c = 0; a + c < 8; ++c) acc[a + c] = MFMA16(A[a], B[c], acc[a + c]);
  }
  QSYNC();
#pragma unroll
  for (int nt = 0; nt < 8; ++nt) {
    const int dw = nt >> 1;
    if (dw != wq) lds_write16(hb + (nt * 3 + (wq > dw ? wq - 1 : wq)) * 1024 + lane * 16, __builtin_bit_cast(u32x4, acc[nt]));
  }
  f32x4 k0 = wq == 0 ? acc[0] : (wq == 1 ? acc[2] : (wq == 2 ? acc[4] : acc[6]));
  f32x4 k1 = wq == 0 ? acc[1] : (wq == 1 ? acc[3] : (wq == 2 ? acc[5] : acc[7]));
  QSYNC();
  const float dco = bf_lo(hr.d);
  u32x2 zz[2];
#pragma unroll
  for (int i = 0; i < 2; ++i) {
    f32x4 o = i ? k1 : k0;
#pragma unroll
    for (int j = 0; j < 3; ++j) o += __builtin_bit_cast(f32x4, lds_read16(hb + ((wq * 2 + i) * 3 + j) * 1024 + lane * 16));
    const int s = (wq * 2 + i) * 256 + 16 * r16 + 4 * g;
    const u32x2 uu = __builtin_bit_cast(u32x2, lds_read8(reg + 2 * (SEQ - 4 - s)));
    const float kv0 = bf_hi(uu[1]), kv1 = bf_lo(uu[1]), kv2 = bf_hi(uu[0]), kv3 = bf_lo(uu[0]);
    const unsigned y01 = pk_bf16(o[0], o[1]), y23 = pk_bf16(o[2], o[3]);
    const unsigned e01 = pk_bf16(kv0 * dco, kv1 * dco), e23 = pk_bf16(kv2 * dco, kv3 * dco);
    zz[i][0] = pk_bf16(bf_lo(y01) + bf_lo(e01), bf_hi(y01) + bf_hi(e01));
    zz[i][1] = pk_bf16(bf_lo(y23) + bf_lo(e23), bf_hi(y23) + bf_hi(e23));
  }
  const auto sx = __builtin_amdgcn_permlane16_swap(zz[0][0], zz[1][0], false, false);
  const auto sy = __builtin_amdgcn_permlane16_swap(zz[0][1], zz[1][1], false, false);
  u16* zrow = (u16*)(ws + WS_ZT) + (size_t)ch * SEQ;
  gstore16c(zrow + (wq * 2 + (g & 1)) * 256 + 16 * r16 + 8 * (g >> 1), (u32x4){sx[0], sy[0], sx[1], sy[1]});
  asm volatile("s_waitcnt lgkmcnt(0)" ::: "memory");
}
DI void conv_publish(unsigned zc, int ch, int lane, char* __restrict__ ws, u64 E) {
  asm volatile("s_waitcnt vmcnt(0)" ::: "memory");
  if (lane == 0) {
    const unsigned old = __hip_atomic_fetch_add((LAS unsigned*)(uintptr_t)zc, 1u, __ATOMIC_RELAXED, __HIP_MEMORY_SCOPE_WORKGROUP);
    if ((old & 3u) == 3u) __hip_atomic_store((u64*)(ws + FL_Z + (size_t)ch * 8), E, __ATOMIC_RELAXED, __HIP_MEMORY_SCOPE_AGENT);
  }
}

DI s16x8 pack8(const f32x16& v, int o) {
  u32x4 r;
  r[0] = pk_bf16(v[o + 0], v[o + 1]); r[1] = pk_bf16(v[o + 2], v[o + 3]);
  r[2] = pk_bf16(v[o + 4], v[o + 5]); r[3] = pk_bf16(v[o + 6], v[o + 7]);
  return __builtin_bit_cast(s16x8, r);
}
DI float max3f(float a, float b, float c) { return __builtin_fmaxf(__builtin_fmaxf(a, b), c); }
DI float max16(const f32x16& v, float m) {
  float a = max3f(v[0], v[1], m), b = max3f(v[2], v[3], v[4]);
  a = max3f(a, v[5], v[6]); b = max3f(b, v[7], v[8]);
  a = max3f(a, v[9], v[10]); b = max3f(b, v[11], v[12]);
  a = max3f(a, v[13], v[14]);
  return max3f(a, b, v[15]);
}
DI float xhalf_max(float m) {
  auto sw = __builtin_amdgcn_permlane32_swap(__float_as_uint(m), __float_as_uint(m), false, false);
  return __builtin_fmaxf(__uint_as_float(sw[0]), __uint_as_float(sw[1]));
}

#define AT_LMAX 65536.0f
#define P3_WADDR() unsigned wv[4]; _Pragma("unroll") for (int i = 0; i < 4; ++i) { \
      const int c = i < 3 ? (i * 4 + w) * 64 + lane : 768 + w * 8 + (lane & 7); const int row = c / 10; \
      wv[i] = (unsigned)(row * DM * 2 + (c - row * 10) * 16); } const char* wog = (const char*)(wo + n3)
#define P3_STW(t) do { _Pragma("unroll") for (int i_ = 0; i_ < 3; ++i_) \
        glds16(wv[i_], wog + (size_t)(t) * 80 * DM * 2, lds0 + p3_w(t) + (i_ * 4 + w) * 1024); \
      if (lane < 8) glds16(wv[3], wog + (size_t)(t) * 80 * DM * 2, lds0 + p3_w(t) + 12288 + w * 128); } while (0)
DI void attn_stage(unsigned lds0, int h, int qb8, int j, int lane, char* __restrict__ ws, u64 E, int zch, Diag& dg) {
  const int qt = qb8 * 8 + j;
  const unsigned l16 = (unsigned)(lane * 16), ct = lds0 + L_CT;
  const int qa = (5 * h) >> 2;
  const bool stg = qb8 > 0 || j >= 2;
  {
    const int l3 = lane & 7, hi = lane & 1;
    const bool kv = stg && l3 >= 2 && l3 < 6;
    const int rb = kv ? 2 * qb8 - 2 + j : 2 * qb8 + (l3 >= 6 ? 1 : 0);
    const int col = !kv ? qa * 16 + hi * 31 : (l3 < 4 ? DM + qa * 16 + hi * 31 : 2 * DM + h * 20 + hi * 19);
    poll(ws, FL_Q + (size_t)(rb * 16 + (col >> 7)) * 8, E);
  }
  STAMP(9);
  conv_publish(ct + CT_ZCNT + 8, zch, lane, ws, E);
  const int kt0 = qb8 * 8 - 8 + 4 * j;
#pragma unroll
  for (int i = 0; i < 4; ++i)
    glds16c(l16, ws + WS_QF + ((size_t)(qt + (i >> 1) * 4) * 40 + qa + (i & 1)) * 1024, lds0 + AT_Q + ((j + (i >> 1) * 4) * 2 + (i & 1)) * 1024);
  if (stg) {
#pragma unroll
    for (int i = 0; i < 8; ++i)
      glds16c(l16, ws + WS_KF + ((size_t)(kt0 + (i >> 1)) * 40 + qa + (i & 1)) * 1024, lds0 + AT_K + (j * 8 + i) * 1024);
  }
  glds_mark(ct + CT_KR8 + j * 4, lane);
  if (stg) {
#pragma unroll
    for (int i = 0; i < 6; ++i) {
      const int c = i * 64 + lane;
      const int kgp = (c * 1561) >> 16, rem = c - kgp * 42, vh = rem >= 21 ? 1 : 0, d = rem - vh * 21;
      const int n = h * 20 + d;
      if (c < 336 && d == 20) lds_write16(lds0 + AT_V + j * 5376 + c * 16, (u32x4){0x3f803f80u, 0x3f803f80u, 0x3f803f80u, 0x3f803f80u});
      if (c < 336 && d < 20)
        glds16c((unsigned)((kgp * 20 + (n >> 5)) * 1024 + (vh * 32 + (n & 31)) * 16), ws + WS_VF + ((size_t)(kt0 * 2) * 20) * 1024,
                lds0 + AT_V + j * 5376 + i * 1024);
    }
    asm volatile("s_waitcnt lgkmcnt(0)" ::: "memory");
  }
  glds_mark(ct + CT_VR8 + j * 4, lane);
}
template <bool FULL>
DI void attn_task(unsigned lds0, int h, int qb8, int j, int lane, char* __restrict__ ws, u64 E, int zch, const u16* __restrict__ wo, int n3, Diag& dg) {
  const int l32 = lane & 31, hh = lane >> 5;
  const int qt = qb8 * 8 + j;
  const int tfirst = FULL ? 0 : 8 - qt;
  const unsigned l16 = (unsigned)(lane * 16), ct = lds0 + L_CT;
  const int qa = (5 * h) >> 2;
  u32x4 q0r, q1r;
  spin_ge4(ct + CT_KR8, 1u);
  if (!FULL) spin_ge4(ct + CT_VR8, 1u);
  q0r = __builtin_bit_cast(u32x4, lds_read16(lds0 + AT_Q + j * 2048 + l16));
  q1r = __builtin_bit_cast(u32x4, lds_read16(lds0 + AT_Q + j * 2048 + 1024 + l16));
  asm volatile("s_waitcnt lgkmcnt(0)" : "+v"(q0r), "+v"(q1r) :: "memory");
  sig(ct + CT_QRD + j * 4, 1u, lane);
  if (zch >= 0) conv_publish(ct + CT_ZCNT + (j >> 2) * 4, zch, lane, ws, E);
  {
    const int g0 = 4 * qa + hh - 5 * h;
    const bool k0 = g0 >= 0, k1 = g0 + 2 >= 0 && g0 + 2 < 5, k2 = g0 + 4 < 5, k3 = g0 + 6 < 5;
    q0r[0] = k0 ? q0r[0] : 0u; q0r[1] = k0 ? q0r[1] : 0u; q0r[2] = k1 ? q0r[2] : 0u; q0r[3] = k1 ? q0r[3] : 0u;
    q1r[0] = k2 ? q1r[0] : 0u; q1r[1] = k2 ? q1r[1] : 0u; q1r[2] = k3 ? q1r[2] : 0u; q1r[3] = k3 ? q1r[3] : 0u;
  }
  s16x8 qlo = __builtin_bit_cast(s16x8, q0r), qhi8 = __builtin_bit_cast(s16x8, q1r);
  STAMP(8);
  const f32x16 zero16 = {0.f, 0.f, 0.f, 0.f, 0.f, 0.f, 0.f, 0.f, 0.f, 0.f, 0.f, 0.f, 0.f, 0.f, 0.f, 0.f};
  const float ninf = -INFINITY;
  const f32x16 ninf16 = {ninf, ninf, ninf, ninf, ninf, ninf, ninf, ninf, ninf, ninf, ninf, ninf, ninf, ninf, ninf, ninf};
  const unsigned kb = lds0 + AT_K + j * 2048 + l16;
#define AT_QK(dst, t, cin) do { if (FULL || (t) >= tfirst) { dst = MFMA32(lds_read16(kb + (t) * 2048), qlo, cin); \
    dst = MFMA32(lds_read16(kb + (t) * 2048 + 1024), qhi8, dst); } else dst = ninf16; } while (0)
  const int vrow = l32 <= 20 ? l32 : (l32 == 24 ? 20 : l32 - 16);
  const unsigned vb = lds0 + AT_V + j * 1344 + hh * 336 + vrow * 16;
#define AT_EXP(sv) do { _Pragma("unroll") for (int r = 0; r < 16; ++r) sv[r] = __builtin_amdgcn_exp2f(sv[r] - m); } while (0)
#define AT_PV(sv, t) do { oacc = MFMA32(lds_read16(vb + (t) * 1344), pack8(sv, 0), oacc); oacc = MFMA32(lds_read16(vb + (t) * 1344 + 672), pack8(sv, 8), oacc); } while (0)
  f32x16 oacc;
  bool exact = !FULL;
  if (FULL) {
    f32x16 sm, sa, sb;
    AT_QK(sm, 8, zero16); AT_QK(sa, 0, zero16);
    const int tq = l32 - 4 * hh;
    const unsigned wm = tq < 0 ? 0xffffffffu : (0xfffffffeu << tq);
    unsigned mk[16];
#pragma unroll
    for (int r = 0; r < 16; ++r) {
      mk[r] = (unsigned)__builtin_amdgcn_sbfe((int)wm, (r & 3) + 8 * (r >> 2), 1);
      sm[r] = __uint_as_float((mk[r] & __float_as_uint(sa[r])) | (~mk[r] & __float_as_uint(sm[r])));
    }
    const float m = xhalf_max(max16(sm, ninf));
    const float nm = -m;
    const f32x16 cneg = {nm, nm, nm, nm, nm, nm, nm, nm, nm, nm, nm, nm, nm, nm, nm, nm};
    AT_QK(sa, 1, cneg);
    AT_EXP(sm);
    u32x4 pl = __builtin_bit_cast(u32x4, pack8(sm, 0)), ph = __builtin_bit_cast(u32x4, pack8(sm, 8)), ql, qh;
#pragma unroll
    for (int i = 0; i < 4; ++i) {
      ql[i] = pl[i] & ((mk[2 * i] & 0xffffu) | (mk[2 * i + 1] & 0xffff0000u)); pl[i] ^= ql[i];
      qh[i] = ph[i] & ((mk[8 + 2 * i] & 0xffffu) | (mk[9 + 2 * i] & 0xffff0000u)); ph[i] ^= qh[i];
    }
    spin_ge4(ct + CT_VR8, 1u);
    STAMP(9);
    oacc = MFMA32(lds_read16(vb + 8 * 1344), __builtin_bit_cast(s16x8, pl), zero16);
    oacc = MFMA32(lds_read16(vb + 8 * 1344 + 672), __builtin_bit_cast(s16x8, ph), oacc);
    oacc = MFMA32(lds_read16(vb), __builtin_bit_cast(s16x8, ql), oacc);
    oacc = MFMA32(lds_read16(vb + 672), __builtin_bit_cast(s16x8, qh), oacc);
#define AT_EX0(sv) do { _Pragma("unroll") for (int r = 0; r < 16; ++r) sv[r] = __builtin_amdgcn_exp2f(sv[r]); } while (0)
    AT_QK(sb, 2, cneg); AT_EX0(sa); AT_PV(sa, 1);
    AT_QK(sa, 3, cneg); AT_EX0(sb); AT_PV(sb, 2);
    AT_QK(sb, 4, cneg); AT_EX0(sa); AT_PV(sa, 3);
    AT_QK(sa, 5, cneg); AT_EX0(sb); AT_PV(sb, 4);
    AT_QK(sb, 6, cneg); AT_EX0(sa); AT_PV(sa, 5);
    AT_QK(sa, 7, cneg); AT_EX0(sb); AT_PV(sb, 6);
    AT_EX0(sa); AT_PV(sa, 7);
    float big = max3f(__builtin_fabsf(oacc[0]), __builtin_fabsf(oacc[1]), __builtin_fabsf(oacc[2]));
    big = max3f(big, __builtin_fabsf(oacc[3]), __builtin_fabsf(oacc[4]));
    big = max3f(big, __builtin_fabsf(oacc[5]), __builtin_fabsf(oacc[6]));
    big = max3f(big, __builtin_fabsf(oacc[7]), __builtin_fabsf(oacc[8]));
    big = max3f(big, __builtin_fabsf(oacc[9]), __builtin_fabsf(oacc[10]));
    big = __builtin_fmaxf(big, __builtin_fabsf(oacc[11]));
    const float ls = hh ? oacc[8] : oacc[12];
    float osum = 0.f;
#pragma unroll
    for (int r = 0; r < 8; ++r) osum += oacc[r];
    const bool ok = (ls < AT_LMAX) && (big < 1e37f) && (osum == osum);
    exact = __builtin_amdgcn_ballot_w64(!ok) != 0ull;
  }
  if (exact) {
    asm volatile("" : "+v"(qlo), "+v"(qhi8) :: "memory");
    f32x16 sm, s1, s2, s3;
    {
      f32x16 s0;
      AT_QK(sm, 8, zero16); AT_QK(s0, 0, zero16);
      AT_QK(s1, 1, zero16); AT_QK(s2, 2, zero16); AT_QK(s3, 3, zero16);
#pragma unroll
      for (int r = 0; r < 16; ++r) {
        const int kk = (r & 3) + 8 * (r >> 2) + 4 * hh;
        sm[r] = kk > l32 ? s0[r] : sm[r];
      }
    }
    float m = max16(sm, ninf);
    m = max16(s1, m); m = max16(s2, m); m = max16(s3, m);
    m = xhalf_max(m);
    AT_EXP(sm);
    oacc = zero16;
    {
      f32x16 p0, p8;
#pragma unroll
      for (int r = 0; r < 16; ++r) {
        const int kk = (r & 3) + 8 * (r >> 2) + 4 * hh;
        p0[r] = kk > l32 ? sm[r] : 0.f;
        p8[r] = kk > l32 ? 0.f : sm[r];
      }
      AT_PV(p8, 8);
      if (FULL) AT_PV(p0, 0);
    }
    if (FULL || 1 >= tfirst) { AT_EXP(s1); AT_PV(s1, 1); }
    AT_QK(sm, 4, zero16); AT_QK(s1, 5, zero16);
    if (FULL || 2 >= tfirst) { AT_EXP(s2); AT_PV(s2, 2); }
    AT_QK(s2, 6, zero16);
    if (FULL || 3 >= tfirst) { AT_EXP(s3); AT_PV(s3, 3); }
    AT_QK(s3, 7, zero16);
    float m2 = max16(sm, m);
    m2 = max16(s1, m2); m2 = max16(s2, m2); m2 = max16(s3, m2);
    m2 = xhalf_max(m2);
    {
      const float alpha = __builtin_amdgcn_exp2f(m - m2);
#pragma unroll
      for (int r = 0; r < 13; ++r) oacc[r] *= alpha;
      m = m2;
    }
    if (FULL || 4 >= tfirst) { AT_EXP(sm); AT_PV(sm, 4); }
    if (FULL || 5 >= tfirst) { AT_EXP(s1); AT_PV(s1, 5); }
    if (FULL || 6 >= tfirst) { AT_EXP(s2); AT_PV(s2, 6); }
    if (FULL || 7 >= tfirst) { AT_EXP(s3); AT_PV(s3, 7); }
  }
  asm volatile("s_waitcnt lgkmcnt(0)" ::: "memory");
  sig(ct + CT_ATT + j * 4, 1u, lane);
  STAMP(10);
  const float l = hh ? oacc[8] : oacc[12];
  const float inv = __builtin_amdgcn_rcpf(l);
  char* oc = ws + WS_OW + ((size_t)qt * NH + h) * 1280;
  u32x4 o;
  o[0] = pk_bf16(oacc[0] * inv, oacc[1] * inv); o[1] = pk_bf16(oacc[2] * inv, oacc[3] * inv);
  o[2] = pk_bf16(oacc[4] * inv, oacc[5] * inv); o[3] = pk_bf16(oacc[6] * inv, oacc[7] * inv);
  gstore16c(oc + lane * 16, o);
  if (!hh) {
    u32x2 o2;
    o2[0] = pk_bf16(oacc[8] * inv, oacc[9] * inv); o2[1] = pk_bf16(oacc[10] * inv, oacc[11] * inv);
    gstore8c(oc + 1024 + l32 * 8, o2);
  }
  publish(ws, FL_O + (size_t)(qt * NH + h) * 8, E, lane);
}

#define HDELAY 0
#define APRIO 0
#define LPRIO 0
#define MPRIO 2
#define ROTG 2
#define A1_DELAY 8
__global__ __launch_bounds__(768, 3) void mega(const u16* __restrict__ x, const u16* __restrict__ wqkv,
                                               const u16* __restrict__ ak, const u16* __restrict__ av,
                                               const u16* __restrict__ shk, const u16* __restrict__ wo,
                                               const u16* __restrict__ ssmk, const u16* __restrict__ ssmd,
                                               const u16* __restrict__ aq, char* __restrict__ ws,
                                               u16* __restrict__ out) {
  extern __shared__ __attribute__((aligned(16))) char smem[];
  const unsigned lds0 = lds_addr(smem);
  const int tid = threadIdx.x, wave = __builtin_amdgcn_readfirstlane(tid >> 6);
  int lane = tid & 63;
#define RELANE() asm volatile("" : "+v"(lane))
  const int role = wave >> 2, w = wave & 3;
  const int bid = blockIdx.x, xcd = bid & 7, idx = bid >> 3;
  const bool has_gemm = idx < 30;
  const unsigned ct = lds0 + L_CT;
  DIAG_INIT();
  u64 E;
  asm volatile("s_load_dwordx2 %0, %1, 0x0" : "=s"(E) : "s"(ws + WS_FL) : "memory");
  const int gt = (xcd & 1) * 30 + idx;
  const int tm = (xcd >> 1) * 4 + (gt & 3), tn = gt >> 2;
  const int m0 = tm * 128, n0 = tn * 128;
  const int rot = ROTG >= 10 ? (((gt & 3) * 5 + 1) / 2 + 3 * tn) % 10 : ((((gt & 3) + tn) % ROTG) * 10) / ROTG;
  const unsigned xo0 = (unsigned)((lane >> 3) * DM * 2 + (((lane & 7) ^ (lane >> 4)) * 16));
  const unsigned xo1 = (unsigned)((lane >> 3) * DM * 2 + (((lane & 7) ^ (4 + (lane >> 4))) * 16));
  const unsigned wof = (unsigned)((lane >> 4) * NQKV * 2 + (((lane & 15) ^ ((lane >> 4) << 2)) * 16));
  const char* xg = (const char*)(x + (size_t)m0 * DM);
  const char* wg = (const char*)(wqkv + n0);
#define K1_PIECE_X(t, kt, j) glds16p(((j) & 1) ? xo1 : xo0, xg + (size_t)(j) * 8 * DM * 2 + (kt) * 128, lds0 + ((t) & 3) * K1_STAGE + (j) * 1024)
#define K1_PIECE_W(t, kt, j) glds16p(wof, wg + (size_t)((kt) * 64 + (j) * 4) * NQKV * 2, lds0 + ((t) & 3) * K1_STAGE + 16384 + (j) * 1024)
  if (has_gemm && role == 0) {
#pragma unroll
    for (int i = 0; i < 4; ++i) { K1_PIECE_X(0, rot, w * 4 + i); K1_PIECE_W(0, rot, w * 4 + i); }
  }
  const unsigned pf = ct + CT_PAIR + (wave >> 1) * 8;
  const int wh = wave & 1;
  unsigned ps = 0;
  const int fb = (idx - 30) * 8 + xcd;
  const int a1t = has_gemm ? (xcd * 30 + idx) * 2 + (w >> 1) : 480 + fb * 10 + (wave >> 1);
  const unsigned a1l = has_gemm ? lds0 + L_AUX + (w >> 1) * A1_PAIR : lds0 + (wave >> 1) * A1_PAIR;
  if (!has_gemm || role == 2) a1_issue(a1l, a1t, lane, wh, ak, av, shk);
  const bool cv0 = role != 0 || !has_gemm || bid < 112;
  const int ch0 = role == 0 ? (has_gemm ? 528 + bid : 512 + fb) : bid * 2 + (role == 1 ? 1 : 0);
  if (role == 1 && tid - 256 < CT_BYTES / 4) lds_st32(ct + (tid - 256) * 4, 0u);
  asm volatile("s_waitcnt lgkmcnt(0)\n\ts_barrier" : "+s"(E) :: "memory");
  E += 1ull;

  const bool ex = has_gemm && role == 1 && bid < 112;
  HRegs hr, h2;
  if (!(has_gemm && role == 0)) {
    hr = conv_load_h(ch0, w * 64 + lane, ssmk, ssmd);
    if (ex) h2 = conv_load_h(528 + bid, w * 64 + lane, ssmk, ssmd);
  }
  auto q_publish = [&]() {
    asm volatile("s_waitcnt vmcnt(0)" ::: "memory");
    if (lane == 0) {
      const unsigned old = __hip_atomic_fetch_add((LAS unsigned*)(uintptr_t)(ct + CT_QCNT), 1u, __ATOMIC_RELAXED, __HIP_MEMORY_SCOPE_WORKGROUP);
      if (old == 3u) __hip_atomic_store((u64*)(ws + FL_Q + (size_t)(tm * 16 + tn) * 8), E, __ATOMIC_RELAXED, __HIP_MEMORY_SCOPE_AGENT);
    }
    STAMP(4);
  };
  if (!has_gemm) {
    const unsigned a2l = lds0 + (6 + (wave >> 1)) * A1_PAIR;
    if (wave < 8) a1_issue(a2l, a1t + 6, lane, wh, ak, av, shk);
    if (wave < 8) wait_vm<6>(); else wait_vm<0>();
    a1_finish(a1l, pf, ps, a1t, lane, wh, ws, E);
    if (wave < 8) { wait_vm<2>(); a1_finish(a2l, pf, ps, a1t + 6, lane, wh, ws, E); }
    a1_publish(a1t, lane, wh, ws, E);
    if (wave < 8) a1_publish(a1t + 6, lane, wh, ws, E);
    bar();
    STAMP(1);
  } else if (role == 0) {
    wait_vm<0>();
    sig(ct + CT_S0 + w * 4, 1u, lane);
    spin_ge4(ct + CT_S0, 1u);
    __builtin_amdgcn_s_setprio(MPRIO);
    if (tn >= 10) k1_gemm<true>(lds0, m0, n0, tid, ws, dg);
    else k1_gemm<false>(lds0, m0, n0, tid, ws, dg);
    STAMP(3);
  } else if (role == 1) {
    __builtin_amdgcn_s_setprio(LPRIO);
#define K1_ST(t) do { const int kt_ = (t) + rot >= 10 ? (t) + rot - 10 : (t) + rot; \
      if ((t) >= 4) spin_ge4(ct + CT_FREE + ((t) & 3) * 16, (unsigned)((t) - 3)); \
      _Pragma("unroll") for (int i_ = 0; i_ < 4; ++i_) { K1_PIECE_X(t, kt_, w * 4 + i_); K1_PIECE_W(t, kt_, w * 4 + i_); } } while (0)
#pragma nounroll
    for (int t = 1; t < 10; ++t) {
      K1_ST(t); glds_mark(ct + CT_FX + t * 16 + w * 4, lane);
    }
    __builtin_amdgcn_s_setprio(0);
    STAMP(1);
  } else {
    __builtin_amdgcn_s_setprio(2);
    wait_vm<0>();
    a1_finish(a1l, pf, ps, a1t, lane, wh, ws, E);
    a1_publish(a1t, lane, wh, ws, E);
    __builtin_amdgcn_s_setprio(0);
    STAMP(1);
  }

  RELANE();
  unsigned qs = 0;
  const unsigned qf = ct + CT_QUAD + role * 16;
  const unsigned creg = lds0 + (role == 2 ? L_AUX : (role == 1 ? 29184 : 0));
  if (role == 2) __builtin_amdgcn_s_setprio(APRIO); else __builtin_amdgcn_s_setprio(0);
  if (has_gemm && role == 0) {
    q_publish();
    if (cv0) {
      hr.d = ssmd[ch0];
      conv_run(creg, qf, qs, ch0, lane, w, hr, ws, ct + CT_XST, dg);
    }
  } else {
    if (has_gemm && role == 1) {
      conv_load_u(hr, ch0, lane, w, ws, E);
      if (ex) conv_load_u(h2, 528 + bid, lane, w, ws, E);
      spin_ge4(ct + CT_FREE + 16, 10u);
      conv_stage(creg, lane, w, hr);
      if (ex) {
        conv_stage(lds0, lane, w, h2);
        lds_write16(lds0 + (w * 64 + lane) * 16, h2.u);
        asm volatile("s_waitcnt lgkmcnt(0)" ::: "memory");
        sig(ct + CT_XST + w * 4, 1u, lane);
      }
    } else {
      if (has_gemm) { const int wq = w; QSYNC(); }
      conv_stage(creg, lane, w, hr);
      conv_load_u(hr, ch0, lane, w, ws, E);
    }
    STAMP(5);
    conv_run(creg, qf, qs, ch0, lane, w, hr, ws, 0u, dg);
  }
  sig(ct + CT_CVD + wave * 4, 1u, lane);
  if (role != 0) STAMP(2);

  const int tm3 = xcd * 4 + (idx >> 3), tn3 = idx & 7;
  const int m3 = tm3 * 64, n3 = tn3 * 80;
  RELANE();

  if (role == 2) {
    const int r16 = lane & 15, g = lane >> 4, q = r16 >> 2, p = lane & 3;
    __builtin_amdgcn_s_setprio(3);
    spin_ge4(ct + CT_CVD + 32, 1u);
    if (has_gemm) spin_ge4(ct + CT_FREE + 16, 10u);
    attn_stage(lds0, xcd * 4 + (idx & 3), idx >> 2, w, lane, ws, E, ch0, dg);
    {
      P3_WADDR();
      P3_STW(5);
      spin_ge8(ct + CT_CVD, 1u);
      P3_STW(0); P3_STW(4); P3_STW(1); P3_STW(2); P3_STW(3);
      glds_mark(ct + CT_WRDY + w * 4, lane);
      STAMP(3);
    }
    __builtin_amdgcn_s_setprio(0);
    u32x4 qv[3];
#pragma unroll
    for (int j = 0; j < 3; ++j) {
      const int dc = j < 2 ? j * 4 + g : 8 + (g & 1);
      qv[j] = *((const u32x4*)(aq + (size_t)(m3 + w * 16 + r16) * DM + n3 + dc * 8));
    }
    {
      const int l5 = lane & 31;
      const int cl = l5 < 8 ? w * 8 + l5 : (l5 < 16 ? 32 + w * 8 + (l5 - 8) : 64 + w * 4 + (l5 & 3));
      poll(ws, FL_Z + (size_t)(n3 + cl) * 8, E);
    }
    {
      const char* zg = ws + WS_ZT + ((size_t)n3 * SEQ + m3) * 2;
      const unsigned zo = (unsigned)((lane >> 3) * SEQ * 2 + (lane & 7) * 16);
      glds16c(zo, zg + (size_t)(w * 8) * SEQ * 2, lds0 + L_AUX + w * 1024);
      glds16c(zo, zg + (size_t)(32 + w * 8) * SEQ * 2, lds0 + L_AUX + 4096 + w * 1024);
      if (lane < 32) glds16c(zo, zg + (size_t)(64 + w * 4) * SEQ * 2, lds0 + L_AUX + 8192 + w * 512);
    }
    wait_vm<0>();
    sig(ct + CT_ZRDY + w * 4, 1u, lane);
    spin_ge4(ct + CT_ZRDY, 1u);
    STAMP(4);
    u32x4 zz[3];
#pragma unroll
    for (int j = 0; j < 3; ++j) {
      const int dc = j < 2 ? j * 4 + g : 8 + (g & 1);
      const unsigned za = lds0 + L_AUX + (dc * 8 + q) * 128 + w * 32 + p * 8;
      zz[j] = __builtin_bit_cast(u32x4, cat8(tr_read(za), tr_read(za + 512)));
    }
#pragma unroll
    for (int j = 0; j < 3; ++j) {
      const int dc = j < 2 ? j * 4 + g : 8 + (g & 1);
      u32x4 o;
#pragma unroll
      for (int i = 0; i < 4; ++i) o[i] = pk_bf16(bf_lo(qv[j][i]) * bf_lo(zz[j][i]), bf_hi(qv[j][i]) * bf_hi(zz[j][i]));
      if (j < 2 || g < 2) ostore16(out + (size_t)(m3 + w * 16 + r16) * (2 * DM) + n3 + dc * 8, o);
    }
    STAMP(7);
    if (bid == 0 && w == 0) {
#pragma unroll
      for (int i = 0; i < 4; ++i) poll(ws, FL_O + (size_t)(((i * 2 + (lane >> 5)) * 8) * NH + (lane & 31)) * 8, E);
      if (lane == 0) __hip_atomic_store((u64*)(ws + WS_FL), E, __ATOMIC_RELAXED, __HIP_MEMORY_SCOPE_AGENT);
    }
    DIAG_DUMP(2);
    return;
  }

  const int h = xcd * 4 + (idx & 3), qb8 = idx >> 2;
  __builtin_amdgcn_s_setprio(1);
  RELANE();
  if (qb8 > 0) attn_task<true>(lds0, h, qb8, wave, lane, ws, E, cv0 ? ch0 : -1, wo, n3, dg);
  else attn_task<false>(lds0, h, qb8, wave, lane, ws, E, cv0 ? ch0 : -1, wo, n3, dg);
  STAMP(11);

  RELANE();
  const int r16 = lane & 15, g = lane >> 4, q = r16 >> 2, p = lane & 3;
  f32x4 acc[5];
#pragma unroll
  for (int i = 0; i < 5; ++i) acc[i] = (f32x4){0.f, 0.f, 0.f, 0.f};
  const unsigned wbp = lds0 + (20 * (g >> 1) + 4 * (g & 1) + q) * 160 + p * 8;
  const unsigned wbs = lds0 + (20 * g + 16 + q) * 160 + p * 8;
  u32x4 op[8]; u32x2 osg[4];
  if (role == 1) {
    P3_WADDR();
    spin_ge8(ct + CT_ATT, 1u);
    P3_STW(6); P3_STW(7);
    glds_mark(ct + CT_WRD2 + w * 4, lane);
  }
  spin_ge4(ct + CT_WRDY, 1u);
  const unsigned w0 = role ? p3_w(4) : p3_w(0), w1 = role ? p3_w(5) : p3_w(1), w2 = role ? p3_w(6) : p3_w(2), w3 = role ? p3_w(7) : p3_w(3);
  struct Fw { s16x8 a[5], b[5]; s16x4 s[5]; };
  Fw fwa, fwb;
#define P3_LOADW(F, bo_) do { _Pragma("unroll") for (int nt = 0; nt < 5; ++nt) { \
      F.a[nt] = cat8(tr_read(wbp + (bo_) + nt * 32), tr_read(wbp + (bo_) + 8 * 160 + nt * 32)); \
      F.b[nt] = cat8(tr_read(wbp + (bo_) + 40 * 160 + nt * 32), tr_read(wbp + (bo_) + 48 * 160 + nt * 32)); \
      F.s[nt] = tr_read(wbs + (bo_) + nt * 32); } } while (0)
#define P3_MM(F, t) do { \
    _Pragma("unroll") for (int nt = 0; nt < 5; ++nt) acc[nt] = MFMA16(F.a[nt], __builtin_bit_cast(s16x8, op[2 * (t)]), acc[nt]); \
    _Pragma("unroll") for (int nt = 0; nt < 5; ++nt) acc[nt] = MFMA16(F.b[nt], __builtin_bit_cast(s16x8, op[2 * (t) + 1]), acc[nt]); } while (0)
#define P3_MS(t) do { const s16x8 os_ = cat8(__builtin_bit_cast(s16x4, osg[t]), __builtin_bit_cast(s16x4, osg[(t) + 1])); \
    _Pragma("unroll") for (int nt = 0; nt < 5; ++nt) acc[nt] = MFMA16(cat8(fwa.s[nt], fwb.s[nt]), os_, acc[nt]); } while (0)
  P3_LOADW(fwa, w0);
  SBAR();
  poll(ws, FL_O + (size_t)(((m3 >> 5) + (w >> 1)) * NH + role * 16 + r16) * 8, E);
  STAMP(12);
  {
    const int orow = (w & 1) * 16 + r16;
    const char* ob = ws + WS_OW + ((size_t)((m3 >> 5) + (w >> 1)) * NH + role * 16) * 1280;
    const char* pp = ob + (g >> 1) * 1280 + (g & 1) * 512 + orow * 16;
    const char* sp = ob + g * 1280 + 1024 + orow * 8;
#pragma unroll
    for (int t = 0; t < 4; ++t) {
      asm volatile("global_load_dwordx4 %0, %3, off sc1\n\tglobal_load_dwordx4 %1, %3, off offset:2560 sc1\n\tglobal_load_dwordx2 %2, %4, off sc1"
                   : "=&v"(op[2 * t]), "=&v"(op[2 * t + 1]), "=&v"(osg[t]) : "v"(pp + t * 5120), "v"(sp + t * 5120) : "memory");
    }
  }
  P3_LOADW(fwb, w1);
  if (role == 1) spin_ge4(ct + CT_WRD2, 1u);
  SBAR();
  asm volatile("s_waitcnt vmcnt(6)" : "+v"(op[0]), "+v"(op[1]), "+v"(op[2]), "+v"(op[3]), "+v"(osg[0]), "+v"(osg[1]) :: "memory");
  STAMP(13);
  P3_MS(0); P3_MM(fwa, 0);
  SBAR();
  P3_LOADW(fwa, w2); P3_MM(fwb, 1);
#pragma unroll
  for (int i = 0; i < 10; ++i) { __builtin_amdgcn_sched_group_barrier(0x008, 1, 0); __builtin_amdgcn_sched_group_barrier(0x100, 3, 0); }
  SBAR();
  asm volatile("s_waitcnt vmcnt(0)" : "+v"(op[4]), "+v"(op[5]), "+v"(op[6]), "+v"(op[7]), "+v"(osg[2]), "+v"(osg[3]) :: "memory");
  P3_LOADW(fwb, w3); P3_MM(fwa, 2);
#pragma unroll
  for (int i = 0; i < 10; ++i) { __builtin_amdgcn_sched_group_barrier(0x008, 1, 0); __builtin_amdgcn_sched_group_barrier(0x100, 3, 0); }
  SBAR();
  P3_MM(fwb, 3); P3_MS(2);
  SBAR();
  const unsigned rb = lds0 + AT_K + 25600 + w * 5120 + lane * 16;
  if (role == 1) {
#pragma unroll
    for (int nt = 0; nt < 5; ++nt) lds_write16(rb + nt * 1024, __builtin_bit_cast(u32x4, acc[nt]));
    asm volatile("s_waitcnt lgkmcnt(0)" ::: "memory");
    sig(ct + CT_RED + w * 4, 1u, lane);
    STAMP(14);
    DIAG_DUMP(1);
    return;
  }
  spin_ge(ct + CT_RED + w * 4, 1u);
  u16* orow_g = out + (size_t)(m3 + w * 16 + r16) * (2 * DM) + DM + n3;
  u32x2 ov[5];
#pragma unroll
  for (int nt = 0; nt < 5; ++nt) {
    const f32x4 o2 = acc[nt] + __builtin_bit_cast(f32x4, lds_read16(rb + nt * 1024));
    ov[nt][0] = pk_bf16(o2[0], o2[1]);
    ov[nt][1] = pk_bf16(o2[2], o2[3]);
  }
#pragma unroll
  for (int pr = 0; pr < 2; ++pr) {
    const auto sx = __builtin_amdgcn_permlane16_swap(ov[2 * pr][0], ov[2 * pr + 1][0], false, false);
    const auto sy = __builtin_amdgcn_permlane16_swap(ov[2 * pr][1], ov[2 * pr + 1][1], false, false);
    ostore16(orow_g + (2 * pr + (g & 1)) * 16 + 8 * (g >> 1), (u32x4){sx[0], sy[0], sx[1], sy[1]});
  }
  ostore8(orow_g + 64 + 4 * g, ov[4]);
  STAMP(14);
  DIAG_DUMP(0);
}

extern "C" void kernel_launch(void* const* d_in, const int* in_sizes, int n_in, void* d_out, int out_size,
                              void* d_ws, size_t ws_size, hipStream_t stream) {
  const u16* aq = (const u16*)d_in[0];
  const u16* ak = (const u16*)d_in[1];
  const u16* av = (const u16*)d_in[2];
  const u16* shk = (const u16*)d_in[3];
  const u16* ssmk = (const u16*)d_in[4];
  const u16* ssmd = (const u16*)d_in[5];
  const u16* x = (const u16*)d_in[6];
  const u16* wqkv = (const u16*)d_in[7];
  const u16* wo = (const u16*)d_in[8];
  (void)hipFuncSetAttribute((const void*)mega, hipFuncAttributeMaxDynamicSharedMemorySize, L_TOTAL);
  mega<<<256, 768, L_TOTAL, stream>>>(x, wqkv, ak, av, shk, wo, ssmk, ssmd, aq, (char*)d_ws, (u16*)d_out);
}
```

```cpp
#define CODE_WARM 44000
#define CW_N 1
#define CW_STRIDE 128
#include <hip/hip_runtime.h>
#include <hip/hip_bf16.h>
#include <stdint.h>

typedef unsigned short u16;
typedef short s16x4 __attribute__((ext_vector_type(4)));
typedef short s16x8 __attribute__((ext_vector_type(8)));
typedef float f32x2 __attribute__((ext_vector_type(2)));
typedef float f32x4 __attribute__((ext_vector_type(4)));
typedef float f32x16 __attribute__((ext_vector_type(16)));
typedef unsigned u32x2 __attribute__((ext_vector_type(2)));
typedef unsigned u32x4 __attribute__((ext_vector_type(4)));
typedef __bf16 bf16x2 __attribute__((ext_vector_type(2)));
#define LAS __attribute__((address_space(3)))
#define DI __device__ __forceinline__

constexpr int SEQ = 2048, DM = 640, NQKV = 1920, NH = 32;
constexpr size_t WS_QF = 0;
constexpr size_t WS_KF = WS_QF + (size_t)DM * SEQ * 2;
constexpr size_t WS_VF = WS_KF + (size_t)DM * SEQ * 2;
constexpr size_t WS_UT = WS_VF + (size_t)DM * SEQ * 2;
constexpr size_t WS_ZT = WS_UT + (size_t)DM * SEQ * 2;
constexpr size_t WS_OW = WS_ZT + (size_t)DM * SEQ * 2;
typedef unsigned long long u64;
constexpr size_t WS_FL = WS_OW + (size_t)DM * SEQ * 2;
constexpr size_t FL_U = WS_FL + 256;
constexpr size_t FL_Q = FL_U + 20 * 64 * 8;
constexpr size_t FL_Z = FL_Q + 16 * 16 * 8;
constexpr size_t FL_O = FL_Z + 640 * 8;

DI unsigned pk_bf16(float lo, float hi) {
  f32x2 f = {lo, hi};
  return __builtin_bit_cast(unsigned, __builtin_convertvector(f, bf16x2));
}
DI float bf_lo(unsigned u) { return __uint_as_float(u << 16); }
DI float bf_hi(unsigned u) { return __uint_as_float(u & 0xffff0000u); }
DI float rbf(float x) { return bf_lo(pk_bf16(x, 0.f)); }

DI unsigned lds_addr(const void* p) { return (unsigned)(uintptr_t)(LAS const char*)p; }

DI void glds16(unsigned voff, const void* sbase, unsigned lds_dst) {
  unsigned keep;
  asm volatile("s_mov_b32 %0, m0\n\ts_mov_b32 m0, %3\n\ts_nop 0\n\tglobal_load_lds_dwordx4 %1, %2\n\ts_mov_b32 m0, %0"
               : "=&s"(keep) : "v"(voff), "s"(sbase), "s"(lds_dst) : "memory");
}
DI void glds16f(unsigned voff, const void* sbase, unsigned lds_dst) {
  asm volatile("s_mov_b32 m0, %2\n\ts_nop 0\n\tglobal_load_lds_dwordx4 %0, %1" :: "v"(voff), "s"(sbase), "s"(lds_dst) : "memory");
}
#define glds16p glds16
DI void glds16nt(unsigned voff, const void* sbase, unsigned lds_dst) {
  unsigned keep;
  asm volatile("s_mov_b32 %0, m0\n\ts_mov_b32 m0, %3\n\ts_nop 0\n\tglobal_load_lds_dwordx4 %1, %2\n\ts_mov_b32 m0, %0"
               : "=&s"(keep) : "v"(voff), "s"(sbase), "s"(lds_dst) : "memory");
}
#define LDPOL "sc1"
DI void glds16c(unsigned voff, const void* sbase, unsigned lds_dst) {
  unsigned keep;
  asm volatile("s_mov_b32 %0, m0\n\ts_mov_b32 m0, %3\n\ts_nop 0\n\tglobal_load_lds_dwordx4 %1, %2 " LDPOL "\n\ts_mov_b32 m0, %0"
               : "=&s"(keep) : "v"(voff), "s"(sbase), "s"(lds_dst) : "memory");
}
struct Mk { const void* src; unsigned x; };
DI void glds_mark(const Mk& mk, unsigned lds_dst, int lane) {
  if (lane == 0) {
    unsigned keep;
    asm volatile("s_mov_b32 %0, m0\n\ts_mov_b32 m0, %3\n\ts_nop 0\n\tglobal_load_lds_dword %1, %2\n\ts_mov_b32 m0, %0"
                 : "=&s"(keep) : "v"(0u), "s"(mk.src), "s"(lds_dst) : "memory");
  }
}
DI float dot2bf(unsigned a, unsigned b, float c) {
  return __builtin_amdgcn_fdot2_f32_bf16(__builtin_bit_cast(bf16x2, a), __builtin_bit_cast(bf16x2, b), c, false);
}
DI void gstore16c(void* p, u32x4 v) {
  asm volatile("global_store_dwordx4 %0, %1, off sc1\n\ts_nop 1" :: "v"(p), "v"(v) : "memory");
}
DI void gstore8c(void* p, u32x2 v) {
  asm volatile("global_store_dwordx2 %0, %1, off sc1" :: "v"(p), "v"(v) : "memory");
}
#define OUTPOL "nt"
DI void ostore16(void* p, u32x4 v) { asm volatile("global_store_dwordx4 %0, %1, off " OUTPOL "\n\ts_nop 1" :: "v"(p), "v"(v) : "memory"); }
DI void ostore8(void* p, u32x2 v) { asm volatile("global_store_dwordx2 %0, %1, off " OUTPOL :: "v"(p), "v"(v) : "memory"); }
template <int N> DI void wait_vm() { asm volatile("s_waitcnt vmcnt(%0)" :: "n"(N) : "memory"); }
DI void bar() { asm volatile("s_waitcnt lgkmcnt(0)\n\ts_barrier" ::: "memory"); }
DI s16x4 tr_read(unsigned a) {
  return __builtin_amdgcn_ds_read_tr16_b64_v4i16((LAS s16x4*)(uintptr_t)a);
}
DI s16x8 lds_read16(unsigned a) { return *(LAS const s16x8*)(uintptr_t)a; }
DI s16x4 lds_read8(unsigned a) { return *(LAS const s16x4*)(uintptr_t)a; }
DI void lds_write16(unsigned a, u32x4 v) { *(LAS u32x4*)(uintptr_t)a = v; }
DI s16x8 cat8(s16x4 a, s16x4 b) { return __builtin_shufflevector(a, b, 0, 1, 2, 3, 4, 5, 6, 7); }
#define MFMA16(a, b, c) __builtin_amdgcn_mfma_f32_16x16x32_bf16((a), (b), (c), 0, 0, 0)
#define MFMA32(a, b, c) __builtin_amdgcn_mfma_f32_32x32x16_bf16((a), (b), (c), 0, 0, 0)

struct Diag {};
#define STAMP(i) do {} while (0)
#define DIAG_INIT() Diag dg
#define DIAG_DUMP(kid) do {} while (0)

#define SBAR() __builtin_amdgcn_sched_barrier(0)
#define SPIN_SLEEP 1
#define SPIN_MAX (1 << 21)
DI unsigned lds_ld32(unsigned a) { return *(volatile LAS unsigned*)(uintptr_t)a; }
DI void lds_st32(unsigned a, unsigned v) { *(volatile LAS unsigned*)(uintptr_t)a = v; }
DI void spin_ge(unsigned a, unsigned want) {
#pragma nounroll
  for (int it = 0; it < SPIN_MAX; ++it) {
    if ((unsigned)__builtin_amdgcn_readfirstlane(lds_ld32(a)) >= want) break;
    __builtin_amdgcn_s_sleep(1);
  }
  asm volatile("" ::: "memory");
}
DI void spin_ge4(unsigned a, unsigned want) {
#pragma nounroll
  for (int it = 0; it < SPIN_MAX; ++it) {
    const u32x4 v = *(volatile LAS u32x4*)(uintptr_t)a;
    const unsigned m = min(min(v[0], v[1]), min(v[2], v[3]));
    if ((unsigned)__builtin_amdgcn_readfirstlane(m) >= want) break;
    __builtin_amdgcn_s_sleep(SPIN_SLEEP);
  }
  asm volatile("" ::: "memory");
}
DI unsigned mk_bad(const u32x4& v, unsigned x) { return (v[0] ^ x) | (v[1] ^ x) | (v[2] ^ x) | (v[3] ^ x); }
DI void spin_mk(unsigned a, unsigned x) {
#pragma nounroll
  for (int it = 0; it < SPIN_MAX; ++it) {
    const u32x4 v = *(volatile LAS u32x4*)(uintptr_t)a;
    if ((unsigned)__builtin_amdgcn_readfirstlane(mk_bad(v, x)) == 0u) break;
    __builtin_amdgcn_s_sleep(SPIN_SLEEP);
  }
  asm volatile("" ::: "memory");
}
DI void spin_ge8(unsigned a, unsigned want) { spin_ge4(a, want); spin_ge4(a + 16, want); }
constexpr int CT_S0 = 0, CT_FREE = 80, CT_PAIR = 144  , CT_QUAD = 192  , CT_ZCNT = 240  ,
              CT_QCNT = 256, CT_ZRDY = 304, CT_CVD = 336  , CT_XST = 416, CT_ATT = 448, CT_RED = 480,
              CT_FX = 512  , CT_KR8 = 688, CT_VR8 = 704, CT_WRDY = 720, CT_WRD2 = 736, CT_BYTES = 1024;
DI void sig(unsigned a, unsigned v, int lane) { if (lane == 0) lds_st32(a, v); }

#define POLL_MAX (1 << 15)
DI void publish(char* ws, size_t off, u64 E, int lane) {
  asm volatile("s_waitcnt vmcnt(0)" ::: "memory");
  if (lane == 0) __hip_atomic_store((u64*)(ws + off), E, __ATOMIC_RELAXED, __HIP_MEMORY_SCOPE_AGENT);
}
#define POLL_SLEEP 6
DI void poll(const char* ws, size_t off, u64 E) {
#pragma nounroll
  for (int it = 0; it < POLL_MAX; ++it) {
    const u64 v = __hip_atomic_load((const u64*)(ws + off), __ATOMIC_RELAXED, __HIP_MEMORY_SCOPE_AGENT);
    if (__builtin_amdgcn_ballot_w64(v != E) == 0ull) break;
    __builtin_amdgcn_s_sleep(POLL_SLEEP);
  }
  asm volatile("" ::: "memory");
}

constexpr int K1_STAGE = 32768;
constexpr int A1_K = 0, A1_V = 3072, A1_W = 5120, A1_WAVE = 5632, A1_O = 2 * A1_WAVE, A1_PAIR = A1_O + 4096;
constexpr int L_AUX = 4 * K1_STAGE;
constexpr int L_CT = L_AUX + 2 * A1_PAIR;
constexpr int L_TOTAL = L_CT + CT_BYTES;
constexpr int K1_CT = L_CT;
constexpr int CV_COPY = 4160, CV_H = 4608, CV_REG = CV_H + 24576;
constexpr int AT_Q = L_AUX + 12288, AT_K = 64000, AT_V = AT_K + 16 * 2048, AT_END = AT_V + 16 * 1344;
constexpr int p3_w(int t) { return t < 5 ? t * 12800 : (t == 5 ? AT_END : AT_K + (t - 6) * 12800); }

#define PSYNC() do { asm volatile("s_waitcnt lgkmcnt(0)" ::: "memory"); ++ps; sig(pf + wh * 4, ps, lane); spin_ge(pf + (wh ^ 1) * 4, ps); } while (0)
DI void a1_issue(unsigned lds, int tile, int lane, int wh, const u16* ak, const u16* av, const u16* shk) {
  const int ts = tile / 10, td = tile - ts * 10;
  const int s0 = ts * 32 + wh * 16, d0 = td * 64;
  const int lr = lane >> 3, lc = (lane & 7) * 16;
  const unsigned wl = lds + wh * A1_WAVE;
#pragma unroll
  for (int i = 0; i < 3; ++i) {
    int r = s0 - 3 + i * 8 + lr; r = r < 0 ? 0 : r;
    if (i < 2 || lane < 24) glds16nt((unsigned)(r * DM * 2 + lc), (const char*)ak + d0 * 2, wl + A1_K + i * 1024);
  }
#pragma unroll
  for (int i = 0; i < 2; ++i)
    glds16nt((unsigned)(lr * DM * 2 + lc), (const char*)av + ((size_t)(s0 + i * 8) * DM + d0) * 2, wl + A1_V + i * 1024);
  if (lane < 32) glds16((unsigned)(lane * 16), (const char*)shk + d0 * 8, wl + A1_W);
}
DI void a1_finish(unsigned lds, unsigned pf, unsigned& ps, int tile, int lane, int wh, char* ws, u64 E) {
  const int ts = tile / 10, td = tile - ts * 10;
  const int c = lane & 7;
  const unsigned wl = lds + wh * A1_WAVE;
  u32x4 wt[4];
#pragma unroll
  for (int i = 0; i < 4; ++i) wt[i] = __builtin_bit_cast(u32x4, lds_read16(wl + A1_W + c * 64 + i * 16));
#pragma unroll
  for (int j = 0; j < 2; ++j) {
    const int r = j * 8 + (lane >> 3);
    const unsigned ka = wl + A1_K + (r + 3) * 128 + c * 16;
    u32x4 k0 = __builtin_bit_cast(u32x4, lds_read16(ka)), k1 = __builtin_bit_cast(u32x4, lds_read16(ka - 128));
    u32x4 k2 = __builtin_bit_cast(u32x4, lds_read16(ka - 256)), k3 = __builtin_bit_cast(u32x4, lds_read16(ka - 384));
    const u32x4 vv = __builtin_bit_cast(u32x4, lds_read16(wl + A1_V + r * 128 + c * 16));
    if (j == 0 && ts == 0 && wh == 0) {
      const u32x4 z = {0u, 0u, 0u, 0u};
      if (r < 1) k1 = z;
      if (r < 2) k2 = z;
      if (r < 3) k3 = z;
    }
    u32x4 o;
#pragma unroll
    for (int i = 0; i < 4; ++i) {
      const unsigned pa0 = __builtin_amdgcn_perm(k1[i], k0[i], 0x05040100u), pb0 = __builtin_amdgcn_perm(k3[i], k2[i], 0x05040100u);
      const unsigned pa1 = __builtin_amdgcn_perm(k1[i], k0[i], 0x07060302u), pb1 = __builtin_amdgcn_perm(k3[i], k2[i], 0x07060302u);
      const float e0 = dot2bf(pb0, wt[i][1], dot2bf(pa0, wt[i][0], 0.f));
      const float e1 = dot2bf(pb1, wt[i][3], dot2bf(pa1, wt[i][2], 0.f));
      o[i] = pk_bf16(e0 * bf_lo(vv[i]), e1 * bf_hi(vv[i]));
    }
    lds_write16(lds + A1_O + (31 - wh * 16 - r) * 128 + c * 16, o);
  }
  PSYNC();
  const int gg = lane >> 4, q = (lane & 15) >> 2, p = lane & 3;
  u16* ut = (u16*)(ws + WS_UT);
#pragma unroll
  for (int j = 0; j < 2; ++j) {
    const unsigned ra = lds + A1_O + (gg * 8 + q) * 128 + wh * 64 + j * 32 + p * 8;
    const s16x4 t0 = tr_read(ra), t1 = tr_read(ra + 512);
    const int d = td * 64 + wh * 32 + j * 16 + (lane & 15);
    gstore16c(ut + (size_t)d * SEQ + (SEQ - 32 - ts * 32) + gg * 8, __builtin_bit_cast(u32x4, cat8(t0, t1)));
  }
}
DI void a1_publish(int tile, int lane, int wh, char* ws, u64 E) {
  const int ts = tile / 10, td = tile - ts * 10;
  publish(ws, FL_U + (size_t)((td * 2 + wh) * 64 + ts) * 8, E, lane);
}

template <bool VT>
DI void k1_gemm(unsigned lds0, int m0, int n0, int tid, char* ws, unsigned mkx, Diag& dg) {
  const int lane = tid & 63, wave = __builtin_amdgcn_readfirstlane(tid >> 6);
  const int wm = wave >> 1, wn = wave & 1;
  const int l32 = lane & 31, hh = lane >> 5, gg = lane >> 4, q = (lane & 15) >> 2, p = lane & 3;
  const unsigned ct = lds0 + K1_CT;
  f32x16 acc[2][2];
#pragma unroll
  for (int i = 0; i < 2; ++i)
#pragma unroll
    for (int j = 0; j < 2; ++j)
#pragma unroll
      for (int r = 0; r < 16; ++r) acc[i][j][r] = 0.f;
  unsigned xb[4];
#pragma unroll
  for (int ks = 0; ks < 4; ++ks) xb[ks] = lds0 + (wm * 64 + l32) * 128 + (((unsigned)(2 * ks + hh) ^ (unsigned)((l32 >> 1) & 7)) * 16);
  unsigned wb[2];
#pragma unroll
  for (int nt = 0; nt < 2; ++nt)
    wb[nt] = lds0 + 16384 + (8 * hh + q) * 256 + (((unsigned)(wn * 4 + 2 * nt + (gg & 1)) ^ (unsigned)(q << 1)) * 32) + p * 8;
  s16x8 x0[2], w0[2], x1[2], w1[2], x2[2], w2[2], x3[2], w3[2];
#define K1_LOADF(XF, WF, bo_, ks) do { \
    _Pragma("unroll") for (int i_ = 0; i_ < 2; ++i_) { \
      WF[i_] = cat8(tr_read(wb[i_] + (bo_) + (ks) * 4096), tr_read(wb[i_] + (bo_) + (ks) * 4096 + 1024)); \
      XF[i_] = lds_read16(xb[ks] + (bo_) + i_ * 4096); } } while (0)
#define K1_MM(XF, WF) do { _Pragma("unroll") for (int mt = 0; mt < 2; ++mt) _Pragma("unroll") for (int nt = 0; nt < 2; ++nt) \
      acc[mt][nt] = VT ? MFMA32(XF[mt], WF[nt], acc[mt][nt]) : MFMA32(WF[nt], XF[mt], acc[mt][nt]); } while (0)
#define ILV_A 2
#define ILV_B 2
#define ILV_C 2
#define K1_ILV() do { \
      __builtin_amdgcn_sched_group_barrier(0x008, 1, 0); __builtin_amdgcn_sched_group_barrier(0x100, ILV_A, 0); \
      __builtin_amdgcn_sched_group_barrier(0x008, 1, 0); __builtin_amdgcn_sched_group_barrier(0x100, ILV_B, 0); \
      __builtin_amdgcn_sched_group_barrier(0x008, 1, 0); __builtin_amdgcn_sched_group_barrier(0x100, ILV_C, 0); \
      __builtin_amdgcn_sched_group_barrier(0x008, 1, 0); __builtin_amdgcn_sched_group_barrier(0x100, 2, 0); } while (0)
  STAMP(1);
  K1_LOADF(x0, w0, 0, 0);
  K1_LOADF(x1, w1, 0, 1);
#pragma nounroll
  for (int t = 0; t < 10; ++t) {
    const unsigned bo = (t & 3) * K1_STAGE, bn = ((t + 1) & 3) * K1_STAGE;
    SBAR();
    const u32x4 fl4 = *(volatile LAS u32x4*)(uintptr_t)(ct + CT_FX + (t + 1) * 16);
    K1_LOADF(x2, w2, bo, 2); K1_MM(x0, w0); K1_ILV();
    SBAR();
    K1_LOADF(x3, w3, bo, 3); K1_MM(x1, w1); K1_ILV();
    SBAR();
    const unsigned flu = (unsigned)__builtin_amdgcn_readfirstlane(mk_bad(fl4, mkx));
    if (t < 9 && flu != 0u) spin_mk(ct + CT_FX + (t + 1) * 16, mkx);
    asm volatile("" ::: "memory");
    SBAR();
    K1_LOADF(x0, w0, bn, 0); K1_MM(x2, w2); K1_ILV();
    SBAR();
    asm volatile("" : "+v"(x3[0]), "+v"(x3[1]), "+v"(w3[0]), "+v"(w3[1]) :: "memory");
    if (lane == 0) lds_st32(ct + CT_FREE + (t & 3) * 16 + wave * 4, (unsigned)(t + 1));
    SBAR();
    K1_LOADF(x1, w1, bn, 1); K1_MM(x3, w3); K1_ILV();
  }
  SBAR();
  STAMP(2);
  if (!VT) {
    const int part = n0 >= DM ? 1 : 0;
    const float sc = part == 0 ? 0.22360679774997896f * 1.4426950408889634f : 1.0f;
    char* base = ws + (part ? WS_KF : WS_QF) + ((size_t)((m0 + wm * 64) >> 5) * 40 + ((n0 - part * DM + wn * 64) >> 4)) * 1024 + lane * 16;
#pragma unroll
    for (int mt = 0; mt < 2; ++mt)
#pragma unroll
      for (int nt = 0; nt < 2; ++nt)
#pragma unroll
        for (int uh = 0; uh < 2; ++uh) {
          u32x4 o;
#pragma unroll
          for (int i = 0; i < 4; ++i) o[i] = pk_bf16(acc[mt][nt][8 * uh + 2 * i] * sc, acc[mt][nt][8 * uh + 2 * i + 1] * sc);
          gstore16c(base + (mt * 40 + nt * 2 + uh) * 1024, o);
        }
  } else {
    char* base = ws + WS_VF + ((size_t)((m0 + wm * 64) >> 4) * 20 + ((n0 - 2 * DM + wn * 64) >> 5)) * 1024 + lane * 16;
#pragma unroll
    for (int mt = 0; mt < 2; ++mt)
#pragma unroll
      for (int nt = 0; nt < 2; ++nt)
#pragma unroll
        for (int kg = 0; kg < 2; ++kg) {
          u32x4 o;
#pragma unroll
          for (int i = 0; i < 4; ++i) o[i] = pk_bf16(acc[mt][nt][8 * kg + 2 * i], acc[mt][nt][8 * kg + 2 * i + 1]);
          gstore16c(base + ((mt * 2 + kg) * 20 + nt) * 1024, o);
        }
  }
}

DI void cv_put(unsigned y, const u32x4& c, const u32x2& n) {
  const unsigned d0 = c[0], d1 = c[1], d2 = c[2], d3 = c[3], d4 = n[0], d5 = n[1];
  const unsigned a0 = __builtin_amdgcn_alignbit(d1, d0, 16), a1 = __builtin_amdgcn_alignbit(d2, d1, 16),
                 a2 = __builtin_amdgcn_alignbit(d3, d2, 16), a3 = __builtin_amdgcn_alignbit(d4, d3, 16),
                 a4 = __builtin_amdgcn_alignbit(d5, d4, 16);
  lds_write16(y, (u32x4){d0, d1, d2, d3});
  lds_write16(y + CV_COPY, (u32x4){a0, a1, a2, a3});
  lds_write16(y + 2 * CV_COPY, (u32x4){d1, d2, d3, d4});
  lds_write16(y + 3 * CV_COPY, (u32x4){a1, a2, a3, a4});
}

#define QSYNC() do { asm volatile("s_waitcnt lgkmcnt(0)" ::: "memory"); ++qs; sig(qf + wq * 4, qs, lane); spin_ge4(qf, qs); } while (0)
struct HRegs { u32x4 c, u; u32x2 n; unsigned d; };
DI HRegs conv_load_h(int ch, int t256, const u16* __restrict__ ssmk, const u16* __restrict__ ssmd) {
  HRegs r;
  const u16* hrow = ssmk + (size_t)ch * SEQ;
  r.c = *((const u32x4*)(hrow + t256 * 8));
  r.n = *(const u32x2*)(hrow + (t256 < 255 ? t256 * 8 + 8 : 0));
  r.d = ssmd[ch];
  return r;
}
DI void conv_load_u(HRegs& r, int ch, int lane, int wq, const char* __restrict__ ws, u64 E) {
  poll(ws, FL_U + (size_t)((ch >> 5) * 64 + 48 - 16 * wq + (lane & 15)) * 8, E);
  const u64* up = (const u64*)(ws + WS_UT + (size_t)ch * SEQ * 2 + wq * 1024 + lane * 16);
  const u64 a = __hip_atomic_load(up, __ATOMIC_RELAXED, __HIP_MEMORY_SCOPE_AGENT);
  const u64 b = __hip_atomic_load(up + 1, __ATOMIC_RELAXED, __HIP_MEMORY_SCOPE_AGENT);
  r.u = (u32x4){(unsigned)a, (unsigned)(a >> 32), (unsigned)b, (unsigned)(b >> 32)};
}
DI void conv_stage(unsigned reg, int lane, int wq, const HRegs& hr) {
  const int t256 = wq * 64 + lane;
  const unsigned hb = reg + CV_H;
  if (t256 < 32) lds_write16(reg + 4096 + t256 * 16, (u32x4){0u, 0u, 0u, 0u});
  cv_put(hb + (t256 + 2) * 16, hr.c, hr.n);
  if (t256 < 2) {
    const u32x4 z = {0u, 0u, 0u, 0u};
    u32x2 nx = {0u, 0u};
    const unsigned h0 = __builtin_amdgcn_readfirstlane(hr.c[0]), h1 = __builtin_amdgcn_readfirstlane(hr.c[1]);
    if (t256 == 1) nx = (u32x2){h0, h1};
    cv_put(hb + t256 * 16, z, nx);
  }
}
DI void conv_run(unsigned reg, unsigned qf, unsigned& qs, int ch, int lane, int wq, const HRegs& hr,
                 char* __restrict__ ws, unsigned xst, Diag& dg) {
  const unsigned hb = reg + CV_H;
  if (xst) spin_ge4(xst, 1u);
  else { lds_write16(reg + (wq * 64 + lane) * 16, hr.u); QSYNC(); }
  STAMP(6);
  const int r16 = lane & 15, g = lane >> 4, q = r16 >> 2, p = lane & 3;
  const unsigned ab0 = hb + ((q + 1) & 3) * CV_COPY + 2 * (8 * g + 4 * p + (q == 3 ? 4 : 0)) + wq * 128;
  const unsigned bb0 = reg + 2 * (2032 - 16 * r16 + 8 * g) + wq * 128 - 7 * 512;
  f32x4 acc[8];
#pragma unroll
  for (int i = 0; i < 8; ++i) acc[i] = (f32x4){0.f, 0.f, 0.f, 0.f};
#define CV_LDA(a_, ad_) cat8(tr_read((ad_) + (a_) * 512), tr_read((ad_) + (a_) * 512 + 8))
#define CV_LDB(c_, bd_) lds_read16((bd_) + (7 - (c_)) * 512)
  {
    s16x8 A[8], B[8];
    B[0] = CV_LDB(0, bb0);
#pragma unroll
    for (int a = 0; a < 8; ++a) A[a] = CV_LDA(a, ab0);
#pragma unroll
    for (int c = 1; c < 8; ++c) B[c] = CV_LDB(c, bb0);
#pragma unroll
    for (int c = 0; c < 8; ++c) {
      SBAR();
#pragma unroll
      for (int a = 0; a + c < 8; ++a) acc[a + c] = MFMA16(A[a], B[c], acc[a + c]);
      B[c] = CV_LDB(c, bb0 + 64);
      A[7 - c] = CV_LDA(7 - c, ab0 + 64);
    }
    SBAR();
#pragma unroll
    for (int a = 7; a >= 0; --a)
#pragma unroll
      for (int c = 0; a + c < 8; ++c) acc[a + c] = MFMA16(A[a], B[c], acc[a + c]);
  }
  QSYNC();
#pragma unroll
  for (int nt = 0; nt < 8; ++nt) {
    const int dw = nt >> 1;
    if (dw != wq) lds_write16(hb + (nt * 3 + (wq > dw ? wq - 1 : wq)) * 1024 + lane * 16, __builtin_bit_cast(u32x4, acc[nt]));
  }
  f32x4 k0 = wq == 0 ? acc[0] : (wq == 1 ? acc[2] : (wq == 2 ? acc[4] : acc[6]));
  f32x4 k1 = wq == 0 ? acc[1] : (wq == 1 ? acc[3] : (wq == 2 ? acc[5] : acc[7]));
  QSYNC();
  const float dco = bf_lo(hr.d);
  u32x2 zz[2];
#pragma unroll
  for (int i = 0; i < 2; ++i) {
    f32x4 o = i ? k1 : k0;
#pragma unroll
    for (int j = 0; j < 3; ++j) o += __builtin_bit_cast(f32x4, lds_read16(hb + ((wq * 2 + i) * 3 + j) * 1024 + lane * 16));
    const int s = (wq * 2 + i) * 256 + 16 * r16 + 4 * g;
    const u32x2 uu = __builtin_bit_cast(u32x2, lds_read8(reg + 2 * (SEQ - 4 - s)));
    const float kv0 = bf_hi(uu[1]), kv1 = bf_lo(uu[1]), kv2 = bf_hi(uu[0]), kv3 = bf_lo(uu[0]);
    const unsigned y01 = pk_bf16(o[0], o[1]), y23 = pk_bf16(o[2], o[3]);
    const unsigned e01 = pk_bf16(kv0 * dco, kv1 * dco), e23 = pk_bf16(kv2 * dco, kv3 * dco);
    zz[i][0] = pk_bf16(bf_lo(y01) + bf_lo(e01), bf_hi(y01) + bf_hi(e01));
    zz[i][1] = pk_bf16(bf_lo(y23) + bf_lo(e23), bf_hi(y23) + bf_hi(e23));
  }
  const auto sx = __builtin_amdgcn_permlane16_swap(zz[0][0], zz[1][0], false, false);
  const auto sy = __builtin_amdgcn_permlane16_swap(zz[0][1], zz[1][1], false, false);
  u16* zrow = (u16*)(ws + WS_ZT) + (size_t)ch * SEQ;
  gstore16c(zrow + (wq * 2 + (g & 1)) * 256 + 16 * r16 + 8 * (g >> 1), (u32x4){sx[0], sy[0], sx[1], sy[1]});
  asm volatile("s_waitcnt lgkmcnt(0)" ::: "memory");
}
DI void conv_publish(unsigned zc, int ch, int lane, char* __restrict__ ws, u64 E) {
  asm volatile("s_waitcnt vmcnt(0)" ::: "memory");
  if (lane == 0) {
    const unsigned old = __hip_atomic_fetch_add((LAS unsigned*)(uintptr_t)zc, 1u, __ATOMIC_RELAXED, __HIP_MEMORY_SCOPE_WORKGROUP);
    if ((old & 3u) == 3u) __hip_atomic_store((u64*)(ws + FL_Z + (size_t)ch * 8), E, __ATOMIC_RELAXED, __HIP_MEMORY_SCOPE_AGENT);
  }
}

DI s16x8 pack8(const f32x16& v, int o) {
  u32x4 r;
  r[0] = pk_bf16(v[o + 0], v[o + 1]); r[1] = pk_bf16(v[o + 2], v[o + 3]);
  r[2] = pk_bf16(v[o + 4], v[o + 5]); r[3] = pk_bf16(v[o + 6], v[o + 7]);
  return __builtin_bit_cast(s16x8, r);
}
DI float max3f(float a, float b, float c) { return __builtin_fmaxf(__builtin_fmaxf(a, b), c); }
DI float max16(const f32x16& v, float m) {
  float a = max3f(v[0], v[1], m), b = max3f(v[2], v[3], v[4]);
  a = max3f(a, v[5], v[6]); b = max3f(b, v[7], v[8]);
  a = max3f(a, v[9], v[10]); b = max3f(b, v[11], v[12]);
  a = max3f(a, v[13], v[14]);
  return max3f(a, b, v[15]);
}
DI float xhalf_max(float m) {
  auto sw = __builtin_amdgcn_permlane32_swap(__float_as_uint(m), __float_as_uint(m), false, false);
  return __builtin_fmaxf(__uint_as_float(sw[0]), __uint_as_float(sw[1]));
}

#define AT_LMAX 65536.0f
#define P3_WADDR() unsigned wv[4]; _Pragma("unroll") for (int i = 0; i < 4; ++i) { \
      const int c = i < 3 ? (i * 4 + w) * 64 + lane : 768 + w * 8 + (lane & 7); const int row = c / 10; \
      wv[i] = (unsigned)(row * DM * 2 + (c - row * 10) * 16); } const char* wog = (const char*)(wo + n3)
#define P3_STW(t) do { _Pragma("unroll") for (int i_ = 0; i_ < 3; ++i_) \
        glds16(wv[i_], wog + (size_t)(t) * 80 * DM * 2, lds0 + p3_w(t) + (i_ * 4 + w) * 1024); \
      if (lane < 8) glds16(wv[3], wog + (size_t)(t) * 80 * DM * 2, lds0 + p3_w(t) + 12288 + w * 128); } while (0)
DI void attn_stage(unsigned lds0, int h, int qb8, int j, int lane, char* __restrict__ ws, u64 E, int zch, const Mk& mk, Diag& dg) {
  const int qt = qb8 * 8 + j;
  const unsigned l16 = (unsigned)(lane * 16), ct = lds0 + L_CT;
  const int qa = (5 * h) >> 2;
  const bool stg = qb8 > 0 || j >= 2;
  {
    const int l3 = lane & 7, hi = lane & 1;
    const bool kv = stg && l3 >= 2 && l3 < 6;
    const int rb = kv ? 2 * qb8 - 2 + j : 2 * qb8 + (l3 >= 6 ? 1 : 0);
    const int col = !kv ? qa * 16 + hi * 31 : (l3 < 4 ? DM + qa * 16 + hi * 31 : 2 * DM + h * 20 + hi * 19);
    poll(ws, FL_Q + (size_t)(rb * 16 + (col >> 7)) * 8, E);
  }
  STAMP(9);
  conv_publish(ct + CT_ZCNT + 8, zch, lane, ws, E);
  const int kt0 = qb8 * 8 - 8 + 4 * j;
#pragma unroll
  for (int i = 0; i < 4; ++i)
    glds16c(l16, ws + WS_QF + ((size_t)(qt + (i >> 1) * 4) * 40 + qa + (i & 1)) * 1024, lds0 + AT_Q + ((j + (i >> 1) * 4) * 2 + (i & 1)) * 1024);
  if (stg) {
#pragma unroll
    for (int i = 0; i < 8; ++i)
      glds16c(l16, ws + WS_KF + ((size_t)(kt0 + (i >> 1)) * 40 + qa + (i & 1)) * 1024, lds0 + AT_K + (j * 8 + i) * 1024);
  }
  glds_mark(mk, ct + CT_KR8 + j * 4, lane);
  if (stg) {
#pragma unroll
    for (int i = 0; i < 6; ++i) {
      const int c = i * 64 + lane;
      const int kgp = (c * 1561) >> 16, rem = c - kgp * 42, vh = rem >= 21 ? 1 : 0, d = rem - vh * 21;
      const int n = h * 20 + d;
      if (c < 336 && d == 20) lds_write16(lds0 + AT_V + j * 5376 + c * 16, (u32x4){0x3f803f80u, 0x3f803f80u, 0x3f803f80u, 0x3f803f80u});
      if (c < 336 && d < 20)
        glds16c((unsigned)((kgp * 20 + (n >> 5)) * 1024 + (vh * 32 + (n & 31)) * 16), ws + WS_VF + ((size_t)(kt0 * 2) * 20) * 1024,
                lds0 + AT_V + j * 5376 + i * 1024);
    }
    asm volatile("s_waitcnt lgkmcnt(0)" ::: "memory");
  }
  glds_mark(mk, ct + CT_VR8 + j * 4, lane);
}
template <bool FULL>
DI void attn_task(unsigned lds0, int h, int qb8, int j, int lane, char* __restrict__ ws, u64 E, int zch, const Mk& mk, Diag& dg) {
  const int l32 = lane & 31, hh = lane >> 5;
  const int qt = qb8 * 8 + j;
  const int tfirst = FULL ? 0 : 8 - qt;
  const unsigned l16 = (unsigned)(lane * 16), ct = lds0 + L_CT;
  const int qa = (5 * h) >> 2;
  u32x4 q0r, q1r;
  spin_mk(ct + CT_KR8, mk.x);
  if (!FULL) spin_mk(ct + CT_VR8, mk.x);
  q0r = __builtin_bit_cast(u32x4, lds_read16(lds0 + AT_Q + j * 2048 + l16));
  q1r = __builtin_bit_cast(u32x4, lds_read16(lds0 + AT_Q + j * 2048 + 1024 + l16));
  asm volatile("s_waitcnt lgkmcnt(0)" : "+v"(q0r), "+v"(q1r) :: "memory");
  if (zch >= 0) conv_publish(ct + CT_ZCNT + (j >> 2) * 4, zch, lane, ws, E);
  {
    const int g0 = 4 * qa + hh - 5 * h;
    const bool k0 = g0 >= 0, k1 = g0 + 2 >= 0 && g0 + 2 < 5, k2 = g0 + 4 < 5, k3 = g0 + 6 < 5;
    q0r[0] = k0 ? q0r[0] : 0u; q0r[1] = k0 ? q0r[1] : 0u; q0r[2] = k1 ? q0r[2] : 0u; q0r[3] = k1 ? q0r[3] : 0u;
    q1r[0] = k2 ? q1r[0] : 0u; q1r[1] = k2 ? q1r[1] : 0u; q1r[2] = k3 ? q1r[2] : 0u; q1r[3] = k3 ? q1r[3] : 0u;
  }
  s16x8 qlo = __builtin_bit_cast(s16x8, q0r), qhi8 = __builtin_bit_cast(s16x8, q1r);
  STAMP(8);
  const f32x16 zero16 = {0.f, 0.f, 0.f, 0.f, 0.f, 0.f, 0.f, 0.f, 0.f, 0.f, 0.f, 0.f, 0.f, 0.f, 0.f, 0.f};
  const float ninf = -INFINITY;
  const f32x16 ninf16 = {ninf, ninf, ninf, ninf, ninf, ninf, ninf, ninf, ninf, ninf, ninf, ninf, ninf, ninf, ninf, ninf};
  const unsigned kb = lds0 + AT_K + j * 2048 + l16;
#define AT_QK(dst, t, cin) do { if (FULL || (t) >= tfirst) { dst = MFMA32(lds_read16(kb + (t) * 2048), qlo, cin); \
    dst = MFMA32(lds_read16(kb + (t) * 2048 + 1024), qhi8, dst); } else dst = ninf16; } while (0)
  const int vrow = l32 <= 20 ? l32 : (l32 == 24 ? 20 : l32 - 16);
  const unsigned vb = lds0 + AT_V + j * 1344 + hh * 336 + vrow * 16;
#define AT_EXP(sv) do { _Pragma("unroll") for (int r = 0; r < 16; ++r) sv[r] = __builtin_amdgcn_exp2f(sv[r] - m); } while (0)
#define AT_PV(sv, t) do { oacc = MFMA32(lds_read16(vb + (t) * 1344), pack8(sv, 0), oacc); oacc = MFMA32(lds_read16(vb + (t) * 1344 + 672), pack8(sv, 8), oacc); } while (0)
  f32x16 oacc;
  bool exact = !FULL;
  if (FULL) {
    f32x16 sm, sa, sb;
    AT_QK(sm, 8, zero16); AT_QK(sa, 0, zero16);
    const int tq = l32 - 4 * hh;
    const unsigned wm = tq < 0 ? 0xffffffffu : (0xfffffffeu << tq);
    unsigned msk[16];
#pragma unroll
    for (int r = 0; r < 16; ++r) {
      msk[r] = (unsigned)__builtin_amdgcn_sbfe((int)wm, (r & 3) + 8 * (r >> 2), 1);
      sm[r] = __uint_as_float((msk[r] & __float_as_uint(sa[r])) | (~msk[r] & __float_as_uint(sm[r])));
    }
    const float m = xhalf_max(max16(sm, ninf));
    const float nm = -m;
    const f32x16 cneg = {nm, nm, nm, nm, nm, nm, nm, nm, nm, nm, nm, nm, nm, nm, nm, nm};
    AT_QK(sa, 1, cneg);
    AT_EXP(sm);
    u32x4 pl = __builtin_bit_cast(u32x4, pack8(sm, 0)), ph = __builtin_bit_cast(u32x4, pack8(sm, 8)), ql, qh;
#pragma unroll
    for (int i = 0; i < 4; ++i) {
      ql[i] = pl[i] & ((msk[2 * i] & 0xffffu) | (msk[2 * i + 1] & 0xffff0000u)); pl[i] ^= ql[i];
      qh[i] = ph[i] & ((msk[8 + 2 * i] & 0xffffu) | (msk[9 + 2 * i] & 0xffff0000u)); ph[i] ^= qh[i];
    }
    spin_mk(ct + CT_VR8, mk.x);
    STAMP(9);
    oacc = MFMA32(lds_read16(vb + 8 * 1344), __builtin_bit_cast(s16x8, pl), zero16);
    oacc = MFMA32(lds_read16(vb + 8 * 1344 + 672), __builtin_bit_cast(s16x8, ph), oacc);
    oacc = MFMA32(lds_read16(vb), __builtin_bit_cast(s16x8, ql), oacc);
    oacc = MFMA32(lds_read16(vb + 672), __builtin_bit_cast(s16x8, qh), oacc);
#define AT_EX0(sv) do { _Pragma("unroll") for (int r = 0; r < 16; ++r) sv[r] = __builtin_amdgcn_exp2f(sv[r]); } while (0)
    AT_QK(sb, 2, cneg); AT_EX0(sa); AT_PV(sa, 1);
    AT_QK(sa, 3, cneg); AT_EX0(sb); AT_PV(sb, 2);
    AT_QK(sb, 4, cneg); AT_EX0(sa); AT_PV(sa, 3);
    AT_QK(sa, 5, cneg); AT_EX0(sb); AT_PV(sb, 4);
    AT_QK(sb, 6, cneg); AT_EX0(sa); AT_PV(sa, 5);
    AT_QK(sa, 7, cneg); AT_EX0(sb); AT_PV(sb, 6);
    AT_EX0(sa); AT_PV(sa, 7);
    float big = max3f(__builtin_fabsf(oacc[0]), __builtin_fabsf(oacc[1]), __builtin_fabsf(oacc[2]));
    big = max3f(big, __builtin_fabsf(oacc[3]), __builtin_fabsf(oacc[4]));
    big = max3f(big, __builtin_fabsf(oacc[5]), __builtin_fabsf(oacc[6]));
    big = max3f(big, __builtin_fabsf(oacc[7]), __builtin_fabsf(oacc[8]));
    big = max3f(big, __builtin_fabsf(oacc[9]), __builtin_fabsf(oacc[10]));
    big = __builtin_fmaxf(big, __builtin_fabsf(oacc[11]));
    const float ls = hh ? oacc[8] : oacc[12];
    float osum = 0.f;
#pragma unroll
    for (int r = 0; r < 8; ++r) osum += oacc[r];
    const bool ok = (ls < AT_LMAX) && (big < 1e37f) && (osum == osum);
    exact = __builtin_amdgcn_ballot_w64(!ok) != 0ull;
  }
  if (exact) {
    asm volatile("" : "+v"(qlo), "+v"(qhi8) :: "memory");
    f32x16 sm, s1, s2, s3;
    {
      f32x16 s0;
      AT_QK(sm, 8, zero16); AT_QK(s0, 0, zero16);
      AT_QK(s1, 1, zero16); AT_QK(s2, 2, zero16); AT_QK(s3, 3, zero16);
#pragma unroll
      for (int r = 0; r < 16; ++r) {
        const int kk = (r & 3) + 8 * (r >> 2) + 4 * hh;
        sm[r] = kk > l32 ? s0[r] : sm[r];
      }
    }
    float m = max16(sm, ninf);
    m = max16(s1, m); m = max16(s2, m); m = max16(s3, m);
    m = xhalf_max(m);
    AT_EXP(sm);
    oacc = zero16;
    {
      f32x16 p0, p8;
#pragma unroll
      for (int r = 0; r < 16; ++r) {
        const int kk = (r & 3) + 8 * (r >> 2) + 4 * hh;
        p0[r] = kk > l32 ? sm[r] : 0.f;
        p8[r] = kk > l32 ? 0.f : sm[r];
      }
      AT_PV(p8, 8);
      if (FULL) AT_PV(p0, 0);
    }
    if (FULL || 1 >= tfirst) { AT_EXP(s1); AT_PV(s1, 1); }
    AT_QK(sm, 4, zero16); AT_QK(s1, 5, zero16);
    if (FULL || 2 >= tfirst) { AT_EXP(s2); AT_PV(s2, 2); }
    AT_QK(s2, 6, zero16);
    if (FULL || 3 >= tfirst) { AT_EXP(s3); AT_PV(s3, 3); }
    AT_QK(s3, 7, zero16);
    float m2 = max16(sm, m);
    m2 = max16(s1, m2); m2 = max16(s2, m2); m2 = max16(s3, m2);
    m2 = xhalf_max(m2);
    {
      const float alpha = __builtin_amdgcn_exp2f(m - m2);
#pragma unroll
      for (int r = 0; r < 13; ++r) oacc[r] *= alpha;
      m = m2;
    }
    if (FULL || 4 >= tfirst) { AT_EXP(sm); AT_PV(sm, 4); }
    if (FULL || 5 >= tfirst) { AT_EXP(s1); AT_PV(s1, 5); }
    if (FULL || 6 >= tfirst) { AT_EXP(s2); AT_PV(s2, 6); }
    if (FULL || 7 >= tfirst) { AT_EXP(s3); AT_PV(s3, 7); }
  }
  asm volatile("s_waitcnt lgkmcnt(0)" ::: "memory");
  sig(ct + CT_ATT + j * 4, 1u, lane);
  STAMP(10);
  const float l = hh ? oacc[8] : oacc[12];
  const float inv = __builtin_amdgcn_rcpf(l);
  char* oc = ws + WS_OW + ((size_t)qt * NH + h) * 1280;
  u32x4 o;
  o[0] = pk_bf16(oacc[0] * inv, oacc[1] * inv); o[1] = pk_bf16(oacc[2] * inv, oacc[3] * inv);
  o[2] = pk_bf16(oacc[4] * inv, oacc[5] * inv); o[3] = pk_bf16(oacc[6] * inv, oacc[7] * inv);
  gstore16c(oc + lane * 16, o);
  if (!hh) {
    u32x2 o2;
    o2[0] = pk_bf16(oacc[8] * inv, oacc[9] * inv); o2[1] = pk_bf16(oacc[10] * inv, oacc[11] * inv);
    gstore8c(oc + 1024 + l32 * 8, o2);
  }
  publish(ws, FL_O + (size_t)(qt * NH + h) * 8, E, lane);
}

#define HDELAY 0
#define APRIO 0
#define LPRIO 0
#define MPRIO 2
#define ROTG 2
#define A1_DELAY 8
__global__ __launch_bounds__(768, 3) void mega(const u16* __restrict__ x, const u16* __restrict__ wqkv,
                                               const u16* __restrict__ ak, const u16* __restrict__ av,
                                               const u16* __restrict__ shk, const u16* __restrict__ wo,
                                               const u16* __restrict__ ssmk, const u16* __restrict__ ssmd,
                                               const u16* __restrict__ aq, char* __restrict__ ws,
                                               u16* __restrict__ out) {
  extern __shared__ __attribute__((aligned(16))) char smem[];
  const unsigned lds0 = lds_addr(smem);
  const int tid = threadIdx.x, wave = __builtin_amdgcn_readfirstlane(tid >> 6);
  int lane = tid & 63;
#define RELANE() asm volatile("" : "+v"(lane))
  const int role = wave >> 2, w = wave & 3;
  const int bid = blockIdx.x, xcd = bid & 7, idx = bid >> 3;
  const bool has_gemm = idx < 30;
  const unsigned ct = lds0 + L_CT;
  if (!has_gemm && role == 2) {
    u64 pc;
    asm volatile("s_getpc_b64 %0" : "=s"(pc));
#pragma unroll
    for (int i = 0; i < CW_N; ++i) {
      unsigned keep;
      asm volatile("s_mov_b32 %0, m0\n\ts_mov_b32 m0, %3\n\ts_nop 0\n\tglobal_load_lds_dword %1, %2\n\ts_mov_b32 m0, %0"
                   : "=&s"(keep) : "v"((unsigned)min((((i * 64 + lane) * 8) + (idx - 30) * 4 + w) * CW_STRIDE, CODE_WARM)), "s"(pc & ~127ull), "s"(ct + 768) : "memory");
    }
  }
  DIAG_INIT();
  u64 E;
  Mk mk; mk.src = wqkv;
  asm volatile("s_load_dwordx2 %0, %2, 0x0\n\ts_load_dword %1, %3, 0x0" : "=&s"(E), "=&s"(mk.x) : "s"(ws + WS_FL), "s"(wqkv) : "memory");
  const int gt = (xcd & 1) * 30 + idx;
  const int tm = (xcd >> 1) * 4 + (gt & 3), tn = gt >> 2;
  const int m0 = tm * 128, n0 = tn * 128;
  const int rot = ROTG >= 10 ? (((gt & 3) * 5 + 1) / 2 + 3 * tn) % 10 : ((((gt & 3) + tn) % ROTG) * 10) / ROTG;
  const unsigned xo0 = (unsigned)((lane >> 3) * DM * 2 + (((lane & 7) ^ (lane >> 4)) * 16));
  const unsigned xo1 = (unsigned)((lane >> 3) * DM * 2 + (((lane & 7) ^ (4 + (lane >> 4))) * 16));
  const unsigned wof = (unsigned)((lane >> 4) * NQKV * 2 + (((lane & 15) ^ ((lane >> 4) << 2)) * 16));
  const char* xg = (const char*)(x + (size_t)m0 * DM);
  const char* wg = (const char*)(wqkv + n0);
#define K1_PIECE_X(t, kt, j) glds16p(((j) & 1) ? xo1 : xo0, xg + (size_t)(j) * 8 * DM * 2 + (kt) * 128, lds0 + ((t) & 3) * K1_STAGE + (j) * 1024)
#define K1_PIECE_W(t, kt, j) glds16p(wof, wg + (size_t)((kt) * 64 + (j) * 4) * NQKV * 2, lds0 + ((t) & 3) * K1_STAGE + 16384 + (j) * 1024)
  if (has_gemm && role == 0) {
#pragma unroll
    for (int i = 0; i < 4; ++i) { K1_PIECE_X(0, rot, w * 4 + i); K1_PIECE_W(0, rot, w * 4 + i); }
  }
  const unsigned pf = ct + CT_PAIR + (wave >> 1) * 8;
  const int wh = wave & 1;
  unsigned ps = 0;
  const int fb = (idx - 30) * 8 + xcd;
  const int a1t = has_gemm ? (xcd * 30 + idx) * 2 + (w >> 1) : 480 + fb * 10 + (wave >> 1);
  const unsigned a1l = has_gemm ? lds0 + L_AUX + (w >> 1) * A1_PAIR : lds0 + (wave >> 1) * A1_PAIR;
  if (!has_gemm || role == 2) a1_issue(a1l, a1t, lane, wh, ak, av, shk);
  const bool cv0 = role != 0 || !has_gemm || bid < 112;
  const int ch0 = role == 0 ? (has_gemm ? 528 + bid : 512 + fb) : bid * 2 + (role == 1 ? 1 : 0);
  asm volatile("s_waitcnt lgkmcnt(0)" : "+s"(E), "+s"(mk.x) :: "memory");
  if (role == 1) lds_st32(ct + (tid - 256) * 4, (tid - 256) * 4 >= CT_FX ? ~mk.x : 0u);
  bar();
  E += 1ull;

  const bool ex = has_gemm && role == 1 && bid < 112;
  HRegs hr, h2;
  if (!(has_gemm && role == 0)) {
    hr = conv_load_h(ch0, w * 64 + lane, ssmk, ssmd);
    if (ex) h2 = conv_load_h(528 + bid, w * 64 + lane, ssmk, ssmd);
  }
  auto q_publish = [&]() {
    asm volatile("s_waitcnt vmcnt(0)" ::: "memory");
    if (lane == 0) {
      const unsigned old = __hip_atomic_fetch_add((LAS unsigned*)(uintptr_t)(ct + CT_QCNT), 1u, __ATOMIC_RELAXED, __HIP_MEMORY_SCOPE_WORKGROUP);
      if (old == 3u) __hip_atomic_store((u64*)(ws + FL_Q + (size_t)(tm * 16 + tn) * 8), E, __ATOMIC_RELAXED, __HIP_MEMORY_SCOPE_AGENT);
    }
    STAMP(4);
  };
  if (!has_gemm) {
    const unsigned a2l = lds0 + (6 + (wave >> 1)) * A1_PAIR;
    if (wave < 8) a1_issue(a2l, a1t + 6, lane, wh, ak, av, shk);
    if (wave < 8) wait_vm<6>(); else wait_vm<0>();
    a1_finish(a1l, pf, ps, a1t, lane, wh, ws, E);
    if (wave < 8) { wait_vm<2>(); a1_finish(a2l, pf, ps, a1t + 6, lane, wh, ws, E); }
    a1_publish(a1t, lane, wh, ws, E);
    if (wave < 8) a1_publish(a1t + 6, lane, wh, ws, E);
    bar();
    STAMP(1);
  } else if (role == 0) {
    wait_vm<0>();
    sig(ct + CT_S0 + w * 4, 1u, lane);
    spin_ge4(ct + CT_S0, 1u);
    __builtin_amdgcn_s_setprio(MPRIO);
    if (tn >= 10) k1_gemm<true>(lds0, m0, n0, tid, ws, mk.x, dg);
    else k1_gemm<false>(lds0, m0, n0, tid, ws, mk.x, dg);
    STAMP(3);
  } else if (role == 1) {
    __builtin_amdgcn_s_setprio(LPRIO);
#define K1_ST(t) do { const int kt_ = (t) + rot >= 10 ? (t) + rot - 10 : (t) + rot; \
      if ((t) >= 4) spin_ge4(ct + CT_FREE + ((t) & 3) * 16, (unsigned)((t) - 3)); \
      _Pragma("unroll") for (int i_ = 0; i_ < 4; ++i_) { K1_PIECE_X(t, kt_, w * 4 + i_); K1_PIECE_W(t, kt_, w * 4 + i_); } } while (0)
#pragma nounroll
    for (int t = 1; t < 10; ++t) {
      K1_ST(t); glds_mark(mk, ct + CT_FX + t * 16 + w * 4, lane);
    }
    __builtin_amdgcn_s_setprio(0);
    STAMP(1);
  } else {
    __builtin_amdgcn_s_setprio(2);
    wait_vm<0>();
    a1_finish(a1l, pf, ps, a1t, lane, wh, ws, E);
    a1_publish(a1t, lane, wh, ws, E);
    __builtin_amdgcn_s_setprio(0);
    STAMP(1);
  }

  RELANE();
  unsigned qs = 0;
  const unsigned qf = ct + CT_QUAD + role * 16;
  const unsigned creg = lds0 + (role == 2 ? L_AUX : (role == 1 ? 29184 : 0));
  if (role == 2) __builtin_amdgcn_s_setprio(APRIO); else __builtin_amdgcn_s_setprio(0);
  if (has_gemm && role == 0) {
    q_publish();
    if (cv0) {
      hr.d = ssmd[ch0];
      conv_run(creg, qf, qs, ch0, lane, w, hr, ws, ct + CT_XST, dg);
    }
  } else {
    if (has_gemm && role == 1) {
      conv_load_u(hr, ch0, lane, w, ws, E);
      if (ex) conv_load_u(h2, 528 + bid, lane, w, ws, E);
      spin_ge4(ct + CT_FREE + 16, 10u);
      conv_stage(creg, lane, w, hr);
      if (ex) {
        conv_stage(lds0, lane, w, h2);
        lds_write16(lds0 + (w * 64 + lane) * 16, h2.u);
        asm volatile("s_waitcnt lgkmcnt(0)" ::: "memory");
        sig(ct + CT_XST + w * 4, 1u, lane);
      }
    } else {
      if (has_gemm) { const int wq = w; QSYNC(); }
      conv_stage(creg, lane, w, hr);
      conv_load_u(hr, ch0, lane, w, ws, E);
    }
    STAMP(5);
    conv_run(creg, qf, qs, ch0, lane, w, hr, ws, 0u, dg);
  }
  sig(ct + CT_CVD + wave * 4, 1u, lane);
  if (role != 0) STAMP(2);

  const int tm3 = xcd * 4 + (idx >> 3), tn3 = idx & 7;
  const int m3 = tm3 * 64, n3 = tn3 * 80;
  RELANE();

  if (role == 2) {
    const int r16 = lane & 15, g = lane >> 4, q = r16 >> 2, p = lane & 3;
    __builtin_amdgcn_s_setprio(3);
    spin_ge4(ct + CT_CVD + 32, 1u);
    if (has_gemm) spin_ge4(ct + CT_FREE + 16, 10u);
    attn_stage(lds0, xcd * 4 + (idx & 3), idx >> 2, w, lane, ws, E, ch0, mk, dg);
    {
      P3_WADDR();
      P3_STW(5);
      spin_ge8(ct + CT_CVD, 1u);
      P3_STW(0); P3_STW(4); P3_STW(1); P3_STW(2); P3_STW(3);
      glds_mark(mk, ct + CT_WRDY + w * 4, lane);
      STAMP(3);
    }
    __builtin_amdgcn_s_setprio(0);
    u32x4 qv[3];
#pragma unroll
    for (int j = 0; j < 3; ++j) {
      const int dc = j < 2 ? j * 4 + g : 8 + (g & 1);
      qv[j] = *((const u32x4*)(aq + (size_t)(m3 + w * 16 + r16) * DM + n3 + dc * 8));
    }
    {
      const int l5 = lane & 31;
      const int cl = l5 < 8 ? w * 8 + l5 : (l5 < 16 ? 32 + w * 8 + (l5 - 8) : 64 + w * 4 + (l5 & 3));
      poll(ws, FL_Z + (size_t)(n3 + cl) * 8, E);
    }
    {
      const char* zg = ws + WS_ZT + ((size_t)n3 * SEQ + m3) * 2;
      const unsigned zo = (unsigned)((lane >> 3) * SEQ * 2 + (lane & 7) * 16);
      glds16c(zo, zg + (size_t)(w * 8) * SEQ * 2, lds0 + L_AUX + w * 1024);
      glds16c(zo, zg + (size_t)(32 + w * 8) * SEQ * 2, lds0 + L_AUX + 4096 + w * 1024);
      if (lane < 32) glds16c(zo, zg + (size_t)(64 + w * 4) * SEQ * 2, lds0 + L_AUX + 8192 + w * 512);
    }
    wait_vm<0>();
    sig(ct + CT_ZRDY + w * 4, 1u, lane);
    spin_ge4(ct + CT_ZRDY, 1u);
    STAMP(4);
    u32x4 zz[3];
#pragma unroll
    for (int j = 0; j < 3; ++j) {
      const int dc = j < 2 ? j * 4 + g : 8 + (g & 1);
      const unsigned za = lds0 + L_AUX + (dc * 8 + q) * 128 + w * 32 + p * 8;
      zz[j] = __builtin_bit_cast(u32x4, cat8(tr_read(za), tr_read(za + 512)));
    }
#pragma unroll
    for (int j = 0; j < 3; ++j) {
      const int dc = j < 2 ? j * 4 + g : 8 + (g & 1);
      u32x4 o;
#pragma unroll
      for (int i = 0; i < 4; ++i) o[i] = pk_bf16(bf_lo(qv[j][i]) * bf_lo(zz[j][i]), bf_hi(qv[j][i]) * bf_hi(zz[j][i]));
      if (j < 2 || g < 2) ostore16(out + (size_t)(m3 + w * 16 + r16) * (2 * DM) + n3 + dc * 8, o);
    }
    STAMP(7);
    if (bid == 0 && w == 0) {
#pragma unroll
      for (int i = 0; i < 4; ++i) poll(ws, FL_O + (size_t)(((i * 2 + (lane >> 5)) * 8) * NH + (lane & 31)) * 8, E);
      if (lane == 0) __hip_atomic_store((u64*)(ws + WS_FL), E, __ATOMIC_RELAXED, __HIP_MEMORY_SCOPE_AGENT);
    }
    DIAG_DUMP(2);
    return;
  }

  const int h = xcd * 4 + (idx & 3), qb8 = idx >> 2;
  __builtin_amdgcn_s_setprio(1);
  RELANE();
  if (qb8 > 0) attn_task<true>(lds0, h, qb8, wave, lane, ws, E, cv0 ? ch0 : -1, mk, dg);
  else attn_task<false>(lds0, h, qb8, wave, lane, ws, E, cv0 ? ch0 : -1, mk, dg);
  STAMP(11);

  RELANE();
  const int r16 = lane & 15, g = lane >> 4, q = r16 >> 2, p = lane & 3;
  f32x4 acc[5];
#pragma unroll
  for (int i = 0; i < 5; ++i) acc[i] = (f32x4){0.f, 0.f, 0.f, 0.f};
  const unsigned wbp = lds0 + (20 * (g >> 1) + 4 * (g & 1) + q) * 160 + p * 8;
  const unsigned wbs = lds0 + (20 * g + 16 + q) * 160 + p * 8;
  u32x4 op[8]; u32x2 osg[4];
  if (role == 1) {
    P3_WADDR();
    spin_ge8(ct + CT_ATT, 1u);
    P3_STW(6); P3_STW(7);
    glds_mark(mk, ct + CT_WRD2 + w * 4, lane);
  }
  spin_mk(ct + CT_WRDY, mk.x);
  const unsigned w0 = role ? p3_w(4) : p3_w(0), w1 = role ? p3_w(5) : p3_w(1), w2 = role ? p3_w(6) : p3_w(2), w3 = role ? p3_w(7) : p3_w(3);
  struct Fw { s16x8 a[5], b[5]; s16x4 s[5]; };
  Fw fwa, fwb;
#define P3_LOADW(F, bo_) do { _Pragma("unroll") for (int nt = 0; nt < 5; ++nt) { \
      F.a[nt] = cat8(tr_read(wbp + (bo_) + nt * 32), tr_read(wbp + (bo_) + 8 * 160 + nt * 32)); \
      F.b[nt] = cat8(tr_read(wbp + (bo_) + 40 * 160 + nt * 32), tr_read(wbp + (bo_) + 48 * 160 + nt * 32)); \
      F.s[nt] = tr_read(wbs + (bo_) + nt * 32); } } while (0)
#define P3_MM(F, t) do { \
    _Pragma("unroll") for (int nt = 0; nt < 5; ++nt) acc[nt] = MFMA16(F.a[nt], __builtin_bit_cast(s16x8, op[2 * (t)]), acc[nt]); \
    _Pragma("unroll") for (int nt = 0; nt < 5; ++nt) acc[nt] = MFMA16(F.b[nt], __builtin_bit_cast(s16x8, op[2 * (t) + 1]), acc[nt]); } while (0)
#define P3_MS(t) do { const s16x8 os_ = cat8(__builtin_bit_cast(s16x4, osg[t]), __builtin_bit_cast(s16x4, osg[(t) + 1])); \
    _Pragma("unroll") for (int nt = 0; nt < 5; ++nt) acc[nt] = MFMA16(cat8(fwa.s[nt], fwb.s[nt]), os_, acc[nt]); } while (0)
  P3_LOADW(fwa, w0);
  SBAR();
  poll(ws, FL_O + (size_t)(((m3 >> 5) + (w >> 1)) * NH + role * 16 + r16) * 8, E);
  STAMP(12);
  {
    const int orow = (w & 1) * 16 + r16;
    const char* ob = ws + WS_OW + ((size_t)((m3 >> 5) + (w >> 1)) * NH + role * 16) * 1280;
    const char* pp = ob + (g >> 1) * 1280 + (g & 1) * 512 + orow * 16;
    const char* sp = ob + g * 1280 + 1024 + orow * 8;
#pragma unroll
    for (int t = 0; t < 4; ++t) {
      asm volatile("global_load_dwordx4 %0, %3, off sc1\n\tglobal_load_dwordx4 %1, %3, off offset:2560 sc1\n\tglobal_load_dwordx2 %2, %4, off sc1"
                   : "=&v"(op[2 * t]), "=&v"(op[2 * t + 1]), "=&v"(osg[t]) : "v"(pp + t * 5120), "v"(sp + t * 5120) : "memory");
    }
  }
  P3_LOADW(fwb, w1);
  if (role == 1) spin_mk(ct + CT_WRD2, mk.x);
  SBAR();
  asm volatile("s_waitcnt vmcnt(6)" : "+v"(op[0]), "+v"(op[1]), "+v"(op[2]), "+v"(op[3]), "+v"(osg[0]), "+v"(osg[1]) :: "memory");
  STAMP(13);
  P3_MS(0); P3_MM(fwa, 0);
  SBAR();
  P3_LOADW(fwa, w2); P3_MM(fwb, 1);
#pragma unroll
  for (int i = 0; i < 10; ++i) { __builtin_amdgcn_sched_group_barrier(0x008, 1, 0); __builtin_amdgcn_sched_group_barrier(0x100, 3, 0); }
  SBAR();
  asm volatile("s_waitcnt vmcnt(0)" : "+v"(op[4]), "+v"(op[5]), "+v"(op[6]), "+v"(op[7]), "+v"(osg[2]), "+v"(osg[3]) :: "memory");
  P3_LOADW(fwb, w3); P3_MM(fwa, 2);
#pragma unroll
  for (int i = 0; i < 10; ++i) { __builtin_amdgcn_sched_group_barrier(0x008, 1, 0); __builtin_amdgcn_sched_group_barrier(0x100, 3, 0); }
  SBAR();
  P3_MM(fwb, 3); P3_MS(2);
  SBAR();
  const unsigned rb = lds0 + AT_K + 25600 + w * 5120 + lane * 16;
  if (role == 1) {
#pragma unroll
    for (int nt = 0; nt < 5; ++nt) lds_write16(rb + nt * 1024, __builtin_bit_cast(u32x4, acc[nt]));
    asm volatile("s_waitcnt lgkmcnt(0)" ::: "memory");
    sig(ct + CT_RED + w * 4, 1u, lane);
    STAMP(14);
    DIAG_DUMP(1);
    return;
  }
  spin_ge(ct + CT_RED + w * 4, 1u);
  u16* orow_g = out + (size_t)(m3 + w * 16 + r16) * (2 * DM) + DM + n3;
  u32x2 ov[5];
#pragma unroll
  for (int nt = 0; nt < 5; ++nt) {
    const f32x4 o2 = acc[nt] + __builtin_bit_cast(f32x4, lds_read16(rb + nt * 1024));
    ov[nt][0] = pk_bf16(o2[0], o2[1]);
    ov[nt][1] = pk_bf16(o2[2], o2[3]);
  }
#pragma unroll
  for (int pr = 0; pr < 2; ++pr) {
    const auto sx = __builtin_amdgcn_permlane16_swap(ov[2 * pr][0], ov[2 * pr + 1][0], false, false);
    const auto sy = __builtin_amdgcn_permlane16_swap(ov[2 * pr][1], ov[2 * pr + 1][1], false, false);
    ostore16(orow_g + (2 * pr + (g & 1)) * 16 + 8 * (g >> 1), (u32x4){sx[0], sy[0], sx[1], sy[1]});
  }
  ostore8(orow_g + 64 + 4 * g, ov[4]);
  STAMP(14);
  DIAG_DUMP(0);
}

extern "C" void kernel_launch(void* const* d_in, const int* in_sizes, int n_in, void* d_out, int out_size,
                              void* d_ws, size_t ws_size, hipStream_t stream) {
  const u16* aq = (const u16*)d_in[0];
  const u16* ak = (const u16*)d_in[1];
  const u16* av = (const u16*)d_in[2];
  const u16* shk = (const u16*)d_in[3];
  const u16* ssmk = (const u16*)d_in[4];
  const u16* ssmd = (const u16*)d_in[5];
  const u16* x = (const u16*)d_in[6];
  const u16* wqkv = (const u16*)d_in[7];
  const u16* wo = (const u16*)d_in[8];
  (void)hipFuncSetAttribute((const void*)mega, hipFuncAttributeMaxDynamicSharedMemorySize, L_TOTAL);
  mega<<<256, 768, L_TOTAL, stream>>>(x, wqkv, ak, av, shk, wo, ssmk, ssmd, aq, (char*)d_ws, (u16*)d_out);
}
```

```cpp
#include <hip/hip_runtime.h>
#include <hip/hip_bf16.h>
#include <stdint.h>

typedef unsigned short u16;
typedef short s16x4 __attribute__((ext_vector_type(4)));
typedef short s16x8 __attribute__((ext_vector_type(8)));
typedef float f32x2 __attribute__((ext_vector_type(2)));
typedef float f32x4 __attribute__((ext_vector_type(4)));
typedef float f32x16 __attribute__((ext_vector_type(16)));
typedef unsigned u32x2 __attribute__((ext_vector_type(2)));
typedef unsigned u32x4 __attribute__((ext_vector_type(4)));
typedef __bf16 bf16x2 __attribute__((ext_vector_type(2)));
#define LAS __attribute__((address_space(3)))
#define DI __device__ __forceinline__

constexpr int SEQ = 2048, DM = 640, NQKV = 1920, NH = 32;
constexpr size_t WS_QF = 0;
constexpr size_t WS_KF = WS_QF + (size_t)DM * SEQ * 2;
constexpr size_t WS_VF = WS_KF + (size_t)DM * SEQ * 2;
constexpr size_t WS_UT = WS_VF + (size_t)DM * SEQ * 2;
constexpr size_t WS_ZT = WS_UT + (size_t)DM * SEQ * 2;
constexpr size_t WS_OW = WS_ZT + (size_t)DM * SEQ * 2;
typedef unsigned long long u64;
constexpr size_t WS_FL = WS_OW + (size_t)DM * SEQ * 2;
constexpr size_t FL_U = WS_FL + 256;
constexpr size_t FL_Q = FL_U + 20 * 64 * 8;
constexpr size_t FL_Z = FL_Q + 16 * 16 * 8;
constexpr size_t FL_O = FL_Z + 640 * 8;

DI unsigned pk_bf16(float lo, float hi) {
  f32x2 f = {lo, hi};
  return __builtin_bit_cast(unsigned, __builtin_convertvector(f, bf16x2));
}
DI float bf_lo(unsigned u) { return __uint_as_float(u << 16); }
DI float bf_hi(unsigned u) { return __uint_as_float(u & 0xffff0000u); }
DI float rbf(float x) { return bf_lo(pk_bf16(x, 0.f)); }

DI unsigned lds_addr(const void* p) { return (unsigned)(uintptr_t)(LAS const char*)p; }

DI void glds16(unsigned voff, const void* sbase, unsigned lds_dst) {
  unsigned keep;
  asm volatile("s_mov_b32 %0, m0\n\ts_mov_b32 m0, %3\n\ts_nop 0\n\tglobal_load_lds_dwordx4 %1, %2\n\ts_mov_b32 m0, %0"
               : "=&s"(keep) : "v"(voff), "s"(sbase), "s"(lds_dst) : "memory");
}
DI void glds16f(unsigned voff, const void* sbase, unsigned lds_dst) {
  asm volatile("s_mov_b32 m0, %2\n\ts_nop 0\n\tglobal_load_lds_dwordx4 %0, %1" :: "v"(voff), "s"(sbase), "s"(lds_dst) : "memory");
}
#define glds16p glds16
DI void glds16nt(unsigned voff, const void* sbase, unsigned lds_dst) {
  unsigned keep;
  asm volatile("s_mov_b32 %0, m0\n\ts_mov_b32 m0, %3\n\ts_nop 0\n\tglobal_load_lds_dwordx4 %1, %2\n\ts_mov_b32 m0, %0"
               : "=&s"(keep) : "v"(voff), "s"(sbase), "s"(lds_dst) : "memory");
}
#define LDPOL "sc1"
DI void glds16c(unsigned voff, const void* sbase, unsigned lds_dst) {
  unsigned keep;
  asm volatile("s_mov_b32 %0, m0\n\ts_mov_b32 m0, %3\n\ts_nop 0\n\tglobal_load_lds_dwordx4 %1, %2 " LDPOL "\n\ts_mov_b32 m0, %0"
               : "=&s"(keep) : "v"(voff), "s"(sbase), "s"(lds_dst) : "memory");
}
struct Mk { const void* src; unsigned x; };
DI void glds_mark(const Mk& mk, unsigned lds_dst, int lane) {
  if (lane == 0) {
    unsigned keep;
    asm volatile("s_mov_b32 %0, m0\n\ts_mov_b32 m0, %3\n\ts_nop 0\n\tglobal_load_lds_dword %1, %2\n\ts_mov_b32 m0, %0"
                 : "=&s"(keep) : "v"(0u), "s"(mk.src), "s"(lds_dst) : "memory");
  }
}
DI float dot2bf(unsigned a, unsigned b, float c) {
  return __builtin_amdgcn_fdot2_f32_bf16(__builtin_bit_cast(bf16x2, a), __builtin_bit_cast(bf16x2, b), c, false);
}
DI void gstore16c(void* p, u32x4 v) {
  asm volatile("global_store_dwordx4 %0, %1, off sc1\n\ts_nop 1" :: "v"(p), "v"(v) : "memory");
}
DI void gstore8c(void* p, u32x2 v) {
  asm volatile("global_store_dwordx2 %0, %1, off sc1" :: "v"(p), "v"(v) : "memory");
}
#define OUTPOL "nt"
DI void ostore16(void* p, u32x4 v) { asm volatile("global_store_dwordx4 %0, %1, off " OUTPOL "\n\ts_nop 1" :: "v"(p), "v"(v) : "memory"); }
DI void ostore8(void* p, u32x2 v) { asm volatile("global_store_dwordx2 %0, %1, off " OUTPOL :: "v"(p), "v"(v) : "memory"); }
template <int N> DI void wait_vm() { asm volatile("s_waitcnt vmcnt(%0)" :: "n"(N) : "memory"); }
DI void bar() { asm volatile("s_waitcnt lgkmcnt(0)\n\ts_barrier" ::: "memory"); }
DI s16x4 tr_read(unsigned a) {
  return __builtin_amdgcn_ds_read_tr16_b64_v4i16((LAS s16x4*)(uintptr_t)a);
}
DI s16x8 lds_read16(unsigned a) { return *(LAS const s16x8*)(uintptr_t)a; }
DI s16x4 lds_read8(unsigned a) { return *(LAS const s16x4*)(uintptr_t)a; }
DI void lds_write16(unsigned a, u32x4 v) { *(LAS u32x4*)(uintptr_t)a = v; }
DI s16x8 cat8(s16x4 a, s16x4 b) { return __builtin_shufflevector(a, b, 0, 1, 2, 3, 4, 5, 6, 7); }
#define MFMA16(a, b, c) __builtin_amdgcn_mfma_f32_16x16x32_bf16((a), (b), (c), 0, 0, 0)
#define MFMA32(a, b, c) __builtin_amdgcn_mfma_f32_32x32x16_bf16((a), (b), (c), 0, 0, 0)

struct Diag {};
#define STAMP(i) do {} while (0)
#define DIAG_INIT() Diag dg
#define DIAG_DUMP(kid) do {} while (0)

#define SBAR() __builtin_amdgcn_sched_barrier(0)
#define SPIN_SLEEP 1
#define SPIN_MAX (1 << 21)
DI unsigned lds_ld32(unsigned a) { return *(volatile LAS unsigned*)(uintptr_t)a; }
DI void lds_st32(unsigned a, unsigned v) { *(volatile LAS unsigned*)(uintptr_t)a = v; }
DI void spin_ge(unsigned a, unsigned want) {
#pragma nounroll
  for (int it = 0; it < SPIN_MAX; ++it) {
    if ((unsigned)__builtin_amdgcn_readfirstlane(lds_ld32(a)) >= want) break;
    __builtin_amdgcn_s_sleep(1);
  }
  asm volatile("" ::: "memory");
}
DI void spin_ge4(unsigned a, unsigned want) {
#pragma nounroll
  for (int it = 0; it < SPIN_MAX; ++it) {
    const u32x4 v = *(volatile LAS u32x4*)(uintptr_t)a;
    const unsigned m = min(min(v[0], v[1]), min(v[2], v[3]));
    if ((unsigned)__builtin_amdgcn_readfirstlane(m) >= want) break;
    __builtin_amdgcn_s_sleep(SPIN_SLEEP);
  }
  asm volatile("" ::: "memory");
}
DI unsigned mk_bad(const u32x4& v, unsigned x) { return (v[0] ^ x) | (v[1] ^ x) | (v[2] ^ x) | (v[3] ^ x); }
DI void spin_mk(unsigned a, unsigned x) {
#pragma nounroll
  for (int it = 0; it < SPIN_MAX; ++it) {
    const u32x4 v = *(volatile LAS u32x4*)(uintptr_t)a;
    if ((unsigned)__builtin_amdgcn_readfirstlane(mk_bad(v, x)) == 0u) break;
    __builtin_amdgcn_s_sleep(SPIN_SLEEP);
  }
  asm volatile("" ::: "memory");
}
DI void spin_ge8(unsigned a, unsigned want) { spin_ge4(a, want); spin_ge4(a + 16, want); }
constexpr int CT_S0 = 0, CT_FREE = 80, CT_PAIR = 144  , CT_QUAD = 192  , CT_ZCNT = 240  ,
              CT_QCNT = 256, CT_ZRDY = 304, CT_CVD = 336  , CT_XST = 416, CT_ATT = 448, CT_RED = 480,
              CT_FX = 512  , CT_KR8 = 688, CT_VR8 = 704, CT_WRDY = 720, CT_WRD2 = 736, CT_BYTES = 1024;
DI void sig(unsigned a, unsigned v, int lane) { if (lane == 0) lds_st32(a, v); }

#define POLL_MAX (1 << 15)
DI void publish(char* ws, size_t off, u64 E, int lane) {
  asm volatile("s_waitcnt vmcnt(0)" ::: "memory");
  if (lane == 0) __hip_atomic_store((u64*)(ws + off), E, __ATOMIC_RELAXED, __HIP_MEMORY_SCOPE_AGENT);
}
#define POLL_SLEEP 6
DI void poll(const char* ws, size_t off, u64 E) {
#pragma nounroll
  for (int it = 0; it < POLL_MAX; ++it) {
    const u64 v = __hip_atomic_load((const u64*)(ws + off), __ATOMIC_RELAXED, __HIP_MEMORY_SCOPE_AGENT);
    if (__builtin_amdgcn_ballot_w64(v != E) == 0ull) break;
    __builtin_amdgcn_s_sleep(POLL_SLEEP);
  }
  asm volatile("" ::: "memory");
}

constexpr int K1_STAGE = 32768;
constexpr int A1_K = 0, A1_V = 3072, A1_W = 5120, A1_WAVE = 5632, A1_O = 2 * A1_WAVE, A1_PAIR = A1_O + 4096;
constexpr int L_AUX = 4 * K1_STAGE;
constexpr int L_CT = L_AUX + 2 * A1_PAIR;
constexpr int L_TOTAL = L_CT + CT_BYTES;
constexpr int K1_CT = L_CT;
constexpr int CV_COPY = 4160, CV_H = 4608, CV_REG = CV_H + 24576;
constexpr int AT_Q = L_AUX + 12288, AT_K = 64000, AT_V = AT_K + 16 * 2048, AT_END = AT_V + 16 * 1344;
constexpr int p3_w(int t) { return t < 5 ? t * 12800 : (t == 5 ? AT_END : AT_K + (t - 6) * 12800); }

#define PSYNC() do { asm volatile("s_waitcnt lgkmcnt(0)" ::: "memory"); ++ps; sig(pf + wh * 4, ps, lane); spin_ge(pf + (wh ^ 1) * 4, ps); } while (0)
DI void a1_issue(unsigned lds, int tile, int lane, int wh, const u16* ak, const u16* av, const u16* shk) {
  const int ts = tile / 10, td = tile - ts * 10;
  const int s0 = ts * 32 + wh * 16, d0 = td * 64;
  const int lr = lane >> 3, lc = (lane & 7) * 16;
  const unsigned wl = lds + wh * A1_WAVE;
#pragma unroll
  for (int i = 0; i < 3; ++i) {
    int r = s0 - 3 + i * 8 + lr; r = r < 0 ? 0 : r;
    if (i < 2 || lane < 24) glds16nt((unsigned)(r * DM * 2 + lc), (const char*)ak + d0 * 2, wl + A1_K + i * 1024);
  }
#pragma unroll
  for (int i = 0; i < 2; ++i)
    glds16nt((unsigned)(lr * DM * 2 + lc), (const char*)av + ((size_t)(s0 + i * 8) * DM + d0) * 2, wl + A1_V + i * 1024);
  if (lane < 32) glds16((unsigned)(lane * 16), (const char*)shk + d0 * 8, wl + A1_W);
}
DI void a1_finish(unsigned lds, unsigned pf, unsigned& ps, int tile, int lane, int wh, char* ws, u64 E) {
  const int ts = tile / 10, td = tile - ts * 10;
  const int c = lane & 7;
  const unsigned wl = lds + wh * A1_WAVE;
  u32x4 wt[4];
#pragma unroll
  for (int i = 0; i < 4; ++i) wt[i] = __builtin_bit_cast(u32x4, lds_read16(wl + A1_W + c * 64 + i * 16));
#pragma unroll
  for (int j = 0; j < 2; ++j) {
    const int r = j * 8 + (lane >> 3);
    const unsigned ka = wl + A1_K + (r + 3) * 128 + c * 16;
    u32x4 k0 = __builtin_bit_cast(u32x4, lds_read16(ka)), k1 = __builtin_bit_cast(u32x4, lds_read16(ka - 128));
    u32x4 k2 = __builtin_bit_cast(u32x4, lds_read16(ka - 256)), k3 = __builtin_bit_cast(u32x4, lds_read16(ka - 384));
    const u32x4 vv = __builtin_bit_cast(u32x4, lds_read16(wl + A1_V + r * 128 + c * 16));
    if (j == 0 && ts == 0 && wh == 0) {
      const u32x4 z = {0u, 0u, 0u, 0u};
      if (r < 1) k1 = z;
      if (r < 2) k2 = z;
      if (r < 3) k3 = z;
    }
    u32x4 o;
#pragma unroll
    for (int i = 0; i < 4; ++i) {
      const unsigned pa0 = __builtin_amdgcn_perm(k1[i], k0[i], 0x05040100u), pb0 = __builtin_amdgcn_perm(k3[i], k2[i], 0x05040100u);
      const unsigned pa1 = __builtin_amdgcn_perm(k1[i], k0[i], 0x07060302u), pb1 = __builtin_amdgcn_perm(k3[i], k2[i], 0x07060302u);
      const float e0 = dot2bf(pb0, wt[i][1], dot2bf(pa0, wt[i][0], 0.f));
      const float e1 = dot2bf(pb1, wt[i][3], dot2bf(pa1, wt[i][2], 0.f));
      o[i] = pk_bf16(e0 * bf_lo(vv[i]), e1 * bf_hi(vv[i]));
    }
    lds_write16(lds + A1_O + (31 - wh * 16 - r) * 128 + c * 16, o);
  }
  PSYNC();
  const int gg = lane >> 4, q = (lane & 15) >> 2, p = lane & 3;
  u16* ut = (u16*)(ws + WS_UT);
#pragma unroll
  for (int j = 0; j < 2; ++j) {
    const unsigned ra = lds + A1_O + (gg * 8 + q) * 128 + wh * 64 + j * 32 + p * 8;
    const s16x4 t0 = tr_read(ra), t1 = tr_read(ra + 512);
    const int d = td * 64 + wh * 32 + j * 16 + (lane & 15);
    gstore16c(ut + (size_t)d * SEQ + (SEQ - 32 - ts * 32) + gg * 8, __builtin_bit_cast(u32x4, cat8(t0, t1)));
  }
}
DI void a1_publish(int tile, int lane, int wh, char* ws, u64 E) {
  const int ts = tile / 10, td = tile - ts * 10;
  publish(ws, FL_U + (size_t)((td * 2 + wh) * 64 + ts) * 8, E, lane);
}

template <bool VT>
DI void k1_gemm(unsigned lds0, int m0, int n0, int tid, char* ws, unsigned mkx, Diag& dg) {
  const int lane = tid & 63, wave = __builtin_amdgcn_readfirstlane(tid >> 6);
  const int wm = wave >> 1, wn = wave & 1;
  const int l32 = lane & 31, hh = lane >> 5, gg = lane >> 4, q = (lane & 15) >> 2, p = lane & 3;
  const unsigned ct = lds0 + K1_CT;
  f32x16 acc[2][2];
#pragma unroll
  for (int i = 0; i < 2; ++i)
#pragma unroll
    for (int j = 0; j < 2; ++j)
#pragma unroll
      for (int r = 0; r < 16; ++r) acc[i][j][r] = 0.f;
  unsigned xb[4];
#pragma unroll
  for (int ks = 0; ks < 4; ++ks) xb[ks] = lds0 + (wm * 64 + l32) * 128 + (((unsigned)(2 * ks + hh) ^ (unsigned)((l32 >> 1) & 7)) * 16);
  unsigned wb[2];
#pragma unroll
  for (int nt = 0; nt < 2; ++nt)
    wb[nt] = lds0 + 16384 + (8 * hh + q) * 256 + (((unsigned)(wn * 4 + 2 * nt + (gg & 1)) ^ (unsigned)(q << 1)) * 32) + p * 8;
  s16x8 x0[2], w0[2], x1[2], w1[2], x2[2], w2[2], x3[2], w3[2];
#define K1_LOADF(XF, WF, bo_, ks) do { \
    _Pragma("unroll") for (int i_ = 0; i_ < 2; ++i_) { \
      WF[i_] = cat8(tr_read(wb[i_] + (bo_) + (ks) * 4096), tr_read(wb[i_] + (bo_) + (ks) * 4096 + 1024)); \
      XF[i_] = lds_read16(xb[ks] + (bo_) + i_ * 4096); } } while (0)
#define K1_MM(XF, WF) do { _Pragma("unroll") for (int mt = 0; mt < 2; ++mt) _Pragma("unroll") for (int nt = 0; nt < 2; ++nt) \
      acc[mt][nt] = VT ? MFMA32(XF[mt], WF[nt], acc[mt][nt]) : MFMA32(WF[nt], XF[mt], acc[mt][nt]); } while (0)
#define ILV_A 2
#define ILV_B 2
#define ILV_C 2
#define K1_ILV() do { \
      __builtin_amdgcn_sched_group_barrier(0x008, 1, 0); __builtin_amdgcn_sched_group_barrier(0x100, ILV_A, 0); \
      __builtin_amdgcn_sched_group_barrier(0x008, 1, 0); __builtin_amdgcn_sched_group_barrier(0x100, ILV_B, 0); \
      __builtin_amdgcn_sched_group_barrier(0x008, 1, 0); __builtin_amdgcn_sched_group_barrier(0x100, ILV_C, 0); \
      __builtin_amdgcn_sched_group_barrier(0x008, 1, 0); __builtin_amdgcn_sched_group_barrier(0x100, 2, 0); } while (0)
  STAMP(1);
  K1_LOADF(x0, w0, 0, 0);
  K1_LOADF(x1, w1, 0, 1);
#pragma nounroll
  for (int t = 0; t < 10; ++t) {
    const unsigned bo = (t & 3) * K1_STAGE, bn = ((t + 1) & 3) * K1_STAGE;
    SBAR();
    const u32x4 fl4 = *(volatile LAS u32x4*)(uintptr_t)(ct + CT_FX + (t + 1) * 16);
    K1_LOADF(x2, w2, bo, 2); K1_MM(x0, w0); K1_ILV();
    SBAR();
    K1_LOADF(x3, w3, bo, 3); K1_MM(x1, w1); K1_ILV();
    SBAR();
    const unsigned flu = (unsigned)__builtin_amdgcn_readfirstlane(mk_bad(fl4, mkx));
    if (t < 9 && flu != 0u) spin_mk(ct + CT_FX + (t + 1) * 16, mkx);
    asm volatile("" ::: "memory");
    SBAR();
    K1_LOADF(x0, w0, bn, 0); K1_MM(x2, w2); K1_ILV();
    SBAR();
    asm volatile("" : "+v"(x3[0]), "+v"(x3[1]), "+v"(w3[0]), "+v"(w3[1]) :: "memory");
    if (lane == 0) lds_st32(ct + CT_FREE + (t & 3) * 16 + wave * 4, (unsigned)(t + 1));
    SBAR();
    K1_LOADF(x1, w1, bn, 1); K1_MM(x3, w3); K1_ILV();
  }
  SBAR();
  STAMP(2);
  if (!VT) {
    const int part = n0 >= DM ? 1 : 0;
    const float sc = part == 0 ? 0.22360679774997896f * 1.4426950408889634f : 1.0f;
    char* base = ws + (part ? WS_KF : WS_QF) + ((size_t)((m0 + wm * 64) >> 5) * 40 + ((n0 - part * DM + wn * 64) >> 4)) * 1024 + lane * 16;
#pragma unroll
    for (int mt = 0; mt < 2; ++mt)
#pragma unroll
      for (int nt = 0; nt < 2; ++nt)
#pragma unroll
        for (int uh = 0; uh < 2; ++uh) {
          u32x4 o;
#pragma unroll
          for (int i = 0; i < 4; ++i) o[i] = pk_bf16(acc[mt][nt][8 * uh + 2 * i] * sc, acc[mt][nt][8 * uh + 2 * i + 1] * sc);
          gstore16c(base + (mt * 40 + nt * 2 + uh) * 1024, o);
        }
  } else {
    char* base = ws + WS_VF + ((size_t)((m0 + wm * 64) >> 4) * 20 + ((n0 - 2 * DM + wn * 64) >> 5)) * 1024 + lane * 16;
#pragma unroll
    for (int mt = 0; mt < 2; ++mt)
#pragma unroll
      for (int nt = 0; nt < 2; ++nt)
#pragma unroll
        for (int kg = 0; kg < 2; ++kg) {
          u32x4 o;
#pragma unroll
          for (int i = 0; i < 4; ++i) o[i] = pk_bf16(acc[mt][nt][8 * kg + 2 * i], acc[mt][nt][8 * kg + 2 * i + 1]);
          gstore16c(base + ((mt * 2 + kg) * 20 + nt) * 1024, o);
        }
  }
}

DI void cv_put(unsigned y, const u32x4& c, const u32x2& n) {
  const unsigned d0 = c[0], d1 = c[1], d2 = c[2], d3 = c[3], d4 = n[0], d5 = n[1];
  const unsigned a0 = __builtin_amdgcn_alignbit(d1, d0, 16), a1 = __builtin_amdgcn_alignbit(d2, d1, 16),
                 a2 = __builtin_amdgcn_alignbit(d3, d2, 16), a3 = __builtin_amdgcn_alignbit(d4, d3, 16),
                 a4 = __builtin_amdgcn_alignbit(d5, d4, 16);
  lds_write16(y, (u32x4){d0, d1, d2, d3});
  lds_write16(y + CV_COPY, (u32x4){a0, a1, a2, a3});
  lds_write16(y + 2 * CV_COPY, (u32x4){d1, d2, d3, d4});
  lds_write16(y + 3 * CV_COPY, (u32x4){a1, a2, a3, a4});
}

#define QSYNC() do { asm volatile("s_waitcnt lgkmcnt(0)" ::: "memory"); ++qs; sig(qf + wq * 4, qs, lane); spin_ge4(qf, qs); } while (0)
struct HRegs { u32x4 c, u; u32x2 n; unsigned d; };
DI HRegs conv_load_h(int ch, int t256, const u16* __restrict__ ssmk, const u16* __restrict__ ssmd) {
  HRegs r;
  const u16* hrow = ssmk + (size_t)ch * SEQ;
  r.c = *((const u32x4*)(hrow + t256 * 8));
  r.n = *(const u32x2*)(hrow + (t256 < 255 ? t256 * 8 + 8 : 0));
  r.d = ssmd[ch];
  return r;
}
DI void conv_load_u(HRegs& r, int ch, int lane, int wq, const char* __restrict__ ws, u64 E) {
  poll(ws, FL_U + (size_t)((ch >> 5) * 64 + 48 - 16 * wq + (lane & 15)) * 8, E);
  const u64* up = (const u64*)(ws + WS_UT + (size_t)ch * SEQ * 2 + wq * 1024 + lane * 16);
  const u64 a = __hip_atomic_load(up, __ATOMIC_RELAXED, __HIP_MEMORY_SCOPE_AGENT);
  const u64 b = __hip_atomic_load(up + 1, __ATOMIC_RELAXED, __HIP_MEMORY_SCOPE_AGENT);
  r.u = (u32x4){(unsigned)a, (unsigned)(a >> 32), (unsigned)b, (unsigned)(b >> 32)};
}
DI void conv_stage(unsigned reg, int lane, int wq, const HRegs& hr) {
  const int t256 = wq * 64 + lane;
  const unsigned hb = reg + CV_H;
  if (t256 < 32) lds_write16(reg + 4096 + t256 * 16, (u32x4){0u, 0u, 0u, 0u});
  cv_put(hb + (t256 + 2) * 16, hr.c, hr.n);
  if (t256 < 2) {
    const u32x4 z = {0u, 0u, 0u, 0u};
    u32x2 nx = {0u, 0u};
    const unsigned h0 = __builtin_amdgcn_readfirstlane(hr.c[0]), h1 = __builtin_amdgcn_readfirstlane(hr.c[1]);
    if (t256 == 1) nx = (u32x2){h0, h1};
    cv_put(hb + t256 * 16, z, nx);
  }
}
DI void conv_run(unsigned reg, unsigned qf, unsigned& qs, int ch, int lane, int wq, const HRegs& hr,
                 char* __restrict__ ws, unsigned xst, Diag& dg) {
  const unsigned hb = reg + CV_H;
  if (xst) spin_ge4(xst, 1u);
  else { lds_write16(reg + (wq * 64 + lane) * 16, hr.u); QSYNC(); }
  STAMP(6);
  const int r16 = lane & 15, g = lane >> 4, q = r16 >> 2, p = lane & 3;
  const unsigned ab0 = hb + ((q + 1) & 3) * CV_COPY + 2 * (8 * g + 4 * p + (q == 3 ? 4 : 0)) + wq * 128;
  const unsigned bb0 = reg + 2 * (2032 - 16 * r16 + 8 * g) + wq * 128 - 7 * 512;
  f32x4 acc[8];
#pragma unroll
  for (int i = 0; i < 8; ++i) acc[i] = (f32x4){0.f, 0.f, 0.f, 0.f};
#define CV_LDA(a_, ad_) cat8(tr_read((ad_) + (a_) * 512), tr_read((ad_) + (a_) * 512 + 8))
#define CV_LDB(c_, bd_) lds_read16((bd_) + (7 - (c_)) * 512)
  {
    s16x8 A[8], B[8];
    B[0] = CV_LDB(0, bb0);
#pragma unroll
    for (int a = 0; a < 8; ++a) A[a] = CV_LDA(a, ab0);
#pragma unroll
    for (int c = 1; c < 8; ++c) B[c] = CV_LDB(c, bb0);
#pragma unroll
    for (int c = 0; c < 8; ++c) {
      SBAR();
#pragma unroll
      for (int a = 0; a + c < 8; ++a) acc[a + c] = MFMA16(A[a], B[c], acc[a + c]);
      B[c] = CV_LDB(c, bb0 + 64);
      A[7 - c] = CV_LDA(7 - c, ab0 + 64);
    }
    SBAR();
#pragma unroll
    for (int a = 7; a >= 0; --a)
#pragma unroll
      for (int c = 0; a + c < 8; ++c) acc[a + c] = MFMA16(A[a], B[c], acc[a + c]);
  }
  QSYNC();
#pragma unroll
  for (int nt = 0; nt < 8; ++nt) {
    const int dw = nt >> 1;
    if (dw != wq) lds_write16(hb + (nt * 3 + (wq > dw ? wq - 1 : wq)) * 1024 + lane * 16, __builtin_bit_cast(u32x4, acc[nt]));
  }
  f32x4 k0 = wq == 0 ? acc[0] : (wq == 1 ? acc[2] : (wq == 2 ? acc[4] : acc[6]));
  f32x4 k1 = wq == 0 ? acc[1] : (wq == 1 ? acc[3] : (wq == 2 ? acc[5] : acc[7]));
  QSYNC();
  const float dco = bf_lo(hr.d);
  u32x2 zz[2];
#pragma unroll
  for (int i = 0; i < 2; ++i) {
    f32x4 o = i ? k1 : k0;
#pragma unroll
    for (int j = 0; j < 3; ++j) o += __builtin_bit_cast(f32x4, lds_read16(hb + ((wq * 2 + i) * 3 + j) * 1024 + lane * 16));
    const int s = (wq * 2 + i) * 256 + 16 * r16 + 4 * g;
    const u32x2 uu = __builtin_bit_cast(u32x2, lds_read8(reg + 2 * (SEQ - 4 - s)));
    const float kv0 = bf_hi(uu[1]), kv1 = bf_lo(uu[1]), kv2 = bf_hi(uu[0]), kv3 = bf_lo(uu[0]);
    const unsigned y01 = pk_bf16(o[0], o[1]), y23 = pk_bf16(o[2], o[3]);
    const unsigned e01 = pk_bf16(kv0 * dco, kv1 * dco), e23 = pk_bf16(kv2 * dco, kv3 * dco);
    zz[i][0] = pk_bf16(bf_lo(y01) + bf_lo(e01), bf_hi(y01) + bf_hi(e01));
    zz[i][1] = pk_bf16(bf_lo(y23) + bf_lo(e23), bf_hi(y23) + bf_hi(e23));
  }
  const auto sx = __builtin_amdgcn_permlane16_swap(zz[0][0], zz[1][0], false, false);
  const auto sy = __builtin_amdgcn_permlane16_swap(zz[0][1], zz[1][1], false, false);
  u16* zrow = (u16*)(ws + WS_ZT) + (size_t)ch * SEQ;
  gstore16c(zrow + (wq * 2 + (g & 1)) * 256 + 16 * r16 + 8 * (g >> 1), (u32x4){sx[0], sy[0], sx[1], sy[1]});
  asm volatile("s_waitcnt lgkmcnt(0)" ::: "memory");
}
DI void conv_publish(unsigned zc, int ch, int lane, char* __restrict__ ws, u64 E) {
  asm volatile("s_waitcnt vmcnt(0)" ::: "memory");
  if (lane == 0) {
    const unsigned old = __hip_atomic_fetch_add((LAS unsigned*)(uintptr_t)zc, 1u, __ATOMIC_RELAXED, __HIP_MEMORY_SCOPE_WORKGROUP);
    if ((old & 3u) == 3u) __hip_atomic_store((u64*)(ws + FL_Z + (size_t)ch * 8), E, __ATOMIC_RELAXED, __HIP_MEMORY_SCOPE_AGENT);
  }
}

DI s16x8 pack8(const f32x16& v, int o) {
  u32x4 r;
  r[0] = pk_bf16(v[o + 0], v[o + 1]); r[1] = pk_bf16(v[o + 2], v[o + 3]);
  r[2] = pk_bf16(v[o + 4], v[o + 5]); r[3] = pk_bf16(v[o + 6], v[o + 7]);
  return __builtin_bit_cast(s16x8, r);
}
DI float max3f(float a, float b, float c) { return __builtin_fmaxf(__builtin_fmaxf(a, b), c); }
DI float max16(const f32x16& v, float m) {
  float a = max3f(v[0], v[1], m), b = max3f(v[2], v[3], v[4]);
  a = max3f(a, v[5], v[6]); b = max3f(b, v[7], v[8]);
  a = max3f(a, v[9], v[10]); b = max3f(b, v[11], v[12]);
  a = max3f(a, v[13], v[14]);
  return max3f(a, b, v[15]);
}
DI float xhalf_max(float m) {
  auto sw = __builtin_amdgcn_permlane32_swap(__float_as_uint(m), __float_as_uint(m), false, false);
  return __builtin_fmaxf(__uint_as_float(sw[0]), __uint_as_float(sw[1]));
}

#define AT_LMAX 65536.0f
#define P3_WADDR() unsigned wv[4]; _Pragma("unroll") for (int i = 0; i < 4; ++i) { \
      const int c = i < 3 ? (i * 4 + w) * 64 + lane : 768 + w * 8 + (lane & 7); const int row = c / 10; \
      wv[i] = (unsigned)(row * DM * 2 + (c - row * 10) * 16); } const char* wog = (const char*)(wo + n3)
#define P3_STW(t) do { _Pragma("unroll") for (int i_ = 0; i_ < 3; ++i_) \
        glds16(wv[i_], wog + (size_t)(t) * 80 * DM * 2, lds0 + p3_w(t) + (i_ * 4 + w) * 1024); \
      if (lane < 8) glds16(wv[3], wog + (size_t)(t) * 80 * DM * 2, lds0 + p3_w(t) + 12288 + w * 128); } while (0)
DI void attn_stage(unsigned lds0, int h, int qb8, int j, int lane, char* __restrict__ ws, u64 E, int zch, const Mk& mk, Diag& dg) {
  const int qt = qb8 * 8 + j;
  const unsigned l16 = (unsigned)(lane * 16), ct = lds0 + L_CT;
  const int qa = (5 * h) >> 2;
  const bool stg = qb8 > 0 || j >= 2;
  {
    const int l3 = lane & 7, hi = lane & 1;
    const bool kv = stg && l3 >= 2 && l3 < 6;
    const int rb = kv ? 2 * qb8 - 2 + j : 2 * qb8 + (l3 >= 6 ? 1 : 0);
    const int col = !kv ? qa * 16 + hi * 31 : (l3 < 4 ? DM + qa * 16 + hi * 31 : 2 * DM + h * 20 + hi * 19);
    poll(ws, FL_Q + (size_t)(rb * 16 + (col >> 7)) * 8, E);
  }
  STAMP(9);
  conv_publish(ct + CT_ZCNT + 8, zch, lane, ws, E);
  const int kt0 = qb8 * 8 - 8 + 4 * j;
#pragma unroll
  for (int i = 0; i < 4; ++i)
    glds16c(l16, ws + WS_QF + ((size_t)(qt + (i >> 1) * 4) * 40 + qa + (i & 1)) * 1024, lds0 + AT_Q + ((j + (i >> 1) * 4) * 2 + (i & 1)) * 1024);
  if (stg) {
#pragma unroll
    for (int i = 0; i < 8; ++i)
      glds16c(l16, ws + WS_KF + ((size_t)(kt0 + (i >> 1)) * 40 + qa + (i & 1)) * 1024, lds0 + AT_K + (j * 8 + i) * 1024);
  }
  glds_mark(mk, ct + CT_KR8 + j * 4, lane);
  if (stg) {
#pragma unroll
    for (int i = 0; i < 6; ++i) {
      const int c = i * 64 + lane;
      const int kgp = (c * 1561) >> 16, rem = c - kgp * 42, vh = rem >= 21 ? 1 : 0, d = rem - vh * 21;
      const int n = h * 20 + d;
      if (c < 336 && d == 20) lds_write16(lds0 + AT_V + j * 5376 + c * 16, (u32x4){0x3f803f80u, 0x3f803f80u, 0x3f803f80u, 0x3f803f80u});
      if (c < 336 && d < 20)
        glds16c((unsigned)((kgp * 20 + (n >> 5)) * 1024 + (vh * 32 + (n & 31)) * 16), ws + WS_VF + ((size_t)(kt0 * 2) * 20) * 1024,
                lds0 + AT_V + j * 5376 + i * 1024);
    }
    asm volatile("s_waitcnt lgkmcnt(0)" ::: "memory");
  }
  glds_mark(mk, ct + CT_VR8 + j * 4, lane);
}
template <bool FULL>
DI void attn_task(unsigned lds0, int h, int qb8, int j, int lane, char* __restrict__ ws, u64 E, int zch, const Mk& mk, Diag& dg) {
  const int l32 = lane & 31, hh = lane >> 5;
  const int qt = qb8 * 8 + j;
  const int tfirst = FULL ? 0 : 8 - qt;
  const unsigned l16 = (unsigned)(lane * 16), ct = lds0 + L_CT;
  const int qa = (5 * h) >> 2;
  u32x4 q0r, q1r;
  spin_mk(ct + CT_KR8, mk.x);
  if (!FULL) spin_mk(ct + CT_VR8, mk.x);
  q0r = __builtin_bit_cast(u32x4, lds_read16(lds0 + AT_Q + j * 2048 + l16));
  q1r = __builtin_bit_cast(u32x4, lds_read16(lds0 + AT_Q + j * 2048 + 1024 + l16));
  asm volatile("s_waitcnt lgkmcnt(0)" : "+v"(q0r), "+v"(q1r) :: "memory");
  if (zch >= 0) conv_publish(ct + CT_ZCNT + (j >> 2) * 4, zch, lane, ws, E);
  {
    const int g0 = 4 * qa + hh - 5 * h;
    const bool k0 = g0 >= 0, k1 = g0 + 2 >= 0 && g0 + 2 < 5, k2 = g0 + 4 < 5, k3 = g0 + 6 < 5;
    q0r[0] = k0 ? q0r[0] : 0u; q0r[1] = k0 ? q0r[1] : 0u; q0r[2] = k1 ? q0r[2] : 0u; q0r[3] = k1 ? q0r[3] : 0u;
    q1r[0] = k2 ? q1r[0] : 0u; q1r[1] = k2 ? q1r[1] : 0u; q1r[2] = k3 ? q1r[2] : 0u; q1r[3] = k3 ? q1r[3] : 0u;
  }
  s16x8 qlo = __builtin_bit_cast(s16x8, q0r), qhi8 = __builtin_bit_cast(s16x8, q1r);
  STAMP(8);
  const f32x16 zero16 = {0.f, 0.f, 0.f, 0.f, 0.f, 0.f, 0.f, 0.f, 0.f, 0.f, 0.f, 0.f, 0.f, 0.f, 0.f, 0.f};
  const float ninf = -INFINITY;
  const f32x16 ninf16 = {ninf, ninf, ninf, ninf, ninf, ninf, ninf, ninf, ninf, ninf, ninf, ninf, ninf, ninf, ninf, ninf};
  const unsigned kb = lds0 + AT_K + j * 2048 + l16;
#define AT_QK(dst, t, cin) do { if (FULL || (t) >= tfirst) { dst = MFMA32(lds_read16(kb + (t) * 2048), qlo, cin); \
    dst = MFMA32(lds_read16(kb + (t) * 2048 + 1024), qhi8, dst); } else dst = ninf16; } while (0)
  const int vrow = l32 <= 20 ? l32 : (l32 == 24 ? 20 : l32 - 16);
  const unsigned vb = lds0 + AT_V + j * 1344 + hh * 336 + vrow * 16;
#define AT_EXP(sv) do { _Pragma("unroll") for (int r = 0; r < 16; ++r) sv[r] = __builtin_amdgcn_exp2f(sv[r] - m); } while (0)
#define AT_PV(sv, t) do { oacc = MFMA32(lds_read16(vb + (t) * 1344), pack8(sv, 0), oacc); oacc = MFMA32(lds_read16(vb + (t) * 1344 + 672), pack8(sv, 8), oacc); } while (0)
  f32x16 oacc;
  bool exact = !FULL;
  if (FULL) {
    f32x16 sm, sa, sb;
    AT_QK(sm, 8, zero16); AT_QK(sa, 0, zero16);
    const int tq = l32 - 4 * hh;
    const unsigned wm = tq < 0 ? 0xffffffffu : (0xfffffffeu << tq);
    unsigned msk[16];
#pragma unroll
    for (int r = 0; r < 16; ++r) {
      msk[r] = (unsigned)__builtin_amdgcn_sbfe((int)wm, (r & 3) + 8 * (r >> 2), 1);
      sm[r] = __uint_as_float((msk[r] & __float_as_uint(sa[r])) | (~msk[r] & __float_as_uint(sm[r])));
    }
    const float m = xhalf_max(max16(sm, ninf));
    const float nm = -m;
    const f32x16 cneg = {nm, nm, nm, nm, nm, nm, nm, nm, nm, nm, nm, nm, nm, nm, nm, nm};
    AT_QK(sa, 1, cneg);
    AT_EXP(sm);
    u32x4 pl = __builtin_bit_cast(u32x4, pack8(sm, 0)), ph = __builtin_bit_cast(u32x4, pack8(sm, 8)), ql, qh;
#pragma unroll
    for (int i = 0; i < 4; ++i) {
      ql[i] = pl[i] & ((msk[2 * i] & 0xffffu) | (msk[2 * i + 1] & 0xffff0000u)); pl[i] ^= ql[i];
      qh[i] = ph[i] & ((msk[8 + 2 * i] & 0xffffu) | (msk[9 + 2 * i] & 0xffff0000u)); ph[i] ^= qh[i];
    }
    spin_mk(ct + CT_VR8, mk.x);
    STAMP(9);
    oacc = MFMA32(lds_read16(vb + 8 * 1344), __builtin_bit_cast(s16x8, pl), zero16);
    oacc = MFMA32(lds_read16(vb + 8 * 1344 + 672), __builtin_bit_cast(s16x8, ph), oacc);
    oacc = MFMA32(lds_read16(vb), __builtin_bit_cast(s16x8, ql), oacc);
    oacc = MFMA32(lds_read16(vb + 672), __builtin_bit_cast(s16x8, qh), oacc);
#define AT_EX0(sv) do { _Pragma("unroll") for (int r = 0; r < 16; ++r) sv[r] = __builtin_amdgcn_exp2f(sv[r]); } while (0)
    AT_QK(sb, 2, cneg); AT_EX0(sa); AT_PV(sa, 1);
    AT_QK(sa, 3, cneg); AT_EX0(sb); AT_PV(sb, 2);
    AT_QK(sb, 4, cneg); AT_EX0(sa); AT_PV(sa, 3);
    AT_QK(sa, 5, cneg); AT_EX0(sb); AT_PV(sb, 4);
    AT_QK(sb, 6, cneg); AT_EX0(sa); AT_PV(sa, 5);
    AT_QK(sa, 7, cneg); AT_EX0(sb); AT_PV(sb, 6);
    AT_EX0(sa); AT_PV(sa, 7);
    float big = max3f(__builtin_fabsf(oacc[0]), __builtin_fabsf(oacc[1]), __builtin_fabsf(oacc[2]));
    big = max3f(big, __builtin_fabsf(oacc[3]), __builtin_fabsf(oacc[4]));
    big = max3f(big, __builtin_fabsf(oacc[5]), __builtin_fabsf(oacc[6]));
    big = max3f(big, __builtin_fabsf(oacc[7]), __builtin_fabsf(oacc[8]));
    big = max3f(big, __builtin_fabsf(oacc[9]), __builtin_fabsf(oacc[10]));
    big = __builtin_fmaxf(big, __builtin_fabsf(oacc[11]));
    const float ls = hh ? oacc[8] : oacc[12];
    float osum = 0.f;
#pragma unroll
    for (int r = 0; r < 8; ++r) osum += oacc[r];
    const bool ok = (ls < AT_LMAX) && (big < 1e37f) && (osum == osum);
    exact = __builtin_amdgcn_ballot_w64(!ok) != 0ull;
  }
  if (exact) {
    asm volatile("" : "+v"(qlo), "+v"(qhi8) :: "memory");
    f32x16 sm, s1, s2, s3;
    {
      f32x16 s0;
      AT_QK(sm, 8, zero16); AT_QK(s0, 0, zero16);
      AT_QK(s1, 1, zero16); AT_QK(s2, 2, zero16); AT_QK(s3, 3, zero16);
#pragma unroll
      for (int r = 0; r < 16; ++r) {
        const int kk = (r & 3) + 8 * (r >> 2) + 4 * hh;
        sm[r] = kk > l32 ? s0[r] : sm[r];
      }
    }
    float m = max16(sm, ninf);
    m = max16(s1, m); m = max16(s2, m); m = max16(s3, m);
    m = xhalf_max(m);
    AT_EXP(sm);
    oacc = zero16;
    {
      f32x16 p0, p8;
#pragma unroll
      for (int r = 0; r < 16; ++r) {
        const int kk = (r & 3) + 8 * (r >> 2) + 4 * hh;
        p0[r] = kk > l32 ? sm[r] : 0.f;
        p8[r] = kk > l32 ? 0.f : sm[r];
      }
      AT_PV(p8, 8);
      if (FULL) AT_PV(p0, 0);
    }
    if (FULL || 1 >= tfirst) { AT_EXP(s1); AT_PV(s1, 1); }
    AT_QK(sm, 4, zero16); AT_QK(s1, 5, zero16);
    if (FULL || 2 >= tfirst) { AT_EXP(s2); AT_PV(s2, 2); }
    AT_QK(s2, 6, zero16);
    if (FULL || 3 >= tfirst) { AT_EXP(s3); AT_PV(s3, 3); }
    AT_QK(s3, 7, zero16);
    float m2 = max16(sm, m);
    m2 = max16(s1, m2); m2 = max16(s2, m2); m2 = max16(s3, m2);
    m2 = xhalf_max(m2);
    {
      const float alpha = __builtin_amdgcn_exp2f(m - m2);
#pragma unroll
      for (int r = 0; r < 13; ++r) oacc[r] *= alpha;
      m = m2;
    }
    if (FULL || 4 >= tfirst) { AT_EXP(sm); AT_PV(sm, 4); }
    if (FULL || 5 >= tfirst) { AT_EXP(s1); AT_PV(s1, 5); }
    if (FULL || 6 >= tfirst) { AT_EXP(s2); AT_PV(s2, 6); }
    if (FULL || 7 >= tfirst) { AT_EXP(s3); AT_PV(s3, 7); }
  }
  asm volatile("s_waitcnt lgkmcnt(0)" ::: "memory");
  sig(ct + CT_ATT + j * 4, 1u, lane);
  STAMP(10);
  const float l = hh ? oacc[8] : oacc[12];
  const float inv = __builtin_amdgcn_rcpf(l);
  char* oc = ws + WS_OW + ((size_t)qt * NH + h) * 1280;
  u32x4 o;
  o[0] = pk_bf16(oacc[0] * inv, oacc[1] * inv); o[1] = pk_bf16(oacc[2] * inv, oacc[3] * inv);
  o[2] = pk_bf16(oacc[4] * inv, oacc[5] * inv); o[3] = pk_bf16(oacc[6] * inv, oacc[7] * inv);
  gstore16c(oc + lane * 16, o);
  if (!hh) {
    u32x2 o2;
    o2[0] = pk_bf16(oacc[8] * inv, oacc[9] * inv); o2[1] = pk_bf16(oacc[10] * inv, oacc[11] * inv);
    gstore8c(oc + 1024 + l32 * 8, o2);
  }
  publish(ws, FL_O + (size_t)(qt * NH + h) * 8, E, lane);
}

#define HDELAY 0
#define APRIO 0
#define LPRIO 0
#define MPRIO 2
#define ROTG 2
#define A1_DELAY 8
__global__ __launch_bounds__(768, 3) void mega(const u16* __restrict__ x, const u16* __restrict__ wqkv,
                                               const u16* __restrict__ ak, const u16* __restrict__ av,
                                               const u16* __restrict__ shk, const u16* __restrict__ wo,
                                               const u16* __restrict__ ssmk, const u16* __restrict__ ssmd,
                                               const u16* __restrict__ aq, char* __restrict__ ws,
                                               u16* __restrict__ out) {
  extern __shared__ __attribute__((aligned(16))) char smem[];
  const unsigned lds0 = lds_addr(smem);
  const int tid = threadIdx.x, wave = __builtin_amdgcn_readfirstlane(tid >> 6);
  int lane = tid & 63;
#define RELANE() asm volatile("" : "+v"(lane))
  const int role = wave >> 2, w = wave & 3;
  const int bid = blockIdx.x, xcd = bid & 7, idx = bid >> 3;
  const bool has_gemm = idx < 30;
  const unsigned ct = lds0 + L_CT;
  DIAG_INIT();
  u64 E;
  Mk mk; mk.src = wqkv;
  asm volatile("s_load_dwordx2 %0, %2, 0x0\n\ts_load_dword %1, %3, 0x0" : "=&s"(E), "=&s"(mk.x) : "s"(ws + WS_FL), "s"(wqkv) : "memory");
  const int gt = (xcd & 1) * 30 + idx;
  const int tm = (xcd >> 1) * 4 + (gt & 3), tn = gt >> 2;
  const int m0 = tm * 128, n0 = tn * 128;
  const int rot = ROTG >= 10 ? (((gt & 3) * 5 + 1) / 2 + 3 * tn) % 10 : ((((gt & 3) + tn) % ROTG) * 10) / ROTG;
  const unsigned xo0 = (unsigned)((lane >> 3) * DM * 2 + (((lane & 7) ^ (lane >> 4)) * 16));
  const unsigned xo1 = (unsigned)((lane >> 3) * DM * 2 + (((lane & 7) ^ (4 + (lane >> 4))) * 16));
  const unsigned wof = (unsigned)((lane >> 4) * NQKV * 2 + (((lane & 15) ^ ((lane >> 4) << 2)) * 16));
  const char* xg = (const char*)(x + (size_t)m0 * DM);
  const char* wg = (const char*)(wqkv + n0);
#define K1_PIECE_X(t, kt, j) glds16p(((j) & 1) ? xo1 : xo0, xg + (size_t)(j) * 8 * DM * 2 + (kt) * 128, lds0 + ((t) & 3) * K1_STAGE + (j) * 1024)
#define K1_PIECE_W(t, kt, j) glds16p(wof, wg + (size_t)((kt) * 64 + (j) * 4) * NQKV * 2, lds0 + ((t) & 3) * K1_STAGE + 16384 + (j) * 1024)
  if (has_gemm && role == 0) {
#pragma unroll
    for (int i = 0; i < 4; ++i) { K1_PIECE_X(0, rot, w * 4 + i); K1_PIECE_W(0, rot, w * 4 + i); }
  }
  const unsigned pf = ct + CT_PAIR + (wave >> 1) * 8;
  const int wh = wave & 1;
  unsigned ps = 0;
  const int fb = (idx - 30) * 8 + xcd;
  const int a1t = has_gemm ? (xcd * 30 + idx) * 2 + (w >> 1) : 480 + fb * 10 + (wave >> 1);
  const unsigned a1l = has_gemm ? lds0 + L_AUX + (w >> 1) * A1_PAIR : lds0 + (wave >> 1) * A1_PAIR;
  if (!has_gemm || role == 2) a1_issue(a1l, a1t, lane, wh, ak, av, shk);
  const bool cv0 = role != 0 || !has_gemm || bid < 112;
  const int ch0 = role == 0 ? (has_gemm ? 528 + bid : 512 + fb) : bid * 2 + (role == 1 ? 1 : 0);
  asm volatile("s_waitcnt lgkmcnt(0)" : "+s"(E), "+s"(mk.x) :: "memory");
  if (role == 1) lds_st32(ct + (tid - 256) * 4, (tid - 256) * 4 >= CT_FX ? ~mk.x : 0u);
  bar();
  E += 1ull;

  const bool ex = has_gemm && role == 1 && bid < 112;
  HRegs hr, h2;
  if (!(has_gemm && role == 0)) {
    hr = conv_load_h(ch0, w * 64 + lane, ssmk, ssmd);
    if (ex) h2 = conv_load_h(528 + bid, w * 64 + lane, ssmk, ssmd);
  }
  auto q_publish = [&]() {
    asm volatile("s_waitcnt vmcnt(0)" ::: "memory");
    if (lane == 0) {
      const unsigned old = __hip_atomic_fetch_add((LAS unsigned*)(uintptr_t)(ct + CT_QCNT), 1u, __ATOMIC_RELAXED, __HIP_MEMORY_SCOPE_WORKGROUP);
      if (old == 3u) __hip_atomic_store((u64*)(ws + FL_Q + (size_t)(tm * 16 + tn) * 8), E, __ATOMIC_RELAXED, __HIP_MEMORY_SCOPE_AGENT);
    }
    STAMP(4);
  };
  if (!has_gemm) {
    const unsigned a2l = lds0 + (6 + (wave >> 1)) * A1_PAIR;
    if (wave < 8) a1_issue(a2l, a1t + 6, lane, wh, ak, av, shk);
    if (wave < 8) wait_vm<6>(); else wait_vm<0>();
    a1_finish(a1l, pf, ps, a1t, lane, wh, ws, E);
    if (wave < 8) { wait_vm<2>(); a1_finish(a2l, pf, ps, a1t + 6, lane, wh, ws, E); }
    a1_publish(a1t, lane, wh, ws, E);
    if (wave < 8) a1_publish(a1t + 6, lane, wh, ws, E);
    bar();
    STAMP(1);
  } else if (role == 0) {
    wait_vm<0>();
    sig(ct + CT_S0 + w * 4, 1u, lane);
    spin_ge4(ct + CT_S0, 1u);
    __builtin_amdgcn_s_setprio(MPRIO);
    if (tn >= 10) k1_gemm<true>(lds0, m0, n0, tid, ws, mk.x, dg);
    else k1_gemm<false>(lds0, m0, n0, tid, ws, mk.x, dg);
    STAMP(3);
  } else if (role == 1) {
    __builtin_amdgcn_s_setprio(LPRIO);
#define K1_ST(t) do { const int kt_ = (t) + rot >= 10 ? (t) + rot - 10 : (t) + rot; \
      if ((t) >= 4) spin_ge4(ct + CT_FREE + ((t) & 3) * 16, (unsigned)((t) - 3)); \
      _Pragma("unroll") for (int i_ = 0; i_ < 4; ++i_) { K1_PIECE_X(t, kt_, w * 4 + i_); K1_PIECE_W(t, kt_, w * 4 + i_); } } while (0)
#pragma nounroll
    for (int t = 1; t < 10; ++t) {
      K1_ST(t); glds_mark(mk, ct + CT_FX + t * 16 + w * 4, lane);
    }
    __builtin_amdgcn_s_setprio(0);
    STAMP(1);
  } else {
    __builtin_amdgcn_s_setprio(2);
    wait_vm<0>();
    a1_finish(a1l, pf, ps, a1t, lane, wh, ws, E);
    a1_publish(a1t, lane, wh, ws, E);
    __builtin_amdgcn_s_setprio(0);
    STAMP(1);
  }

  RELANE();
  unsigned qs = 0;
  const unsigned qf = ct + CT_QUAD + role * 16;
  const unsigned creg = lds0 + (role == 2 ? L_AUX : (role == 1 ? 29184 : 0));
  if (role == 2) __builtin_amdgcn_s_setprio(APRIO); else __builtin_amdgcn_s_setprio(0);
  if (has_gemm && role == 0) {
    q_publish();
    if (cv0) {
      hr.d = ssmd[ch0];
      conv_run(creg, qf, qs, ch0, lane, w, hr, ws, ct + CT_XST, dg);
    }
  } else {
    if (has_gemm && role == 1) {
      conv_load_u(hr, ch0, lane, w, ws, E);
      if (ex) conv_load_u(h2, 528 + bid, lane, w, ws, E);
      spin_ge4(ct + CT_FREE + 16, 10u);
      conv_stage(creg, lane, w, hr);
      if (ex) {
        conv_stage(lds0, lane, w, h2);
        lds_write16(lds0 + (w * 64 + lane) * 16, h2.u);
        asm volatile("s_waitcnt lgkmcnt(0)" ::: "memory");
        sig(ct + CT_XST + w * 4, 1u, lane);
      }
    } else {
      if (has_gemm) { const int wq = w; QSYNC(); }
      conv_stage(creg, lane, w, hr);
      conv_load_u(hr, ch0, lane, w, ws, E);
    }
    STAMP(5);
    conv_run(creg, qf, qs, ch0, lane, w, hr, ws, 0u, dg);
  }
  sig(ct + CT_CVD + wave * 4, 1u, lane);
  if (role != 0) STAMP(2);

  const int tm3 = xcd * 4 + (idx >> 3), tn3 = idx & 7;
  const int m3 = tm3 * 64, n3 = tn3 * 80;
  RELANE();

  if (role == 2) {
    const int r16 = lane & 15, g = lane >> 4, q = r16 >> 2, p = lane & 3;
    __builtin_amdgcn_s_setprio(3);
    spin_ge4(ct + CT_CVD + 32, 1u);
    if (has_gemm) spin_ge4(ct + CT_FREE + 16, 10u);
    attn_stage(lds0, xcd * 4 + (idx & 3), idx >> 2, w, lane, ws, E, ch0, mk, dg);
    {
      P3_WADDR();
      P3_STW(5);
      spin_ge8(ct + CT_CVD, 1u);
      P3_STW(0); P3_STW(4); P3_STW(1); P3_STW(2); P3_STW(3);
      glds_mark(mk, ct + CT_WRDY + w * 4, lane);
      STAMP(3);
    }
    __builtin_amdgcn_s_setprio(0);
    u32x4 qv[3];
#pragma unroll
    for (int j = 0; j < 3; ++j) {
      const int dc = j < 2 ? j * 4 + g : 8 + (g & 1);
      qv[j] = *((const u32x4*)(aq + (size_t)(m3 + w * 16 + r16) * DM + n3 + dc * 8));
    }
    {
      const int l5 = lane & 31;
      const int cl = l5 < 8 ? w * 8 + l5 : (l5 < 16 ? 32 + w * 8 + (l5 - 8) : 64 + w * 4 + (l5 & 3));
      poll(ws, FL_Z + (size_t)(n3 + cl) * 8, E);
    }
    {
      const char* zg = ws + WS_ZT + ((size_t)n3 * SEQ + m3) * 2;
      const unsigned zo = (unsigned)((lane >> 3) * SEQ * 2 + (lane & 7) * 16);
      glds16c(zo, zg + (size_t)(w * 8) * SEQ * 2, lds0 + L_AUX + w * 1024);
      glds16c(zo, zg + (size_t)(32 + w * 8) * SEQ * 2, lds0 + L_AUX + 4096 + w * 1024);
      if (lane < 32) glds16c(zo, zg + (size_t)(64 + w * 4) * SEQ * 2, lds0 + L_AUX + 8192 + w * 512);
    }
    wait_vm<0>();
    sig(ct + CT_ZRDY + w * 4, 1u, lane);
    spin_ge4(ct + CT_ZRDY, 1u);
    STAMP(4);
    u32x4 zz[3];
#pragma unroll
    for (int j = 0; j < 3; ++j) {
      const int dc = j < 2 ? j * 4 + g : 8 + (g & 1);
      const unsigned za = lds0 + L_AUX + (dc * 8 + q) * 128 + w * 32 + p * 8;
      zz[j] = __builtin_bit_cast(u32x4, cat8(tr_read(za), tr_read(za + 512)));
    }
#pragma unroll
    for (int j = 0; j < 3; ++j) {
      const int dc = j < 2 ? j * 4 + g : 8 + (g & 1);
      u32x4 o;
#pragma unroll
      for (int i = 0; i < 4; ++i) o[i] = pk_bf16(bf_lo(qv[j][i]) * bf_lo(zz[j][i]), bf_hi(qv[j][i]) * bf_hi(zz[j][i]));
      if (j < 2 || g < 2) ostore16(out + (size_t)(m3 + w * 16 + r16) * (2 * DM) + n3 + dc * 8, o);
    }
    STAMP(7);
    if (bid == 0 && w == 0) {
#pragma unroll
      for (int i = 0; i < 4; ++i) poll(ws, FL_O + (size_t)(((i * 2 + (lane >> 5)) * 8) * NH + (lane & 31)) * 8, E);
      if (lane == 0) __hip_atomic_store((u64*)(ws + WS_FL), E, __ATOMIC_RELAXED, __HIP_MEMORY_SCOPE_AGENT);
    }
    DIAG_DUMP(2);
    return;
  }

  const int h = xcd * 4 + (idx & 3), qb8 = idx >> 2;
  __builtin_amdgcn_s_setprio(1);
  RELANE();
  if (qb8 > 0) attn_task<true>(lds0, h, qb8, wave, lane, ws, E, cv0 ? ch0 : -1, mk, dg);
  else attn_task<false>(lds0, h, qb8, wave, lane, ws, E, cv0 ? ch0 : -1, mk, dg);
  STAMP(11);

  RELANE();
  const int r16 = lane & 15, g = lane >> 4, q = r16 >> 2, p = lane & 3;
  f32x4 acc[5];
#pragma unroll
  for (int i = 0; i < 5; ++i) acc[i] = (f32x4){0.f, 0.f, 0.f, 0.f};
  const unsigned wbp = lds0 + (20 * (g >> 1) + 4 * (g & 1) + q) * 160 + p * 8;
  const unsigned wbs = lds0 + (20 * g + 16 + q) * 160 + p * 8;
  u32x4 op[8]; u32x2 osg[4];
  if (role == 1) {
    P3_WADDR();
    spin_ge8(ct + CT_ATT, 1u);
    P3_STW(6); P3_STW(7);
    glds_mark(mk, ct + CT_WRD2 + w * 4, lane);
  }
  spin_mk(ct + CT_WRDY, mk.x);
  const unsigned w0 = role ? p3_w(4) : p3_w(0), w1 = role ? p3_w(5) : p3_w(1), w2 = role ? p3_w(6) : p3_w(2), w3 = role ? p3_w(7) : p3_w(3);
  struct Fw { s16x8 a[5], b[5]; s16x4 s[5]; };
  Fw fwa, fwb;
#define P3_LOADW(F, bo_) do { _Pragma("unroll") for (int nt = 0; nt < 5; ++nt) { \
      F.a[nt] = cat8(tr_read(wbp + (bo_) + nt * 32), tr_read(wbp + (bo_) + 8 * 160 + nt * 32)); \
      F.b[nt] = cat8(tr_read(wbp + (bo_) + 40 * 160 + nt * 32), tr_read(wbp + (bo_) + 48 * 160 + nt * 32)); \
      F.s[nt] = tr_read(wbs + (bo_) + nt * 32); } } while (0)
#define P3_MM(F, t) do { \
    _Pragma("unroll") for (int nt = 0; nt < 5; ++nt) acc[nt] = MFMA16(F.a[nt], __builtin_bit_cast(s16x8, op[2 * (t)]), acc[nt]); \
    _Pragma("unroll") for (int nt = 0; nt < 5; ++nt) acc[nt] = MFMA16(F.b[nt], __builtin_bit_cast(s16x8, op[2 * (t) + 1]), acc[nt]); } while (0)
#define P3_MS(t) do { const s16x8 os_ = cat8(__builtin_bit_cast(s16x4, osg[t]), __builtin_bit_cast(s16x4, osg[(t) + 1])); \
    _Pragma("unroll") for (int nt = 0; nt < 5; ++nt) acc[nt] = MFMA16(cat8(fwa.s[nt], fwb.s[nt]), os_, acc[nt]); } while (0)
  P3_LOADW(fwa, w0);
  SBAR();
  poll(ws, FL_O + (size_t)(((m3 >> 5) + (w >> 1)) * NH + role * 16 + r16) * 8, E);
  STAMP(12);
  {
    const int orow = (w & 1) * 16 + r16;
    const char* ob = ws + WS_OW + ((size_t)((m3 >> 5) + (w >> 1)) * NH + role * 16) * 1280;
    const char* pp = ob + (g >> 1) * 1280 + (g & 1) * 512 + orow * 16;
    const char* sp = ob + g * 1280 + 1024 + orow * 8;
#pragma unroll
    for (int t = 0; t < 4; ++t) {
      asm volatile("global_load_dwordx4 %0, %3, off sc1\n\tglobal_load_dwordx4 %1, %3, off offset:2560 sc1\n\tglobal_load_dwordx2 %2, %4, off sc1"
                   : "=&v"(op[2 * t]), "=&v"(op[2 * t + 1]), "=&v"(osg[t]) : "v"(pp + t * 5120), "v"(sp + t * 5120) : "memory");
    }
  }
  P3_LOADW(fwb, w1);
  if (role == 1) spin_mk(ct + CT_WRD2, mk.x);
  SBAR();
  asm volatile("s_waitcnt vmcnt(6)" : "+v"(op[0]), "+v"(op[1]), "+v"(op[2]), "+v"(op[3]), "+v"(osg[0]), "+v"(osg[1]) :: "memory");
  STAMP(13);
  P3_MS(0); P3_MM(fwa, 0);
  SBAR();
  P3_LOADW(fwa, w2); P3_MM(fwb, 1);
#pragma unroll
  for (int i = 0; i < 10; ++i) { __builtin_amdgcn_sched_group_barrier(0x008, 1, 0); __builtin_amdgcn_sched_group_barrier(0x100, 3, 0); }
  SBAR();
  asm volatile("s_waitcnt vmcnt(0)" : "+v"(op[4]), "+v"(op[5]), "+v"(op[6]), "+v"(op[7]), "+v"(osg[2]), "+v"(osg[3]) :: "memory");
  P3_LOADW(fwb, w3); P3_MM(fwa, 2);
#pragma unroll
  for (int i = 0; i < 10; ++i) { __builtin_amdgcn_sched_group_barrier(0x008, 1, 0); __builtin_amdgcn_sched_group_barrier(0x100, 3, 0); }
  SBAR();
  P3_MM(fwb, 3); P3_MS(2);
  SBAR();
  const unsigned rb = lds0 + AT_K + 25600 + w * 5120 + lane * 16;
  if (role == 1) {
#pragma unroll
    for (int nt = 0; nt < 5; ++nt) lds_write16(rb + nt * 1024, __builtin_bit_cast(u32x4, acc[nt]));
    asm volatile("s_waitcnt lgkmcnt(0)" ::: "memory");
    sig(ct + CT_RED + w * 4, 1u, lane);
    STAMP(14);
    DIAG_DUMP(1);
    return;
  }
  spin_ge(ct + CT_RED + w * 4, 1u);
  u16* orow_g = out + (size_t)(m3 + w * 16 + r16) * (2 * DM) + DM + n3;
  u32x2 ov[5];
#pragma unroll
  for (int nt = 0; nt < 5; ++nt) {
    const f32x4 o2 = acc[nt] + __builtin_bit_cast(f32x4, lds_read16(rb + nt * 1024));
    ov[nt][0] = pk_bf16(o2[0], o2[1]);
    ov[nt][1] = pk_bf16(o2[2], o2[3]);
  }
#pragma unroll
  for (int pr = 0; pr < 2; ++pr) {
    const auto sx = __builtin_amdgcn_permlane16_swap(ov[2 * pr][0], ov[2 * pr + 1][0], false, false);
    const auto sy = __builtin_amdgcn_permlane16_swap(ov[2 * pr][1], ov[2 * pr + 1][1], false, false);
    ostore16(orow_g + (2 * pr + (g & 1)) * 16 + 8 * (g >> 1), (u32x4){sx[0], sy[0], sx[1], sy[1]});
  }
  ostore8(orow_g + 64 + 4 * g, ov[4]);
  STAMP(14);
  DIAG_DUMP(0);
}

extern "C" void kernel_launch(void* const* d_in, const int* in_sizes, int n_in, void* d_out, int out_size,
                              void* d_ws, size_t ws_size, hipStream_t stream) {
  const u16* aq = (const u16*)d_in[0];
  const u16* ak = (const u16*)d_in[1];
  const u16* av = (const u16*)d_in[2];
  const u16* shk = (const u16*)d_in[3];
  const u16* ssmk = (const u16*)d_in[4];
  const u16* ssmd = (const u16*)d_in[5];
  const u16* x = (const u16*)d_in[6];
  const u16* wqkv = (const u16*)d_in[7];
  const u16* wo = (const u16*)d_in[8];
  (void)hipFuncSetAttribute((const void*)mega, hipFuncAttributeMaxDynamicSharedMemorySize, L_TOTAL);
  mega<<<256, 768, L_TOTAL, stream>>>(x, wqkv, ak, av, shk, wo, ssmk, ssmd, aq, (char*)d_ws, (u16*)d_out);
}
```

```cpp
#include <hip/hip_runtime.h>
#include <hip/hip_bf16.h>
#include <stdint.h>

typedef unsigned short u16;
typedef short s16x4 __attribute__((ext_vector_type(4)));
typedef short s16x8 __attribute__((ext_vector_type(8)));
typedef float f32x2 __attribute__((ext_vector_type(2)));
typedef float f32x4 __attribute__((ext_vector_type(4)));
typedef float f32x16 __attribute__((ext_vector_type(16)));
typedef unsigned u32x2 __attribute__((ext_vector_type(2)));
typedef unsigned u32x4 __attribute__((ext_vector_type(4)));
typedef __bf16 bf16x2 __attribute__((ext_vector_type(2)));
#define LAS __attribute__((address_space(3)))
#define DI __device__ __forceinline__

constexpr int SEQ = 2048, DM = 640, NQKV = 1920, NH = 32;
constexpr size_t WS_QF = 0;
constexpr size_t WS_KF = WS_QF + (size_t)DM * SEQ * 2;
constexpr size_t WS_VF = WS_KF + (size_t)DM * SEQ * 2;
constexpr size_t WS_UT = WS_VF + (size_t)DM * SEQ * 2;
constexpr size_t WS_ZT = WS_UT + (size_t)DM * SEQ * 2;
constexpr size_t WS_OW = WS_ZT + (size_t)DM * SEQ * 2;
typedef unsigned long long u64;
constexpr size_t WS_FL = WS_OW + (size_t)DM * SEQ * 2;
constexpr size_t FL_U = WS_FL + 256;
constexpr size_t FL_Q = FL_U + 20 * 64 * 8;
constexpr size_t FL_Z = FL_Q + 16 * 16 * 8;
constexpr size_t FL_O = FL_Z + 640 * 8;

DI unsigned pk_bf16(float lo, float hi) {
  f32x2 f = {lo, hi};
  return __builtin_bit_cast(unsigned, __builtin_convertvector(f, bf16x2));
}
DI float bf_lo(unsigned u) { return __uint_as_float(u << 16); }
DI float bf_hi(unsigned u) { return __uint_as_float(u & 0xffff0000u); }
DI float rbf(float x) { return bf_lo(pk_bf16(x, 0.f)); }

DI unsigned lds_addr(const void* p) { return (unsigned)(uintptr_t)(LAS const char*)p; }

DI void glds16(unsigned voff, const void* sbase, unsigned lds_dst) {
  unsigned keep;
  asm volatile("s_mov_b32 %0, m0\n\ts_mov_b32 m0, %3\n\ts_nop 0\n\tglobal_load_lds_dwordx4 %1, %2\n\ts_mov_b32 m0, %0"
               : "=&s"(keep) : "v"(voff), "s"(sbase), "s"(lds_dst) : "memory");
}
DI void glds16f(unsigned voff, const void* sbase, unsigned lds_dst) {
  asm volatile("s_mov_b32 m0, %2\n\ts_nop 0\n\tglobal_load_lds_dwordx4 %0, %1" :: "v"(voff), "s"(sbase), "s"(lds_dst) : "memory");
}
#define glds16p glds16
DI void glds16nt(unsigned voff, const void* sbase, unsigned lds_dst) {
  unsigned keep;
  asm volatile("s_mov_b32 %0, m0\n\ts_mov_b32 m0, %3\n\ts_nop 0\n\tglobal_load_lds_dwordx4 %1, %2\n\ts_mov_b32 m0, %0"
               : "=&s"(keep) : "v"(voff), "s"(sbase), "s"(lds_dst) : "memory");
}
#define LDPOL "sc1"
DI void glds16c(unsigned voff, const void* sbase, unsigned lds_dst) {
  unsigned keep;
  asm volatile("s_mov_b32 %0, m0\n\ts_mov_b32 m0, %3\n\ts_nop 0\n\tglobal_load_lds_dwordx4 %1, %2 " LDPOL "\n\ts_mov_b32 m0, %0"
               : "=&s"(keep) : "v"(voff), "s"(sbase), "s"(lds_dst) : "memory");
}
struct Mk { const void* src; unsigned x; };
DI void glds_mark(const Mk& mk, unsigned lds_dst, int lane) {
  if (lane == 0) {
    unsigned keep;
    asm volatile("s_mov_b32 %0, m0\n\ts_mov_b32 m0, %3\n\ts_nop 0\n\tglobal_load_lds_dword %1, %2\n\ts_mov_b32 m0, %0"
                 : "=&s"(keep) : "v"(0u), "s"(mk.src), "s"(lds_dst) : "memory");
  }
}
DI float dot2bf(unsigned a, unsigned b, float c) {
  return __builtin_amdgcn_fdot2_f32_bf16(__builtin_bit_cast(bf16x2, a), __builtin_bit_cast(bf16x2, b), c, false);
}
DI void gstore16c(void* p, u32x4 v) {
  asm volatile("global_store_dwordx4 %0, %1, off sc1\n\ts_nop 1" :: "v"(p), "v"(v) : "memory");
}
DI void gstore8c(void* p, u32x2 v) {
  asm volatile("global_store_dwordx2 %0, %1, off sc1" :: "v"(p), "v"(v) : "memory");
}
#define OUTPOL "nt"
DI void ostore16(void* p, u32x4 v) { asm volatile("global_store_dwordx4 %0, %1, off " OUTPOL "\n\ts_nop 1" :: "v"(p), "v"(v) : "memory"); }
DI void ostore8(void* p, u32x2 v) { asm volatile("global_store_dwordx2 %0, %1, off " OUTPOL :: "v"(p), "v"(v) : "memory"); }
template <int N> DI void wait_vm() { asm volatile("s_waitcnt vmcnt(%0)" :: "n"(N) : "memory"); }
DI void bar() { asm volatile("s_waitcnt lgkmcnt(0)\n\ts_barrier" ::: "memory"); }
DI s16x4 tr_read(unsigned a) {
  return __builtin_amdgcn_ds_read_tr16_b64_v4i16((LAS s16x4*)(uintptr_t)a);
}
DI s16x8 lds_read16(unsigned a) { return *(LAS const s16x8*)(uintptr_t)a; }
DI s16x4 lds_read8(unsigned a) { return *(LAS const s16x4*)(uintptr_t)a; }
DI void lds_write16(unsigned a, u32x4 v) { *(LAS u32x4*)(uintptr_t)a = v; }
DI s16x8 cat8(s16x4 a, s16x4 b) { return __builtin_shufflevector(a, b, 0, 1, 2, 3, 4, 5, 6, 7); }
#define MFMA16(a, b, c) __builtin_amdgcn_mfma_f32_16x16x32_bf16((a), (b), (c), 0, 0, 0)
#define MFMA32(a, b, c) __builtin_amdgcn_mfma_f32_32x32x16_bf16((a), (b), (c), 0, 0, 0)

struct Diag {};
#define STAMP(i) do {} while (0)
#define DIAG_INIT() Diag dg
#define DIAG_DUMP(kid) do {} while (0)

#define SBAR() __builtin_amdgcn_sched_barrier(0)
#define SPIN_SLEEP 1
#define SPIN_MAX (1 << 21)
DI unsigned lds_ld32(unsigned a) { return *(volatile LAS unsigned*)(uintptr_t)a; }
DI void lds_st32(unsigned a, unsigned v) { *(volatile LAS unsigned*)(uintptr_t)a = v; }
DI void spin_ge(unsigned a, unsigned want) {
#pragma nounroll
  for (int it = 0; it < SPIN_MAX; ++it) {
    if ((unsigned)__builtin_amdgcn_readfirstlane(lds_ld32(a)) >= want) break;
    __builtin_amdgcn_s_sleep(1);
  }
  asm volatile("" ::: "memory");
}
DI void spin_ge4(unsigned a, unsigned want) {
#pragma nounroll
  for (int it = 0; it < SPIN_MAX; ++it) {
    const u32x4 v = *(volatile LAS u32x4*)(uintptr_t)a;
    const unsigned m = min(min(v[0], v[1]), min(v[2], v[3]));
    if ((unsigned)__builtin_amdgcn_readfirstlane(m) >= want) break;
    __builtin_amdgcn_s_sleep(SPIN_SLEEP);
  }
  asm volatile("" ::: "memory");
}
DI unsigned mk_bad(const u32x4& v, unsigned x) { return (v[0] ^ x) | (v[1] ^ x) | (v[2] ^ x) | (v[3] ^ x); }
DI void spin_mk(unsigned a, unsigned x) {
#pragma nounroll
  for (int it = 0; it < SPIN_MAX; ++it) {
    const u32x4 v = *(volatile LAS u32x4*)(uintptr_t)a;
    if ((unsigned)__builtin_amdgcn_readfirstlane(mk_bad(v, x)) == 0u) break;
    __builtin_amdgcn_s_sleep(SPIN_SLEEP);
  }
  asm volatile("" ::: "memory");
}
DI void spin_ge8(unsigned a, unsigned want) { spin_ge4(a, want); spin_ge4(a + 16, want); }
constexpr int CT_S0 = 0, CT_FREE = 80, CT_PAIR = 144  , CT_QUAD = 192  , CT_ZCNT = 240  ,
              CT_QCNT = 256, CT_ZRDY = 304, CT_CVD = 336  , CT_XST = 416, CT_ATT = 448, CT_RED = 480,
              CT_FX = 512  , CT_KR8 = 688, CT_VR8 = 704, CT_WRDY = 720, CT_WRD2 = 736, CT_BYTES = 1024;
DI void sig(unsigned a, unsigned v, int lane) { if (lane == 0) lds_st32(a, v); }

#define POLL_MAX (1 << 15)
DI void publish(char* ws, size_t off, u64 E, int lane) {
  asm volatile("s_waitcnt vmcnt(0)" ::: "memory");
  if (lane == 0) __hip_atomic_store((u64*)(ws + off), E, __ATOMIC_RELAXED, __HIP_MEMORY_SCOPE_AGENT);
}
#define POLL_SLEEP 6
DI void poll(const char* ws, size_t off, u64 E) {
#pragma nounroll
  for (int it = 0; it < POLL_MAX; ++it) {
    const u64 v = __hip_atomic_load((const u64*)(ws + off), __ATOMIC_RELAXED, __HIP_MEMORY_SCOPE_AGENT);
    if (__builtin_amdgcn_ballot_w64(v != E) == 0ull) break;
    __builtin_amdgcn_s_sleep(POLL_SLEEP);
  }
  asm volatile("" ::: "memory");
}

constexpr int K1_STAGE = 32768;
constexpr int A1_K = 0, A1_V = 3072, A1_W = 5120, A1_WAVE = 5632, A1_O = 2 * A1_WAVE, A1_PAIR = A1_O + 4096;
constexpr int L_AUX = 4 * K1_STAGE;
constexpr int L_CT = L_AUX + 2 * A1_PAIR;
constexpr int L_TOTAL = L_CT + CT_BYTES;
constexpr int K1_CT = L_CT;
constexpr int CV_COPY = 4160, CV_H = 4608, CV_REG = CV_H + 24576;
constexpr int AT_Q = L_AUX + 12288, AT_K = 64000, AT_V = AT_K + 16 * 2048, AT_END = AT_V + 16 * 1344;
constexpr int p3_w(int t) { return t < 5 ? t * 12800 : (t == 5 ? AT_END : AT_K + (t - 6) * 12800); }

#define PSYNC() do { asm volatile("s_waitcnt lgkmcnt(0)" ::: "memory"); ++ps; sig(pf + wh * 4, ps, lane); spin_ge(pf + (wh ^ 1) * 4, ps); } while (0)
DI void a1_issue(unsigned lds, int tile, int lane, int wh, const u16* ak, const u16* av, const u16* shk) {
  const int ts = tile & 63, td = tile >> 6;
  const int s0 = ts * 32 + wh * 16, d0 = td * 64;
  const int lr = lane >> 3, lc = (lane & 7) * 16;
  const unsigned wl = lds + wh * A1_WAVE;
#pragma unroll
  for (int i = 0; i < 3; ++i) {
    int r = s0 - 3 + i * 8 + lr; r = r < 0 ? 0 : r;
    if (i < 2 || lane < 24) glds16nt((unsigned)(r * DM * 2 + lc), (const char*)ak + d0 * 2, wl + A1_K + i * 1024);
  }
#pragma unroll
  for (int i = 0; i < 2; ++i)
    glds16nt((unsigned)(lr * DM * 2 + lc), (const char*)av + ((size_t)(s0 + i * 8) * DM + d0) * 2, wl + A1_V + i * 1024);
  if (lane < 32) glds16((unsigned)(lane * 16), (const char*)shk + d0 * 8, wl + A1_W);
}
DI void a1_finish(unsigned lds, unsigned pf, unsigned& ps, int tile, int lane, int wh, char* ws, u64 E) {
  const int ts = tile & 63, td = tile >> 6;
  const int c = lane & 7;
  const unsigned wl = lds + wh * A1_WAVE;
  u32x4 wt[4];
#pragma unroll
  for (int i = 0; i < 4; ++i) wt[i] = __builtin_bit_cast(u32x4, lds_read16(wl + A1_W + c * 64 + i * 16));
#pragma unroll
  for (int j = 0; j < 2; ++j) {
    const int r = j * 8 + (lane >> 3);
    const unsigned ka = wl + A1_K + (r + 3) * 128 + c * 16;
    u32x4 k0 = __builtin_bit_cast(u32x4, lds_read16(ka)), k1 = __builtin_bit_cast(u32x4, lds_read16(ka - 128));
    u32x4 k2 = __builtin_bit_cast(u32x4, lds_read16(ka - 256)), k3 = __builtin_bit_cast(u32x4, lds_read16(ka - 384));
    const u32x4 vv = __builtin_bit_cast(u32x4, lds_read16(wl + A1_V + r * 128 + c * 16));
    if (j == 0 && ts == 0 && wh == 0) {
      const u32x4 z = {0u, 0u, 0u, 0u};
      if (r < 1) k1 = z;
      if (r < 2) k2 = z;
      if (r < 3) k3 = z;
    }
    u32x4 o;
#pragma unroll
    for (int i = 0; i < 4; ++i) {
      const unsigned pa0 = __builtin_amdgcn_perm(k1[i], k0[i], 0x05040100u), pb0 = __builtin_amdgcn_perm(k3[i], k2[i], 0x05040100u);
      const unsigned pa1 = __builtin_amdgcn_perm(k1[i], k0[i], 0x07060302u), pb1 = __builtin_amdgcn_perm(k3[i], k2[i], 0x07060302u);
      const float e0 = dot2bf(pb0, wt[i][1], dot2bf(pa0, wt[i][0], 0.f));
      const float e1 = dot2bf(pb1, wt[i][3], dot2bf(pa1, wt[i][2], 0.f));
      o[i] = pk_bf16(e0 * bf_lo(vv[i]), e1 * bf_hi(vv[i]));
    }
    lds_write16(lds + A1_O + (31 - wh * 16 - r) * 128 + c * 16, o);
  }
  PSYNC();
  const int gg = lane >> 4, q = (lane & 15) >> 2, p = lane & 3;
  u16* ut = (u16*)(ws + WS_UT);
#pragma unroll
  for (int j = 0; j < 2; ++j) {
    const unsigned ra = lds + A1_O + (gg * 8 + q) * 128 + wh * 64 + j * 32 + p * 8;
    const s16x4 t0 = tr_read(ra), t1 = tr_read(ra + 512);
    const int d = td * 64 + wh * 32 + j * 16 + (lane & 15);
    gstore16c(ut + (size_t)d * SEQ + (SEQ - 32 - ts * 32) + gg * 8, __builtin_bit_cast(u32x4, cat8(t0, t1)));
  }
}
DI void a1_publish(int tile, int lane, int wh, char* ws, u64 E) {
  const int ts = tile & 63, td = tile >> 6;
  publish(ws, FL_U + (size_t)((td * 2 + wh) * 64 + ts) * 8, E, lane);
}

template <bool VT>
DI void k1_gemm(unsigned lds0, int m0, int n0, int tid, char* ws, unsigned mkx, Diag& dg) {
  const int lane = tid & 63, wave = __builtin_amdgcn_readfirstlane(tid >> 6);
  const int wm = wave >> 1, wn = wave & 1;
  const int l32 = lane & 31, hh = lane >> 5, gg = lane >> 4, q = (lane & 15) >> 2, p = lane & 3;
  const unsigned ct = lds0 + K1_CT;
  f32x16 acc[2][2];
#pragma unroll
  for (int i = 0; i < 2; ++i)
#pragma unroll
    for (int j = 0; j < 2; ++j)
#pragma unroll
      for (int r = 0; r < 16; ++r) acc[i][j][r] = 0.f;
  unsigned xb[4];
#pragma unroll
  for (int ks = 0; ks < 4; ++ks) xb[ks] = lds0 + (wm * 64 + l32) * 128 + (((unsigned)(2 * ks + hh) ^ (unsigned)((l32 >> 1) & 7)) * 16);
  unsigned wb[2];
#pragma unroll
  for (int nt = 0; nt < 2; ++nt)
    wb[nt] = lds0 + 16384 + (8 * hh + q) * 256 + (((unsigned)(wn * 4 + 2 * nt + (gg & 1)) ^ (unsigned)(q << 1)) * 32) + p * 8;
  s16x8 x0[2], w0[2], x1[2], w1[2], x2[2], w2[2], x3[2], w3[2];
#define K1_LOADF(XF, WF, bo_, ks) do { \
    _Pragma("unroll") for (int i_ = 0; i_ < 2; ++i_) { \
      WF[i_] = cat8(tr_read(wb[i_] + (bo_) + (ks) * 4096), tr_read(wb[i_] + (bo_) + (ks) * 4096 + 1024)); \
      XF[i_] = lds_read16(xb[ks] + (bo_) + i_ * 4096); } } while (0)
#define K1_MM(XF, WF) do { _Pragma("unroll") for (int mt = 0; mt < 2; ++mt) _Pragma("unroll") for (int nt = 0; nt < 2; ++nt) \
      acc[mt][nt] = VT ? MFMA32(XF[mt], WF[nt], acc[mt][nt]) : MFMA32(WF[nt], XF[mt], acc[mt][nt]); } while (0)
#define ILV_A 2
#define ILV_B 2
#define ILV_C 2
#define K1_ILV() do { \
      __builtin_amdgcn_sched_group_barrier(0x008, 1, 0); __builtin_amdgcn_sched_group_barrier(0x100, ILV_A, 0); \
      __builtin_amdgcn_sched_group_barrier(0x008, 1, 0); __builtin_amdgcn_sched_group_barrier(0x100, ILV_B, 0); \
      __builtin_amdgcn_sched_group_barrier(0x008, 1, 0); __builtin_amdgcn_sched_group_barrier(0x100, ILV_C, 0); \
      __builtin_amdgcn_sched_group_barrier(0x008, 1, 0); __builtin_amdgcn_sched_group_barrier(0x100, 2, 0); } while (0)
  STAMP(1);
  K1_LOADF(x0, w0, 0, 0);
  K1_LOADF(x1, w1, 0, 1);
#pragma nounroll
  for (int t = 0; t < 10; ++t) {
    const unsigned bo = (t & 3) * K1_STAGE, bn = ((t + 1) & 3) * K1_STAGE;
    SBAR();
    const u32x4 fl4 = *(volatile LAS u32x4*)(uintptr_t)(ct + CT_FX + (t + 1) * 16);
    K1_LOADF(x2, w2, bo, 2); K1_MM(x0, w0); K1_ILV();
    SBAR();
    K1_LOADF(x3, w3, bo, 3); K1_MM(x1, w1); K1_ILV();
    SBAR();
    const unsigned flu = (unsigned)__builtin_amdgcn_readfirstlane(mk_bad(fl4, mkx));
    if (t < 9 && flu != 0u) spin_mk(ct + CT_FX + (t + 1) * 16, mkx);
    asm volatile("" ::: "memory");
    SBAR();
    K1_LOADF(x0, w0, bn, 0); K1_MM(x2, w2); K1_ILV();
    SBAR();
    asm volatile("" : "+v"(x3[0]), "+v"(x3[1]), "+v"(w3[0]), "+v"(w3[1]) :: "memory");
    if (lane == 0) lds_st32(ct + CT_FREE + (t & 3) * 16 + wave * 4, (unsigned)(t + 1));
    SBAR();
    K1_LOADF(x1, w1, bn, 1); K1_MM(x3, w3); K1_ILV();
  }
  SBAR();
  STAMP(2);
  if (!VT) {
    const int part = n0 >= DM ? 1 : 0;
    const float sc = part == 0 ? 0.22360679774997896f * 1.4426950408889634f : 1.0f;
    char* base = ws + (part ? WS_KF : WS_QF) + ((size_t)((m0 + wm * 64) >> 5) * 40 + ((n0 - part * DM + wn * 64) >> 4)) * 1024 + lane * 16;
#pragma unroll
    for (int mt = 0; mt < 2; ++mt)
#pragma unroll
      for (int nt = 0; nt < 2; ++nt)
#pragma unroll
        for (int uh = 0; uh < 2; ++uh) {
          u32x4 o;
#pragma unroll
          for (int i = 0; i < 4; ++i) o[i] = pk_bf16(acc[mt][nt][8 * uh + 2 * i] * sc, acc[mt][nt][8 * uh + 2 * i + 1] * sc);
          gstore16c(base + (mt * 40 + nt * 2 + uh) * 1024, o);
        }
  } else {
    char* base = ws + WS_VF + ((size_t)((m0 + wm * 64) >> 4) * 20 + ((n0 - 2 * DM + wn * 64) >> 5)) * 1024 + lane * 16;
#pragma unroll
    for (int mt = 0; mt < 2; ++mt)
#pragma unroll
      for (int nt = 0; nt < 2; ++nt)
#pragma unroll
        for (int kg = 0; kg < 2; ++kg) {
          u32x4 o;
#pragma unroll
          for (int i = 0; i < 4; ++i) o[i] = pk_bf16(acc[mt][nt][8 * kg + 2 * i], acc[mt][nt][8 * kg + 2 * i + 1]);
          gstore16c(base + ((mt * 2 + kg) * 20 + nt) * 1024, o);
        }
  }
}

DI void cv_put(unsigned y, const u32x4& c, const u32x2& n) {
  const unsigned d0 = c[0], d1 = c[1], d2 = c[2], d3 = c[3], d4 = n[0], d5 = n[1];
  const unsigned a0 = __builtin_amdgcn_alignbit(d1, d0, 16), a1 = __builtin_amdgcn_alignbit(d2, d1, 16),
                 a2 = __builtin_amdgcn_alignbit(d3, d2, 16), a3 = __builtin_amdgcn_alignbit(d4, d3, 16),
                 a4 = __builtin_amdgcn_alignbit(d5, d4, 16);
  lds_write16(y, (u32x4){d0, d1, d2, d3});
  lds_write16(y + CV_COPY, (u32x4){a0, a1, a2, a3});
  lds_write16(y + 2 * CV_COPY, (u32x4){d1, d2, d3, d4});
  lds_write16(y + 3 * CV_COPY, (u32x4){a1, a2, a3, a4});
}

#define QSYNC() do { asm volatile("s_waitcnt lgkmcnt(0)" ::: "memory"); ++qs; sig(qf + wq * 4, qs, lane); spin_ge4(qf, qs); } while (0)
struct HRegs { u32x4 c, u; u32x2 n; unsigned d; };
DI HRegs conv_load_h(int ch, int t256, const u16* __restrict__ ssmk, const u16* __restrict__ ssmd) {
  HRegs r;
  const u16* hrow = ssmk + (size_t)ch * SEQ;
  r.c = *((const u32x4*)(hrow + t256 * 8));
  r.n = *(const u32x2*)(hrow + (t256 < 255 ? t256 * 8 + 8 : 0));
  r.d = ssmd[ch];
  return r;
}
DI void conv_load_u(HRegs& r, int ch, int lane, int wq, const char* __restrict__ ws, u64 E) {
  poll(ws, FL_U + (size_t)((ch >> 5) * 64 + 48 - 16 * wq + (lane & 15)) * 8, E);
  const u64* up = (const u64*)(ws + WS_UT + (size_t)ch * SEQ * 2 + wq * 1024 + lane * 16);
  const u64 a = __hip_atomic_load(up, __ATOMIC_RELAXED, __HIP_MEMORY_SCOPE_AGENT);
  const u64 b = __hip_atomic_load(up + 1, __ATOMIC_RELAXED, __HIP_MEMORY_SCOPE_AGENT);
  r.u = (u32x4){(unsigned)a, (unsigned)(a >> 32), (unsigned)b, (unsigned)(b >> 32)};
}
DI void conv_stage(unsigned reg, int lane, int wq, const HRegs& hr) {
  const int t256 = wq * 64 + lane;
  const unsigned hb = reg + CV_H;
  if (t256 < 32) lds_write16(reg + 4096 + t256 * 16, (u32x4){0u, 0u, 0u, 0u});
  cv_put(hb + (t256 + 2) * 16, hr.c, hr.n);
  if (t256 < 2) {
    const u32x4 z = {0u, 0u, 0u, 0u};
    u32x2 nx = {0u, 0u};
    const unsigned h0 = __builtin_amdgcn_readfirstlane(hr.c[0]), h1 = __builtin_amdgcn_readfirstlane(hr.c[1]);
    if (t256 == 1) nx = (u32x2){h0, h1};
    cv_put(hb + t256 * 16, z, nx);
  }
}
DI void conv_run(unsigned reg, unsigned qf, unsigned& qs, int ch, int lane, int wq, const HRegs& hr,
                 char* __restrict__ ws, unsigned xst, Diag& dg) {
  const unsigned hb = reg + CV_H;
  if (xst) spin_ge4(xst, 1u);
  else { lds_write16(reg + (wq * 64 + lane) * 16, hr.u); QSYNC(); }
  STAMP(6);
  const int r16 = lane & 15, g = lane >> 4, q = r16 >> 2, p = lane & 3;
  const unsigned ab0 = hb + ((q + 1) & 3) * CV_COPY + 2 * (8 * g + 4 * p + (q == 3 ? 4 : 0)) + wq * 128;
  const unsigned bb0 = reg + 2 * (2032 - 16 * r16 + 8 * g) + wq * 128 - 7 * 512;
  f32x4 acc[8];
#pragma unroll
  for (int i = 0; i < 8; ++i) acc[i] = (f32x4){0.f, 0.f, 0.f, 0.f};
#define CV_LDA(a_, ad_) cat8(tr_read((ad_) + (a_) * 512), tr_read((ad_) + (a_) * 512 + 8))
#define CV_LDB(c_, bd_) lds_read16((bd_) + (7 - (c_)) * 512)
  {
    s16x8 A[8], B[8];
    B[0] = CV_LDB(0, bb0);
#pragma unroll
    for (int a = 0; a < 8; ++a) A[a] = CV_LDA(a, ab0);
#pragma unroll
    for (int c = 1; c < 8; ++c) B[c] = CV_LDB(c, bb0);
#pragma unroll
    for (int c = 0; c < 8; ++c) {
      SBAR();
#pragma unroll
      for (int a = 0; a + c < 8; ++a) acc[a + c] = MFMA16(A[a], B[c], acc[a + c]);
      B[c] = CV_LDB(c, bb0 + 64);
      A[7 - c] = CV_LDA(7 - c, ab0 + 64);
    }
    SBAR();
#pragma unroll
    for (int a = 7; a >= 0; --a)
#pragma unroll
      for (int c = 0; a + c < 8; ++c) acc[a + c] = MFMA16(A[a], B[c], acc[a + c]);
  }
  QSYNC();
#pragma unroll
  for (int nt = 0; nt < 8; ++nt) {
    const int dw = nt >> 1;
    if (dw != wq) lds_write16(hb + (nt * 3 + (wq > dw ? wq - 1 : wq)) * 1024 + lane * 16, __builtin_bit_cast(u32x4, acc[nt]));
  }
  f32x4 k0 = wq == 0 ? acc[0] : (wq == 1 ? acc[2] : (wq == 2 ? acc[4] : acc[6]));
  f32x4 k1 = wq == 0 ? acc[1] : (wq == 1 ? acc[3] : (wq == 2 ? acc[5] : acc[7]));
  QSYNC();
  const float dco = bf_lo(hr.d);
  u32x2 zz[2];
#pragma unroll
  for (int i = 0; i < 2; ++i) {
    f32x4 o = i ? k1 : k0;
#pragma unroll
    for (int j = 0; j < 3; ++j) o += __builtin_bit_cast(f32x4, lds_read16(hb + ((wq * 2 + i) * 3 + j) * 1024 + lane * 16));
    const int s = (wq * 2 + i) * 256 + 16 * r16 + 4 * g;
    const u32x2 uu = __builtin_bit_cast(u32x2, lds_read8(reg + 2 * (SEQ - 4 - s)));
    const float kv0 = bf_hi(uu[1]), kv1 = bf_lo(uu[1]), kv2 = bf_hi(uu[0]), kv3 = bf_lo(uu[0]);
    const unsigned y01 = pk_bf16(o[0], o[1]), y23 = pk_bf16(o[2], o[3]);
    const unsigned e01 = pk_bf16(kv0 * dco, kv1 * dco), e23 = pk_bf16(kv2 * dco, kv3 * dco);
    zz[i][0] = pk_bf16(bf_lo(y01) + bf_lo(e01), bf_hi(y01) + bf_hi(e01));
    zz[i][1] = pk_bf16(bf_lo(y23) + bf_lo(e23), bf_hi(y23) + bf_hi(e23));
  }
  const auto sx = __builtin_amdgcn_permlane16_swap(zz[0][0], zz[1][0], false, false);
  const auto sy = __builtin_amdgcn_permlane16_swap(zz[0][1], zz[1][1], false, false);
  u16* zrow = (u16*)(ws + WS_ZT) + (size_t)ch * SEQ;
  gstore16c(zrow + (wq * 2 + (g & 1)) * 256 + 16 * r16 + 8 * (g >> 1), (u32x4){sx[0], sy[0], sx[1], sy[1]});
  asm volatile("s_waitcnt lgkmcnt(0)" ::: "memory");
}
DI void conv_publish(unsigned zc, int ch, int lane, char* __restrict__ ws, u64 E) {
  asm volatile("s_waitcnt vmcnt(0)" ::: "memory");
  if (lane == 0) {
    const unsigned old = __hip_atomic_fetch_add((LAS unsigned*)(uintptr_t)zc, 1u, __ATOMIC_RELAXED, __HIP_MEMORY_SCOPE_WORKGROUP);
    if ((old & 3u) == 3u) __hip_atomic_store((u64*)(ws + FL_Z + (size_t)ch * 8), E, __ATOMIC_RELAXED, __HIP_MEMORY_SCOPE_AGENT);
  }
}

DI s16x8 pack8(const f32x16& v, int o) {
  u32x4 r;
  r[0] = pk_bf16(v[o + 0], v[o + 1]); r[1] = pk_bf16(v[o + 2], v[o + 3]);
  r[2] = pk_bf16(v[o + 4], v[o + 5]); r[3] = pk_bf16(v[o + 6], v[o + 7]);
  return __builtin_bit_cast(s16x8, r);
}
DI float max3f(float a, float b, float c) { return __builtin_fmaxf(__builtin_fmaxf(a, b), c); }
DI float max16(const f32x16& v, float m) {
  float a = max3f(v[0], v[1], m), b = max3f(v[2], v[3], v[4]);
  a = max3f(a, v[5], v[6]); b = max3f(b, v[7], v[8]);
  a = max3f(a, v[9], v[10]); b = max3f(b, v[11], v[12]);
  a = max3f(a, v[13], v[14]);
  return max3f(a, b, v[15]);
}
DI float xhalf_max(float m) {
  auto sw = __builtin_amdgcn_permlane32_swap(__float_as_uint(m), __float_as_uint(m), false, false);
  return __builtin_fmaxf(__uint_as_float(sw[0]), __uint_as_float(sw[1]));
}

#define AT_LMAX 65536.0f
#define P3_WADDR() unsigned wv[4]; _Pragma("unroll") for (int i = 0; i < 4; ++i) { \
      const int c = i < 3 ? (i * 4 + w) * 64 + lane : 768 + w * 8 + (lane & 7); const int row = c / 10; \
      wv[i] = (unsigned)(row * DM * 2 + (c - row * 10) * 16); } const char* wog = (const char*)(wo + n3)
#define P3_STW(t) do { _Pragma("unroll") for (int i_ = 0; i_ < 3; ++i_) \
        glds16(wv[i_], wog + (size_t)(t) * 80 * DM * 2, lds0 + p3_w(t) + (i_ * 4 + w) * 1024); \
      if (lane < 8) glds16(wv[3], wog + (size_t)(t) * 80 * DM * 2, lds0 + p3_w(t) + 12288 + w * 128); } while (0)
DI void attn_stage(unsigned lds0, int h, int qb8, int j, int lane, char* __restrict__ ws, u64 E, int zch, const Mk& mk, Diag& dg) {
  const int qt = qb8 * 8 + j;
  const unsigned l16 = (unsigned)(lane * 16), ct = lds0 + L_CT;
  const int qa = (5 * h) >> 2;
  const bool stg = qb8 > 0 || j >= 2;
  {
    const int l3 = lane & 7, hi = lane & 1;
    const bool kv = stg && l3 >= 2 && l3 < 6;
    const int rb = kv ? 2 * qb8 - 2 + j : 2 * qb8 + (l3 >= 6 ? 1 : 0);
    const int col = !kv ? qa * 16 + hi * 31 : (l3 < 4 ? DM + qa * 16 + hi * 31 : 2 * DM + h * 20 + hi * 19);
    poll(ws, FL_Q + (size_t)(rb * 16 + (col >> 7)) * 8, E);
  }
  STAMP(9);
  conv_publish(ct + CT_ZCNT + 8, zch, lane, ws, E);
  const int kt0 = qb8 * 8 - 8 + 4 * j;
#pragma unroll
  for (int i = 0; i < 4; ++i)
    glds16c(l16, ws + WS_QF + ((size_t)(qt + (i >> 1) * 4) * 40 + qa + (i & 1)) * 1024, lds0 + AT_Q + ((j + (i >> 1) * 4) * 2 + (i & 1)) * 1024);
  if (stg) {
#pragma unroll
    for (int i = 0; i < 8; ++i)
      glds16c(l16, ws + WS_KF + ((size_t)(kt0 + (i >> 1)) * 40 + qa + (i & 1)) * 1024, lds0 + AT_K + (j * 8 + i) * 1024);
  }
  glds_mark(mk, ct + CT_KR8 + j * 4, lane);
  if (stg) {
#pragma unroll
    for (int i = 0; i < 6; ++i) {
      const int c = i * 64 + lane;
      const int kgp = (c * 1561) >> 16, rem = c - kgp * 42, vh = rem >= 21 ? 1 : 0, d = rem - vh * 21;
      const int n = h * 20 + d;
      if (c < 336 && d == 20) lds_write16(lds0 + AT_V + j * 5376 + c * 16, (u32x4){0x3f803f80u, 0x3f803f80u, 0x3f803f80u, 0x3f803f80u});
      if (c < 336 && d < 20)
        glds16c((unsigned)((kgp * 20 + (n >> 5)) * 1024 + (vh * 32 + (n & 31)) * 16), ws + WS_VF + ((size_t)(kt0 * 2) * 20) * 1024,
                lds0 + AT_V + j * 5376 + i * 1024);
    }
    asm volatile("s_waitcnt lgkmcnt(0)" ::: "memory");
  }
  glds_mark(mk, ct + CT_VR8 + j * 4, lane);
}
template <bool FULL>
DI void attn_task(unsigned lds0, int h, int qb8, int j, int lane, char* __restrict__ ws, u64 E, int zch, const Mk& mk, Diag& dg) {
  const int l32 = lane & 31, hh = lane >> 5;
  const int qt = qb8 * 8 + j;
  const int tfirst = FULL ? 0 : 8 - qt;
  const unsigned l16 = (unsigned)(lane * 16), ct = lds0 + L_CT;
  const int qa = (5 * h) >> 2;
  u32x4 q0r, q1r;
  spin_mk(ct + CT_KR8, mk.x);
  if (!FULL) spin_mk(ct + CT_VR8, mk.x);
  q0r = __builtin_bit_cast(u32x4, lds_read16(lds0 + AT_Q + j * 2048 + l16));
  q1r = __builtin_bit_cast(u32x4, lds_read16(lds0 + AT_Q + j * 2048 + 1024 + l16));
  asm volatile("s_waitcnt lgkmcnt(0)" : "+v"(q0r), "+v"(q1r) :: "memory");
  if (zch >= 0) conv_publish(ct + CT_ZCNT + (j >> 2) * 4, zch, lane, ws, E);
  {
    const int g0 = 4 * qa + hh - 5 * h;
    const bool k0 = g0 >= 0, k1 = g0 + 2 >= 0 && g0 + 2 < 5, k2 = g0 + 4 < 5, k3 = g0 + 6 < 5;
    q0r[0] = k0 ? q0r[0] : 0u; q0r[1] = k0 ? q0r[1] : 0u; q0r[2] = k1 ? q0r[2] : 0u; q0r[3] = k1 ? q0r[3] : 0u;
    q1r[0] = k2 ? q1r[0] : 0u; q1r[1] = k2 ? q1r[1] : 0u; q1r[2] = k3 ? q1r[2] : 0u; q1r[3] = k3 ? q1r[3] : 0u;
  }
  s16x8 qlo = __builtin_bit_cast(s16x8, q0r), qhi8 = __builtin_bit_cast(s16x8, q1r);
  STAMP(8);
  const f32x16 zero16 = {0.f, 0.f, 0.f, 0.f, 0.f, 0.f, 0.f, 0.f, 0.f, 0.f, 0.f, 0.f, 0.f, 0.f, 0.f, 0.f};
  const float ninf = -INFINITY;
  const f32x16 ninf16 = {ninf, ninf, ninf, ninf, ninf, ninf, ninf, ninf, ninf, ninf, ninf, ninf, ninf, ninf, ninf, ninf};
  const unsigned kb = lds0 + AT_K + j * 2048 + l16;
#define AT_QK(dst, t, cin) do { if (FULL || (t) >= tfirst) { dst = MFMA32(lds_read16(kb + (t) * 2048), qlo, cin); \
    dst = MFMA32(lds_read16(kb + (t) * 2048 + 1024), qhi8, dst); } else dst = ninf16; } while (0)
  const int vrow = l32 <= 20 ? l32 : (l32 == 24 ? 20 : l32 - 16);
  const unsigned vb = lds0 + AT_V + j * 1344 + hh * 336 + vrow * 16;
#define AT_EXP(sv) do { _Pragma("unroll") for (int r = 0; r < 16; ++r) sv[r] = __builtin_amdgcn_exp2f(sv[r] - m); } while (0)
#define AT_PV(sv, t) do { oacc = MFMA32(lds_read16(vb + (t) * 1344), pack8(sv, 0), oacc); oacc = MFMA32(lds_read16(vb + (t) * 1344 + 672), pack8(sv, 8), oacc); } while (0)
  f32x16 oacc;
  bool exact = !FULL;
  if (FULL) {
    f32x16 sm, sa, sb;
    AT_QK(sm, 8, zero16); AT_QK(sa, 0, zero16);
    const int tq = l32 - 4 * hh;
    const unsigned wm = tq < 0 ? 0xffffffffu : (0xfffffffeu << tq);
    unsigned msk[16];
#pragma unroll
    for (int r = 0; r < 16; ++r) {
      msk[r] = (unsigned)__builtin_amdgcn_sbfe((int)wm, (r & 3) + 8 * (r >> 2), 1);
      sm[r] = __uint_as_float((msk[r] & __float_as_uint(sa[r])) | (~msk[r] & __float_as_uint(sm[r])));
    }
    const float m = xhalf_max(max16(sm, ninf));
    const float nm = -m;
    const f32x16 cneg = {nm, nm, nm, nm, nm, nm, nm, nm, nm, nm, nm, nm, nm, nm, nm, nm};
    AT_QK(sa, 1, cneg);
    AT_EXP(sm);
    u32x4 pl = __builtin_bit_cast(u32x4, pack8(sm, 0)), ph = __builtin_bit_cast(u32x4, pack8(sm, 8)), ql, qh;
#pragma unroll
    for (int i = 0; i < 4; ++i) {
      ql[i] = pl[i] & ((msk[2 * i] & 0xffffu) | (msk[2 * i + 1] & 0xffff0000u)); pl[i] ^= ql[i];
      qh[i] = ph[i] & ((msk[8 + 2 * i] & 0xffffu) | (msk[9 + 2 * i] & 0xffff0000u)); ph[i] ^= qh[i];
    }
    spin_mk(ct + CT_VR8, mk.x);
    STAMP(9);
    oacc = MFMA32(lds_read16(vb + 8 * 1344), __builtin_bit_cast(s16x8, pl), zero16);
    oacc = MFMA32(lds_read16(vb + 8 * 1344 + 672), __builtin_bit_cast(s16x8, ph), oacc);
    oacc = MFMA32(lds_read16(vb), __builtin_bit_cast(s16x8, ql), oacc);
    oacc = MFMA32(lds_read16(vb + 672), __builtin_bit_cast(s16x8, qh), oacc);
#define AT_EX0(sv) do { _Pragma("unroll") for (int r = 0; r < 16; ++r) sv[r] = __builtin_amdgcn_exp2f(sv[r]); } while (0)
    AT_QK(sb, 2, cneg); AT_EX0(sa); AT_PV(sa, 1);
    AT_QK(sa, 3, cneg); AT_EX0(sb); AT_PV(sb, 2);
    AT_QK(sb, 4, cneg); AT_EX0(sa); AT_PV(sa, 3);
    AT_QK(sa, 5, cneg); AT_EX0(sb); AT_PV(sb, 4);
    AT_QK(sb, 6, cneg); AT_EX0(sa); AT_PV(sa, 5);
    AT_QK(sa, 7, cneg); AT_EX0(sb); AT_PV(sb, 6);
    AT_EX0(sa); AT_PV(sa, 7);
    float big = max3f(__builtin_fabsf(oacc[0]), __builtin_fabsf(oacc[1]), __builtin_fabsf(oacc[2]));
    big = max3f(big, __builtin_fabsf(oacc[3]), __builtin_fabsf(oacc[4]));
    big = max3f(big, __builtin_fabsf(oacc[5]), __builtin_fabsf(oacc[6]));
    big = max3f(big, __builtin_fabsf(oacc[7]), __builtin_fabsf(oacc[8]));
    big = max3f(big, __builtin_fabsf(oacc[9]), __builtin_fabsf(oacc[10]));
    big = __builtin_fmaxf(big, __builtin_fabsf(oacc[11]));
    const float ls = hh ? oacc[8] : oacc[12];
    float osum = 0.f;
#pragma unroll
    for (int r = 0; r < 8; ++r) osum += oacc[r];
    const bool ok = (ls < AT_LMAX) && (big < 1e37f) && (osum == osum);
    exact = __builtin_amdgcn_ballot_w64(!ok) != 0ull;
  }
  if (exact) {
    asm volatile("" : "+v"(qlo), "+v"(qhi8) :: "memory");
    f32x16 sm, s1, s2, s3;
    {
      f32x16 s0;
      AT_QK(sm, 8, zero16); AT_QK(s0, 0, zero16);
      AT_QK(s1, 1, zero16); AT_QK(s2, 2, zero16); AT_QK(s3, 3, zero16);
#pragma unroll
      for (int r = 0; r < 16; ++r) {
        const int kk = (r & 3) + 8 * (r >> 2) + 4 * hh;
        sm[r] = kk > l32 ? s0[r] : sm[r];
      }
    }
    float m = max16(sm, ninf);
    m = max16(s1, m); m = max16(s2, m); m = max16(s3, m);
    m = xhalf_max(m);
    AT_EXP(sm);
    oacc = zero16;
    {
      f32x16 p0, p8;
#pragma unroll
      for (int r = 0; r < 16; ++r) {
        const int kk = (r & 3) + 8 * (r >> 2) + 4 * hh;
        p0[r] = kk > l32 ? sm[r] : 0.f;
        p8[r] = kk > l32 ? 0.f : sm[r];
      }
      AT_PV(p8, 8);
      if (FULL) AT_PV(p0, 0);
    }
    if (FULL || 1 >= tfirst) { AT_EXP(s1); AT_PV(s1, 1); }
    AT_QK(sm, 4, zero16); AT_QK(s1, 5, zero16);
    if (FULL || 2 >= tfirst) { AT_EXP(s2); AT_PV(s2, 2); }
    AT_QK(s2, 6, zero16);
    if (FULL || 3 >= tfirst) { AT_EXP(s3); AT_PV(s3, 3); }
    AT_QK(s3, 7, zero16);
    float m2 = max16(sm, m);
    m2 = max16(s1, m2); m2 = max16(s2, m2); m2 = max16(s3, m2);
    m2 = xhalf_max(m2);
    {
      const float alpha = __builtin_amdgcn_exp2f(m - m2);
#pragma unroll
      for (int r = 0; r < 13; ++r) oacc[r] *= alpha;
      m = m2;
    }
    if (FULL || 4 >= tfirst) { AT_EXP(sm); AT_PV(sm, 4); }
    if (FULL || 5 >= tfirst) { AT_EXP(s1); AT_PV(s1, 5); }
    if (FULL || 6 >= tfirst) { AT_EXP(s2); AT_PV(s2, 6); }
    if (FULL || 7 >= tfirst) { AT_EXP(s3); AT_PV(s3, 7); }
  }
  asm volatile("s_waitcnt lgkmcnt(0)" ::: "memory");
  sig(ct + CT_ATT + j * 4, 1u, lane);
  STAMP(10);
  const float l = hh ? oacc[8] : oacc[12];
  const float inv = __builtin_amdgcn_rcpf(l);
  char* oc = ws + WS_OW + ((size_t)qt * NH + h) * 1280;
  u32x4 o;
  o[0] = pk_bf16(oacc[0] * inv, oacc[1] * inv); o[1] = pk_bf16(oacc[2] * inv, oacc[3] * inv);
  o[2] = pk_bf16(oacc[4] * inv, oacc[5] * inv); o[3] = pk_bf16(oacc[6] * inv, oacc[7] * inv);
  gstore16c(oc + lane * 16, o);
  if (!hh) {
    u32x2 o2;
    o2[0] = pk_bf16(oacc[8] * inv, oacc[9] * inv); o2[1] = pk_bf16(oacc[10] * inv, oacc[11] * inv);
    gstore8c(oc + 1024 + l32 * 8, o2);
  }
  publish(ws, FL_O + (size_t)(qt * NH + h) * 8, E, lane);
}

#define HDELAY 0
#define APRIO 1
#define LPRIO 0
#define MPRIO 2
#define ROTG 2
#define A1_DELAY 8
__global__ __launch_bounds__(768, 3) void mega(const u16* __restrict__ x, const u16* __restrict__ wqkv,
                                               const u16* __restrict__ ak, const u16* __restrict__ av,
                                               const u16* __restrict__ shk, const u16* __restrict__ wo,
                                               const u16* __restrict__ ssmk, const u16* __restrict__ ssmd,
                                               const u16* __restrict__ aq, char* __restrict__ ws,
                                               u16* __restrict__ out) {
  extern __shared__ __attribute__((aligned(16))) char smem[];
  const unsigned lds0 = lds_addr(smem);
  const int tid = threadIdx.x, wave = __builtin_amdgcn_readfirstlane(tid >> 6);
  int lane = tid & 63;
#define RELANE() asm volatile("" : "+v"(lane))
  const int role = wave >> 2, w = wave & 3;
  const int bid = blockIdx.x, xcd = bid & 7, idx = bid >> 3;
  const bool has_gemm = idx < 30;
  const unsigned ct = lds0 + L_CT;
  DIAG_INIT();
  u64 E;
  Mk mk; mk.src = wqkv;
  asm volatile("s_load_dwordx2 %0, %2, 0x0\n\ts_load_dword %1, %3, 0x0" : "=&s"(E), "=&s"(mk.x) : "s"(ws + WS_FL), "s"(wqkv) : "memory");
  const int gt = (xcd & 1) * 30 + idx;
  const int tm = (xcd >> 1) * 4 + (gt & 3), tn = gt >> 2;
  const int m0 = tm * 128, n0 = tn * 128;
  const int rot = ROTG >= 10 ? (((gt & 3) * 5 + 1) / 2 + 3 * tn) % 10 : ((((gt & 3) + tn) % ROTG) * 10) / ROTG;
  const unsigned xo0 = (unsigned)((lane >> 3) * DM * 2 + (((lane & 7) ^ (lane >> 4)) * 16));
  const unsigned xo1 = (unsigned)((lane >> 3) * DM * 2 + (((lane & 7) ^ (4 + (lane >> 4))) * 16));
  const unsigned wof = (unsigned)((lane >> 4) * NQKV * 2 + (((lane & 15) ^ ((lane >> 4) << 2)) * 16));
  const char* xg = (const char*)(x + (size_t)m0 * DM);
  const char* wg = (const char*)(wqkv + n0);
#define K1_PIECE_X(t, kt, j) glds16p(((j) & 1) ? xo1 : xo0, xg + (size_t)(j) * 8 * DM * 2 + (kt) * 128, lds0 + ((t) & 3) * K1_STAGE + (j) * 1024)
#define K1_PIECE_W(t, kt, j) glds16p(wof, wg + (size_t)((kt) * 64 + (j) * 4) * NQKV * 2, lds0 + ((t) & 3) * K1_STAGE + 16384 + (j) * 1024)
  if (has_gemm && role == 0) {
#pragma unroll
    for (int i = 0; i < 4; ++i) { K1_PIECE_X(0, rot, w * 4 + i); K1_PIECE_W(0, rot, w * 4 + i); }
  }
  const unsigned pf = ct + CT_PAIR + (wave >> 1) * 8;
  const int wh = wave & 1;
  unsigned ps = 0;
  const int fb = (idx - 30) * 8 + xcd;
  const int a1t = has_gemm ? (xcd * 30 + idx) * 2 + (w >> 1) : 480 + fb * 10 + (wave >> 1);
  const unsigned a1l = has_gemm ? lds0 + L_AUX + (w >> 1) * A1_PAIR : lds0 + (wave >> 1) * A1_PAIR;
  if (!has_gemm || role == 2) a1_issue(a1l, a1t, lane, wh, ak, av, shk);
  const bool cv0 = role != 0 || !has_gemm || bid < 112;
  const int ch0 = role == 0 ? (has_gemm ? 528 + bid : 512 + fb) : bid + (role == 1 ? 256 : 0);
  asm volatile("s_waitcnt lgkmcnt(0)" : "+s"(E), "+s"(mk.x) :: "memory");
  if (role == 1) lds_st32(ct + (tid - 256) * 4, (tid - 256) * 4 >= CT_FX ? ~mk.x : 0u);
  bar();
  E += 1ull;

  const bool ex = has_gemm && role == 1 && bid < 112;
  HRegs hr, h2;
#define LOAD_H() do { hr = conv_load_h(ch0, w * 64 + lane, ssmk, ssmd); \
    if (ex) h2 = conv_load_h(528 + bid, w * 64 + lane, ssmk, ssmd); } while (0)
  auto q_publish = [&]() {
    asm volatile("s_waitcnt vmcnt(0)" ::: "memory");
    if (lane == 0) {
      const unsigned old = __hip_atomic_fetch_add((LAS unsigned*)(uintptr_t)(ct + CT_QCNT), 1u, __ATOMIC_RELAXED, __HIP_MEMORY_SCOPE_WORKGROUP);
      if (old == 3u) __hip_atomic_store((u64*)(ws + FL_Q + (size_t)(tm * 16 + tn) * 8), E, __ATOMIC_RELAXED, __HIP_MEMORY_SCOPE_AGENT);
    }
    STAMP(4);
  };
  if (!has_gemm) {
    LOAD_H();
    const unsigned a2l = lds0 + (6 + (wave >> 1)) * A1_PAIR;
    if (wave < 8) a1_issue(a2l, a1t + 6, lane, wh, ak, av, shk);
    if (wave < 8) wait_vm<6>(); else wait_vm<0>();
    a1_finish(a1l, pf, ps, a1t, lane, wh, ws, E);
    if (wave < 8) { wait_vm<2>(); a1_finish(a2l, pf, ps, a1t + 6, lane, wh, ws, E); }
    a1_publish(a1t, lane, wh, ws, E);
    if (wave < 8) a1_publish(a1t + 6, lane, wh, ws, E);
    bar();
    STAMP(1);
  } else if (role == 0) {
    wait_vm<0>();
    sig(ct + CT_S0 + w * 4, 1u, lane);
    spin_ge4(ct + CT_S0, 1u);
    __builtin_amdgcn_s_setprio(MPRIO);
    if (tn >= 10) k1_gemm<true>(lds0, m0, n0, tid, ws, mk.x, dg);
    else k1_gemm<false>(lds0, m0, n0, tid, ws, mk.x, dg);
    STAMP(3);
  } else if (role == 1) {
    __builtin_amdgcn_s_setprio(LPRIO);
#define K1_ST(t) do { const int kt_ = (t) + rot >= 10 ? (t) + rot - 10 : (t) + rot; \
      if ((t) >= 4) spin_ge4(ct + CT_FREE + ((t) & 3) * 16, (unsigned)((t) - 3)); \
      _Pragma("unroll") for (int i_ = 0; i_ < 4; ++i_) { K1_PIECE_X(t, kt_, w * 4 + i_); K1_PIECE_W(t, kt_, w * 4 + i_); } } while (0)
#pragma nounroll
    for (int t = 1; t < 4; ++t) { K1_ST(t); glds_mark(mk, ct + CT_FX + t * 16 + w * 4, lane); }
    LOAD_H();
#pragma nounroll
    for (int t = 4; t < 10; ++t) { K1_ST(t); glds_mark(mk, ct + CT_FX + t * 16 + w * 4, lane); }
    __builtin_amdgcn_s_setprio(0);
    STAMP(1);
  } else {
    LOAD_H();
    __builtin_amdgcn_s_setprio(2);
    wait_vm<0>();
    a1_finish(a1l, pf, ps, a1t, lane, wh, ws, E);
    a1_publish(a1t, lane, wh, ws, E);
    __builtin_amdgcn_s_setprio(0);
    STAMP(1);
  }

  RELANE();
  unsigned qs = 0;
  const unsigned qf = ct + CT_QUAD + role * 16;
  const unsigned creg = lds0 + (role == 2 ? L_AUX : (role == 1 ? 29184 : 0));
  if (role == 2) __builtin_amdgcn_s_setprio(APRIO); else __builtin_amdgcn_s_setprio(0);
  if (has_gemm && role == 0) {
    q_publish();
    if (cv0) {
      hr.d = ssmd[ch0];
      conv_run(creg, qf, qs, ch0, lane, w, hr, ws, ct + CT_XST, dg);
    }
  } else {
    if (has_gemm && role == 1) {
      conv_load_u(hr, ch0, lane, w, ws, E);
      if (ex) conv_load_u(h2, 528 + bid, lane, w, ws, E);
      spin_ge4(ct + CT_FREE + 16, 10u);
      conv_stage(creg, lane, w, hr);
      if (ex) {
        conv_stage(lds0, lane, w, h2);
        lds_write16(lds0 + (w * 64 + lane) * 16, h2.u);
        asm volatile("s_waitcnt lgkmcnt(0)" ::: "memory");
        sig(ct + CT_XST + w * 4, 1u, lane);
      }
    } else {
      if (has_gemm) { const int wq = w; QSYNC(); }
      conv_stage(creg, lane, w, hr);
      conv_load_u(hr, ch0, lane, w, ws, E);
    }
    STAMP(5);
    conv_run(creg, qf, qs, ch0, lane, w, hr, ws, 0u, dg);
  }
  sig(ct + CT_CVD + wave * 4, 1u, lane);
  if (role != 0) STAMP(2);

  const int tm3 = xcd * 4 + (idx >> 3), tn3 = idx & 7;
  const int m3 = tm3 * 64, n3 = tn3 * 80;
  RELANE();

  if (role == 2) {
    const int r16 = lane & 15, g = lane >> 4, q = r16 >> 2, p = lane & 3;
    __builtin_amdgcn_s_setprio(3);
    spin_ge4(ct + CT_CVD + 32, 1u);
    if (has_gemm) spin_ge4(ct + CT_FREE + 16, 10u);
    attn_stage(lds0, xcd * 4 + (idx & 3), idx >> 2, w, lane, ws, E, ch0, mk, dg);
    {
      P3_WADDR();
      P3_STW(5);
      spin_ge8(ct + CT_CVD, 1u);
      P3_STW(0); P3_STW(4); P3_STW(1); P3_STW(2); P3_STW(3);
      glds_mark(mk, ct + CT_WRDY + w * 4, lane);
      STAMP(3);
    }
    __builtin_amdgcn_s_setprio(0);
    u32x4 qv[3];
#pragma unroll
    for (int j = 0; j < 3; ++j) {
      const int dc = j < 2 ? j * 4 + g : 8 + (g & 1);
      qv[j] = *((const u32x4*)(aq + (size_t)(m3 + w * 16 + r16) * DM + n3 + dc * 8));
    }
    {
      const int l5 = lane & 31;
      const int cl = l5 < 8 ? w * 8 + l5 : (l5 < 16 ? 32 + w * 8 + (l5 - 8) : 64 + w * 4 + (l5 & 3));
      poll(ws, FL_Z + (size_t)(n3 + cl) * 8, E);
    }
    {
      const char* zg = ws + WS_ZT + ((size_t)n3 * SEQ + m3) * 2;
      const unsigned zo = (unsigned)((lane >> 3) * SEQ * 2 + (lane & 7) * 16);
      glds16c(zo, zg + (size_t)(w * 8) * SEQ * 2, lds0 + L_AUX + w * 1024);
      glds16c(zo, zg + (size_t)(32 + w * 8) * SEQ * 2, lds0 + L_AUX + 4096 + w * 1024);
      if (lane < 32) glds16c(zo, zg + (size_t)(64 + w * 4) * SEQ * 2, lds0 + L_AUX + 8192 + w * 512);
    }
    wait_vm<0>();
    sig(ct + CT_ZRDY + w * 4, 1u, lane);
    spin_ge4(ct + CT_ZRDY, 1u);
    STAMP(4);
    u32x4 zz[3];
#pragma unroll
    for (int j = 0; j < 3; ++j) {
      const int dc = j < 2 ? j * 4 + g : 8 + (g & 1);
      const unsigned za = lds0 + L_AUX + (dc * 8 + q) * 128 + w * 32 + p * 8;
      zz[j] = __builtin_bit_cast(u32x4, cat8(tr_read(za), tr_read(za + 512)));
    }
#pragma unroll
    for (int j = 0; j < 3; ++j) {
      const int dc = j < 2 ? j * 4 + g : 8 + (g & 1);
      u32x4 o;
#pragma unroll
      for (int i = 0; i < 4; ++i) o[i] = pk_bf16(bf_lo(qv[j][i]) * bf_lo(zz[j][i]), bf_hi(qv[j][i]) * bf_hi(zz[j][i]));
      if (j < 2 || g < 2) ostore16(out + (size_t)(m3 + w * 16 + r16) * (2 * DM) + n3 + dc * 8, o);
    }
    STAMP(7);
    if (bid == 0 && w == 0) {
#pragma unroll
      for (int i = 0; i < 4; ++i) poll(ws, FL_O + (size_t)(((i * 2 + (lane >> 5)) * 8) * NH + (lane & 31)) * 8, E);
      if (lane == 0) __hip_atomic_store((u64*)(ws + WS_FL), E, __ATOMIC_RELAXED, __HIP_MEMORY_SCOPE_AGENT);
    }
    DIAG_DUMP(2);
    return;
  }

  const int h = xcd * 4 + (idx & 3), qb8 = idx >> 2;
  __builtin_amdgcn_s_setprio(1);
  RELANE();
  if (qb8 > 0) attn_task<true>(lds0, h, qb8, wave, lane, ws, E, cv0 ? ch0 : -1, mk, dg);
  else attn_task<false>(lds0, h, qb8, wave, lane, ws, E, cv0 ? ch0 : -1, mk, dg);
  STAMP(11);

  RELANE();
  const int r16 = lane & 15, g = lane >> 4, q = r16 >> 2, p = lane & 3;
  f32x4 acc[5];
#pragma unroll
  for (int i = 0; i < 5; ++i) acc[i] = (f32x4){0.f, 0.f, 0.f, 0.f};
  const unsigned wbp = lds0 + (20 * (g >> 1) + 4 * (g & 1) + q) * 160 + p * 8;
  const unsigned wbs = lds0 + (20 * g + 16 + q) * 160 + p * 8;
  u32x4 op[8]; u32x2 osg[4];
  if (role == 1) {
    P3_WADDR();
    spin_ge8(ct + CT_ATT, 1u);
    P3_STW(6); P3_STW(7);
    glds_mark(mk, ct + CT_WRD2 + w * 4, lane);
  }
  spin_mk(ct + CT_WRDY, mk.x);
  const unsigned w0 = role ? p3_w(4) : p3_w(0), w1 = role ? p3_w(5) : p3_w(1), w2 = role ? p3_w(6) : p3_w(2), w3 = role ? p3_w(7) : p3_w(3);
  struct Fw { s16x8 a[5], b[5]; s16x4 s[5]; };
  Fw fwa, fwb;
#define P3_LOADW(F, bo_) do { _Pragma("unroll") for (int nt = 0; nt < 5; ++nt) { \
      F.a[nt] = cat8(tr_read(wbp + (bo_) + nt * 32), tr_read(wbp + (bo_) + 8 * 160 + nt * 32)); \
      F.b[nt] = cat8(tr_read(wbp + (bo_) + 40 * 160 + nt * 32), tr_read(wbp + (bo_) + 48 * 160 + nt * 32)); \
      F.s[nt] = tr_read(wbs + (bo_) + nt * 32); } } while (0)
#define P3_MM(F, t) do { \
    _Pragma("unroll") for (int nt = 0; nt < 5; ++nt) acc[nt] = MFMA16(F.a[nt], __builtin_bit_cast(s16x8, op[2 * (t)]), acc[nt]); \
    _Pragma("unroll") for (int nt = 0; nt < 5; ++nt) acc[nt] = MFMA16(F.b[nt], __builtin_bit_cast(s16x8, op[2 * (t) + 1]), acc[nt]); } while (0)
#define P3_MS(t) do { const s16x8 os_ = cat8(__builtin_bit_cast(s16x4, osg[t]), __builtin_bit_cast(s16x4, osg[(t) + 1])); \
    _Pragma("unroll") for (int nt = 0; nt < 5; ++nt) acc[nt] = MFMA16(cat8(fwa.s[nt], fwb.s[nt]), os_, acc[nt]); } while (0)
  P3_LOADW(fwa, w0);
  SBAR();
  poll(ws, FL_O + (size_t)(((m3 >> 5) + (w >> 1)) * NH + role * 16 + r16) * 8, E);
  STAMP(12);
  {
    const int orow = (w & 1) * 16 + r16;
    const char* ob = ws + WS_OW + ((size_t)((m3 >> 5) + (w >> 1)) * NH + role * 16) * 1280;
    const char* pp = ob + (g >> 1) * 1280 + (g & 1) * 512 + orow * 16;
    const char* sp = ob + g * 1280 + 1024 + orow * 8;
#pragma unroll
    for (int t = 0; t < 4; ++t) {
      asm volatile("global_load_dwordx4 %0, %3, off sc1\n\tglobal_load_dwordx4 %1, %3, off offset:2560 sc1\n\tglobal_load_dwordx2 %2, %4, off sc1"
                   : "=&v"(op[2 * t]), "=&v"(op[2 * t + 1]), "=&v"(osg[t]) : "v"(pp + t * 5120), "v"(sp + t * 5120) : "memory");
    }
  }
  P3_LOADW(fwb, w1);
  if (role == 1) spin_mk(ct + CT_WRD2, mk.x);
  SBAR();
  asm volatile("s_waitcnt vmcnt(6)" : "+v"(op[0]), "+v"(op[1]), "+v"(op[2]), "+v"(op[3]), "+v"(osg[0]), "+v"(osg[1]) :: "memory");
  STAMP(13);
  P3_MS(0); P3_MM(fwa, 0);
  SBAR();
  P3_LOADW(fwa, w2); P3_MM(fwb, 1);
#pragma unroll
  for (int i = 0; i < 10; ++i) { __builtin_amdgcn_sched_group_barrier(0x008, 1, 0); __builtin_amdgcn_sched_group_barrier(0x100, 3, 0); }
  SBAR();
  asm volatile("s_waitcnt vmcnt(0)" : "+v"(op[4]), "+v"(op[5]), "+v"(op[6]), "+v"(op[7]), "+v"(osg[2]), "+v"(osg[3]) :: "memory");
  P3_LOADW(fwb, w3); P3_MM(fwa, 2);
#pragma unroll
  for (int i = 0; i < 10; ++i) { __builtin_amdgcn_sched_group_barrier(0x008, 1, 0); __builtin_amdgcn_sched_group_barrier(0x100, 3, 0); }
  SBAR();
  P3_MM(fwb, 3); P3_MS(2);
  SBAR();
  const unsigned rb = lds0 + AT_K + 25600 + w * 5120 + lane * 16;
  if (role == 1) {
#pragma unroll
    for (int nt = 0; nt < 5; ++nt) lds_write16(rb + nt * 1024, __builtin_bit_cast(u32x4, acc[nt]));
    asm volatile("s_waitcnt lgkmcnt(0)" ::: "memory");
    sig(ct + CT_RED + w * 4, 1u, lane);
    STAMP(14);
    DIAG_DUMP(1);
    return;
  }
  spin_ge(ct + CT_RED + w * 4, 1u);
  u16* orow_g = out + (size_t)(m3 + w * 16 + r16) * (2 * DM) + DM + n3;
  u32x2 ov[5];
#pragma unroll
  for (int nt = 0; nt < 5; ++nt) {
    const f32x4 o2 = acc[nt] + __builtin_bit_cast(f32x4, lds_read16(rb + nt * 1024));
    ov[nt][0] = pk_bf16(o2[0], o2[1]);
    ov[nt][1] = pk_bf16(o2[2], o2[3]);
  }
#pragma unroll
  for (int pr = 0; pr < 2; ++pr) {
    const auto sx = __builtin_amdgcn_permlane16_swap(ov[2 * pr][0], ov[2 * pr + 1][0], false, false);
    const auto sy = __builtin_amdgcn_permlane16_swap(ov[2 * pr][1], ov[2 * pr + 1][1], false, false);
    ostore16(orow_g + (2 * pr + (g & 1)) * 16 + 8 * (g >> 1), (u32x4){sx[0], sy[0], sx[1], sy[1]});
  }
  ostore8(orow_g + 64 + 4 * g, ov[4]);
  STAMP(14);
  DIAG_DUMP(0);
}

extern "C" void kernel_launch(void* const* d_in, const int* in_sizes, int n_in, void* d_out, int out_size,
                              void* d_ws, size_t ws_size, hipStream_t stream) {
  const u16* aq = (const u16*)d_in[0];
  const u16* ak = (const u16*)d_in[1];
  const u16* av = (const u16*)d_in[2];
  const u16* shk = (const u16*)d_in[3];
  const u16* ssmk = (const u16*)d_in[4];
  const u16* ssmd = (const u16*)d_in[5];
  const u16* x = (const u16*)d_in[6];
  const u16* wqkv = (const u16*)d_in[7];
  const u16* wo = (const u16*)d_in[8];
  (void)hipFuncSetAttribute((const void*)mega, hipFuncAttributeMaxDynamicSharedMemorySize, L_TOTAL);
  mega<<<256, 768, L_TOTAL, stream>>>(x, wqkv, ak, av, shk, wo, ssmk, ssmd, aq, (char*)d_ws, (u16*)d_out);
}
```

```cpp
#include <hip/hip_runtime.h>
#include <hip/hip_bf16.h>
#include <stdint.h>

typedef unsigned short u16;
typedef short s16x4 __attribute__((ext_vector_type(4)));
typedef short s16x8 __attribute__((ext_vector_type(8)));
typedef float f32x2 __attribute__((ext_vector_type(2)));
typedef float f32x4 __attribute__((ext_vector_type(4)));
typedef float f32x16 __attribute__((ext_vector_type(16)));
typedef unsigned u32x2 __attribute__((ext_vector_type(2)));
typedef unsigned u32x4 __attribute__((ext_vector_type(4)));
typedef __bf16 bf16x2 __attribute__((ext_vector_type(2)));
#define LAS __attribute__((address_space(3)))
#define DI __device__ __forceinline__

constexpr int SEQ = 2048, DM = 640, NQKV = 1920, NH = 32;
constexpr size_t WS_QF = 0;
constexpr size_t WS_KF = WS_QF + (size_t)DM * SEQ * 2;
constexpr size_t WS_VF = WS_KF + (size_t)DM * SEQ * 2;
constexpr size_t WS_UT = WS_VF + (size_t)DM * SEQ * 2;
constexpr size_t WS_ZT = WS_UT + (size_t)DM * SEQ * 2;
constexpr size_t WS_OW = WS_ZT + (size_t)DM * SEQ * 2;
typedef unsigned long long u64;
constexpr size_t WS_FL = WS_OW + (size_t)DM * SEQ * 2;
constexpr size_t FL_U = WS_FL + 256;
constexpr size_t FL_Q = FL_U + 20 * 64 * 8;
constexpr size_t FL_Z = FL_Q + 16 * 16 * 8;
constexpr size_t FL_O = FL_Z + 640 * 8;

DI unsigned pk_bf16(float lo, float hi) {
  f32x2 f = {lo, hi};
  return __builtin_bit_cast(unsigned, __builtin_convertvector(f, bf16x2));
}
DI float bf_lo(unsigned u) { return __uint_as_float(u << 16); }
DI float bf_hi(unsigned u) { return __uint_as_float(u & 0xffff0000u); }
DI float rbf(float x) { return bf_lo(pk_bf16(x, 0.f)); }

DI unsigned lds_addr(const void* p) { return (unsigned)(uintptr_t)(LAS const char*)p; }

DI void glds16(unsigned voff, const void* sbase, unsigned lds_dst) {
  unsigned keep;
  asm volatile("s_mov_b32 %0, m0\n\ts_mov_b32 m0, %3\n\ts_nop 0\n\tglobal_load_lds_dwordx4 %1, %2\n\ts_mov_b32 m0, %0"
               : "=&s"(keep) : "v"(voff), "s"(sbase), "s"(lds_dst) : "memory");
}
#define LDPOL "sc1"
DI void glds16c(unsigned voff, const void* sbase, unsigned lds_dst) {
  unsigned keep;
  asm volatile("s_mov_b32 %0, m0\n\ts_mov_b32 m0, %3\n\ts_nop 0\n\tglobal_load_lds_dwordx4 %1, %2 " LDPOL "\n\ts_mov_b32 m0, %0"
               : "=&s"(keep) : "v"(voff), "s"(sbase), "s"(lds_dst) : "memory");
}
struct Mk { const void* src; unsigned x; };
DI void glds_mark(const Mk& mk, unsigned lds_dst, int lane) {
  if (lane == 0) {
    unsigned keep;
    asm volatile("s_mov_b32 %0, m0\n\ts_mov_b32 m0, %3\n\ts_nop 0\n\tglobal_load_lds_dword %1, %2\n\ts_mov_b32 m0, %0"
                 : "=&s"(keep) : "v"(0u), "s"(mk.src), "s"(lds_dst) : "memory");
  }
}
DI float dot2bf(unsigned a, unsigned b, float c) {
  return __builtin_amdgcn_fdot2_f32_bf16(__builtin_bit_cast(bf16x2, a), __builtin_bit_cast(bf16x2, b), c, false);
}
DI void gstore16c(void* p, u32x4 v) {
  asm volatile("global_store_dwordx4 %0, %1, off sc1\n\ts_nop 1" :: "v"(p), "v"(v) : "memory");
}
DI void gstore8c(void* p, u32x2 v) {
  asm volatile("global_store_dwordx2 %0, %1, off sc1" :: "v"(p), "v"(v) : "memory");
}
#define OUTPOL "nt"
DI void ostore16(void* p, u32x4 v) { asm volatile("global_store_dwordx4 %0, %1, off " OUTPOL "\n\ts_nop 1" :: "v"(p), "v"(v) : "memory"); }
DI void ostore8(void* p, u32x2 v) { asm volatile("global_store_dwordx2 %0, %1, off " OUTPOL :: "v"(p), "v"(v) : "memory"); }
template <int N> DI void wait_vm() { asm volatile("s_waitcnt vmcnt(%0)" :: "n"(N) : "memory"); }
DI void bar() { asm volatile("s_waitcnt lgkmcnt(0)\n\ts_barrier" ::: "memory"); }
DI s16x4 tr_read(unsigned a) {
  return __builtin_amdgcn_ds_read_tr16_b64_v4i16((LAS s16x4*)(uintptr_t)a);
}
DI s16x8 lds_read16(unsigned a) { return *(LAS const s16x8*)(uintptr_t)a; }
DI s16x4 lds_read8(unsigned a) { return *(LAS const s16x4*)(uintptr_t)a; }
DI void lds_write16(unsigned a, u32x4 v) { *(LAS u32x4*)(uintptr_t)a = v; }
DI s16x8 cat8(s16x4 a, s16x4 b) { return __builtin_shufflevector(a, b, 0, 1, 2, 3, 4, 5, 6, 7); }
#define MFMA16(a, b, c) __builtin_amdgcn_mfma_f32_16x16x32_bf16((a), (b), (c), 0, 0, 0)
#define MFMA32(a, b, c) __builtin_amdgcn_mfma_f32_32x32x16_bf16((a), (b), (c), 0, 0, 0)

#define SBAR() __builtin_amdgcn_sched_barrier(0)
#define SPIN_SLEEP 1
#define SPIN_MAX (1 << 21)
DI unsigned lds_ld32(unsigned a) { return *(volatile LAS unsigned*)(uintptr_t)a; }
DI void lds_st32(unsigned a, unsigned v) { *(volatile LAS unsigned*)(uintptr_t)a = v; }
DI void spin_ge(unsigned a, unsigned want) {
#pragma nounroll
  for (int it = 0; it < SPIN_MAX; ++it) {
    if ((unsigned)__builtin_amdgcn_readfirstlane(lds_ld32(a)) >= want) break;
    __builtin_amdgcn_s_sleep(1);
  }
  asm volatile("" ::: "memory");
}
DI void spin_ge4(unsigned a, unsigned want) {
#pragma nounroll
  for (int it = 0; it < SPIN_MAX; ++it) {
    const u32x4 v = *(volatile LAS u32x4*)(uintptr_t)a;
    const unsigned m = min(min(v[0], v[1]), min(v[2], v[3]));
    if ((unsigned)__builtin_amdgcn_readfirstlane(m) >= want) break;
    __builtin_amdgcn_s_sleep(SPIN_SLEEP);
  }
  asm volatile("" ::: "memory");
}
DI unsigned mk_bad(const u32x4& v, unsigned x) { return (v[0] ^ x) | (v[1] ^ x) | (v[2] ^ x) | (v[3] ^ x); }
DI void spin_mk(unsigned a, unsigned x) {
#pragma nounroll
  for (int it = 0; it < SPIN_MAX; ++it) {
    const u32x4 v = *(volatile LAS u32x4*)(uintptr_t)a;
    if ((unsigned)__builtin_amdgcn_readfirstlane(mk_bad(v, x)) == 0u) break;
    __builtin_amdgcn_s_sleep(SPIN_SLEEP);
  }
  asm volatile("" ::: "memory");
}
DI void spin_ge8(unsigned a, unsigned want) { spin_ge4(a, want); spin_ge4(a + 16, want); }
constexpr int CT_S0 = 0, CT_FREE = 80, CT_PAIR = 144  , CT_QUAD = 192  , CT_ZCNT = 240  ,
              CT_QCNT = 256, CT_ZRDY = 304, CT_CVD = 336  , CT_XST = 416, CT_ATT = 448, CT_RED = 480,
              CT_FX = 512  , CT_KR8 = 688, CT_VR8 = 704, CT_WRDY = 720, CT_WRD2 = 736, CT_BYTES = 1024;
DI void sig(unsigned a, unsigned v, int lane) { if (lane == 0) lds_st32(a, v); }

#define POLL_MAX (1 << 15)
DI void publish(char* ws, size_t off, u64 E, int lane) {
  asm volatile("s_waitcnt vmcnt(0)" ::: "memory");
  if (lane == 0) __hip_atomic_store((u64*)(ws + off), E, __ATOMIC_RELAXED, __HIP_MEMORY_SCOPE_AGENT);
}
#define POLL_SLEEP 6
DI void poll(const char* ws, size_t off, u64 E) {
#pragma nounroll
  for (int it = 0; it < POLL_MAX; ++it) {
    const u64 v = __hip_atomic_load((const u64*)(ws + off), __ATOMIC_RELAXED, __HIP_MEMORY_SCOPE_AGENT);
    if (__builtin_amdgcn_ballot_w64(v != E) == 0ull) break;
    __builtin_amdgcn_s_sleep(POLL_SLEEP);
  }
  asm volatile("" ::: "memory");
}

constexpr int K1_STAGE = 32768;
constexpr int A1_K = 0, A1_V = 3072, A1_W = 5120, A1_WAVE = 5632, A1_O = 2 * A1_WAVE, A1_PAIR = A1_O + 4096;
constexpr int L_AUX = 4 * K1_STAGE;
constexpr int L_CT = L_AUX + 2 * A1_PAIR;
constexpr int L_TOTAL = L_CT + CT_BYTES;
constexpr int K1_CT = L_CT;
constexpr int CV_COPY = 4160, CV_H = 4608, CV_REG = CV_H + 24576;
constexpr int AT_Q = L_AUX + 12288, AT_K = 64000, AT_V = AT_K + 16 * 2048, AT_END = AT_V + 16 * 1344;
constexpr int p3_w(int t) { return t < 5 ? t * 12800 : (t == 5 ? AT_END : AT_K + (t - 6) * 12800); }

#define PSYNC() do { asm volatile("s_waitcnt lgkmcnt(0)" ::: "memory"); ++ps; sig(pf + wh * 4, ps, lane); spin_ge(pf + (wh ^ 1) * 4, ps); } while (0)
DI void a1_issue(unsigned lds, int tile, int lane, int wh, const u16* ak, const u16* av, const u16* shk) {
  const int ts = tile & 63, td = tile >> 6;
  const int s0 = ts * 32 + wh * 16, d0 = td * 64;
  const int lr = lane >> 3, lc = (lane & 7) * 16;
  const unsigned wl = lds + wh * A1_WAVE;
#pragma unroll
  for (int i = 0; i < 3; ++i) {
    int r = s0 - 3 + i * 8 + lr; r = r < 0 ? 0 : r;
    if (i < 2 || lane < 24) glds16((unsigned)(r * DM * 2 + lc), (const char*)ak + d0 * 2, wl + A1_K + i * 1024);
  }
#pragma unroll
  for (int i = 0; i < 2; ++i)
    glds16((unsigned)(lr * DM * 2 + lc), (const char*)av + ((size_t)(s0 + i * 8) * DM + d0) * 2, wl + A1_V + i * 1024);
  if (lane < 32) glds16((unsigned)(lane * 16), (const char*)shk + d0 * 8, wl + A1_W);
}
DI void a1_finish(unsigned lds, unsigned pf, unsigned& ps, int tile, int lane, int wh, char* ws, u64 E) {
  const int ts = tile & 63, td = tile >> 6;
  const int c = lane & 7;
  const unsigned wl = lds + wh * A1_WAVE;
  u32x4 wt[4];
#pragma unroll
  for (int i = 0; i < 4; ++i) wt[i] = __builtin_bit_cast(u32x4, lds_read16(wl + A1_W + c * 64 + i * 16));
#pragma unroll
  for (int j = 0; j < 2; ++j) {
    const int r = j * 8 + (lane >> 3);
    const unsigned ka = wl + A1_K + (r + 3) * 128 + c * 16;
    u32x4 k0 = __builtin_bit_cast(u32x4, lds_read16(ka)), k1 = __builtin_bit_cast(u32x4, lds_read16(ka - 128));
    u32x4 k2 = __builtin_bit_cast(u32x4, lds_read16(ka - 256)), k3 = __builtin_bit_cast(u32x4, lds_read16(ka - 384));
    const u32x4 vv = __builtin_bit_cast(u32x4, lds_read16(wl + A1_V + r * 128 + c * 16));
    if (j == 0 && ts == 0 && wh == 0) {
      const u32x4 z = {0u, 0u, 0u, 0u};
      if (r < 1) k1 = z;
      if (r < 2) k2 = z;
      if (r < 3) k3 = z;
    }
    u32x4 o;
#pragma unroll
    for (int i = 0; i < 4; ++i) {
      const unsigned pa0 = __builtin_amdgcn_perm(k1[i], k0[i], 0x05040100u), pb0 = __builtin_amdgcn_perm(k3[i], k2[i], 0x05040100u);
      const unsigned pa1 = __builtin_amdgcn_perm(k1[i], k0[i], 0x07060302u), pb1 = __builtin_amdgcn_perm(k3[i], k2[i], 0x07060302u);
      const float e0 = dot2bf(pb0, wt[i][1], dot2bf(pa0, wt[i][0], 0.f));
      const float e1 = dot2bf(pb1, wt[i][3], dot2bf(pa1, wt[i][2], 0.f));
      o[i] = pk_bf16(e0 * bf_lo(vv[i]), e1 * bf_hi(vv[i]));
    }
    lds_write16(lds + A1_O + (31 - wh * 16 - r) * 128 + c * 16, o);
  }
  PSYNC();
  const int gg = lane >> 4, q = (lane & 15) >> 2, p = lane & 3;
  u16* ut = (u16*)(ws + WS_UT);
#pragma unroll
  for (int j = 0; j < 2; ++j) {
    const unsigned ra = lds + A1_O + (gg * 8 + q) * 128 + wh * 64 + j * 32 + p * 8;
    const s16x4 t0 = tr_read(ra), t1 = tr_read(ra + 512);
    const int d = td * 64 + wh * 32 + j * 16 + (lane & 15);
    gstore16c(ut + (size_t)d * SEQ + (SEQ - 32 - ts * 32) + gg * 8, __builtin_bit_cast(u32x4, cat8(t0, t1)));
  }
}
DI void a1_publish(int tile, int lane, int wh, char* ws, u64 E) {
  const int ts = tile & 63, td = tile >> 6;
  publish(ws, FL_U + (size_t)((td * 2 + wh) * 64 + ts) * 8, E, lane);
}

template <bool VT>
DI void k1_gemm(unsigned lds0, int m0, int n0, int tid, char* ws, unsigned mkx) {
  const int lane = tid & 63, wave = __builtin_amdgcn_readfirstlane(tid >> 6);
  const int wm = wave >> 1, wn = wave & 1;
  const int l32 = lane & 31, hh = lane >> 5, gg = lane >> 4, q = (lane & 15) >> 2, p = lane & 3;
  const unsigned ct = lds0 + K1_CT;
  f32x16 acc[2][2];
#pragma unroll
  for (int i = 0; i < 2; ++i)
#pragma unroll
    for (int j = 0; j < 2; ++j)
#pragma unroll
      for (int r = 0; r < 16; ++r) acc[i][j][r] = 0.f;
  unsigned xb[4];
#pragma unroll
  for (int ks = 0; ks < 4; ++ks) xb[ks] = lds0 + (wm * 64 + l32) * 128 + (((unsigned)(2 * ks + hh) ^ (unsigned)((l32 >> 1) & 7)) * 16);
  unsigned wb[2];
#pragma unroll
  for (int nt = 0; nt < 2; ++nt)
    wb[nt] = lds0 + 16384 + (8 * hh + q) * 256 + (((unsigned)(wn * 4 + 2 * nt + (gg & 1)) ^ (unsigned)(q << 1)) * 32) + p * 8;
  s16x8 x0[2], w0[2], x1[2], w1[2], x2[2], w2[2], x3[2], w3[2];
#define K1_LOADF(XF, WF, bo_, ks) do { \
    _Pragma("unroll") for (int i_ = 0; i_ < 2; ++i_) { \
      WF[i_] = cat8(tr_read(wb[i_] + (bo_) + (ks) * 4096), tr_read(wb[i_] + (bo_) + (ks) * 4096 + 1024)); \
      XF[i_] = lds_read16(xb[ks] + (bo_) + i_ * 4096); } } while (0)
#define K1_MM(XF, WF) do { _Pragma("unroll") for (int mt = 0; mt < 2; ++mt) _Pragma("unroll") for (int nt = 0; nt < 2; ++nt) \
      acc[mt][nt] = VT ? MFMA32(XF[mt], WF[nt], acc[mt][nt]) : MFMA32(WF[nt], XF[mt], acc[mt][nt]); } while (0)
#define ILV_A 2
#define ILV_B 2
#define ILV_C 2
#define K1_ILV() do { \
      __builtin_amdgcn_sched_group_barrier(0x008, 1, 0); __builtin_amdgcn_sched_group_barrier(0x100, ILV_A, 0); \
      __builtin_amdgcn_sched_group_barrier(0x008, 1, 0); __builtin_amdgcn_sched_group_barrier(0x100, ILV_B, 0); \
      __builtin_amdgcn_sched_group_barrier(0x008, 1, 0); __builtin_amdgcn_sched_group_barrier(0x100, ILV_C, 0); \
      __builtin_amdgcn_sched_group_barrier(0x008, 1, 0); __builtin_amdgcn_sched_group_barrier(0x100, 2, 0); } while (0)
  K1_LOADF(x0, w0, 0, 0);
  K1_LOADF(x1, w1, 0, 1);
#pragma nounroll
  for (int t = 0; t < 10; ++t) {
    const unsigned bo = (t & 3) * K1_STAGE, bn = ((t + 1) & 3) * K1_STAGE;
    SBAR();
    const u32x4 fl4 = *(volatile LAS u32x4*)(uintptr_t)(ct + CT_FX + (t + 1) * 16);
    K1_LOADF(x2, w2, bo, 2); K1_MM(x0, w0); K1_ILV();
    SBAR();
    K1_LOADF(x3, w3, bo, 3); K1_MM(x1, w1); K1_ILV();
    SBAR();
    const unsigned flu = (unsigned)__builtin_amdgcn_readfirstlane(mk_bad(fl4, mkx));
    if (t < 9 && flu != 0u) spin_mk(ct + CT_FX + (t + 1) * 16, mkx);
    asm volatile("" ::: "memory");
    SBAR();
    K1_LOADF(x0, w0, bn, 0); K1_MM(x2, w2); K1_ILV();
    SBAR();
    asm volatile("" : "+v"(x3[0]), "+v"(x3[1]), "+v"(w3[0]), "+v"(w3[1]) :: "memory");
    if (lane == 0) lds_st32(ct + CT_FREE + (t & 3) * 16 + wave * 4, (unsigned)(t + 1));
    SBAR();
    K1_LOADF(x1, w1, bn, 1); K1_MM(x3, w3); K1_ILV();
  }
  SBAR();
  if (!VT) {
    const int part = n0 >= DM ? 1 : 0;
    const float sc = part == 0 ? 0.22360679774997896f * 1.4426950408889634f : 1.0f;
    char* base = ws + (part ? WS_KF : WS_QF) + ((size_t)((m0 + wm * 64) >> 5) * 40 + ((n0 - part * DM + wn * 64) >> 4)) * 1024 + lane * 16;
#pragma unroll
    for (int mt = 0; mt < 2; ++mt)
#pragma unroll
      for (int nt = 0; nt < 2; ++nt)
#pragma unroll
        for (int uh = 0; uh < 2; ++uh) {
          u32x4 o;
#pragma unroll
          for (int i = 0; i < 4; ++i) o[i] = pk_bf16(acc[mt][nt][8 * uh + 2 * i] * sc, acc[mt][nt][8 * uh + 2 * i + 1] * sc);
          gstore16c(base + (mt * 40 + nt * 2 + uh) * 1024, o);
        }
  } else {
    char* base = ws + WS_VF + ((size_t)((m0 + wm * 64) >> 4) * 20 + ((n0 - 2 * DM + wn * 64) >> 5)) * 1024 + lane * 16;
#pragma unroll
    for (int mt = 0; mt < 2; ++mt)
#pragma unroll
      for (int nt = 0; nt < 2; ++nt)
#pragma unroll
        for (int kg = 0; kg < 2; ++kg) {
          u32x4 o;
#pragma unroll
          for (int i = 0; i < 4; ++i) o[i] = pk_bf16(acc[mt][nt][8 * kg + 2 * i], acc[mt][nt][8 * kg + 2 * i + 1]);
          gstore16c(base + ((mt * 2 + kg) * 20 + nt) * 1024, o);
        }
  }
}

DI void cv_put(unsigned y, const u32x4& c, const u32x2& n) {
  const unsigned d0 = c[0], d1 = c[1], d2 = c[2], d3 = c[3], d4 = n[0], d5 = n[1];
  const unsigned a0 = __builtin_amdgcn_alignbit(d1, d0, 16), a1 = __builtin_amdgcn_alignbit(d2, d1, 16),
                 a2 = __builtin_amdgcn_alignbit(d3, d2, 16), a3 = __builtin_amdgcn_alignbit(d4, d3, 16),
                 a4 = __builtin_amdgcn_alignbit(d5, d4, 16);
  lds_write16(y, (u32x4){d0, d1, d2, d3});
  lds_write16(y + CV_COPY, (u32x4){a0, a1, a2, a3});
  lds_write16(y + 2 * CV_COPY, (u32x4){d1, d2, d3, d4});
  lds_write16(y + 3 * CV_COPY, (u32x4){a1, a2, a3, a4});
}

#define QSYNC() do { asm volatile("s_waitcnt lgkmcnt(0)" ::: "memory"); ++qs; sig(qf + wq * 4, qs, lane); spin_ge4(qf, qs); } while (0)
struct HRegs { u32x4 c, u; u32x2 n; unsigned d; };
DI HRegs conv_load_h(int ch, int t256, const u16* __restrict__ ssmk, const u16* __restrict__ ssmd) {
  HRegs r;
  const u16* hrow = ssmk + (size_t)ch * SEQ;
  r.c = *((const u32x4*)(hrow + t256 * 8));
  r.n = *(const u32x2*)(hrow + (t256 < 255 ? t256 * 8 + 8 : 0));
  r.d = ssmd[ch];
  return r;
}
DI void conv_load_u(HRegs& r, int ch, int lane, int wq, const char* __restrict__ ws, u64 E) {
  poll(ws, FL_U + (size_t)((ch >> 5) * 64 + 48 - 16 * wq + (lane & 15)) * 8, E);
  const u64* up = (const u64*)(ws + WS_UT + (size_t)ch * SEQ * 2 + wq * 1024 + lane * 16);
  const u64 a = __hip_atomic_load(up, __ATOMIC_RELAXED, __HIP_MEMORY_SCOPE_AGENT);
  const u64 b = __hip_atomic_load(up + 1, __ATOMIC_RELAXED, __HIP_MEMORY_SCOPE_AGENT);
  r.u = (u32x4){(unsigned)a, (unsigned)(a >> 32), (unsigned)b, (unsigned)(b >> 32)};
}
DI void conv_stage(unsigned reg, int lane, int wq, const HRegs& hr) {
  const int t256 = wq * 64 + lane;
  const unsigned hb = reg + CV_H;
  if (t256 < 32) lds_write16(reg + 4096 + t256 * 16, (u32x4){0u, 0u, 0u, 0u});
  cv_put(hb + (t256 + 2) * 16, hr.c, hr.n);
  if (t256 < 2) {
    const u32x4 z = {0u, 0u, 0u, 0u};
    u32x2 nx = {0u, 0u};
    const unsigned h0 = __builtin_amdgcn_readfirstlane(hr.c[0]), h1 = __builtin_amdgcn_readfirstlane(hr.c[1]);
    if (t256 == 1) nx = (u32x2){h0, h1};
    cv_put(hb + t256 * 16, z, nx);
  }
}
DI void conv_run(unsigned reg, unsigned qf, unsigned& qs, int ch, int lane, int wq, const HRegs& hr,
                 char* __restrict__ ws, unsigned xst) {
  const unsigned hb = reg + CV_H;
  if (xst) spin_ge4(xst, 1u);
  else { lds_write16(reg + (wq * 64 + lane) * 16, hr.u); QSYNC(); }
  const int r16 = lane & 15, g = lane >> 4, q = r16 >> 2, p = lane & 3;
  const unsigned ab0 = hb + ((q + 1) & 3) * CV_COPY + 2 * (8 * g + 4 * p + (q == 3 ? 4 : 0)) + wq * 128;
  const unsigned bb0 = reg + 2 * (2032 - 16 * r16 + 8 * g) + wq * 128 - 7 * 512;
  f32x4 acc[8];
#pragma unroll
  for (int i = 0; i < 8; ++i) acc[i] = (f32x4){0.f, 0.f, 0.f, 0.f};
#define CV_LDA(a_, ad_) cat8(tr_read((ad_) + (a_) * 512), tr_read((ad_) + (a_) * 512 + 8))
#define CV_LDB(c_, bd_) lds_read16((bd_) + (7 - (c_)) * 512)
  {
    s16x8 A[8], B[8];
    B[0] = CV_LDB(0, bb0);
#pragma unroll
    for (int a = 0; a < 8; ++a) A[a] = CV_LDA(a, ab0);
#pragma unroll
    for (int c = 1; c < 8; ++c) B[c] = CV_LDB(c, bb0);
#pragma unroll
    for (int c = 0; c < 8; ++c) {
      SBAR();
#pragma unroll
      for (int a = 0; a + c < 8; ++a) acc[a + c] = MFMA16(A[a], B[c], acc[a + c]);
      B[c] = CV_LDB(c, bb0 + 64);
      A[7 - c] = CV_LDA(7 - c, ab0 + 64);
    }
    SBAR();
#pragma unroll
    for (int a = 7; a >= 0; --a)
#pragma unroll
      for (int c = 0; a + c < 8; ++c) acc[a + c] = MFMA16(A[a], B[c], acc[a + c]);
  }
  QSYNC();
#pragma unroll
  for (int nt = 0; nt < 8; ++nt) {
    const int dw = nt >> 1;
    if (dw != wq) lds_write16(hb + (nt * 3 + (wq > dw ? wq - 1 : wq)) * 1024 + lane * 16, __builtin_bit_cast(u32x4, acc[nt]));
  }
  f32x4 k0 = wq == 0 ? acc[0] : (wq == 1 ? acc[2] : (wq == 2 ? acc[4] : acc[6]));
  f32x4 k1 = wq == 0 ? acc[1] : (wq == 1 ? acc[3] : (wq == 2 ? acc[5] : acc[7]));
  QSYNC();
  const float dco = bf_lo(hr.d);
  u32x2 zz[2];
#pragma unroll
  for (int i = 0; i < 2; ++i) {
    f32x4 o = i ? k1 : k0;
#pragma unroll
    for (int j = 0; j < 3; ++j) o += __builtin_bit_cast(f32x4, lds_read16(hb + ((wq * 2 + i) * 3 + j) * 1024 + lane * 16));
    const int s = (wq * 2 + i) * 256 + 16 * r16 + 4 * g;
    const u32x2 uu = __builtin_bit_cast(u32x2, lds_read8(reg + 2 * (SEQ - 4 - s)));
    const float kv0 = bf_hi(uu[1]), kv1 = bf_lo(uu[1]), kv2 = bf_hi(uu[0]), kv3 = bf_lo(uu[0]);
    const unsigned y01 = pk_bf16(o[0], o[1]), y23 = pk_bf16(o[2], o[3]);
    const unsigned e01 = pk_bf16(kv0 * dco, kv1 * dco), e23 = pk_bf16(kv2 * dco, kv3 * dco);
    zz[i][0] = pk_bf16(bf_lo(y01) + bf_lo(e01), bf_hi(y01) + bf_hi(e01));
    zz[i][1] = pk_bf16(bf_lo(y23) + bf_lo(e23), bf_hi(y23) + bf_hi(e23));
  }
  const auto sx = __builtin_amdgcn_permlane16_swap(zz[0][0], zz[1][0], false, false);
  const auto sy = __builtin_amdgcn_permlane16_swap(zz[0][1], zz[1][1], false, false);
  u16* zrow = (u16*)(ws + WS_ZT) + (size_t)ch * SEQ;
  gstore16c(zrow + (wq * 2 + (g & 1)) * 256 + 16 * r16 + 8 * (g >> 1), (u32x4){sx[0], sy[0], sx[1], sy[1]});
  asm volatile("s_waitcnt lgkmcnt(0)" ::: "memory");
}
DI void conv_publish(unsigned zc, int ch, int lane, char* __restrict__ ws, u64 E) {
  asm volatile("s_waitcnt vmcnt(0)" ::: "memory");
  if (lane == 0) {
    const unsigned old = __hip_atomic_fetch_add((LAS unsigned*)(uintptr_t)zc, 1u, __ATOMIC_RELAXED, __HIP_MEMORY_SCOPE_WORKGROUP);
    if ((old & 3u) == 3u) __hip_atomic_store((u64*)(ws + FL_Z + (size_t)ch * 8), E, __ATOMIC_RELAXED, __HIP_MEMORY_SCOPE_AGENT);
  }
}

DI s16x8 pack8(const f32x16& v, int o) {
  u32x4 r;
  r[0] = pk_bf16(v[o + 0], v[o + 1]); r[1] = pk_bf16(v[o + 2], v[o + 3]);
  r[2] = pk_bf16(v[o + 4], v[o + 5]); r[3] = pk_bf16(v[o + 6], v[o + 7]);
  return __builtin_bit_cast(s16x8, r);
}
DI float max3f(float a, float b, float c) { return __builtin_fmaxf(__builtin_fmaxf(a, b), c); }
DI float max16(const f32x16& v, float m) {
  float a = max3f(v[0], v[1], m), b = max3f(v[2], v[3], v[4]);
  a = max3f(a, v[5], v[6]); b = max3f(b, v[7], v[8]);
  a = max3f(a, v[9], v[10]); b = max3f(b, v[11], v[12]);
  a = max3f(a, v[13], v[14]);
  return max3f(a, b, v[15]);
}
DI float xhalf_max(float m) {
  auto sw = __builtin_amdgcn_permlane32_swap(__float_as_uint(m), __float_as_uint(m), false, false);
  return __builtin_fmaxf(__uint_as_float(sw[0]), __uint_as_float(sw[1]));
}

#define AT_LMAX 65536.0f
#define P3_WADDR() unsigned wv[4]; _Pragma("unroll") for (int i = 0; i < 4; ++i) { \
      const int c = i < 3 ? (i * 4 + w) * 64 + lane : 768 + w * 8 + (lane & 7); const int row = c / 10; \
      wv[i] = (unsigned)(row * DM * 2 + (c - row * 10) * 16); } const char* wog = (const char*)(wo + n3)
#define P3_STW(t) do { _Pragma("unroll") for (int i_ = 0; i_ < 3; ++i_) \
        glds16(wv[i_], wog + (size_t)(t) * 80 * DM * 2, lds0 + p3_w(t) + (i_ * 4 + w) * 1024); \
      if (lane < 8) glds16(wv[3], wog + (size_t)(t) * 80 * DM * 2, lds0 + p3_w(t) + 12288 + w * 128); } while (0)
DI void attn_stage(unsigned lds0, int h, int qb8, int j, int lane, char* __restrict__ ws, u64 E, int zch, const Mk& mk) {
  const int qt = qb8 * 8 + j;
  const unsigned l16 = (unsigned)(lane * 16), ct = lds0 + L_CT;
  const int qa = (5 * h) >> 2;
  const bool stg = qb8 > 0 || j >= 2;
  {
    const int l3 = lane & 7, hi = lane & 1;
    const bool kv = stg && l3 >= 2 && l3 < 6;
    const int rb = kv ? 2 * qb8 - 2 + j : 2 * qb8 + (l3 >= 6 ? 1 : 0);
    const int col = !kv ? qa * 16 + hi * 31 : (l3 < 4 ? DM + qa * 16 + hi * 31 : 2 * DM + h * 20 + hi * 19);
    poll(ws, FL_Q + (size_t)(rb * 16 + (col >> 7)) * 8, E);
  }
  conv_publish(ct + CT_ZCNT + 8, zch, lane, ws, E);
  const int kt0 = qb8 * 8 - 8 + 4 * j;
#pragma unroll
  for (int i = 0; i < 4; ++i)
    glds16c(l16, ws + WS_QF + ((size_t)(qt + (i >> 1) * 4) * 40 + qa + (i & 1)) * 1024, lds0 + AT_Q + ((j + (i >> 1) * 4) * 2 + (i & 1)) * 1024);
  if (stg) {
#pragma unroll
    for (int i = 0; i < 8; ++i)
      glds16c(l16, ws + WS_KF + ((size_t)(kt0 + (i >> 1)) * 40 + qa + (i & 1)) * 1024, lds0 + AT_K + (j * 8 + i) * 1024);
  }
  glds_mark(mk, ct + CT_KR8 + j * 4, lane);
  if (stg) {
#pragma unroll
    for (int i = 0; i < 6; ++i) {
      const int c = i * 64 + lane;
      const int kgp = (c * 1561) >> 16, rem = c - kgp * 42, vh = rem >= 21 ? 1 : 0, d = rem - vh * 21;
      const int n = h * 20 + d;
      if (c < 336 && d == 20) lds_write16(lds0 + AT_V + j * 5376 + c * 16, (u32x4){0x3f803f80u, 0x3f803f80u, 0x3f803f80u, 0x3f803f80u});
      if (c < 336 && d < 20)
        glds16c((unsigned)((kgp * 20 + (n >> 5)) * 1024 + (vh * 32 + (n & 31)) * 16), ws + WS_VF + ((size_t)(kt0 * 2) * 20) * 1024,
                lds0 + AT_V + j * 5376 + i * 1024);
    }
    asm volatile("s_waitcnt lgkmcnt(0)" ::: "memory");
  }
  glds_mark(mk, ct + CT_VR8 + j * 4, lane);
}
template <bool FULL>
DI void attn_task(unsigned lds0, int h, int qb8, int j, int lane, char* __restrict__ ws, u64 E, int zch, const Mk& mk) {
  const int l32 = lane & 31, hh = lane >> 5;
  const int qt = qb8 * 8 + j;
  const int tfirst = FULL ? 0 : 8 - qt;
  const unsigned l16 = (unsigned)(lane * 16), ct = lds0 + L_CT;
  const int qa = (5 * h) >> 2;
  u32x4 q0r, q1r;
  spin_mk(ct + CT_KR8, mk.x);
  if (!FULL) spin_mk(ct + CT_VR8, mk.x);
  q0r = __builtin_bit_cast(u32x4, lds_read16(lds0 + AT_Q + j * 2048 + l16));
  q1r = __builtin_bit_cast(u32x4, lds_read16(lds0 + AT_Q + j * 2048 + 1024 + l16));
  asm volatile("s_waitcnt lgkmcnt(0)" : "+v"(q0r), "+v"(q1r) :: "memory");
  if (zch >= 0) conv_publish(ct + CT_ZCNT + (j >> 2) * 4, zch, lane, ws, E);
  {
    const int g0 = 4 * qa + hh - 5 * h;
    const bool k0 = g0 >= 0, k1 = g0 + 2 >= 0 && g0 + 2 < 5, k2 = g0 + 4 < 5, k3 = g0 + 6 < 5;
    q0r[0] = k0 ? q0r[0] : 0u; q0r[1] = k0 ? q0r[1] : 0u; q0r[2] = k1 ? q0r[2] : 0u; q0r[3] = k1 ? q0r[3] : 0u;
    q1r[0] = k2 ? q1r[0] : 0u; q1r[1] = k2 ? q1r[1] : 0u; q1r[2] = k3 ? q1r[2] : 0u; q1r[3] = k3 ? q1r[3] : 0u;
  }
  s16x8 qlo = __builtin_bit_cast(s16x8, q0r), qhi8 = __builtin_bit_cast(s16x8, q1r);
  const f32x16 zero16 = {0.f, 0.f, 0.f, 0.f, 0.f, 0.f, 0.f, 0.f, 0.f, 0.f, 0.f, 0.f, 0.f, 0.f, 0.f, 0.f};
  const float ninf = -INFINITY;
  const f32x16 ninf16 = {ninf, ninf, ninf, ninf, ninf, ninf, ninf, ninf, ninf, ninf, ninf, ninf, ninf, ninf, ninf, ninf};
  const unsigned kb = lds0 + AT_K + j * 2048 + l16;
#define AT_QK(dst, t, cin) do { if (FULL || (t) >= tfirst) { dst = MFMA32(lds_read16(kb + (t) * 2048), qlo, cin); \
    dst = MFMA32(lds_read16(kb + (t) * 2048 + 1024), qhi8, dst); } else dst = ninf16; } while (0)
  const int vrow = l32 <= 20 ? l32 : (l32 == 24 ? 20 : l32 - 16);
  const unsigned vb = lds0 + AT_V + j * 1344 + hh * 336 + vrow * 16;
#define AT_EXP(sv) do { _Pragma("unroll") for (int r = 0; r < 16; ++r) sv[r] = __builtin_amdgcn_exp2f(sv[r] - m); } while (0)
#define AT_PV(sv, t) do { oacc = MFMA32(lds_read16(vb + (t) * 1344), pack8(sv, 0), oacc); oacc = MFMA32(lds_read16(vb + (t) * 1344 + 672), pack8(sv, 8), oacc); } while (0)
  f32x16 oacc;
  bool exact = !FULL;
  if (FULL) {
    f32x16 sm, sa, sb;
    AT_QK(sm, 8, zero16); AT_QK(sa, 0, zero16);
    const int tq = l32 - 4 * hh;
    const unsigned wm = tq < 0 ? 0xffffffffu : (0xfffffffeu << tq);
    unsigned msk[16];
#pragma unroll
    for (int r = 0; r < 16; ++r) {
      msk[r] = (unsigned)__builtin_amdgcn_sbfe((int)wm, (r & 3) + 8 * (r >> 2), 1);
      sm[r] = __uint_as_float((msk[r] & __float_as_uint(sa[r])) | (~msk[r] & __float_as_uint(sm[r])));
    }
    const float m = xhalf_max(max16(sm, ninf));
    const float nm = -m;
    const f32x16 cneg = {nm, nm, nm, nm, nm, nm, nm, nm, nm, nm, nm, nm, nm, nm, nm, nm};
    AT_QK(sa, 1, cneg);
    AT_EXP(sm);
    u32x4 pl = __builtin_bit_cast(u32x4, pack8(sm, 0)), ph = __builtin_bit_cast(u32x4, pack8(sm, 8)), ql, qh;
#pragma unroll
    for (int i = 0; i < 4; ++i) {
      ql[i] = pl[i] & ((msk[2 * i] & 0xffffu) | (msk[2 * i + 1] & 0xffff0000u)); pl[i] ^= ql[i];
      qh[i] = ph[i] & ((msk[8 + 2 * i] & 0xffffu) | (msk[9 + 2 * i] & 0xffff0000u)); ph[i] ^= qh[i];
    }
    spin_mk(ct + CT_VR8, mk.x);
    oacc = MFMA32(lds_read16(vb + 8 * 1344), __builtin_bit_cast(s16x8, pl), zero16);
    oacc = MFMA32(lds_read16(vb + 8 * 1344 + 672), __builtin_bit_cast(s16x8, ph), oacc);
    oacc = MFMA32(lds_read16(vb), __builtin_bit_cast(s16x8, ql), oacc);
    oacc = MFMA32(lds_read16(vb + 672), __builtin_bit_cast(s16x8, qh), oacc);
#define AT_EX0(sv) do { _Pragma("unroll") for (int r = 0; r < 16; ++r) sv[r] = __builtin_amdgcn_exp2f(sv[r]); } while (0)
    AT_QK(sb, 2, cneg); AT_EX0(sa); AT_PV(sa, 1);
    AT_QK(sa, 3, cneg); AT_EX0(sb); AT_PV(sb, 2);
    AT_QK(sb, 4, cneg); AT_EX0(sa); AT_PV(sa, 3);
    AT_QK(sa, 5, cneg); AT_EX0(sb); AT_PV(sb, 4);
    AT_QK(sb, 6, cneg); AT_EX0(sa); AT_PV(sa, 5);
    AT_QK(sa, 7, cneg); AT_EX0(sb); AT_PV(sb, 6);
    AT_EX0(sa); AT_PV(sa, 7);
    float big = max3f(__builtin_fabsf(oacc[0]), __builtin_fabsf(oacc[1]), __builtin_fabsf(oacc[2]));
    big = max3f(big, __builtin_fabsf(oacc[3]), __builtin_fabsf(oacc[4]));
    big = max3f(big, __builtin_fabsf(oacc[5]), __builtin_fabsf(oacc[6]));
    big = max3f(big, __builtin_fabsf(oacc[7]), __builtin_fabsf(oacc[8]));
    big = max3f(big, __builtin_fabsf(oacc[9]), __builtin_fabsf(oacc[10]));
    big = __builtin_fmaxf(big, __builtin_fabsf(oacc[11]));
    const float ls = hh ? oacc[8] : oacc[12];
    float osum = 0.f;
#pragma unroll
    for (int r = 0; r < 8; ++r) osum += oacc[r];
    const bool ok = (ls < AT_LMAX) && (big < 1e37f) && (osum == osum);
    exact = __builtin_amdgcn_ballot_w64(!ok) != 0ull;
  }
  if (exact) {
    asm volatile("" : "+v"(qlo), "+v"(qhi8) :: "memory");
    f32x16 sm, s1, s2, s3;
    {
      f32x16 s0;
      AT_QK(sm, 8, zero16); AT_QK(s0, 0, zero16);
      AT_QK(s1, 1, zero16); AT_QK(s2, 2, zero16); AT_QK(s3, 3, zero16);
#pragma unroll
      for (int r = 0; r < 16; ++r) {
        const int kk = (r & 3) + 8 * (r >> 2) + 4 * hh;
        sm[r] = kk > l32 ? s0[r] : sm[r];
      }
    }
    float m = max16(sm, ninf);
    m = max16(s1, m); m = max16(s2, m); m = max16(s3, m);
    m = xhalf_max(m);
    AT_EXP(sm);
    oacc = zero16;
    {
      f32x16 p0, p8;
#pragma unroll
      for (int r = 0; r < 16; ++r) {
        const int kk = (r & 3) + 8 * (r >> 2) + 4 * hh;
        p0[r] = kk > l32 ? sm[r] : 0.f;
        p8[r] = kk > l32 ? 0.f : sm[r];
      }
      AT_PV(p8, 8);
      if (FULL) AT_PV(p0, 0);
    }
    if (FULL || 1 >= tfirst) { AT_EXP(s1); AT_PV(s1, 1); }
    AT_QK(sm, 4, zero16); AT_QK(s1, 5, zero16);
    if (FULL || 2 >= tfirst) { AT_EXP(s2); AT_PV(s2, 2); }
    AT_QK(s2, 6, zero16);
    if (FULL || 3 >= tfirst) { AT_EXP(s3); AT_PV(s3, 3); }
    AT_QK(s3, 7, zero16);
    float m2 = max16(sm, m);
    m2 = max16(s1, m2); m2 = max16(s2, m2); m2 = max16(s3, m2);
    m2 = xhalf_max(m2);
    {
      const float alpha = __builtin_amdgcn_exp2f(m - m2);
#pragma unroll
      for (int r = 0; r < 13; ++r) oacc[r] *= alpha;
      m = m2;
    }
    if (FULL || 4 >= tfirst) { AT_EXP(sm); AT_PV(sm, 4); }
    if (FULL || 5 >= tfirst) { AT_EXP(s1); AT_PV(s1, 5); }
    if (FULL || 6 >= tfirst) { AT_EXP(s2); AT_PV(s2, 6); }
    if (FULL || 7 >= tfirst) { AT_EXP(s3); AT_PV(s3, 7); }
  }
  asm volatile("s_waitcnt lgkmcnt(0)" ::: "memory");
  sig(ct + CT_ATT + j * 4, 1u, lane);
  const float l = hh ? oacc[8] : oacc[12];
  const float inv = __builtin_amdgcn_rcpf(l);
  char* oc = ws + WS_OW + ((size_t)qt * NH + h) * 1280;
  u32x4 o;
  o[0] = pk_bf16(oacc[0] * inv, oacc[1] * inv); o[1] = pk_bf16(oacc[2] * inv, oacc[3] * inv);
  o[2] = pk_bf16(oacc[4] * inv, oacc[5] * inv); o[3] = pk_bf16(oacc[6] * inv, oacc[7] * inv);
  gstore16c(oc + lane * 16, o);
  if (!hh) {
    u32x2 o2;
    o2[0] = pk_bf16(oacc[8] * inv, oacc[9] * inv); o2[1] = pk_bf16(oacc[10] * inv, oacc[11] * inv);
    gstore8c(oc + 1024 + l32 * 8, o2);
  }
  publish(ws, FL_O + (size_t)(qt * NH + h) * 8, E, lane);
}

#define APRIO 1
#define LPRIO 0
#define MPRIO 2
#define ROTG 2
__global__ __launch_bounds__(768, 3) void mega(const u16* __restrict__ x, const u16* __restrict__ wqkv,
                                               const u16* __restrict__ ak, const u16* __restrict__ av,
                                               const u16* __restrict__ shk, const u16* __restrict__ wo,
                                               const u16* __restrict__ ssmk, const u16* __restrict__ ssmd,
                                               const u16* __restrict__ aq, char* __restrict__ ws,
                                               u16* __restrict__ out) {
  extern __shared__ __attribute__((aligned(16))) char smem[];
  const unsigned lds0 = lds_addr(smem);
  const int tid = threadIdx.x, wave = __builtin_amdgcn_readfirstlane(tid >> 6);
  int lane = tid & 63;
#define RELANE() asm volatile("" : "+v"(lane))
  const int role = wave >> 2, w = wave & 3;
  const int bid = blockIdx.x, xcd = bid & 7, idx = bid >> 3;
  const bool has_gemm = idx < 30;
  const unsigned ct = lds0 + L_CT;
  u64 E;
  Mk mk; mk.src = wqkv;
  asm volatile("s_load_dwordx2 %0, %2, 0x0\n\ts_load_dword %1, %3, 0x0" : "=&s"(E), "=&s"(mk.x) : "s"(ws + WS_FL), "s"(wqkv) : "memory");
  const int gt = (xcd & 1) * 30 + idx;
  const int tm = (xcd >> 1) * 4 + (gt & 3), tn = gt >> 2;
  const int m0 = tm * 128, n0 = tn * 128;
  const int rot = ROTG >= 10 ? (((gt & 3) * 5 + 1) / 2 + 3 * tn) % 10 : ((((gt & 3) + tn) % ROTG) * 10) / ROTG;
  const unsigned xo0 = (unsigned)((lane >> 3) * DM * 2 + (((lane & 7) ^ (lane >> 4)) * 16));
  const unsigned xo1 = (unsigned)((lane >> 3) * DM * 2 + (((lane & 7) ^ (4 + (lane >> 4))) * 16));
  const unsigned wof = (unsigned)((lane >> 4) * NQKV * 2 + (((lane & 15) ^ ((lane >> 4) << 2)) * 16));
  const char* xg = (const char*)(x + (size_t)m0 * DM);
  const char* wg = (const char*)(wqkv + n0);
#define K1_PIECE_X(t, kt, j) glds16(((j) & 1) ? xo1 : xo0, xg + (size_t)(j) * 8 * DM * 2 + (kt) * 128, lds0 + ((t) & 3) * K1_STAGE + (j) * 1024)
#define K1_PIECE_W(t, kt, j) glds16(wof, wg + (size_t)((kt) * 64 + (j) * 4) * NQKV * 2, lds0 + ((t) & 3) * K1_STAGE + 16384 + (j) * 1024)
  if (has_gemm && role < 2) {
    const int kt0 = role + rot >= 10 ? role + rot - 10 : role + rot;
#pragma unroll
    for (int i = 0; i < 4; ++i) { K1_PIECE_X(role, kt0, w * 4 + i); K1_PIECE_W(role, kt0, w * 4 + i); }
  }
  const unsigned pf = ct + CT_PAIR + (wave >> 1) * 8;
  const int wh = wave & 1;
  unsigned ps = 0;
  const int fb = (idx - 30) * 8 + xcd;
  const int a1t = has_gemm ? (xcd * 30 + idx) * 2 + (w >> 1) : 480 + fb * 10 + (wave >> 1);
  const unsigned a1l = has_gemm ? lds0 + L_AUX + (w >> 1) * A1_PAIR : lds0 + (wave >> 1) * A1_PAIR;
  if (!has_gemm || role == 2) a1_issue(a1l, a1t, lane, wh, ak, av, shk);
  const bool cv0 = role != 0 || !has_gemm || bid < 112;
  const int ch0 = role == 0 ? (has_gemm ? 528 + bid : 512 + fb) : bid + (role == 1 ? 256 : 0);
  asm volatile("s_waitcnt lgkmcnt(0)" : "+s"(E), "+s"(mk.x) :: "memory");
  if (role == 1) lds_st32(ct + (tid - 256) * 4, (tid - 256) * 4 >= CT_FX ? ~mk.x : 0u);
  bar();
  E += 1ull;

  const bool ex = has_gemm && role == 1 && bid < 112;
  HRegs hr, h2;
#define LOAD_H() do { hr = conv_load_h(ch0, w * 64 + lane, ssmk, ssmd); \
    if (ex) h2 = conv_load_h(528 + bid, w * 64 + lane, ssmk, ssmd); } while (0)
  auto q_publish = [&]() {
    asm volatile("s_waitcnt vmcnt(0)" ::: "memory");
    if (lane == 0) {
      const unsigned old = __hip_atomic_fetch_add((LAS unsigned*)(uintptr_t)(ct + CT_QCNT), 1u, __ATOMIC_RELAXED, __HIP_MEMORY_SCOPE_WORKGROUP);
      if (old == 3u) __hip_atomic_store((u64*)(ws + FL_Q + (size_t)(tm * 16 + tn) * 8), E, __ATOMIC_RELAXED, __HIP_MEMORY_SCOPE_AGENT);
    }
  };
  if (!has_gemm) {
    LOAD_H();
    const unsigned a2l = lds0 + (6 + (wave >> 1)) * A1_PAIR;
    if (wave < 8) a1_issue(a2l, a1t + 6, lane, wh, ak, av, shk);
    if (wave < 8) wait_vm<6>(); else wait_vm<0>();
    a1_finish(a1l, pf, ps, a1t, lane, wh, ws, E);
    if (wave < 8) { wait_vm<2>(); a1_finish(a2l, pf, ps, a1t + 6, lane, wh, ws, E); }
    a1_publish(a1t, lane, wh, ws, E);
    if (wave < 8) a1_publish(a1t + 6, lane, wh, ws, E);
    bar();
  } else if (role == 0) {
    wait_vm<0>();
    sig(ct + CT_S0 + w * 4, 1u, lane);
    spin_ge4(ct + CT_S0, 1u);
    __builtin_amdgcn_s_setprio(MPRIO);
    if (tn >= 10) k1_gemm<true>(lds0, m0, n0, tid, ws, mk.x);
    else k1_gemm<false>(lds0, m0, n0, tid, ws, mk.x);
  } else if (role == 1) {
    __builtin_amdgcn_s_setprio(LPRIO);
#define K1_ST(t) do { const int kt_ = (t) + rot >= 10 ? (t) + rot - 10 : (t) + rot; \
      if ((t) >= 4) spin_ge4(ct + CT_FREE + ((t) & 3) * 16, (unsigned)((t) - 3)); \
      _Pragma("unroll") for (int i_ = 0; i_ < 4; ++i_) { K1_PIECE_X(t, kt_, w * 4 + i_); K1_PIECE_W(t, kt_, w * 4 + i_); } } while (0)
    glds_mark(mk, ct + CT_FX + 16 + w * 4, lane);
#pragma nounroll
    for (int t = 2; t < 4; ++t) { K1_ST(t); glds_mark(mk, ct + CT_FX + t * 16 + w * 4, lane); }
    LOAD_H();
#pragma nounroll
    for (int t = 4; t < 10; ++t) { K1_ST(t); glds_mark(mk, ct + CT_FX + t * 16 + w * 4, lane); }
    __builtin_amdgcn_s_setprio(0);
  } else {
    __builtin_amdgcn_s_setprio(2);
    wait_vm<0>();
    LOAD_H();
    a1_finish(a1l, pf, ps, a1t, lane, wh, ws, E);
    a1_publish(a1t, lane, wh, ws, E);
    __builtin_amdgcn_s_setprio(0);
  }

  RELANE();
  unsigned qs = 0;
  const unsigned qf = ct + CT_QUAD + role * 16;
  const unsigned creg = lds0 + (role == 2 ? L_AUX : (role == 1 ? 29184 : 0));
  if (role == 2) __builtin_amdgcn_s_setprio(APRIO); else __builtin_amdgcn_s_setprio(0);
  if (has_gemm && role == 0) {
    q_publish();
    if (cv0) {
      hr.d = ssmd[ch0];
      conv_run(creg, qf, qs, ch0, lane, w, hr, ws, ct + CT_XST);
    }
  } else {
    if (has_gemm && role == 1) {
      conv_load_u(hr, ch0, lane, w, ws, E);
      if (ex) conv_load_u(h2, 528 + bid, lane, w, ws, E);
      spin_ge4(ct + CT_FREE + 16, 10u);
      conv_stage(creg, lane, w, hr);
      if (ex) {
        conv_stage(lds0, lane, w, h2);
        lds_write16(lds0 + (w * 64 + lane) * 16, h2.u);
        asm volatile("s_waitcnt lgkmcnt(0)" ::: "memory");
        sig(ct + CT_XST + w * 4, 1u, lane);
      }
    } else {
      if (has_gemm) { const int wq = w; QSYNC(); }
      conv_stage(creg, lane, w, hr);
      conv_load_u(hr, ch0, lane, w, ws, E);
    }
    conv_run(creg, qf, qs, ch0, lane, w, hr, ws, 0u);
  }
  sig(ct + CT_CVD + wave * 4, 1u, lane);

  const int tm3 = xcd * 4 + (idx >> 3), tn3 = idx & 7;
  const int m3 = tm3 * 64, n3 = tn3 * 80;
  RELANE();

  if (role == 2) {
    const int r16 = lane & 15, g = lane >> 4, q = r16 >> 2, p = lane & 3;
    __builtin_amdgcn_s_setprio(3);
    spin_ge4(ct + CT_CVD + 32, 1u);
    if (has_gemm) spin_ge4(ct + CT_FREE + 16, 10u);
    attn_stage(lds0, xcd * 4 + (idx & 3), idx >> 2, w, lane, ws, E, ch0, mk);
    {
      P3_WADDR();
      P3_STW(5);
      spin_ge8(ct + CT_CVD, 1u);
      P3_STW(0); P3_STW(4); P3_STW(1); P3_STW(2); P3_STW(3);
      glds_mark(mk, ct + CT_WRDY + w * 4, lane);
    }
    __builtin_amdgcn_s_setprio(0);
    u32x4 qv[3];
#pragma unroll
    for (int j = 0; j < 3; ++j) {
      const int dc = j < 2 ? j * 4 + g : 8 + (g & 1);
      qv[j] = *((const u32x4*)(aq + (size_t)(m3 + w * 16 + r16) * DM + n3 + dc * 8));
    }
    {
      const int l5 = lane & 31;
      const int cl = l5 < 8 ? w * 8 + l5 : (l5 < 16 ? 32 + w * 8 + (l5 - 8) : 64 + w * 4 + (l5 & 3));
      poll(ws, FL_Z + (size_t)(n3 + cl) * 8, E);
    }
    {
      const char* zg = ws + WS_ZT + ((size_t)n3 * SEQ + m3) * 2;
      const unsigned zo = (unsigned)((lane >> 3) * SEQ * 2 + (lane & 7) * 16);
      glds16c(zo, zg + (size_t)(w * 8) * SEQ * 2, lds0 + L_AUX + w * 1024);
      glds16c(zo, zg + (size_t)(32 + w * 8) * SEQ * 2, lds0 + L_AUX + 4096 + w * 1024);
      if (lane < 32) glds16c(zo, zg + (size_t)(64 + w * 4) * SEQ * 2, lds0 + L_AUX + 8192 + w * 512);
    }
    wait_vm<0>();
    sig(ct + CT_ZRDY + w * 4, 1u, lane);
    spin_ge4(ct + CT_ZRDY, 1u);
    u32x4 zz[3];
#pragma unroll
    for (int j = 0; j < 3; ++j) {
      const int dc = j < 2 ? j * 4 + g : 8 + (g & 1);
      const unsigned za = lds0 + L_AUX + (dc * 8 + q) * 128 + w * 32 + p * 8;
      zz[j] = __builtin_bit_cast(u32x4, cat8(tr_read(za), tr_read(za + 512)));
    }
#pragma unroll
    for (int j = 0; j < 3; ++j) {
      const int dc = j < 2 ? j * 4 + g : 8 + (g & 1);
      u32x4 o;
#pragma unroll
      for (int i = 0; i < 4; ++i) o[i] = pk_bf16(bf_lo(qv[j][i]) * bf_lo(zz[j][i]), bf_hi(qv[j][i]) * bf_hi(zz[j][i]));
      if (j < 2 || g < 2) ostore16(out + (size_t)(m3 + w * 16 + r16) * (2 * DM) + n3 + dc * 8, o);
    }
    if (bid == 0 && w == 0) {
#pragma unroll
      for (int i = 0; i < 4; ++i) poll(ws, FL_O + (size_t)(((i * 2 + (lane >> 5)) * 8) * NH + (lane & 31)) * 8, E);
      if (lane == 0) __hip_atomic_store((u64*)(ws + WS_FL), E, __ATOMIC_RELAXED, __HIP_MEMORY_SCOPE_AGENT);
    }
    return;
  }

  const int h = xcd * 4 + (idx & 3), qb8 = idx >> 2;
  __builtin_amdgcn_s_setprio(1);
  RELANE();
  if (qb8 > 0) attn_task<true>(lds0, h, qb8, wave, lane, ws, E, cv0 ? ch0 : -1, mk);
  else attn_task<false>(lds0, h, qb8, wave, lane, ws, E, cv0 ? ch0 : -1, mk);

  RELANE();
  const int r16 = lane & 15, g = lane >> 4, q = r16 >> 2, p = lane & 3;
  f32x4 acc[5];
#pragma unroll
  for (int i = 0; i < 5; ++i) acc[i] = (f32x4){0.f, 0.f, 0.f, 0.f};
  const unsigned wbp = lds0 + (20 * (g >> 1) + 4 * (g & 1) + q) * 160 + p * 8;
  const unsigned wbs = lds0 + (20 * g + 16 + q) * 160 + p * 8;
  u32x4 op[8]; u32x2 osg[4];
  if (role == 1) {
    P3_WADDR();
    spin_ge8(ct + CT_ATT, 1u);
    P3_STW(6); P3_STW(7);
    glds_mark(mk, ct + CT_WRD2 + w * 4, lane);
  }
  spin_mk(ct + CT_WRDY, mk.x);
  const unsigned w0 = role ? p3_w(4) : p3_w(0), w1 = role ? p3_w(5) : p3_w(1), w2 = role ? p3_w(6) : p3_w(2), w3 = role ? p3_w(7) : p3_w(3);
  struct Fw { s16x8 a[5], b[5]; s16x4 s[5]; };
  Fw fwa, fwb;
#define P3_LOADW(F, bo_) do { _Pragma("unroll") for (int nt = 0; nt < 5; ++nt) { \
      F.a[nt] = cat8(tr_read(wbp + (bo_) + nt * 32), tr_read(wbp + (bo_) + 8 * 160 + nt * 32)); \
      F.b[nt] = cat8(tr_read(wbp + (bo_) + 40 * 160 + nt * 32), tr_read(wbp + (bo_) + 48 * 160 + nt * 32)); \
      F.s[nt] = tr_read(wbs + (bo_) + nt * 32); } } while (0)
#define P3_MM(F, t) do { \
    _Pragma("unroll") for (int nt = 0; nt < 5; ++nt) acc[nt] = MFMA16(F.a[nt], __builtin_bit_cast(s16x8, op[2 * (t)]), acc[nt]); \
    _Pragma("unroll") for (int nt = 0; nt < 5; ++nt) acc[nt] = MFMA16(F.b[nt], __builtin_bit_cast(s16x8, op[2 * (t) + 1]), acc[nt]); } while (0)
#define P3_MS(t) do { const s16x8 os_ = cat8(__builtin_bit_cast(s16x4, osg[t]), __builtin_bit_cast(s16x4, osg[(t) + 1])); \
    _Pragma("unroll") for (int nt = 0; nt < 5; ++nt) acc[nt] = MFMA16(cat8(fwa.s[nt], fwb.s[nt]), os_, acc[nt]); } while (0)
  P3_LOADW(fwa, w0);
  SBAR();
  poll(ws, FL_O + (size_t)(((m3 >> 5) + (w >> 1)) * NH + role * 16 + r16) * 8, E);
  {
    const int orow = (w & 1) * 16 + r16;
    const char* ob = ws + WS_OW + ((size_t)((m3 >> 5) + (w >> 1)) * NH + role * 16) * 1280;
    const char* pp = ob + (g >> 1) * 1280 + (g & 1) * 512 + orow * 16;
    const char* sp = ob + g * 1280 + 1024 + orow * 8;
#pragma unroll
    for (int t = 0; t < 4; ++t) {
      asm volatile("global_load_dwordx4 %0, %3, off sc1\n\tglobal_load_dwordx4 %1, %3, off offset:2560 sc1\n\tglobal_load_dwordx2 %2, %4, off sc1"
                   : "=&v"(op[2 * t]), "=&v"(op[2 * t + 1]), "=&v"(osg[t]) : "v"(pp + t * 5120), "v"(sp + t * 5120) : "memory");
    }
  }
  P3_LOADW(fwb, w1);
  if (role == 1) spin_mk(ct + CT_WRD2, mk.x);
  SBAR();
  asm volatile("s_waitcnt vmcnt(6)" : "+v"(op[0]), "+v"(op[1]), "+v"(op[2]), "+v"(op[3]), "+v"(osg[0]), "+v"(osg[1]) :: "memory");
  P3_MS(0); P3_MM(fwa, 0);
  SBAR();
  P3_LOADW(fwa, w2); P3_MM(fwb, 1);
#pragma unroll
  for (int i = 0; i < 10; ++i) { __builtin_amdgcn_sched_group_barrier(0x008, 1, 0); __builtin_amdgcn_sched_group_barrier(0x100, 3, 0); }
  SBAR();
  asm volatile("s_waitcnt vmcnt(0)" : "+v"(op[4]), "+v"(op[5]), "+v"(op[6]), "+v"(op[7]), "+v"(osg[2]), "+v"(osg[3]) :: "memory");
  P3_LOADW(fwb, w3); P3_MM(fwa, 2);
#pragma unroll
  for (int i = 0; i < 10; ++i) { __builtin_amdgcn_sched_group_barrier(0x008, 1, 0); __builtin_amdgcn_sched_group_barrier(0x100, 3, 0); }
  SBAR();
  P3_MM(fwb, 3); P3_MS(2);
  SBAR();
  const unsigned rb = lds0 + AT_K + 25600 + w * 5120 + lane * 16;
  if (role == 1) {
#pragma unroll
    for (int nt = 0; nt < 5; ++nt) lds_write16(rb + nt * 1024, __builtin_bit_cast(u32x4, acc[nt]));
    asm volatile("s_waitcnt lgkmcnt(0)" ::: "memory");
    sig(ct + CT_RED + w * 4, 1u, lane);
    return;
  }
  spin_ge(ct + CT_RED + w * 4, 1u);
  u16* orow_g = out + (size_t)(m3 + w * 16 + r16) * (2 * DM) + DM + n3;
  u32x2 ov[5];
#pragma unroll
  for (int nt = 0; nt < 5; ++nt) {
    const f32x4 o2 = acc[nt] + __builtin_bit_cast(f32x4, lds_read16(rb + nt * 1024));
    ov[nt][0] = pk_bf16(o2[0], o2[1]);
    ov[nt][1] = pk_bf16(o2[2], o2[3]);
  }
#pragma unroll
  for (int pr = 0; pr < 2; ++pr) {
    const auto sx = __builtin_amdgcn_permlane16_swap(ov[2 * pr][0], ov[2 * pr + 1][0], false, false);
    const auto sy = __builtin_amdgcn_permlane16_swap(ov[2 * pr][1], ov[2 * pr + 1][1], false, false);
    ostore16(orow_g + (2 * pr + (g & 1)) * 16 + 8 * (g >> 1), (u32x4){sx[0], sy[0], sx[1], sy[1]});
  }
  ostore8(orow_g + 64 + 4 * g, ov[4]);
}

extern "C" void kernel_launch(void* const* d_in, const int* in_sizes, int n_in, void* d_out, int out_size,
                              void* d_ws, size_t ws_size, hipStream_t stream) {
  const u16* aq = (const u16*)d_in[0];
  const u16* ak = (const u16*)d_in[1];
  const u16* av = (const u16*)d_in[2];
  const u16* shk = (const u16*)d_in[3];
  const u16* ssmk = (const u16*)d_in[4];
  const u16* ssmd = (const u16*)d_in[5];
  const u16* x = (const u16*)d_in[6];
  const u16* wqkv = (const u16*)d_in[7];
  const u16* wo = (const u16*)d_in[8];
  (void)hipFuncSetAttribute((const void*)mega, hipFuncAttributeMaxDynamicSharedMemorySize, L_TOTAL);
  mega<<<256, 768, L_TOTAL, stream>>>(x, wqkv, ak, av, shk, wo, ssmk, ssmd, aq, (char*)d_ws, (u16*)d_out);
}
```

```cpp
#include <hip/hip_runtime.h>
#include <hip/hip_bf16.h>
#include <stdint.h>

typedef unsigned short u16;
typedef short s16x4 __attribute__((ext_vector_type(4)));
typedef short s16x8 __attribute__((ext_vector_type(8)));
typedef float f32x2 __attribute__((ext_vector_type(2)));
typedef float f32x4 __attribute__((ext_vector_type(4)));
typedef float f32x16 __attribute__((ext_vector_type(16)));
typedef unsigned u32x2 __attribute__((ext_vector_type(2)));
typedef unsigned u32x4 __attribute__((ext_vector_type(4)));
typedef __bf16 bf16x2 __attribute__((ext_vector_type(2)));
#define LAS __attribute__((address_space(3)))
#define DI __device__ __forceinline__

constexpr int SEQ = 2048, DM = 640, NQKV = 1920, NH = 32;
constexpr size_t WS_QF = 0;
constexpr size_t WS_KF = WS_QF + (size_t)DM * SEQ * 2;
constexpr size_t WS_VF = WS_KF + (size_t)DM * SEQ * 2;
constexpr size_t WS_UT = WS_VF + (size_t)DM * SEQ * 2;
constexpr size_t WS_ZT = WS_UT + (size_t)DM * SEQ * 2;
constexpr size_t WS_OW = WS_ZT + (size_t)DM * SEQ * 2;
typedef unsigned long long u64;
constexpr size_t WS_FL = WS_OW + (size_t)DM * SEQ * 2;
constexpr size_t FL_U = WS_FL + 256;
constexpr size_t FL_Q = FL_U + 20 * 64 * 8;
constexpr size_t FL_Z = FL_Q + 16 * 16 * 8;
constexpr size_t FL_O = FL_Z + 640 * 8;

DI unsigned pk_bf16(float lo, float hi) {
  f32x2 f = {lo, hi};
  return __builtin_bit_cast(unsigned, __builtin_convertvector(f, bf16x2));
}
DI float bf_lo(unsigned u) { return __uint_as_float(u << 16); }
DI float bf_hi(unsigned u) { return __uint_as_float(u & 0xffff0000u); }
DI float rbf(float x) { return bf_lo(pk_bf16(x, 0.f)); }

DI unsigned lds_addr(const void* p) { return (unsigned)(uintptr_t)(LAS const char*)p; }

DI void glds16(unsigned voff, const void* sbase, unsigned lds_dst) {
  unsigned keep;
  asm volatile("s_mov_b32 %0, m0\n\ts_mov_b32 m0, %3\n\ts_nop 0\n\tglobal_load_lds_dwordx4 %1, %2\n\ts_mov_b32 m0, %0"
               : "=&s"(keep) : "v"(voff), "s"(sbase), "s"(lds_dst) : "memory");
}
#define LDPOL "sc1"
DI void glds16c(unsigned voff, const void* sbase, unsigned lds_dst) {
  unsigned keep;
  asm volatile("s_mov_b32 %0, m0\n\ts_mov_b32 m0, %3\n\ts_nop 0\n\tglobal_load_lds_dwordx4 %1, %2 " LDPOL "\n\ts_mov_b32 m0, %0"
               : "=&s"(keep) : "v"(voff), "s"(sbase), "s"(lds_dst) : "memory");
}
struct Mk { const void* src; unsigned x; };
DI void glds_mark(const Mk& mk, unsigned lds_dst, int lane) {
  if (lane == 0) {
    unsigned keep;
    asm volatile("s_mov_b32 %0, m0\n\ts_mov_b32 m0, %3\n\ts_nop 0\n\tglobal_load_lds_dword %1, %2\n\ts_mov_b32 m0, %0"
                 : "=&s"(keep) : "v"(0u), "s"(mk.src), "s"(lds_dst) : "memory");
  }
}
DI float dot2bf(unsigned a, unsigned b, float c) {
  return __builtin_amdgcn_fdot2_f32_bf16(__builtin_bit_cast(bf16x2, a), __builtin_bit_cast(bf16x2, b), c, false);
}
DI void gstore16c(void* p, u32x4 v) {
  asm volatile("global_store_dwordx4 %0, %1, off sc1\n\ts_nop 1" :: "v"(p), "v"(v) : "memory");
}
DI void gstore8c(void* p, u32x2 v) {
  asm volatile("global_store_dwordx2 %0, %1, off sc1" :: "v"(p), "v"(v) : "memory");
}
#define OUTPOL "nt"
DI void ostore16(void* p, u32x4 v) { asm volatile("global_store_dwordx4 %0, %1, off " OUTPOL "\n\ts_nop 1" :: "v"(p), "v"(v) : "memory"); }
DI void ostore8(void* p, u32x2 v) { asm volatile("global_store_dwordx2 %0, %1, off " OUTPOL :: "v"(p), "v"(v) : "memory"); }
template <int N> DI void wait_vm() { asm volatile("s_waitcnt vmcnt(%0)" :: "n"(N) : "memory"); }
DI void bar() { asm volatile("s_waitcnt lgkmcnt(0)\n\ts_barrier" ::: "memory"); }
DI s16x4 tr_read(unsigned a) {
  return __builtin_amdgcn_ds_read_tr16_b64_v4i16((LAS s16x4*)(uintptr_t)a);
}
DI s16x8 lds_read16(unsigned a) { return *(LAS const s16x8*)(uintptr_t)a; }
DI s16x4 lds_read8(unsigned a) { return *(LAS const s16x4*)(uintptr_t)a; }
DI void lds_write16(unsigned a, u32x4 v) { *(LAS u32x4*)(uintptr_t)a = v; }
DI s16x8 cat8(s16x4 a, s16x4 b) { return __builtin_shufflevector(a, b, 0, 1, 2, 3, 4, 5, 6, 7); }
#define MFMA16(a, b, c) __builtin_amdgcn_mfma_f32_16x16x32_bf16((a), (b), (c), 0, 0, 0)
#define MFMA32(a, b, c) __builtin_amdgcn_mfma_f32_32x32x16_bf16((a), (b), (c), 0, 0, 0)

#define SBAR() __builtin_amdgcn_sched_barrier(0)
#define SPIN_SLEEP 1
#define SPIN_MAX (1 << 21)
DI unsigned lds_ld32(unsigned a) { return *(volatile LAS unsigned*)(uintptr_t)a; }
DI void lds_st32(unsigned a, unsigned v) { *(volatile LAS unsigned*)(uintptr_t)a = v; }
DI void spin_ge(unsigned a, unsigned want) {
#pragma nounroll
  for (int it = 0; it < SPIN_MAX; ++it) {
    if ((unsigned)__builtin_amdgcn_readfirstlane(lds_ld32(a)) >= want) break;
    __builtin_amdgcn_s_sleep(1);
  }
  asm volatile("" ::: "memory");
}
DI void spin_ge4(unsigned a, unsigned want) {
#pragma nounroll
  for (int it = 0; it < SPIN_MAX; ++it) {
    const u32x4 v = *(volatile LAS u32x4*)(uintptr_t)a;
    const unsigned m = min(min(v[0], v[1]), min(v[2], v[3]));
    if ((unsigned)__builtin_amdgcn_readfirstlane(m) >= want) break;
    __builtin_amdgcn_s_sleep(SPIN_SLEEP);
  }
  asm volatile("" ::: "memory");
}
DI unsigned mk_bad(const u32x4& v, unsigned x) { return (v[0] ^ x) | (v[1] ^ x) | (v[2] ^ x) | (v[3] ^ x); }
DI void spin_mk(unsigned a, unsigned x) {
#pragma nounroll
  for (int it = 0; it < SPIN_MAX; ++it) {
    const u32x4 v = *(volatile LAS u32x4*)(uintptr_t)a;
    if ((unsigned)__builtin_amdgcn_readfirstlane(mk_bad(v, x)) == 0u) break;
    __builtin_amdgcn_s_sleep(SPIN_SLEEP);
  }
  asm volatile("" ::: "memory");
}
DI void spin_ge8(unsigned a, unsigned want) { spin_ge4(a, want); spin_ge4(a + 16, want); }
constexpr int CT_S0 = 0, CT_FREE = 80, CT_PAIR = 144  , CT_QUAD = 192  , CT_ZCNT = 240  ,
              CT_QCNT = 256, CT_ZRDY = 304, CT_CVD = 336  , CT_XST = 416, CT_ATT = 448, CT_RED = 480,
              CT_FX = 512  , CT_KR8 = 688, CT_VR8 = 704, CT_WRDY = 720, CT_WRD2 = 736, CT_BYTES = 1024;
DI void sig(unsigned a, unsigned v, int lane) { if (lane == 0) lds_st32(a, v); }

#define POLL_MAX (1 << 15)
DI void publish(char* ws, size_t off, u64 E, int lane) {
  asm volatile("s_waitcnt vmcnt(0)" ::: "memory");
  if (lane == 0) __hip_atomic_store((u64*)(ws + off), E, __ATOMIC_RELAXED, __HIP_MEMORY_SCOPE_AGENT);
}
#define POLL_SLEEP 6
DI void poll(const char* ws, size_t off, u64 E) {
#pragma nounroll
  for (int it = 0; it < POLL_MAX; ++it) {
    const u64 v = __hip_atomic_load((const u64*)(ws + off), __ATOMIC_RELAXED, __HIP_MEMORY_SCOPE_AGENT);
    if (__builtin_amdgcn_ballot_w64(v != E) == 0ull) break;
    __builtin_amdgcn_s_sleep(POLL_SLEEP);
  }
  asm volatile("" ::: "memory");
}

constexpr int K1_STAGE = 32768;
constexpr int A1_K = 0, A1_V = 3072, A1_W = 5120, A1_WAVE = 5632, A1_O = 2 * A1_WAVE, A1_PAIR = A1_O + 4096;
constexpr int L_AUX = 4 * K1_STAGE;
constexpr int L_CT = L_AUX + 2 * A1_PAIR;
constexpr int L_TOTAL = L_CT + CT_BYTES;
constexpr int K1_CT = L_CT;
constexpr int CV_COPY = 4160, CV_H = 4608, CV_REG = CV_H + 24576;
constexpr int AT_Q = L_AUX + 12288, AT_K = 64000, AT_V = AT_K + 16 * 2048, AT_END = AT_V + 16 * 1344;
constexpr int p3_w(int t) { return t < 5 ? t * 12800 : (t == 5 ? AT_END : AT_K + (t - 6) * 12800); }

#define PSYNC() do { asm volatile("s_waitcnt lgkmcnt(0)" ::: "memory"); ++ps; sig(pf + wh * 4, ps, lane); spin_ge(pf + (wh ^ 1) * 4, ps); } while (0)
DI void a1_issue(unsigned lds, int tile, int lane, int wh, const u16* ak, const u16* av, const u16* shk) {
  const int ts = tile & 63, td = tile >> 6;
  const int s0 = ts * 32 + wh * 16, d0 = td * 64;
  const int lr = lane >> 3, lc = (lane & 7) * 16;
  const unsigned wl = lds + wh * A1_WAVE;
#pragma unroll
  for (int i = 0; i < 3; ++i) {
    int r = s0 - 3 + i * 8 + lr; r = r < 0 ? 0 : r;
    if (i < 2 || lane < 24) glds16((unsigned)(r * DM * 2 + lc), (const char*)ak + d0 * 2, wl + A1_K + i * 1024);
  }
#pragma unroll
  for (int i = 0; i < 2; ++i)
    glds16((unsigned)(lr * DM * 2 + lc), (const char*)av + ((size_t)(s0 + i * 8) * DM + d0) * 2, wl + A1_V + i * 1024);
  if (lane < 32) glds16((unsigned)(lane * 16), (const char*)shk + d0 * 8, wl + A1_W);
}
DI void a1_finish(unsigned lds, unsigned pf, unsigned& ps, int tile, int lane, int wh, char* ws, u64 E) {
  const int ts = tile & 63, td = tile >> 6;
  const int c = lane & 7;
  const unsigned wl = lds + wh * A1_WAVE;
  u32x4 wt[4];
#pragma unroll
  for (int i = 0; i < 4; ++i) wt[i] = __builtin_bit_cast(u32x4, lds_read16(wl + A1_W + c * 64 + i * 16));
#pragma unroll
  for (int j = 0; j < 2; ++j) {
    const int r = j * 8 + (lane >> 3);
    const unsigned ka = wl + A1_K + (r + 3) * 128 + c * 16;
    u32x4 k0 = __builtin_bit_cast(u32x4, lds_read16(ka)), k1 = __builtin_bit_cast(u32x4, lds_read16(ka - 128));
    u32x4 k2 = __builtin_bit_cast(u32x4, lds_read16(ka - 256)), k3 = __builtin_bit_cast(u32x4, lds_read16(ka - 384));
    const u32x4 vv = __builtin_bit_cast(u32x4, lds_read16(wl + A1_V + r * 128 + c * 16));
    if (j == 0 && ts == 0 && wh == 0) {
      const u32x4 z = {0u, 0u, 0u, 0u};
      if (r < 1) k1 = z;
      if (r < 2) k2 = z;
      if (r < 3) k3 = z;
    }
    u32x4 o;
#pragma unroll
    for (int i = 0; i < 4; ++i) {
      const unsigned pa0 = __builtin_amdgcn_perm(k1[i], k0[i], 0x05040100u), pb0 = __builtin_amdgcn_perm(k3[i], k2[i], 0x05040100u);
      const unsigned pa1 = __builtin_amdgcn_perm(k1[i], k0[i], 0x07060302u), pb1 = __builtin_amdgcn_perm(k3[i], k2[i], 0x07060302u);
      const float e0 = dot2bf(pb0, wt[i][1], dot2bf(pa0, wt[i][0], 0.f));
      const float e1 = dot2bf(pb1, wt[i][3], dot2bf(pa1, wt[i][2], 0.f));
      o[i] = pk_bf16(e0 * bf_lo(vv[i]), e1 * bf_hi(vv[i]));
    }
    lds_write16(lds + A1_O + (31 - wh * 16 - r) * 128 + c * 16, o);
  }
  PSYNC();
  const int gg = lane >> 4, q = (lane & 15) >> 2, p = lane & 3;
  u16* ut = (u16*)(ws + WS_UT);
#pragma unroll
  for (int j = 0; j < 2; ++j) {
    const unsigned ra = lds + A1_O + (gg * 8 + q) * 128 + wh * 64 + j * 32 + p * 8;
    const s16x4 t0 = tr_read(ra), t1 = tr_read(ra + 512);
    const int d = td * 64 + wh * 32 + j * 16 + (lane & 15);
    gstore16c(ut + (size_t)d * SEQ + (SEQ - 32 - ts * 32) + gg * 8, __builtin_bit_cast(u32x4, cat8(t0, t1)));
  }
}
DI void a1_publish(int tile, int lane, int wh, char* ws, u64 E) {
  const int ts = tile & 63, td = tile >> 6;
  publish(ws, FL_U + (size_t)((td * 2 + wh) * 64 + ts) * 8, E, lane);
}

template <bool VT>
DI void k1_gemm(unsigned lds0, int m0, int n0, int tid, char* ws, unsigned mkx) {
  const int lane = tid & 63, wave = __builtin_amdgcn_readfirstlane(tid >> 6);
  const int wm = wave >> 1, wn = wave & 1;
  const int l32 = lane & 31, hh = lane >> 5, gg = lane >> 4, q = (lane & 15) >> 2, p = lane & 3;
  const unsigned ct = lds0 + K1_CT;
  f32x16 acc[2][2];
#pragma unroll
  for (int i = 0; i < 2; ++i)
#pragma unroll
    for (int j = 0; j < 2; ++j)
#pragma unroll
      for (int r = 0; r < 16; ++r) acc[i][j][r] = 0.f;
  unsigned xb[4];
#pragma unroll
  for (int ks = 0; ks < 4; ++ks) xb[ks] = lds0 + (wm * 64 + l32) * 128 + (((unsigned)(2 * ks + hh) ^ (unsigned)((l32 >> 1) & 7)) * 16);
  unsigned wb[2];
#pragma unroll
  for (int nt = 0; nt < 2; ++nt)
    wb[nt] = lds0 + 16384 + (8 * hh + q) * 256 + (((unsigned)(wn * 4 + 2 * nt + (gg & 1)) ^ (unsigned)(q << 1)) * 32) + p * 8;
  s16x8 x0[2], w0[2], x1[2], w1[2], x2[2], w2[2], x3[2], w3[2];
#define K1_LOADF(XF, WF, bo_, ks) do { \
    _Pragma("unroll") for (int i_ = 0; i_ < 2; ++i_) { \
      WF[i_] = cat8(tr_read(wb[i_] + (bo_) + (ks) * 4096), tr_read(wb[i_] + (bo_) + (ks) * 4096 + 1024)); \
      XF[i_] = lds_read16(xb[ks] + (bo_) + i_ * 4096); } } while (0)
#define K1_MM(XF, WF) do { _Pragma("unroll") for (int mt = 0; mt < 2; ++mt) _Pragma("unroll") for (int nt = 0; nt < 2; ++nt) \
      acc[mt][nt] = VT ? MFMA32(XF[mt], WF[nt], acc[mt][nt]) : MFMA32(WF[nt], XF[mt], acc[mt][nt]); } while (0)
#define ILV_A 2
#define ILV_B 2
#define ILV_C 2
#define K1_ILV() do { \
      __builtin_amdgcn_sched_group_barrier(0x008, 1, 0); __builtin_amdgcn_sched_group_barrier(0x100, ILV_A, 0); \
      __builtin_amdgcn_sched_group_barrier(0x008, 1, 0); __builtin_amdgcn_sched_group_barrier(0x100, ILV_B, 0); \
      __builtin_amdgcn_sched_group_barrier(0x008, 1, 0); __builtin_amdgcn_sched_group_barrier(0x100, ILV_C, 0); \
      __builtin_amdgcn_sched_group_barrier(0x008, 1, 0); __builtin_amdgcn_sched_group_barrier(0x100, 2, 0); } while (0)
  K1_LOADF(x0, w0, 0, 0);
  K1_LOADF(x1, w1, 0, 1);
#pragma nounroll
  for (int t = 0; t < 10; ++t) {
    const unsigned bo = (t & 3) * K1_STAGE, bn = ((t + 1) & 3) * K1_STAGE;
    SBAR();
    const u32x4 fl4 = *(volatile LAS u32x4*)(uintptr_t)(ct + CT_FX + (t + 1) * 16);
    K1_LOADF(x2, w2, bo, 2); K1_MM(x0, w0); K1_ILV();
    SBAR();
    K1_LOADF(x3, w3, bo, 3); K1_MM(x1, w1); K1_ILV();
    SBAR();
    const unsigned flu = (unsigned)__builtin_amdgcn_readfirstlane(mk_bad(fl4, mkx));
    if (t < 9 && flu != 0u) spin_mk(ct + CT_FX + (t + 1) * 16, mkx);
    asm volatile("" ::: "memory");
    SBAR();
    K1_LOADF(x0, w0, bn, 0); K1_MM(x2, w2); K1_ILV();
    SBAR();
    asm volatile("" : "+v"(x3[0]), "+v"(x3[1]), "+v"(w3[0]), "+v"(w3[1]) :: "memory");
    if (lane == 0) lds_st32(ct + CT_FREE + (t & 3) * 16 + wave * 4, (unsigned)(t + 1));
    SBAR();
    K1_LOADF(x1, w1, bn, 1); K1_MM(x3, w3); K1_ILV();
  }
  SBAR();
  if (!VT) {
    const int part = n0 >= DM ? 1 : 0;
    const float sc = part == 0 ? 0.22360679774997896f * 1.4426950408889634f : 1.0f;
    char* base = ws + (part ? WS_KF : WS_QF) + ((size_t)((m0 + wm * 64) >> 5) * 40 + ((n0 - part * DM + wn * 64) >> 4)) * 1024 + lane * 16;
#pragma unroll
    for (int mt = 0; mt < 2; ++mt)
#pragma unroll
      for (int nt = 0; nt < 2; ++nt)
#pragma unroll
        for (int uh = 0; uh < 2; ++uh) {
          u32x4 o;
#pragma unroll
          for (int i = 0; i < 4; ++i) o[i] = pk_bf16(acc[mt][nt][8 * uh + 2 * i] * sc, acc[mt][nt][8 * uh + 2 * i + 1] * sc);
          gstore16c(base + (mt * 40 + nt * 2 + uh) * 1024, o);
        }
  } else {
    char* base = ws + WS_VF + ((size_t)((m0 + wm * 64) >> 4) * 20 + ((n0 - 2 * DM + wn * 64) >> 5)) * 1024 + lane * 16;
#pragma unroll
    for (int mt = 0; mt < 2; ++mt)
#pragma unroll
      for (int nt = 0; nt < 2; ++nt)
#pragma unroll
        for (int kg = 0; kg < 2; ++kg) {
          u32x4 o;
#pragma unroll
          for (int i = 0; i < 4; ++i) o[i] = pk_bf16(acc[mt][nt][8 * kg + 2 * i], acc[mt][nt][8 * kg + 2 * i + 1]);
          gstore16c(base + ((mt * 2 + kg) * 20 + nt) * 1024, o);
        }
  }
}

DI void cv_put(unsigned y, const u32x4& c, const u32x2& n) {
  const unsigned d0 = c[0], d1 = c[1], d2 = c[2], d3 = c[3], d4 = n[0], d5 = n[1];
  const unsigned a0 = __builtin_amdgcn_alignbit(d1, d0, 16), a1 = __builtin_amdgcn_alignbit(d2, d1, 16),
                 a2 = __builtin_amdgcn_alignbit(d3, d2, 16), a3 = __builtin_amdgcn_alignbit(d4, d3, 16),
                 a4 = __builtin_amdgcn_alignbit(d5, d4, 16);
  lds_write16(y, (u32x4){d0, d1, d2, d3});
  lds_write16(y + CV_COPY, (u32x4){a0, a1, a2, a3});
  lds_write16(y + 2 * CV_COPY, (u32x4){d1, d2, d3, d4});
  lds_write16(y + 3 * CV_COPY, (u32x4){a1, a2, a3, a4});
}

#define QSYNC() do { asm volatile("s_waitcnt lgkmcnt(0)" ::: "memory"); ++qs; sig(qf + wq * 4, qs, lane); spin_ge4(qf, qs); } while (0)
struct HRegs { u32x4 c, u; u32x2 n; unsigned d; };
DI HRegs conv_load_h(int ch, int t256, const u16* __restrict__ ssmk, const u16* __restrict__ ssmd) {
  HRegs r;
  const u16* hrow = ssmk + (size_t)ch * SEQ;
  r.c = *((const u32x4*)(hrow + t256 * 8));
  r.n = *(const u32x2*)(hrow + (t256 < 255 ? t256 * 8 + 8 : 0));
  r.d = ssmd[ch];
  return r;
}
DI void conv_load_u(HRegs& r, int ch, int lane, int wq, const char* __restrict__ ws, u64 E) {
  poll(ws, FL_U + (size_t)((ch >> 5) * 64 + 48 - 16 * wq + (lane & 15)) * 8, E);
  const u64* up = (const u64*)(ws + WS_UT + (size_t)ch * SEQ * 2 + wq * 1024 + lane * 16);
  const u64 a = __hip_atomic_load(up, __ATOMIC_RELAXED, __HIP_MEMORY_SCOPE_AGENT);
  const u64 b = __hip_atomic_load(up + 1, __ATOMIC_RELAXED, __HIP_MEMORY_SCOPE_AGENT);
  r.u = (u32x4){(unsigned)a, (unsigned)(a >> 32), (unsigned)b, (unsigned)(b >> 32)};
}
DI void conv_stage(unsigned reg, int lane, int wq, const HRegs& hr) {
  const int t256 = wq * 64 + lane;
  const unsigned hb = reg + CV_H;
  if (t256 < 32) lds_write16(reg + 4096 + t256 * 16, (u32x4){0u, 0u, 0u, 0u});
  cv_put(hb + (t256 + 2) * 16, hr.c, hr.n);
  if (t256 < 2) {
    const u32x4 z = {0u, 0u, 0u, 0u};
    u32x2 nx = {0u, 0u};
    const unsigned h0 = __builtin_amdgcn_readfirstlane(hr.c[0]), h1 = __builtin_amdgcn_readfirstlane(hr.c[1]);
    if (t256 == 1) nx = (u32x2){h0, h1};
    cv_put(hb + t256 * 16, z, nx);
  }
}
DI void conv_run(unsigned reg, unsigned qf, unsigned& qs, int ch, int lane, int wq, const HRegs& hr,
                 char* __restrict__ ws, unsigned xst) {
  const unsigned hb = reg + CV_H;
  if (xst) spin_ge4(xst, 1u);
  else { lds_write16(reg + (wq * 64 + lane) * 16, hr.u); QSYNC(); }
  const int r16 = lane & 15, g = lane >> 4, q = r16 >> 2, p = lane & 3;
  const unsigned ab0 = hb + ((q + 1) & 3) * CV_COPY + 2 * (8 * g + 4 * p + (q == 3 ? 4 : 0)) + wq * 128;
  const unsigned bb0 = reg + 2 * (2032 - 16 * r16 + 8 * g) + wq * 128 - 7 * 512;
  f32x4 acc[8];
#pragma unroll
  for (int i = 0; i < 8; ++i) acc[i] = (f32x4){0.f, 0.f, 0.f, 0.f};
#define CV_LDA(a_, ad_) cat8(tr_read((ad_) + (a_) * 512), tr_read((ad_) + (a_) * 512 + 8))
#define CV_LDB(c_, bd_) lds_read16((bd_) + (7 - (c_)) * 512)
  {
    s16x8 A[8], B[8];
    B[0] = CV_LDB(0, bb0);
#pragma unroll
    for (int a = 0; a < 8; ++a) A[a] = CV_LDA(a, ab0);
#pragma unroll
    for (int c = 1; c < 8; ++c) B[c] = CV_LDB(c, bb0);
#pragma unroll
    for (int c = 0; c < 8; ++c) {
      SBAR();
#pragma unroll
      for (int a = 0; a + c < 8; ++a) acc[a + c] = MFMA16(A[a], B[c], acc[a + c]);
      B[c] = CV_LDB(c, bb0 + 64);
      A[7 - c] = CV_LDA(7 - c, ab0 + 64);
    }
    SBAR();
#pragma unroll
    for (int a = 7; a >= 0; --a)
#pragma unroll
      for (int c = 0; a + c < 8; ++c) acc[a + c] = MFMA16(A[a], B[c], acc[a + c]);
  }
  QSYNC();
#pragma unroll
  for (int nt = 0; nt < 8; ++nt) {
    const int dw = nt >> 1;
    if (dw != wq) lds_write16(hb + (nt * 3 + (wq > dw ? wq - 1 : wq)) * 1024 + lane * 16, __builtin_bit_cast(u32x4, acc[nt]));
  }
  f32x4 k0 = wq == 0 ? acc[0] : (wq == 1 ? acc[2] : (wq == 2 ? acc[4] : acc[6]));
  f32x4 k1 = wq == 0 ? acc[1] : (wq == 1 ? acc[3] : (wq == 2 ? acc[5] : acc[7]));
  QSYNC();
  const float dco = bf_lo(hr.d);
  u32x2 zz[2];
#pragma unroll
  for (int i = 0; i < 2; ++i) {
    f32x4 o = i ? k1 : k0;
#pragma unroll
    for (int j = 0; j < 3; ++j) o += __builtin_bit_cast(f32x4, lds_read16(hb + ((wq * 2 + i) * 3 + j) * 1024 + lane * 16));
    const int s = (wq * 2 + i) * 256 + 16 * r16 + 4 * g;
    const u32x2 uu = __builtin_bit_cast(u32x2, lds_read8(reg + 2 * (SEQ - 4 - s)));
    const float kv0 = bf_hi(uu[1]), kv1 = bf_lo(uu[1]), kv2 = bf_hi(uu[0]), kv3 = bf_lo(uu[0]);
    const unsigned y01 = pk_bf16(o[0], o[1]), y23 = pk_bf16(o[2], o[3]);
    const unsigned e01 = pk_bf16(kv0 * dco, kv1 * dco), e23 = pk_bf16(kv2 * dco, kv3 * dco);
    zz[i][0] = pk_bf16(bf_lo(y01) + bf_lo(e01), bf_hi(y01) + bf_hi(e01));
    zz[i][1] = pk_bf16(bf_lo(y23) + bf_lo(e23), bf_hi(y23) + bf_hi(e23));
  }
  const auto sx = __builtin_amdgcn_permlane16_swap(zz[0][0], zz[1][0], false, false);
  const auto sy = __builtin_amdgcn_permlane16_swap(zz[0][1], zz[1][1], false, false);
  u16* zrow = (u16*)(ws + WS_ZT) + (size_t)ch * SEQ;
  gstore16c(zrow + (wq * 2 + (g & 1)) * 256 + 16 * r16 + 8 * (g >> 1), (u32x4){sx[0], sy[0], sx[1], sy[1]});
  asm volatile("s_waitcnt lgkmcnt(0)" ::: "memory");
}
DI void conv_publish(unsigned zc, int ch, int lane, char* __restrict__ ws, u64 E) {
  asm volatile("s_waitcnt vmcnt(0)" ::: "memory");
  if (lane == 0) {
    const unsigned old = __hip_atomic_fetch_add((LAS unsigned*)(uintptr_t)zc, 1u, __ATOMIC_RELAXED, __HIP_MEMORY_SCOPE_WORKGROUP);
    if ((old & 3u) == 3u) __hip_atomic_store((u64*)(ws + FL_Z + (size_t)ch * 8), E, __ATOMIC_RELAXED, __HIP_MEMORY_SCOPE_AGENT);
  }
}

DI s16x8 pack8(const f32x16& v, int o) {
  u32x4 r;
  r[0] = pk_bf16(v[o + 0], v[o + 1]); r[1] = pk_bf16(v[o + 2], v[o + 3]);
  r[2] = pk_bf16(v[o + 4], v[o + 5]); r[3] = pk_bf16(v[o + 6], v[o + 7]);
  return __builtin_bit_cast(s16x8, r);
}
DI float max3f(float a, float b, float c) { return __builtin_fmaxf(__builtin_fmaxf(a, b), c); }
DI float max16(const f32x16& v, float m) {
  float a = max3f(v[0], v[1], m), b = max3f(v[2], v[3], v[4]);
  a = max3f(a, v[5], v[6]); b = max3f(b, v[7], v[8]);
  a = max3f(a, v[9], v[10]); b = max3f(b, v[11], v[12]);
  a = max3f(a, v[13], v[14]);
  return max3f(a, b, v[15]);
}
DI float xhalf_max(float m) {
  auto sw = __builtin_amdgcn_permlane32_swap(__float_as_uint(m), __float_as_uint(m), false, false);
  return __builtin_fmaxf(__uint_as_float(sw[0]), __uint_as_float(sw[1]));
}

#define AT_LMAX 65536.0f
#define P3_WADDR() unsigned wv[4]; _Pragma("unroll") for (int i = 0; i < 4; ++i) { \
      const int c = i < 3 ? (i * 4 + w) * 64 + lane : 768 + w * 8 + (lane & 7); const int row = c / 10; \
      wv[i] = (unsigned)(row * DM * 2 + (c - row * 10) * 16); } const char* wog = (const char*)(wo + n3)
#define P3_STW(t) do { _Pragma("unroll") for (int i_ = 0; i_ < 3; ++i_) \
        glds16(wv[i_], wog + (size_t)(t) * 80 * DM * 2, lds0 + p3_w(t) + (i_ * 4 + w) * 1024); \
      if (lane < 8) glds16(wv[3], wog + (size_t)(t) * 80 * DM * 2, lds0 + p3_w(t) + 12288 + w * 128); } while (0)
DI void attn_stage(unsigned lds0, int h, int qb8, int j, int lane, char* __restrict__ ws, u64 E, int zch, const Mk& mk) {
  const int qt = qb8 * 8 + j;
  const unsigned l16 = (unsigned)(lane * 16), ct = lds0 + L_CT;
  const int qa = (5 * h) >> 2;
  const bool stg = qb8 > 0 || j >= 2;
  {
    const int l3 = lane & 7, hi = lane & 1;
    const bool kv = stg && l3 >= 2 && l3 < 6;
    const int rb = kv ? 2 * qb8 - 2 + j : 2 * qb8 + (l3 >= 6 ? 1 : 0);
    const int col = !kv ? qa * 16 + hi * 31 : (l3 < 4 ? DM + qa * 16 + hi * 31 : 2 * DM + h * 20 + hi * 19);
    poll(ws, FL_Q + (size_t)(rb * 16 + (col >> 7)) * 8, E);
  }
  conv_publish(ct + CT_ZCNT + 8, zch, lane, ws, E);
  const int kt0 = qb8 * 8 - 8 + 4 * j;
#pragma unroll
  for (int i = 0; i < 4; ++i)
    glds16c(l16, ws + WS_QF + ((size_t)(qt + (i >> 1) * 4) * 40 + qa + (i & 1)) * 1024, lds0 + AT_Q + ((j + (i >> 1) * 4) * 2 + (i & 1)) * 1024);
  if (stg) {
#pragma unroll
    for (int i = 0; i < 8; ++i)
      glds16c(l16, ws + WS_KF + ((size_t)(kt0 + (i >> 1)) * 40 + qa + (i & 1)) * 1024, lds0 + AT_K + (j * 8 + i) * 1024);
  }
  glds_mark(mk, ct + CT_KR8 + j * 4, lane);
  if (stg) {
#pragma unroll
    for (int i = 0; i < 6; ++i) {
      const int c = i * 64 + lane;
      const int kgp = (c * 1561) >> 16, rem = c - kgp * 42, vh = rem >= 21 ? 1 : 0, d = rem - vh * 21;
      const int n = h * 20 + d;
      if (c < 336 && d == 20) lds_write16(lds0 + AT_V + j * 5376 + c * 16, (u32x4){0x3f803f80u, 0x3f803f80u, 0x3f803f80u, 0x3f803f80u});
      if (c < 336 && d < 20)
        glds16c((unsigned)((kgp * 20 + (n >> 5)) * 1024 + (vh * 32 + (n & 31)) * 16), ws + WS_VF + ((size_t)(kt0 * 2) * 20) * 1024,
                lds0 + AT_V + j * 5376 + i * 1024);
    }
    asm volatile("s_waitcnt lgkmcnt(0)" ::: "memory");
  }
  glds_mark(mk, ct + CT_VR8 + j * 4, lane);
}
template <bool FULL>
DI void attn_task(unsigned lds0, int h, int qb8, int j, int lane, char* __restrict__ ws, u64 E, int zch, const Mk& mk) {
  const int l32 = lane & 31, hh = lane >> 5;
  const int qt = qb8 * 8 + j;
  const int tfirst = FULL ? 0 : 8 - qt;
  const unsigned l16 = (unsigned)(lane * 16), ct = lds0 + L_CT;
  const int qa = (5 * h) >> 2;
  u32x4 q0r, q1r;
  spin_mk(ct + CT_KR8, mk.x);
  if (!FULL) spin_mk(ct + CT_VR8, mk.x);
  q0r = __builtin_bit_cast(u32x4, lds_read16(lds0 + AT_Q + j * 2048 + l16));
  q1r = __builtin_bit_cast(u32x4, lds_read16(lds0 + AT_Q + j * 2048 + 1024 + l16));
  asm volatile("s_waitcnt lgkmcnt(0)" : "+v"(q0r), "+v"(q1r) :: "memory");
  if (zch >= 0) conv_publish(ct + CT_ZCNT + (j >> 2) * 4, zch, lane, ws, E);
  {
    const int g0 = 4 * qa + hh - 5 * h;
    const bool k0 = g0 >= 0, k1 = g0 + 2 >= 0 && g0 + 2 < 5, k2 = g0 + 4 < 5, k3 = g0 + 6 < 5;
    q0r[0] = k0 ? q0r[0] : 0u; q0r[1] = k0 ? q0r[1] : 0u; q0r[2] = k1 ? q0r[2] : 0u; q0r[3] = k1 ? q0r[3] : 0u;
    q1r[0] = k2 ? q1r[0] : 0u; q1r[1] = k2 ? q1r[1] : 0u; q1r[2] = k3 ? q1r[2] : 0u; q1r[3] = k3 ? q1r[3] : 0u;
  }
  s16x8 qlo = __builtin_bit_cast(s16x8, q0r), qhi8 = __builtin_bit_cast(s16x8, q1r);
  const f32x16 zero16 = {0.f, 0.f, 0.f, 0.f, 0.f, 0.f, 0.f, 0.f, 0.f, 0.f, 0.f, 0.f, 0.f, 0.f, 0.f, 0.f};
  const float ninf = -INFINITY;
  const f32x16 ninf16 = {ninf, ninf, ninf, ninf, ninf, ninf, ninf, ninf, ninf, ninf, ninf, ninf, ninf, ninf, ninf, ninf};
  const unsigned kb = lds0 + AT_K + j * 2048 + l16;
#define AT_QK(dst, t, cin) do { if (FULL || (t) >= tfirst) { dst = MFMA32(lds_read16(kb + (t) * 2048), qlo, cin); \
    dst = MFMA32(lds_read16(kb + (t) * 2048 + 1024), qhi8, dst); } else dst = ninf16; } while (0)
  const int vrow = l32 <= 20 ? l32 : (l32 == 24 ? 20 : l32 - 16);
  const unsigned vb = lds0 + AT_V + j * 1344 + hh * 336 + vrow * 16;
#define AT_EXP(sv) do { _Pragma("unroll") for (int r = 0; r < 16; ++r) sv[r] = __builtin_amdgcn_exp2f(sv[r] - m); } while (0)
#define AT_PV(sv, t) do { oacc = MFMA32(lds_read16(vb + (t) * 1344), pack8(sv, 0), oacc); oacc = MFMA32(lds_read16(vb + (t) * 1344 + 672), pack8(sv, 8), oacc); } while (0)
  f32x16 oacc;
  bool exact = !FULL;
  if (FULL) {
    f32x16 sm, sa, sb;
    AT_QK(sm, 8, zero16); AT_QK(sa, 0, zero16);
    const int tq = l32 - 4 * hh;
    const unsigned wm = tq < 0 ? 0xffffffffu : (0xfffffffeu << tq);
    unsigned msk[16];
#pragma unroll
    for (int r = 0; r < 16; ++r) {
      msk[r] = (unsigned)__builtin_amdgcn_sbfe((int)wm, (r & 3) + 8 * (r >> 2), 1);
      sm[r] = __uint_as_float((msk[r] & __float_as_uint(sa[r])) | (~msk[r] & __float_as_uint(sm[r])));
    }
    const float m = xhalf_max(max16(sm, ninf));
    const float nm = -m;
    const f32x16 cneg = {nm, nm, nm, nm, nm, nm, nm, nm, nm, nm, nm, nm, nm, nm, nm, nm};
    AT_QK(sa, 1, cneg);
    AT_EXP(sm);
    u32x4 pl = __builtin_bit_cast(u32x4, pack8(sm, 0)), ph = __builtin_bit_cast(u32x4, pack8(sm, 8)), ql, qh;
#pragma unroll
    for (int i = 0; i < 4; ++i) {
      ql[i] = pl[i] & ((msk[2 * i] & 0xffffu) | (msk[2 * i + 1] & 0xffff0000u)); pl[i] ^= ql[i];
      qh[i] = ph[i] & ((msk[8 + 2 * i] & 0xffffu) | (msk[9 + 2 * i] & 0xffff0000u)); ph[i] ^= qh[i];
    }
    spin_mk(ct + CT_VR8, mk.x);
    oacc = MFMA32(lds_read16(vb + 8 * 1344), __builtin_bit_cast(s16x8, pl), zero16);
    oacc = MFMA32(lds_read16(vb + 8 * 1344 + 672), __builtin_bit_cast(s16x8, ph), oacc);
    oacc = MFMA32(lds_read16(vb), __builtin_bit_cast(s16x8, ql), oacc);
    oacc = MFMA32(lds_read16(vb + 672), __builtin_bit_cast(s16x8, qh), oacc);
#define AT_EX0(sv) do { _Pragma("unroll") for (int r = 0; r < 16; ++r) sv[r] = __builtin_amdgcn_exp2f(sv[r]); } while (0)
    AT_QK(sb, 2, cneg); AT_EX0(sa); AT_PV(sa, 1);
    AT_QK(sa, 3, cneg); AT_EX0(sb); AT_PV(sb, 2);
    AT_QK(sb, 4, cneg); AT_EX0(sa); AT_PV(sa, 3);
    AT_QK(sa, 5, cneg); AT_EX0(sb); AT_PV(sb, 4);
    AT_QK(sb, 6, cneg); AT_EX0(sa); AT_PV(sa, 5);
    AT_QK(sa, 7, cneg); AT_EX0(sb); AT_PV(sb, 6);
    AT_EX0(sa); AT_PV(sa, 7);
    float big = max3f(__builtin_fabsf(oacc[0]), __builtin_fabsf(oacc[1]), __builtin_fabsf(oacc[2]));
    big = max3f(big, __builtin_fabsf(oacc[3]), __builtin_fabsf(oacc[4]));
    big = max3f(big, __builtin_fabsf(oacc[5]), __builtin_fabsf(oacc[6]));
    big = max3f(big, __builtin_fabsf(oacc[7]), __builtin_fabsf(oacc[8]));
    big = max3f(big, __builtin_fabsf(oacc[9]), __builtin_fabsf(oacc[10]));
    big = __builtin_fmaxf(big, __builtin_fabsf(oacc[11]));
    const float ls = hh ? oacc[8] : oacc[12];
    float osum = 0.f;
#pragma unroll
    for (int r = 0; r < 8; ++r) osum += oacc[r];
    const bool ok = (ls < AT_LMAX) && (big < 1e37f) && (osum == osum);
    exact = __builtin_amdgcn_ballot_w64(!ok) != 0ull;
  }
  if (exact) {
    asm volatile("" : "+v"(qlo), "+v"(qhi8) :: "memory");
    f32x16 sm, s1, s2, s3;
    {
      f32x16 s0;
      AT_QK(sm, 8, zero16); AT_QK(s0, 0, zero16);
      AT_QK(s1, 1, zero16); AT_QK(s2, 2, zero16); AT_QK(s3, 3, zero16);
#pragma unroll
      for (int r = 0; r < 16; ++r) {
        const int kk = (r & 3) + 8 * (r >> 2) + 4 * hh;
        sm[r] = kk > l32 ? s0[r] : sm[r];
      }
    }
    float m = max16(sm, ninf);
    m = max16(s1, m); m = max16(s2, m); m = max16(s3, m);
    m = xhalf_max(m);
    AT_EXP(sm);
    oacc = zero16;
    {
      f32x16 p0, p8;
#pragma unroll
      for (int r = 0; r < 16; ++r) {
        const int kk = (r & 3) + 8 * (r >> 2) + 4 * hh;
        p0[r] = kk > l32 ? sm[r] : 0.f;
        p8[r] = kk > l32 ? 0.f : sm[r];
      }
      AT_PV(p8, 8);
      if (FULL) AT_PV(p0, 0);
    }
    if (FULL || 1 >= tfirst) { AT_EXP(s1); AT_PV(s1, 1); }
    AT_QK(sm, 4, zero16); AT_QK(s1, 5, zero16);
    if (FULL || 2 >= tfirst) { AT_EXP(s2); AT_PV(s2, 2); }
    AT_QK(s2, 6, zero16);
    if (FULL || 3 >= tfirst) { AT_EXP(s3); AT_PV(s3, 3); }
    AT_QK(s3, 7, zero16);
    float m2 = max16(sm, m);
    m2 = max16(s1, m2); m2 = max16(s2, m2); m2 = max16(s3, m2);
    m2 = xhalf_max(m2);
    {
      const float alpha = __builtin_amdgcn_exp2f(m - m2);
#pragma unroll
      for (int r = 0; r < 13; ++r) oacc[r] *= alpha;
      m = m2;
    }
    if (FULL || 4 >= tfirst) { AT_EXP(sm); AT_PV(sm, 4); }
    if (FULL || 5 >= tfirst) { AT_EXP(s1); AT_PV(s1, 5); }
    if (FULL || 6 >= tfirst) { AT_EXP(s2); AT_PV(s2, 6); }
    if (FULL || 7 >= tfirst) { AT_EXP(s3); AT_PV(s3, 7); }
  }
  asm volatile("s_waitcnt lgkmcnt(0)" ::: "memory");
  sig(ct + CT_ATT + j * 4, 1u, lane);
  const float l = hh ? oacc[8] : oacc[12];
  const float inv = __builtin_amdgcn_rcpf(l);
  char* oc = ws + WS_OW + ((size_t)qt * NH + h) * 1280;
  u32x4 o;
  o[0] = pk_bf16(oacc[0] * inv, oacc[1] * inv); o[1] = pk_bf16(oacc[2] * inv, oacc[3] * inv);
  o[2] = pk_bf16(oacc[4] * inv, oacc[5] * inv); o[3] = pk_bf16(oacc[6] * inv, oacc[7] * inv);
  gstore16c(oc + lane * 16, o);
  if (!hh) {
    u32x2 o2;
    o2[0] = pk_bf16(oacc[8] * inv, oacc[9] * inv); o2[1] = pk_bf16(oacc[10] * inv, oacc[11] * inv);
    gstore8c(oc + 1024 + l32 * 8, o2);
  }
  publish(ws, FL_O + (size_t)(qt * NH + h) * 8, E, lane);
}

#define APRIO 1
#define LPRIO 0
#define MPRIO 2
#define ROTG 2
__global__ __launch_bounds__(768, 3) void mega(const u16* __restrict__ x, const u16* __restrict__ wqkv,
                                               const u16* __restrict__ ak, const u16* __restrict__ av,
                                               const u16* __restrict__ shk, const u16* __restrict__ wo,
                                               const u16* __restrict__ ssmk, const u16* __restrict__ ssmd,
                                               const u16* __restrict__ aq, char* __restrict__ ws,
                                               u16* __restrict__ out) {
  extern __shared__ __attribute__((aligned(16))) char smem[];
  const unsigned lds0 = lds_addr(smem);
  const int tid = threadIdx.x, wave = __builtin_amdgcn_readfirstlane(tid >> 6);
  int lane = tid & 63;
#define RELANE() asm volatile("" : "+v"(lane))
  const int role = wave >> 2, w = wave & 3;
  const int bid = blockIdx.x, xcd = bid & 7, idx = bid >> 3;
  const bool has_gemm = idx < 30;
  const unsigned ct = lds0 + L_CT;
  u64 E;
  Mk mk; mk.src = wqkv;
  asm volatile("s_load_dwordx2 %0, %2, 0x0\n\ts_load_dword %1, %3, 0x0" : "=&s"(E), "=&s"(mk.x) : "s"(ws + WS_FL), "s"(wqkv) : "memory");
  const int gt = (xcd & 1) * 30 + idx;
  const int tm = (xcd >> 1) * 4 + (gt & 3), tn = gt >> 2;
  const int m0 = tm * 128, n0 = tn * 128;
  const int rot = ROTG >= 10 ? (((gt & 3) * 5 + 1) / 2 + 3 * tn) % 10 : ((((gt & 3) + tn) % ROTG) * 10) / ROTG;
  const unsigned xo0 = (unsigned)((lane >> 3) * DM * 2 + (((lane & 7) ^ (lane >> 4)) * 16));
  const unsigned xo1 = (unsigned)((lane >> 3) * DM * 2 + (((lane & 7) ^ (4 + (lane >> 4))) * 16));
  const unsigned wof = (unsigned)((lane >> 4) * NQKV * 2 + (((lane & 15) ^ ((lane >> 4) << 2)) * 16));
  const char* xg = (const char*)(x + (size_t)m0 * DM);
  const char* wg = (const char*)(wqkv + n0);
#define K1_PIECE_X(t, kt, j) glds16(((j) & 1) ? xo1 : xo0, xg + (size_t)(j) * 8 * DM * 2 + (kt) * 128, lds0 + ((t) & 3) * K1_STAGE + (j) * 1024)
#define K1_PIECE_W(t, kt, j) glds16(wof, wg + (size_t)((kt) * 64 + (j) * 4) * NQKV * 2, lds0 + ((t) & 3) * K1_STAGE + 16384 + (j) * 1024)
  if (has_gemm && role == 0) {
#pragma unroll
    for (int i = 0; i < 4; ++i) { K1_PIECE_X(0, rot, w * 4 + i); K1_PIECE_W(0, rot, w * 4 + i); }
  }
  const unsigned pf = ct + CT_PAIR + (wave >> 1) * 8;
  const int wh = wave & 1;
  unsigned ps = 0;
  const int fb = (idx - 30) * 8 + xcd;
  const int a1t = has_gemm ? (xcd * 30 + idx) * 2 + (w >> 1) : 480 + fb * 10 + (wave >> 1);
  const unsigned a1l = has_gemm ? lds0 + L_AUX + (w >> 1) * A1_PAIR : lds0 + (wave >> 1) * A1_PAIR;
  if (!has_gemm || role == 2) a1_issue(a1l, a1t, lane, wh, ak, av, shk);
  const bool cv0 = role != 0 || !has_gemm || bid < 112;
  const int ch0 = role == 0 ? (has_gemm ? 528 + bid : 512 + fb) : bid + (role == 1 ? 256 : 0);
  asm volatile("s_waitcnt lgkmcnt(0)" : "+s"(E), "+s"(mk.x) :: "memory");
  if (role == 1) lds_st32(ct + (tid - 256) * 4, (tid - 256) * 4 >= CT_FX ? ~mk.x : 0u);
  bar();
  E += 1ull;

  const bool ex = has_gemm && role == 1 && bid < 112;
  HRegs hr, h2;
#define LOAD_H() do { hr = conv_load_h(ch0, w * 64 + lane, ssmk, ssmd); \
    if (ex) h2 = conv_load_h(528 + bid, w * 64 + lane, ssmk, ssmd); } while (0)
  auto q_publish = [&]() {
    asm volatile("s_waitcnt vmcnt(0)" ::: "memory");
    if (lane == 0) {
      const unsigned old = __hip_atomic_fetch_add((LAS unsigned*)(uintptr_t)(ct + CT_QCNT), 1u, __ATOMIC_RELAXED, __HIP_MEMORY_SCOPE_WORKGROUP);
      if (old == 3u) __hip_atomic_store((u64*)(ws + FL_Q + (size_t)(tm * 16 + tn) * 8), E, __ATOMIC_RELAXED, __HIP_MEMORY_SCOPE_AGENT);
    }
  };
  if (!has_gemm) {
    LOAD_H();
    const unsigned a2l = lds0 + (6 + (wave >> 1)) * A1_PAIR;
    if (wave < 8) a1_issue(a2l, a1t + 6, lane, wh, ak, av, shk);
    if (wave < 8) wait_vm<6>(); else wait_vm<0>();
    a1_finish(a1l, pf, ps, a1t, lane, wh, ws, E);
    if (wave < 8) { wait_vm<2>(); a1_finish(a2l, pf, ps, a1t + 6, lane, wh, ws, E); }
    a1_publish(a1t, lane, wh, ws, E);
    if (wave < 8) a1_publish(a1t + 6, lane, wh, ws, E);
    bar();
  } else if (role == 0) {
    wait_vm<0>();
    sig(ct + CT_S0 + w * 4, 1u, lane);
    spin_ge4(ct + CT_S0, 1u);
    __builtin_amdgcn_s_setprio(MPRIO);
    if (tn >= 10) k1_gemm<true>(lds0, m0, n0, tid, ws, mk.x);
    else k1_gemm<false>(lds0, m0, n0, tid, ws, mk.x);
  } else if (role == 1) {
    __builtin_amdgcn_s_setprio(LPRIO);
#define K1_ST(t) do { const int kt_ = (t) + rot >= 10 ? (t) + rot - 10 : (t) + rot; \
      if ((t) >= 4) spin_ge4(ct + CT_FREE + ((t) & 3) * 16, (unsigned)((t) - 3)); \
      _Pragma("unroll") for (int i_ = 0; i_ < 4; ++i_) { K1_PIECE_X(t, kt_, w * 4 + i_); K1_PIECE_W(t, kt_, w * 4 + i_); } } while (0)
#pragma nounroll
    for (int t = 1; t < 4; ++t) { K1_ST(t); glds_mark(mk, ct + CT_FX + t * 16 + w * 4, lane); }
    LOAD_H();
#pragma nounroll
    for (int t = 4; t < 10; ++t) { K1_ST(t); glds_mark(mk, ct + CT_FX + t * 16 + w * 4, lane); }
    __builtin_amdgcn_s_setprio(0);
  } else {
    LOAD_H();
    __builtin_amdgcn_s_setprio(2);
    wait_vm<0>();
    a1_finish(a1l, pf, ps, a1t, lane, wh, ws, E);
    a1_publish(a1t, lane, wh, ws, E);
    __builtin_amdgcn_s_setprio(0);
  }

  RELANE();
  unsigned qs = 0;
  const unsigned qf = ct + CT_QUAD + role * 16;
  const unsigned creg = lds0 + (role == 2 ? L_AUX : (role == 1 ? 29184 : 0));
  if (role == 2) __builtin_amdgcn_s_setprio(APRIO); else __builtin_amdgcn_s_setprio(0);
  if (has_gemm && role == 0) {
    q_publish();
    if (cv0) {
      hr.d = ssmd[ch0];
      conv_run(creg, qf, qs, ch0, lane, w, hr, ws, ct + CT_XST);
    }
  } else {
    if (has_gemm && role == 1) {
      conv_load_u(hr, ch0, lane, w, ws, E);
      if (ex) conv_load_u(h2, 528 + bid, lane, w, ws, E);
      spin_ge4(ct + CT_FREE + 16, 10u);
      conv_stage(creg, lane, w, hr);
      if (ex) {
        conv_stage(lds0, lane, w, h2);
        lds_write16(lds0 + (w * 64 + lane) * 16, h2.u);
        asm volatile("s_waitcnt lgkmcnt(0)" ::: "memory");
        sig(ct + CT_XST + w * 4, 1u, lane);
      }
    } else {
      if (has_gemm) { const int wq = w; QSYNC(); }
      conv_stage(creg, lane, w, hr);
      conv_load_u(hr, ch0, lane, w, ws, E);
    }
    conv_run(creg, qf, qs, ch0, lane, w, hr, ws, 0u);
  }
  sig(ct + CT_CVD + wave * 4, 1u, lane);

  const int tm3 = xcd * 4 + (idx >> 3), tn3 = idx & 7;
  const int m3 = tm3 * 64, n3 = tn3 * 80;
  RELANE();

  if (role == 2) {
    const int r16 = lane & 15, g = lane >> 4, q = r16 >> 2, p = lane & 3;
    __builtin_amdgcn_s_setprio(3);
    spin_ge4(ct + CT_CVD + 32, 1u);
    if (has_gemm) spin_ge4(ct + CT_FREE + 16, 10u);
    attn_stage(lds0, xcd * 4 + (idx & 3), idx >> 2, w, lane, ws, E, ch0, mk);
    {
      P3_WADDR();
      P3_STW(5);
      spin_ge8(ct + CT_CVD, 1u);
      P3_STW(0); P3_STW(4); P3_STW(1); P3_STW(2); P3_STW(3);
      glds_mark(mk, ct + CT_WRDY + w * 4, lane);
    }
    __builtin_amdgcn_s_setprio(0);
    u32x4 qv[3];
#pragma unroll
    for (int j = 0; j < 3; ++j) {
      const int dc = j < 2 ? j * 4 + g : 8 + (g & 1);
      qv[j] = *((const u32x4*)(aq + (size_t)(m3 + w * 16 + r16) * DM + n3 + dc * 8));
    }
    {
      const int l5 = lane & 31;
      const int cl = l5 < 8 ? w * 8 + l5 : (l5 < 16 ? 32 + w * 8 + (l5 - 8) : 64 + w * 4 + (l5 & 3));
      poll(ws, FL_Z + (size_t)(n3 + cl) * 8, E);
    }
    {
      const char* zg = ws + WS_ZT + ((size_t)n3 * SEQ + m3) * 2;
      const unsigned zo = (unsigned)((lane >> 3) * SEQ * 2 + (lane & 7) * 16);
      glds16c(zo, zg + (size_t)(w * 8) * SEQ * 2, lds0 + L_AUX + w * 1024);
      glds16c(zo, zg + (size_t)(32 + w * 8) * SEQ * 2, lds0 + L_AUX + 4096 + w * 1024);
      if (lane < 32) glds16c(zo, zg + (size_t)(64 + w * 4) * SEQ * 2, lds0 + L_AUX + 8192 + w * 512);
    }
    wait_vm<0>();
    sig(ct + CT_ZRDY + w * 4, 1u, lane);
    spin_ge4(ct + CT_ZRDY, 1u);
    u32x4 zz[3];
#pragma unroll
    for (int j = 0; j < 3; ++j) {
      const int dc = j < 2 ? j * 4 + g : 8 + (g & 1);
      const unsigned za = lds0 + L_AUX + (dc * 8 + q) * 128 + w * 32 + p * 8;
      zz[j] = __builtin_bit_cast(u32x4, cat8(tr_read(za), tr_read(za + 512)));
    }
#pragma unroll
    for (int j = 0; j < 3; ++j) {
      const int dc = j < 2 ? j * 4 + g : 8 + (g & 1);
      u32x4 o;
#pragma unroll
      for (int i = 0; i < 4; ++i) o[i] = pk_bf16(bf_lo(qv[j][i]) * bf_lo(zz[j][i]), bf_hi(qv[j][i]) * bf_hi(zz[j][i]));
      if (j < 2 || g < 2) ostore16(out + (size_t)(m3 + w * 16 + r16) * (2 * DM) + n3 + dc * 8, o);
    }
    if (bid == 0 && w == 0) {
#pragma unroll
      for (int i = 0; i < 4; ++i) poll(ws, FL_O + (size_t)(((i * 2 + (lane >> 5)) * 8) * NH + (lane & 31)) * 8, E);
      if (lane == 0) __hip_atomic_store((u64*)(ws + WS_FL), E, __ATOMIC_RELAXED, __HIP_MEMORY_SCOPE_AGENT);
    }
    return;
  }

  const int h = xcd * 4 + (idx & 3), qb8 = idx >> 2;
  __builtin_amdgcn_s_setprio(1);
  RELANE();
  if (qb8 > 0) attn_task<true>(lds0, h, qb8, wave, lane, ws, E, cv0 ? ch0 : -1, mk);
  else attn_task<false>(lds0, h, qb8, wave, lane, ws, E, cv0 ? ch0 : -1, mk);

  RELANE();
  const int r16 = lane & 15, g = lane >> 4, q = r16 >> 2, p = lane & 3;
  f32x4 acc[5];
#pragma unroll
  for (int i = 0; i < 5; ++i) acc[i] = (f32x4){0.f, 0.f, 0.f, 0.f};
  const unsigned wbp = lds0 + (20 * (g >> 1) + 4 * (g & 1) + q) * 160 + p * 8;
  const unsigned wbs = lds0 + (20 * g + 16 + q) * 160 + p * 8;
  u32x4 op[8]; u32x2 osg[4];
  if (role == 1) {
    P3_WADDR();
    spin_ge8(ct + CT_ATT, 1u);
    P3_STW(6); P3_STW(7);
    glds_mark(mk, ct + CT_WRD2 + w * 4, lane);
  }
  spin_mk(ct + CT_WRDY, mk.x);
  const unsigned w0 = role ? p3_w(4) : p3_w(0), w1 = role ? p3_w(5) : p3_w(1), w2 = role ? p3_w(6) : p3_w(2), w3 = role ? p3_w(7) : p3_w(3);
  struct Fw { s16x8 a[5], b[5]; s16x4 s[5]; };
  Fw fwa, fwb;
#define P3_LOADW(F, bo_) do { _Pragma("unroll") for (int nt = 0; nt < 5; ++nt) { \
      F.a[nt] = cat8(tr_read(wbp + (bo_) + nt * 32), tr_read(wbp + (bo_) + 8 * 160 + nt * 32)); \
      F.b[nt] = cat8(tr_read(wbp + (bo_) + 40 * 160 + nt * 32), tr_read(wbp + (bo_) + 48 * 160 + nt * 32)); \
      F.s[nt] = tr_read(wbs + (bo_) + nt * 32); } } while (0)
#define P3_MM(F, t) do { \
    _Pragma("unroll") for (int nt = 0; nt < 5; ++nt) acc[nt] = MFMA16(F.a[nt], __builtin_bit_cast(s16x8, op[2 * (t)]), acc[nt]); \
    _Pragma("unroll") for (int nt = 0; nt < 5; ++nt) acc[nt] = MFMA16(F.b[nt], __builtin_bit_cast(s16x8, op[2 * (t) + 1]), acc[nt]); } while (0)
#define P3_MS(t) do { const s16x8 os_ = cat8(__builtin_bit_cast(s16x4, osg[t]), __builtin_bit_cast(s16x4, osg[(t) + 1])); \
    _Pragma("unroll") for (int nt = 0; nt < 5; ++nt) acc[nt] = MFMA16(cat8(fwa.s[nt], fwb.s[nt]), os_, acc[nt]); } while (0)
  P3_LOADW(fwa, w0);
  SBAR();
  poll(ws, FL_O + (size_t)(((m3 >> 5) + (w >> 1)) * NH + role * 16 + r16) * 8, E);
  {
    const int orow = (w & 1) * 16 + r16;
    const char* ob = ws + WS_OW + ((size_t)((m3 >> 5) + (w >> 1)) * NH + role * 16) * 1280;
    const char* pp = ob + (g >> 1) * 1280 + (g & 1) * 512 + orow * 16;
    const char* sp = ob + g * 1280 + 1024 + orow * 8;
#pragma unroll
    for (int t = 0; t < 4; ++t) {
      asm volatile("global_load_dwordx4 %0, %3, off sc1\n\tglobal_load_dwordx4 %1, %3, off offset:2560 sc1\n\tglobal_load_dwordx2 %2, %4, off sc1"
                   : "=&v"(op[2 * t]), "=&v"(op[2 * t + 1]), "=&v"(osg[t]) : "v"(pp + t * 5120), "v"(sp + t * 5120) : "memory");
    }
  }
  P3_LOADW(fwb, w1);
  if (role == 1) spin_mk(ct + CT_WRD2, mk.x);
  SBAR();
  asm volatile("s_waitcnt vmcnt(6)" : "+v"(op[0]), "+v"(op[1]), "+v"(op[2]), "+v"(op[3]), "+v"(osg[0]), "+v"(osg[1]) :: "memory");
  P3_MS(0); P3_MM(fwa, 0);
  SBAR();
  P3_LOADW(fwa, w2); P3_MM(fwb, 1);
#pragma unroll
  for (int i = 0; i < 10; ++i) { __builtin_amdgcn_sched_group_barrier(0x008, 1, 0); __builtin_amdgcn_sched_group_barrier(0x100, 3, 0); }
  SBAR();
  asm volatile("s_waitcnt vmcnt(0)" : "+v"(op[4]), "+v"(op[5]), "+v"(op[6]), "+v"(op[7]), "+v"(osg[2]), "+v"(osg[3]) :: "memory");
  P3_LOADW(fwb, w3); P3_MM(fwa, 2);
#pragma unroll
  for (int i = 0; i < 10; ++i) { __builtin_amdgcn_sched_group_barrier(0x008, 1, 0); __builtin_amdgcn_sched_group_barrier(0x100, 3, 0); }
  SBAR();
  P3_MM(fwb, 3); P3_MS(2);
  SBAR();
  const unsigned rb = lds0 + AT_K + 25600 + w * 5120 + lane * 16;
  if (role == 1) {
#pragma unroll
    for (int nt = 0; nt < 5; ++nt) lds_write16(rb + nt * 1024, __builtin_bit_cast(u32x4, acc[nt]));
    asm volatile("s_waitcnt lgkmcnt(0)" ::: "memory");
    sig(ct + CT_RED + w * 4, 1u, lane);
    return;
  }
  spin_ge(ct + CT_RED + w * 4, 1u);
  u16* orow_g = out + (size_t)(m3 + w * 16 + r16) * (2 * DM) + DM + n3;
  u32x2 ov[5];
#pragma unroll
  for (int nt = 0; nt < 5; ++nt) {
    const f32x4 o2 = acc[nt] + __builtin_bit_cast(f32x4, lds_read16(rb + nt * 1024));
    ov[nt][0] = pk_bf16(o2[0], o2[1]);
    ov[nt][1] = pk_bf16(o2[2], o2[3]);
  }
#pragma unroll
  for (int pr = 0; pr < 2; ++pr) {
    const auto sx = __builtin_amdgcn_permlane16_swap(ov[2 * pr][0], ov[2 * pr + 1][0], false, false);
    const auto sy = __builtin_amdgcn_permlane16_swap(ov[2 * pr][1], ov[2 * pr + 1][1], false, false);
    ostore16(orow_g + (2 * pr + (g & 1)) * 16 + 8 * (g >> 1), (u32x4){sx[0], sy[0], sx[1], sy[1]});
  }
  ostore8(orow_g + 64 + 4 * g, ov[4]);
}

extern "C" void kernel_launch(void* const* d_in, const int* in_sizes, int n_in, void* d_out, int out_size,
                              void* d_ws, size_t ws_size, hipStream_t stream) {
  const u16* aq = (const u16*)d_in[0];
  const u16* ak = (const u16*)d_in[1];
  const u16* av = (const u16*)d_in[2];
  const u16* shk = (const u16*)d_in[3];
  const u16* ssmk = (const u16*)d_in[4];
  const u16* ssmd = (const u16*)d_in[5];
  const u16* x = (const u16*)d_in[6];
  const u16* wqkv = (const u16*)d_in[7];
  const u16* wo = (const u16*)d_in[8];
  (void)hipFuncSetAttribute((const void*)mega, hipFuncAttributeMaxDynamicSharedMemorySize, L_TOTAL);
  mega<<<256, 768, L_TOTAL, stream>>>(x, wqkv, ak, av, shk, wo, ssmk, ssmd, aq, (char*)d_ws, (u16*)d_out);
}
```

```cpp
#define LDELAY 6
#include <hip/hip_runtime.h>
#include <hip/hip_bf16.h>
#include <stdint.h>

typedef unsigned short u16;
typedef short s16x4 __attribute__((ext_vector_type(4)));
typedef short s16x8 __attribute__((ext_vector_type(8)));
typedef float f32x2 __attribute__((ext_vector_type(2)));
typedef float f32x4 __attribute__((ext_vector_type(4)));
typedef float f32x16 __attribute__((ext_vector_type(16)));
typedef unsigned u32x2 __attribute__((ext_vector_type(2)));
typedef unsigned u32x4 __attribute__((ext_vector_type(4)));
typedef __bf16 bf16x2 __attribute__((ext_vector_type(2)));
#define LAS __attribute__((address_space(3)))
#define DI __device__ __forceinline__

constexpr int SEQ = 2048, DM = 640, NQKV = 1920, NH = 32;
constexpr size_t WS_QF = 0;
constexpr size_t WS_KF = WS_QF + (size_t)DM * SEQ * 2;
constexpr size_t WS_VF = WS_KF + (size_t)DM * SEQ * 2;
constexpr size_t WS_UT = WS_VF + (size_t)DM * SEQ * 2;
constexpr size_t WS_ZT = WS_UT + (size_t)DM * SEQ * 2;
constexpr size_t WS_OW = WS_ZT + (size_t)DM * SEQ * 2;
typedef unsigned long long u64;
constexpr size_t WS_FL = WS_OW + (size_t)DM * SEQ * 2;
constexpr size_t FL_U = WS_FL + 256;
constexpr size_t FL_Q = FL_U + 20 * 64 * 8;
constexpr size_t FL_Z = FL_Q + 16 * 16 * 8;
constexpr size_t FL_O = FL_Z + 640 * 8;

DI unsigned pk_bf16(float lo, float hi) {
  f32x2 f = {lo, hi};
  return __builtin_bit_cast(unsigned, __builtin_convertvector(f, bf16x2));
}
DI float bf_lo(unsigned u) { return __uint_as_float(u << 16); }
DI float bf_hi(unsigned u) { return __uint_as_float(u & 0xffff0000u); }
DI float rbf(float x) { return bf_lo(pk_bf16(x, 0.f)); }

DI unsigned lds_addr(const void* p) { return (unsigned)(uintptr_t)(LAS const char*)p; }

DI void glds16(unsigned voff, const void* sbase, unsigned lds_dst) {
  unsigned keep;
  asm volatile("s_mov_b32 %0, m0\n\ts_mov_b32 m0, %3\n\ts_nop 0\n\tglobal_load_lds_dwordx4 %1, %2\n\ts_mov_b32 m0, %0"
               : "=&s"(keep) : "v"(voff), "s"(sbase), "s"(lds_dst) : "memory");
}
DI void glds16f(unsigned voff, const void* sbase, unsigned lds_dst) {
  asm volatile("s_mov_b32 m0, %2\n\ts_nop 0\n\tglobal_load_lds_dwordx4 %0, %1" :: "v"(voff), "s"(sbase), "s"(lds_dst) : "memory");
}
#define glds16p glds16
DI void glds16nt(unsigned voff, const void* sbase, unsigned lds_dst) {
  unsigned keep;
  asm volatile("s_mov_b32 %0, m0\n\ts_mov_b32 m0, %3\n\ts_nop 0\n\tglobal_load_lds_dwordx4 %1, %2\n\ts_mov_b32 m0, %0"
               : "=&s"(keep) : "v"(voff), "s"(sbase), "s"(lds_dst) : "memory");
}
#define LDPOL "sc1"
DI void glds16c(unsigned voff, const void* sbase, unsigned lds_dst) {
  unsigned keep;
  asm volatile("s_mov_b32 %0, m0\n\ts_mov_b32 m0, %3\n\ts_nop 0\n\tglobal_load_lds_dwordx4 %1, %2 " LDPOL "\n\ts_mov_b32 m0, %0"
               : "=&s"(keep) : "v"(voff), "s"(sbase), "s"(lds_dst) : "memory");
}
struct Mk { const void* src; unsigned x; };
DI void glds_mark(const Mk& mk, unsigned lds_dst, int lane) {
  if (lane == 0) {
    unsigned keep;
    asm volatile("s_mov_b32 %0, m0\n\ts_mov_b32 m0, %3\n\ts_nop 0\n\tglobal_load_lds_dword %1, %2\n\ts_mov_b32 m0, %0"
                 : "=&s"(keep) : "v"(0u), "s"(mk.src), "s"(lds_dst) : "memory");
  }
}
DI float dot2bf(unsigned a, unsigned b, float c) {
  return __builtin_amdgcn_fdot2_f32_bf16(__builtin_bit_cast(bf16x2, a), __builtin_bit_cast(bf16x2, b), c, false);
}
DI void gstore16c(void* p, u32x4 v) {
  asm volatile("global_store_dwordx4 %0, %1, off sc1\n\ts_nop 1" :: "v"(p), "v"(v) : "memory");
}
DI void gstore8c(void* p, u32x2 v) {
  asm volatile("global_store_dwordx2 %0, %1, off sc1" :: "v"(p), "v"(v) : "memory");
}
#define OUTPOL "nt"
DI void ostore16(void* p, u32x4 v) { asm volatile("global_store_dwordx4 %0, %1, off " OUTPOL "\n\ts_nop 1" :: "v"(p), "v"(v) : "memory"); }
DI void ostore8(void* p, u32x2 v) { asm volatile("global_store_dwordx2 %0, %1, off " OUTPOL :: "v"(p), "v"(v) : "memory"); }
template <int N> DI void wait_vm() { asm volatile("s_waitcnt vmcnt(%0)" :: "n"(N) : "memory"); }
DI void bar() { asm volatile("s_waitcnt lgkmcnt(0)\n\ts_barrier" ::: "memory"); }
DI s16x4 tr_read(unsigned a) {
  return __builtin_amdgcn_ds_read_tr16_b64_v4i16((LAS s16x4*)(uintptr_t)a);
}
DI s16x8 lds_read16(unsigned a) { return *(LAS const s16x8*)(uintptr_t)a; }
DI s16x4 lds_read8(unsigned a) { return *(LAS const s16x4*)(uintptr_t)a; }
DI void lds_write16(unsigned a, u32x4 v) { *(LAS u32x4*)(uintptr_t)a = v; }
DI s16x8 cat8(s16x4 a, s16x4 b) { return __builtin_shufflevector(a, b, 0, 1, 2, 3, 4, 5, 6, 7); }
#define MFMA16(a, b, c) __builtin_amdgcn_mfma_f32_16x16x32_bf16((a), (b), (c), 0, 0, 0)
#define MFMA32(a, b, c) __builtin_amdgcn_mfma_f32_32x32x16_bf16((a), (b), (c), 0, 0, 0)

struct Diag {};
#define STAMP(i) do {} while (0)
#define DIAG_INIT() Diag dg
#define DIAG_DUMP(kid) do {} while (0)

#define SBAR() __builtin_amdgcn_sched_barrier(0)
#define SPIN_SLEEP 1
#define SPIN_MAX (1 << 21)
DI unsigned lds_ld32(unsigned a) { return *(volatile LAS unsigned*)(uintptr_t)a; }
DI void lds_st32(unsigned a, unsigned v) { *(volatile LAS unsigned*)(uintptr_t)a = v; }
DI void spin_ge(unsigned a, unsigned want) {
#pragma nounroll
  for (int it = 0; it < SPIN_MAX; ++it) {
    if ((unsigned)__builtin_amdgcn_readfirstlane(lds_ld32(a)) >= want) break;
    __builtin_amdgcn_s_sleep(1);
  }
  asm volatile("" ::: "memory");
}
DI void spin_ge4(unsigned a, unsigned want) {
#pragma nounroll
  for (int it = 0; it < SPIN_MAX; ++it) {
    const u32x4 v = *(volatile LAS u32x4*)(uintptr_t)a;
    const unsigned m = min(min(v[0], v[1]), min(v[2], v[3]));
    if ((unsigned)__builtin_amdgcn_readfirstlane(m) >= want) break;
    __builtin_amdgcn_s_sleep(SPIN_SLEEP);
  }
  asm volatile("" ::: "memory");
}
DI unsigned mk_bad(const u32x4& v, unsigned x) { return (v[0] ^ x) | (v[1] ^ x) | (v[2] ^ x) | (v[3] ^ x); }
DI void spin_mk(unsigned a, unsigned x) {
#pragma nounroll
  for (int it = 0; it < SPIN_MAX; ++it) {
    const u32x4 v = *(volatile LAS u32x4*)(uintptr_t)a;
    if ((unsigned)__builtin_amdgcn_readfirstlane(mk_bad(v, x)) == 0u) break;
    __builtin_amdgcn_s_sleep(SPIN_SLEEP);
  }
  asm volatile("" ::: "memory");
}
DI void spin_ge8(unsigned a, unsigned want) { spin_ge4(a, want); spin_ge4(a + 16, want); }
constexpr int CT_S0 = 0, CT_FREE = 80, CT_PAIR = 144  , CT_QUAD = 192  , CT_ZCNT = 240  ,
              CT_QCNT = 256, CT_ZRDY = 304, CT_CVD = 336  , CT_XST = 416, CT_ATT = 448, CT_RED = 480,
              CT_FX = 512  , CT_KR8 = 688, CT_VR8 = 704, CT_WRDY = 720, CT_WRD2 = 736, CT_BYTES = 1024;
DI void sig(unsigned a, unsigned v, int lane) { if (lane == 0) lds_st32(a, v); }

#define POLL_MAX (1 << 15)
DI void publish(char* ws, size_t off, u64 E, int lane) {
  asm volatile("s_waitcnt vmcnt(0)" ::: "memory");
  if (lane == 0) __hip_atomic_store((u64*)(ws + off), E, __ATOMIC_RELAXED, __HIP_MEMORY_SCOPE_AGENT);
}
#define POLL_SLEEP 6
DI void poll(const char* ws, size_t off, u64 E) {
#pragma nounroll
  for (int it = 0; it < POLL_MAX; ++it) {
    const u64 v = __hip_atomic_load((const u64*)(ws + off), __ATOMIC_RELAXED, __HIP_MEMORY_SCOPE_AGENT);
    if (__builtin_amdgcn_ballot_w64(v != E) == 0ull) break;
    __builtin_amdgcn_s_sleep(POLL_SLEEP);
  }
  asm volatile("" ::: "memory");
}

constexpr int K1_STAGE = 32768;
constexpr int A1_K = 0, A1_V = 3072, A1_W = 5120, A1_WAVE = 5632, A1_O = 2 * A1_WAVE, A1_PAIR = A1_O + 4096;
constexpr int L_AUX = 4 * K1_STAGE;
constexpr int L_CT = L_AUX + 2 * A1_PAIR;
constexpr int L_TOTAL = L_CT + CT_BYTES;
constexpr int K1_CT = L_CT;
constexpr int CV_COPY = 4160, CV_H = 4608, CV_REG = CV_H + 24576;
constexpr int AT_Q = L_AUX + 12288, AT_K = 64000, AT_V = AT_K + 16 * 2048, AT_END = AT_V + 16 * 1344;
constexpr int p3_w(int t) { return t < 5 ? t * 12800 : (t == 5 ? AT_END : AT_K + (t - 6) * 12800); }

#define PSYNC() do { asm volatile("s_waitcnt lgkmcnt(0)" ::: "memory"); ++ps; sig(pf + wh * 4, ps, lane); spin_ge(pf + (wh ^ 1) * 4, ps); } while (0)
DI void a1_issue(unsigned lds, int tile, int lane, int wh, const u16* ak, const u16* av, const u16* shk) {
  const int ts = tile & 63, td = tile >> 6;
  const int s0 = ts * 32 + wh * 16, d0 = td * 64;
  const int lr = lane >> 3, lc = (lane & 7) * 16;
  const unsigned wl = lds + wh * A1_WAVE;
#pragma unroll
  for (int i = 0; i < 3; ++i) {
    int r = s0 - 3 + i * 8 + lr; r = r < 0 ? 0 : r;
    if (i < 2 || lane < 24) glds16nt((unsigned)(r * DM * 2 + lc), (const char*)ak + d0 * 2, wl + A1_K + i * 1024);
  }
#pragma unroll
  for (int i = 0; i < 2; ++i)
    glds16nt((unsigned)(lr * DM * 2 + lc), (const char*)av + ((size_t)(s0 + i * 8) * DM + d0) * 2, wl + A1_V + i * 1024);
  if (lane < 32) glds16((unsigned)(lane * 16), (const char*)shk + d0 * 8, wl + A1_W);
}
DI void a1_finish(unsigned lds, unsigned pf, unsigned& ps, int tile, int lane, int wh, char* ws, u64 E) {
  const int ts = tile & 63, td = tile >> 6;
  const int c = lane & 7;
  const unsigned wl = lds + wh * A1_WAVE;
  u32x4 wt[4];
#pragma unroll
  for (int i = 0; i < 4; ++i) wt[i] = __builtin_bit_cast(u32x4, lds_read16(wl + A1_W + c * 64 + i * 16));
#pragma unroll
  for (int j = 0; j < 2; ++j) {
    const int r = j * 8 + (lane >> 3);
    const unsigned ka = wl + A1_K + (r + 3) * 128 + c * 16;
    u32x4 k0 = __builtin_bit_cast(u32x4, lds_read16(ka)), k1 = __builtin_bit_cast(u32x4, lds_read16(ka - 128));
    u32x4 k2 = __builtin_bit_cast(u32x4, lds_read16(ka - 256)), k3 = __builtin_bit_cast(u32x4, lds_read16(ka - 384));
    const u32x4 vv = __builtin_bit_cast(u32x4, lds_read16(wl + A1_V + r * 128 + c * 16));
    if (j == 0 && ts == 0 && wh == 0) {
      const u32x4 z = {0u, 0u, 0u, 0u};
      if (r < 1) k1 = z;
      if (r < 2) k2 = z;
      if (r < 3) k3 = z;
    }
    u32x4 o;
#pragma unroll
    for (int i = 0; i < 4; ++i) {
      const unsigned pa0 = __builtin_amdgcn_perm(k1[i], k0[i], 0x05040100u), pb0 = __builtin_amdgcn_perm(k3[i], k2[i], 0x05040100u);
      const unsigned pa1 = __builtin_amdgcn_perm(k1[i], k0[i], 0x07060302u), pb1 = __builtin_amdgcn_perm(k3[i], k2[i], 0x07060302u);
      const float e0 = dot2bf(pb0, wt[i][1], dot2bf(pa0, wt[i][0], 0.f));
      const float e1 = dot2bf(pb1, wt[i][3], dot2bf(pa1, wt[i][2], 0.f));
      o[i] = pk_bf16(e0 * bf_lo(vv[i]), e1 * bf_hi(vv[i]));
    }
    lds_write16(lds + A1_O + (31 - wh * 16 - r) * 128 + c * 16, o);
  }
  PSYNC();
  const int gg = lane >> 4, q = (lane & 15) >> 2, p = lane & 3;
  u16* ut = (u16*)(ws + WS_UT);
#pragma unroll
  for (int j = 0; j < 2; ++j) {
    const unsigned ra = lds + A1_O + (gg * 8 + q) * 128 + wh * 64 + j * 32 + p * 8;
    const s16x4 t0 = tr_read(ra), t1 = tr_read(ra + 512);
    const int d = td * 64 + wh * 32 + j * 16 + (lane & 15);
    gstore16c(ut + (size_t)d * SEQ + (SEQ - 32 - ts * 32) + gg * 8, __builtin_bit_cast(u32x4, cat8(t0, t1)));
  }
}
DI void a1_publish(int tile, int lane, int wh, char* ws, u64 E) {
  const int ts = tile & 63, td = tile >> 6;
  publish(ws, FL_U + (size_t)((td * 2 + wh) * 64 + ts) * 8, E, lane);
}

template <bool VT>
DI void k1_gemm(unsigned lds0, int m0, int n0, int tid, char* ws, unsigned mkx, Diag& dg) {
  const int lane = tid & 63, wave = __builtin_amdgcn_readfirstlane(tid >> 6);
  const int wm = wave >> 1, wn = wave & 1;
  const int l32 = lane & 31, hh = lane >> 5, gg = lane >> 4, q = (lane & 15) >> 2, p = lane & 3;
  const unsigned ct = lds0 + K1_CT;
  f32x16 acc[2][2];
#pragma unroll
  for (int i = 0; i < 2; ++i)
#pragma unroll
    for (int j = 0; j < 2; ++j)
#pragma unroll
      for (int r = 0; r < 16; ++r) acc[i][j][r] = 0.f;
  unsigned xb[4];
#pragma unroll
  for (int ks = 0; ks < 4; ++ks) xb[ks] = lds0 + (wm * 64 + l32) * 128 + (((unsigned)(2 * ks + hh) ^ (unsigned)((l32 >> 1) & 7)) * 16);
  unsigned wb[2];
#pragma unroll
  for (int nt = 0; nt < 2; ++nt)
    wb[nt] = lds0 + 16384 + (8 * hh + q) * 256 + (((unsigned)(wn * 4 + 2 * nt + (gg & 1)) ^ (unsigned)(q << 1)) * 32) + p * 8;
  s16x8 x0[2], w0[2], x1[2], w1[2], x2[2], w2[2], x3[2], w3[2];
#define K1_LOADF(XF, WF, bo_, ks) do { \
    _Pragma("unroll") for (int i_ = 0; i_ < 2; ++i_) { \
      WF[i_] = cat8(tr_read(wb[i_] + (bo_) + (ks) * 4096), tr_read(wb[i_] + (bo_) + (ks) * 4096 + 1024)); \
      XF[i_] = lds_read16(xb[ks] + (bo_) + i_ * 4096); } } while (0)
#define K1_MM(XF, WF) do { _Pragma("unroll") for (int mt = 0; mt < 2; ++mt) _Pragma("unroll") for (int nt = 0; nt < 2; ++nt) \
      acc[mt][nt] = VT ? MFMA32(XF[mt], WF[nt], acc[mt][nt]) : MFMA32(WF[nt], XF[mt], acc[mt][nt]); } while (0)
#define ILV_A 2
#define ILV_B 2
#define ILV_C 2
#define K1_ILV() do { \
      __builtin_amdgcn_sched_group_barrier(0x008, 1, 0); __builtin_amdgcn_sched_group_barrier(0x100, ILV_A, 0); \
      __builtin_amdgcn_sched_group_barrier(0x008, 1, 0); __builtin_amdgcn_sched_group_barrier(0x100, ILV_B, 0); \
      __builtin_amdgcn_sched_group_barrier(0x008, 1, 0); __builtin_amdgcn_sched_group_barrier(0x100, ILV_C, 0); \
      __builtin_amdgcn_sched_group_barrier(0x008, 1, 0); __builtin_amdgcn_sched_group_barrier(0x100, 2, 0); } while (0)
  STAMP(1);
  K1_LOADF(x0, w0, 0, 0);
  K1_LOADF(x1, w1, 0, 1);
#pragma nounroll
  for (int t = 0; t < 10; ++t) {
    const unsigned bo = (t & 3) * K1_STAGE, bn = ((t + 1) & 3) * K1_STAGE;
    SBAR();
    const u32x4 fl4 = *(volatile LAS u32x4*)(uintptr_t)(ct + CT_FX + (t + 1) * 16);
    K1_LOADF(x2, w2, bo, 2); K1_MM(x0, w0); K1_ILV();
    SBAR();
    K1_LOADF(x3, w3, bo, 3); K1_MM(x1, w1); K1_ILV();
    SBAR();
    const unsigned flu = (unsigned)__builtin_amdgcn_readfirstlane(mk_bad(fl4, mkx));
    if (t < 9 && flu != 0u) spin_mk(ct + CT_FX + (t + 1) * 16, mkx);
    asm volatile("" ::: "memory");
    SBAR();
    K1_LOADF(x0, w0, bn, 0); K1_MM(x2, w2); K1_ILV();
    SBAR();
    asm volatile("" : "+v"(x3[0]), "+v"(x3[1]), "+v"(w3[0]), "+v"(w3[1]) :: "memory");
    if (lane == 0) lds_st32(ct + CT_FREE + (t & 3) * 16 + wave * 4, (unsigned)(t + 1));
    SBAR();
    K1_LOADF(x1, w1, bn, 1); K1_MM(x3, w3); K1_ILV();
  }
  SBAR();
  STAMP(2);
  if (!VT) {
    const int part = n0 >= DM ? 1 : 0;
    const float sc = part == 0 ? 0.22360679774997896f * 1.4426950408889634f : 1.0f;
    char* base = ws + (part ? WS_KF : WS_QF) + ((size_t)((m0 + wm * 64) >> 5) * 40 + ((n0 - part * DM + wn * 64) >> 4)) * 1024 + lane * 16;
#pragma unroll
    for (int mt = 0; mt < 2; ++mt)
#pragma unroll
      for (int nt = 0; nt < 2; ++nt)
#pragma unroll
        for (int uh = 0; uh < 2; ++uh) {
          u32x4 o;
#pragma unroll
          for (int i = 0; i < 4; ++i) o[i] = pk_bf16(acc[mt][nt][8 * uh + 2 * i] * sc, acc[mt][nt][8 * uh + 2 * i + 1] * sc);
          gstore16c(base + (mt * 40 + nt * 2 + uh) * 1024, o);
        }
  } else {
    char* base = ws + WS_VF + ((size_t)((m0 + wm * 64) >> 4) * 20 + ((n0 - 2 * DM + wn * 64) >> 5)) * 1024 + lane * 16;
#pragma unroll
    for (int mt = 0; mt < 2; ++mt)
#pragma unroll
      for (int nt = 0; nt < 2; ++nt)
#pragma unroll
        for (int kg = 0; kg < 2; ++kg) {
          u32x4 o;
#pragma unroll
          for (int i = 0; i < 4; ++i) o[i] = pk_bf16(acc[mt][nt][8 * kg + 2 * i], acc[mt][nt][8 * kg + 2 * i + 1]);
          gstore16c(base + ((mt * 2 + kg) * 20 + nt) * 1024, o);
        }
  }
}

DI void cv_put(unsigned y, const u32x4& c, const u32x2& n) {
  const unsigned d0 = c[0], d1 = c[1], d2 = c[2], d3 = c[3], d4 = n[0], d5 = n[1];
  const unsigned a0 = __builtin_amdgcn_alignbit(d1, d0, 16), a1 = __builtin_amdgcn_alignbit(d2, d1, 16),
                 a2 = __builtin_amdgcn_alignbit(d3, d2, 16), a3 = __builtin_amdgcn_alignbit(d4, d3, 16),
                 a4 = __builtin_amdgcn_alignbit(d5, d4, 16);
  lds_write16(y, (u32x4){d0, d1, d2, d3});
  lds_write16(y + CV_COPY, (u32x4){a0, a1, a2, a3});
  lds_write16(y + 2 * CV_COPY, (u32x4){d1, d2, d3, d4});
  lds_write16(y + 3 * CV_COPY, (u32x4){a1, a2, a3, a4});
}

#define QSYNC() do { asm volatile("s_waitcnt lgkmcnt(0)" ::: "memory"); ++qs; sig(qf + wq * 4, qs, lane); spin_ge4(qf, qs); } while (0)
struct HRegs { u32x4 c, u; u32x2 n; unsigned d; };
DI HRegs conv_load_h(int ch, int t256, const u16* __restrict__ ssmk, const u16* __restrict__ ssmd) {
  HRegs r;
  const u16* hrow = ssmk + (size_t)ch * SEQ;
  r.c = *((const u32x4*)(hrow + t256 * 8));
  r.n = *(const u32x2*)(hrow + (t256 < 255 ? t256 * 8 + 8 : 0));
  r.d = ssmd[ch];
  return r;
}
DI void conv_load_u(HRegs& r, int ch, int lane, int wq, const char* __restrict__ ws, u64 E) {
  poll(ws, FL_U + (size_t)((ch >> 5) * 64 + 48 - 16 * wq + (lane & 15)) * 8, E);
  const u64* up = (const u64*)(ws + WS_UT + (size_t)ch * SEQ * 2 + wq * 1024 + lane * 16);
  const u64 a = __hip_atomic_load(up, __ATOMIC_RELAXED, __HIP_MEMORY_SCOPE_AGENT);
  const u64 b = __hip_atomic_load(up + 1, __ATOMIC_RELAXED, __HIP_MEMORY_SCOPE_AGENT);
  r.u = (u32x4){(unsigned)a, (unsigned)(a >> 32), (unsigned)b, (unsigned)(b >> 32)};
}
DI void conv_stage(unsigned reg, int lane, int wq, const HRegs& hr) {
  const int t256 = wq * 64 + lane;
  const unsigned hb = reg + CV_H;
  if (t256 < 32) lds_write16(reg + 4096 + t256 * 16, (u32x4){0u, 0u, 0u, 0u});
  cv_put(hb + (t256 + 2) * 16, hr.c, hr.n);
  if (t256 < 2) {
    const u32x4 z = {0u, 0u, 0u, 0u};
    u32x2 nx = {0u, 0u};
    const unsigned h0 = __builtin_amdgcn_readfirstlane(hr.c[0]), h1 = __builtin_amdgcn_readfirstlane(hr.c[1]);
    if (t256 == 1) nx = (u32x2){h0, h1};
    cv_put(hb + t256 * 16, z, nx);
  }
}
DI void conv_run(unsigned reg, unsigned qf, unsigned& qs, int ch, int lane, int wq, const HRegs& hr,
                 char* __restrict__ ws, unsigned xst, Diag& dg) {
  const unsigned hb = reg + CV_H;
  if (xst) spin_ge4(xst, 1u);
  else { lds_write16(reg + (wq * 64 + lane) * 16, hr.u); QSYNC(); }
  STAMP(6);
  const int r16 = lane & 15, g = lane >> 4, q = r16 >> 2, p = lane & 3;
  const unsigned ab0 = hb + ((q + 1) & 3) * CV_COPY + 2 * (8 * g + 4 * p + (q == 3 ? 4 : 0)) + wq * 128;
  const unsigned bb0 = reg + 2 * (2032 - 16 * r16 + 8 * g) + wq * 128 - 7 * 512;
  f32x4 acc[8];
#pragma unroll
  for (int i = 0; i < 8; ++i) acc[i] = (f32x4){0.f, 0.f, 0.f, 0.f};
#define CV_LDA(a_, ad_) cat8(tr_read((ad_) + (a_) * 512), tr_read((ad_) + (a_) * 512 + 8))
#define CV_LDB(c_, bd_) lds_read16((bd_) + (7 - (c_)) * 512)
  {
    s16x8 A[8], B[8];
    B[0] = CV_LDB(0, bb0);
#pragma unroll
    for (int a = 0; a < 8; ++a) A[a] = CV_LDA(a, ab0);
#pragma unroll
    for (int c = 1; c < 8; ++c) B[c] = CV_LDB(c, bb0);
#pragma unroll
    for (int c = 0; c < 8; ++c) {
      SBAR();
#pragma unroll
      for (int a = 0; a + c < 8; ++a) acc[a + c] = MFMA16(A[a], B[c], acc[a + c]);
      B[c] = CV_LDB(c, bb0 + 64);
      A[7 - c] = CV_LDA(7 - c, ab0 + 64);
    }
    SBAR();
#pragma unroll
    for (int a = 7; a >= 0; --a)
#pragma unroll
      for (int c = 0; a + c < 8; ++c) acc[a + c] = MFMA16(A[a], B[c], acc[a + c]);
  }
  QSYNC();
#pragma unroll
  for (int nt = 0; nt < 8; ++nt) {
    const int dw = nt >> 1;
    if (dw != wq) lds_write16(hb + (nt * 3 + (wq > dw ? wq - 1 : wq)) * 1024 + lane * 16, __builtin_bit_cast(u32x4, acc[nt]));
  }
  f32x4 k0 = wq == 0 ? acc[0] : (wq == 1 ? acc[2] : (wq == 2 ? acc[4] : acc[6]));
  f32x4 k1 = wq == 0 ? acc[1] : (wq == 1 ? acc[3] : (wq == 2 ? acc[5] : acc[7]));
  QSYNC();
  const float dco = bf_lo(hr.d);
  u32x2 zz[2];
#pragma unroll
  for (int i = 0; i < 2; ++i) {
    f32x4 o = i ? k1 : k0;
#pragma unroll
    for (int j = 0; j < 3; ++j) o += __builtin_bit_cast(f32x4, lds_read16(hb + ((wq * 2 + i) * 3 + j) * 1024 + lane * 16));
    const int s = (wq * 2 + i) * 256 + 16 * r16 + 4 * g;
    const u32x2 uu = __builtin_bit_cast(u32x2, lds_read8(reg + 2 * (SEQ - 4 - s)));
    const float kv0 = bf_hi(uu[1]), kv1 = bf_lo(uu[1]), kv2 = bf_hi(uu[0]), kv3 = bf_lo(uu[0]);
    const unsigned y01 = pk_bf16(o[0], o[1]), y23 = pk_bf16(o[2], o[3]);
    const unsigned e01 = pk_bf16(kv0 * dco, kv1 * dco), e23 = pk_bf16(kv2 * dco, kv3 * dco);
    zz[i][0] = pk_bf16(bf_lo(y01) + bf_lo(e01), bf_hi(y01) + bf_hi(e01));
    zz[i][1] = pk_bf16(bf_lo(y23) + bf_lo(e23), bf_hi(y23) + bf_hi(e23));
  }
  const auto sx = __builtin_amdgcn_permlane16_swap(zz[0][0], zz[1][0], false, false);
  const auto sy = __builtin_amdgcn_permlane16_swap(zz[0][1], zz[1][1], false, false);
  u16* zrow = (u16*)(ws + WS_ZT) + (size_t)ch * SEQ;
  gstore16c(zrow + (wq * 2 + (g & 1)) * 256 + 16 * r16 + 8 * (g >> 1), (u32x4){sx[0], sy[0], sx[1], sy[1]});
  asm volatile("s_waitcnt lgkmcnt(0)" ::: "memory");
}
DI void conv_publish(unsigned zc, int ch, int lane, char* __restrict__ ws, u64 E) {
  asm volatile("s_waitcnt vmcnt(0)" ::: "memory");
  if (lane == 0) {
    const unsigned old = __hip_atomic_fetch_add((LAS unsigned*)(uintptr_t)zc, 1u, __ATOMIC_RELAXED, __HIP_MEMORY_SCOPE_WORKGROUP);
    if ((old & 3u) == 3u) __hip_atomic_store((u64*)(ws + FL_Z + (size_t)ch * 8), E, __ATOMIC_RELAXED, __HIP_MEMORY_SCOPE_AGENT);
  }
}

DI s16x8 pack8(const f32x16& v, int o) {
  u32x4 r;
  r[0] = pk_bf16(v[o + 0], v[o + 1]); r[1] = pk_bf16(v[o + 2], v[o + 3]);
  r[2] = pk_bf16(v[o + 4], v[o + 5]); r[3] = pk_bf16(v[o + 6], v[o + 7]);
  return __builtin_bit_cast(s16x8, r);
}
DI float max3f(float a, float b, float c) { return __builtin_fmaxf(__builtin_fmaxf(a, b), c); }
DI float max16(const f32x16& v, float m) {
  float a = max3f(v[0], v[1], m), b = max3f(v[2], v[3], v[4]);
  a = max3f(a, v[5], v[6]); b = max3f(b, v[7], v[8]);
  a = max3f(a, v[9], v[10]); b = max3f(b, v[11], v[12]);
  a = max3f(a, v[13], v[14]);
  return max3f(a, b, v[15]);
}
DI float xhalf_max(float m) {
  auto sw = __builtin_amdgcn_permlane32_swap(__float_as_uint(m), __float_as_uint(m), false, false);
  return __builtin_fmaxf(__uint_as_float(sw[0]), __uint_as_float(sw[1]));
}

#define AT_LMAX 65536.0f
#define P3_WADDR() unsigned wv[4]; _Pragma("unroll") for (int i = 0; i < 4; ++i) { \
      const int c = i < 3 ? (i * 4 + w) * 64 + lane : 768 + w * 8 + (lane & 7); const int row = c / 10; \
      wv[i] = (unsigned)(row * DM * 2 + (c - row * 10) * 16); } const char* wog = (const char*)(wo + n3)
#define P3_STW(t) do { _Pragma("unroll") for (int i_ = 0; i_ < 3; ++i_) \
        glds16(wv[i_], wog + (size_t)(t) * 80 * DM * 2, lds0 + p3_w(t) + (i_ * 4 + w) * 1024); \
      if (lane < 8) glds16(wv[3], wog + (size_t)(t) * 80 * DM * 2, lds0 + p3_w(t) + 12288 + w * 128); } while (0)
DI void attn_stage(unsigned lds0, int h, int qb8, int j, int lane, char* __restrict__ ws, u64 E, int zch, const Mk& mk, Diag& dg) {
  const int qt = qb8 * 8 + j;
  const unsigned l16 = (unsigned)(lane * 16), ct = lds0 + L_CT;
  const int qa = (5 * h) >> 2;
  const bool stg = qb8 > 0 || j >= 2;
  {
    const int l3 = lane & 7, hi = lane & 1;
    const bool kv = stg && l3 >= 2 && l3 < 6;
    const int rb = kv ? 2 * qb8 - 2 + j : 2 * qb8 + (l3 >= 6 ? 1 : 0);
    const int col = !kv ? qa * 16 + hi * 31 : (l3 < 4 ? DM + qa * 16 + hi * 31 : 2 * DM + h * 20 + hi * 19);
    poll(ws, FL_Q + (size_t)(rb * 16 + (col >> 7)) * 8, E);
  }
  STAMP(9);
  conv_publish(ct + CT_ZCNT + 8, zch, lane, ws, E);
  const int kt0 = qb8 * 8 - 8 + 4 * j;
#pragma unroll
  for (int i = 0; i < 4; ++i)
    glds16c(l16, ws + WS_QF + ((size_t)(qt + (i >> 1) * 4) * 40 + qa + (i & 1)) * 1024, lds0 + AT_Q + ((j + (i >> 1) * 4) * 2 + (i & 1)) * 1024);
  if (stg) {
#pragma unroll
    for (int i = 0; i < 8; ++i)
      glds16c(l16, ws + WS_KF + ((size_t)(kt0 + (i >> 1)) * 40 + qa + (i & 1)) * 1024, lds0 + AT_K + (j * 8 + i) * 1024);
  }
  glds_mark(mk, ct + CT_KR8 + j * 4, lane);
  if (stg) {
#pragma unroll
    for (int i = 0; i < 6; ++i) {
      const int c = i * 64 + lane;
      const int kgp = (c * 1561) >> 16, rem = c - kgp * 42, vh = rem >= 21 ? 1 : 0, d = rem - vh * 21;
      const int n = h * 20 + d;
      if (c < 336 && d == 20) lds_write16(lds0 + AT_V + j * 5376 + c * 16, (u32x4){0x3f803f80u, 0x3f803f80u, 0x3f803f80u, 0x3f803f80u});
      if (c < 336 && d < 20)
        glds16c((unsigned)((kgp * 20 + (n >> 5)) * 1024 + (vh * 32 + (n & 31)) * 16), ws + WS_VF + ((size_t)(kt0 * 2) * 20) * 1024,
                lds0 + AT_V + j * 5376 + i * 1024);
    }
    asm volatile("s_waitcnt lgkmcnt(0)" ::: "memory");
  }
  glds_mark(mk, ct + CT_VR8 + j * 4, lane);
}
template <bool FULL>
DI void attn_task(unsigned lds0, int h, int qb8, int j, int lane, char* __restrict__ ws, u64 E, int zch, const Mk& mk, Diag& dg) {
  const int l32 = lane & 31, hh = lane >> 5;
  const int qt = qb8 * 8 + j;
  const int tfirst = FULL ? 0 : 8 - qt;
  const unsigned l16 = (unsigned)(lane * 16), ct = lds0 + L_CT;
  const int qa = (5 * h) >> 2;
  u32x4 q0r, q1r;
  spin_mk(ct + CT_KR8, mk.x);
  if (!FULL) spin_mk(ct + CT_VR8, mk.x);
  q0r = __builtin_bit_cast(u32x4, lds_read16(lds0 + AT_Q + j * 2048 + l16));
  q1r = __builtin_bit_cast(u32x4, lds_read16(lds0 + AT_Q + j * 2048 + 1024 + l16));
  asm volatile("s_waitcnt lgkmcnt(0)" : "+v"(q0r), "+v"(q1r) :: "memory");
  if (zch >= 0) conv_publish(ct + CT_ZCNT + (j >> 2) * 4, zch, lane, ws, E);
  {
    const int g0 = 4 * qa + hh - 5 * h;
    const bool k0 = g0 >= 0, k1 = g0 + 2 >= 0 && g0 + 2 < 5, k2 = g0 + 4 < 5, k3 = g0 + 6 < 5;
    q0r[0] = k0 ? q0r[0] : 0u; q0r[1] = k0 ? q0r[1] : 0u; q0r[2] = k1 ? q0r[2] : 0u; q0r[3] = k1 ? q0r[3] : 0u;
    q1r[0] = k2 ? q1r[0] : 0u; q1r[1] = k2 ? q1r[1] : 0u; q1r[2] = k3 ? q1r[2] : 0u; q1r[3] = k3 ? q1r[3] : 0u;
  }
  s16x8 qlo = __builtin_bit_cast(s16x8, q0r), qhi8 = __builtin_bit_cast(s16x8, q1r);
  STAMP(8);
  const f32x16 zero16 = {0.f, 0.f, 0.f, 0.f, 0.f, 0.f, 0.f, 0.f, 0.f, 0.f, 0.f, 0.f, 0.f, 0.f, 0.f, 0.f};
  const float ninf = -INFINITY;
  const f32x16 ninf16 = {ninf, ninf, ninf, ninf, ninf, ninf, ninf, ninf, ninf, ninf, ninf, ninf, ninf, ninf, ninf, ninf};
  const unsigned kb = lds0 + AT_K + j * 2048 + l16;
#define AT_QK(dst, t, cin) do { if (FULL || (t) >= tfirst) { dst = MFMA32(lds_read16(kb + (t) * 2048), qlo, cin); \
    dst = MFMA32(lds_read16(kb + (t) * 2048 + 1024), qhi8, dst); } else dst = ninf16; } while (0)
  const int vrow = l32 <= 20 ? l32 : (l32 == 24 ? 20 : l32 - 16);
  const unsigned vb = lds0 + AT_V + j * 1344 + hh * 336 + vrow * 16;
#define AT_EXP(sv) do { _Pragma("unroll") for (int r = 0; r < 16; ++r) sv[r] = __builtin_amdgcn_exp2f(sv[r] - m); } while (0)
#define AT_PV(sv, t) do { oacc = MFMA32(lds_read16(vb + (t) * 1344), pack8(sv, 0), oacc); oacc = MFMA32(lds_read16(vb + (t) * 1344 + 672), pack8(sv, 8), oacc); } while (0)
  f32x16 oacc;
  bool exact = !FULL;
  if (FULL) {
    f32x16 sm, sa, sb;
    AT_QK(sm, 8, zero16); AT_QK(sa, 0, zero16);
    const int tq = l32 - 4 * hh;
    const unsigned wm = tq < 0 ? 0xffffffffu : (0xfffffffeu << tq);
    unsigned msk[16];
#pragma unroll
    for (int r = 0; r < 16; ++r) {
      msk[r] = (unsigned)__builtin_amdgcn_sbfe((int)wm, (r & 3) + 8 * (r >> 2), 1);
      sm[r] = __uint_as_float((msk[r] & __float_as_uint(sa[r])) | (~msk[r] & __float_as_uint(sm[r])));
    }
    const float m = xhalf_max(max16(sm, ninf));
    const float nm = -m;
    const f32x16 cneg = {nm, nm, nm, nm, nm, nm, nm, nm, nm, nm, nm, nm, nm, nm, nm, nm};
    AT_QK(sa, 1, cneg);
    AT_EXP(sm);
    u32x4 pl = __builtin_bit_cast(u32x4, pack8(sm, 0)), ph = __builtin_bit_cast(u32x4, pack8(sm, 8)), ql, qh;
#pragma unroll
    for (int i = 0; i < 4; ++i) {
      ql[i] = pl[i] & ((msk[2 * i] & 0xffffu) | (msk[2 * i + 1] & 0xffff0000u)); pl[i] ^= ql[i];
      qh[i] = ph[i] & ((msk[8 + 2 * i] & 0xffffu) | (msk[9 + 2 * i] & 0xffff0000u)); ph[i] ^= qh[i];
    }
    spin_mk(ct + CT_VR8, mk.x);
    STAMP(9);
    oacc = MFMA32(lds_read16(vb + 8 * 1344), __builtin_bit_cast(s16x8, pl), zero16);
    oacc = MFMA32(lds_read16(vb + 8 * 1344 + 672), __builtin_bit_cast(s16x8, ph), oacc);
    oacc = MFMA32(lds_read16(vb), __builtin_bit_cast(s16x8, ql), oacc);
    oacc = MFMA32(lds_read16(vb + 672), __builtin_bit_cast(s16x8, qh), oacc);
#define AT_EX0(sv) do { _Pragma("unroll") for (int r = 0; r < 16; ++r) sv[r] = __builtin_amdgcn_exp2f(sv[r]); } while (0)
    AT_QK(sb, 2, cneg); AT_EX0(sa); AT_PV(sa, 1);
    AT_QK(sa, 3, cneg); AT_EX0(sb); AT_PV(sb, 2);
    AT_QK(sb, 4, cneg); AT_EX0(sa); AT_PV(sa, 3);
    AT_QK(sa, 5, cneg); AT_EX0(sb); AT_PV(sb, 4);
    AT_QK(sb, 6, cneg); AT_EX0(sa); AT_PV(sa, 5);
    AT_QK(sa, 7, cneg); AT_EX0(sb); AT_PV(sb, 6);
    AT_EX0(sa); AT_PV(sa, 7);
    float big = max3f(__builtin_fabsf(oacc[0]), __builtin_fabsf(oacc[1]), __builtin_fabsf(oacc[2]));
    big = max3f(big, __builtin_fabsf(oacc[3]), __builtin_fabsf(oacc[4]));
    big = max3f(big, __builtin_fabsf(oacc[5]), __builtin_fabsf(oacc[6]));
    big = max3f(big, __builtin_fabsf(oacc[7]), __builtin_fabsf(oacc[8]));
    big = max3f(big, __builtin_fabsf(oacc[9]), __builtin_fabsf(oacc[10]));
    big = __builtin_fmaxf(big, __builtin_fabsf(oacc[11]));
    const float ls = hh ? oacc[8] : oacc[12];
    float osum = 0.f;
#pragma unroll
    for (int r = 0; r < 8; ++r) osum += oacc[r];
    const bool ok = (ls < AT_LMAX) && (big < 1e37f) && (osum == osum);
    exact = __builtin_amdgcn_ballot_w64(!ok) != 0ull;
  }
  if (exact) {
    asm volatile("" : "+v"(qlo), "+v"(qhi8) :: "memory");
    f32x16 sm, s1, s2, s3;
    {
      f32x16 s0;
      AT_QK(sm, 8, zero16); AT_QK(s0, 0, zero16);
      AT_QK(s1, 1, zero16); AT_QK(s2, 2, zero16); AT_QK(s3, 3, zero16);
#pragma unroll
      for (int r = 0; r < 16; ++r) {
        const int kk = (r & 3) + 8 * (r >> 2) + 4 * hh;
        sm[r] = kk > l32 ? s0[r] : sm[r];
      }
    }
    float m = max16(sm, ninf);
    m = max16(s1, m); m = max16(s2, m); m = max16(s3, m);
    m = xhalf_max(m);
    AT_EXP(sm);
    oacc = zero16;
    {
      f32x16 p0, p8;
#pragma unroll
      for (int r = 0; r < 16; ++r) {
        const int kk = (r & 3) + 8 * (r >> 2) + 4 * hh;
        p0[r] = kk > l32 ? sm[r] : 0.f;
        p8[r] = kk > l32 ? 0.f : sm[r];
      }
      AT_PV(p8, 8);
      if (FULL) AT_PV(p0, 0);
    }
    if (FULL || 1 >= tfirst) { AT_EXP(s1); AT_PV(s1, 1); }
    AT_QK(sm, 4, zero16); AT_QK(s1, 5, zero16);
    if (FULL || 2 >= tfirst) { AT_EXP(s2); AT_PV(s2, 2); }
    AT_QK(s2, 6, zero16);
    if (FULL || 3 >= tfirst) { AT_EXP(s3); AT_PV(s3, 3); }
    AT_QK(s3, 7, zero16);
    float m2 = max16(sm, m);
    m2 = max16(s1, m2); m2 = max16(s2, m2); m2 = max16(s3, m2);
    m2 = xhalf_max(m2);
    {
      const float alpha = __builtin_amdgcn_exp2f(m - m2);
#pragma unroll
      for (int r = 0; r < 13; ++r) oacc[r] *= alpha;
      m = m2;
    }
    if (FULL || 4 >= tfirst) { AT_EXP(sm); AT_PV(sm, 4); }
    if (FULL || 5 >= tfirst) { AT_EXP(s1); AT_PV(s1, 5); }
    if (FULL || 6 >= tfirst) { AT_EXP(s2); AT_PV(s2, 6); }
    if (FULL || 7 >= tfirst) { AT_EXP(s3); AT_PV(s3, 7); }
  }
  asm volatile("s_waitcnt lgkmcnt(0)" ::: "memory");
  sig(ct + CT_ATT + j * 4, 1u, lane);
  STAMP(10);
  const float l = hh ? oacc[8] : oacc[12];
  const float inv = __builtin_amdgcn_rcpf(l);
  char* oc = ws + WS_OW + ((size_t)qt * NH + h) * 1280;
  u32x4 o;
  o[0] = pk_bf16(oacc[0] * inv, oacc[1] * inv); o[1] = pk_bf16(oacc[2] * inv, oacc[3] * inv);
  o[2] = pk_bf16(oacc[4] * inv, oacc[5] * inv); o[3] = pk_bf16(oacc[6] * inv, oacc[7] * inv);
  gstore16c(oc + lane * 16, o);
  if (!hh) {
    u32x2 o2;
    o2[0] = pk_bf16(oacc[8] * inv, oacc[9] * inv); o2[1] = pk_bf16(oacc[10] * inv, oacc[11] * inv);
    gstore8c(oc + 1024 + l32 * 8, o2);
  }
  publish(ws, FL_O + (size_t)(qt * NH + h) * 8, E, lane);
}

#define HDELAY 0
#define APRIO 1
#define LPRIO 0
#define MPRIO 2
#define ROTG 2
#define A1_DELAY 8
__global__ __launch_bounds__(768, 3) void mega(const u16* __restrict__ x, const u16* __restrict__ wqkv,
                                               const u16* __restrict__ ak, const u16* __restrict__ av,
                                               const u16* __restrict__ shk, const u16* __restrict__ wo,
                                               const u16* __restrict__ ssmk, const u16* __restrict__ ssmd,
                                               const u16* __restrict__ aq, char* __restrict__ ws,
                                               u16* __restrict__ out) {
  extern __shared__ __attribute__((aligned(16))) char smem[];
  const unsigned lds0 = lds_addr(smem);
  const int tid = threadIdx.x, wave = __builtin_amdgcn_readfirstlane(tid >> 6);
  int lane = tid & 63;
#define RELANE() asm volatile("" : "+v"(lane))
  const int role = wave >> 2, w = wave & 3;
  const int bid = blockIdx.x, xcd = bid & 7, idx = bid >> 3;
  const bool has_gemm = idx < 30;
  const unsigned ct = lds0 + L_CT;
  DIAG_INIT();
  u64 E;
  Mk mk; mk.src = wqkv;
  asm volatile("s_load_dwordx2 %0, %2, 0x0\n\ts_load_dword %1, %3, 0x0" : "=&s"(E), "=&s"(mk.x) : "s"(ws + WS_FL), "s"(wqkv) : "memory");
  const int gt = (xcd & 1) * 30 + idx;
  const int tm = (xcd >> 1) * 4 + (gt & 3), tn = gt >> 2;
  const int m0 = tm * 128, n0 = tn * 128;
  const int rot = ROTG >= 10 ? (((gt & 3) * 5 + 1) / 2 + 3 * tn) % 10 : ((((gt & 3) + tn) % ROTG) * 10) / ROTG;
  const unsigned xo0 = (unsigned)((lane >> 3) * DM * 2 + (((lane & 7) ^ (lane >> 4)) * 16));
  const unsigned xo1 = (unsigned)((lane >> 3) * DM * 2 + (((lane & 7) ^ (4 + (lane >> 4))) * 16));
  const unsigned wof = (unsigned)((lane >> 4) * NQKV * 2 + (((lane & 15) ^ ((lane >> 4) << 2)) * 16));
  const char* xg = (const char*)(x + (size_t)m0 * DM);
  const char* wg = (const char*)(wqkv + n0);
#define K1_PIECE_X(t, kt, j) glds16p(((j) & 1) ? xo1 : xo0, xg + (size_t)(j) * 8 * DM * 2 + (kt) * 128, lds0 + ((t) & 3) * K1_STAGE + (j) * 1024)
#define K1_PIECE_W(t, kt, j) glds16p(wof, wg + (size_t)((kt) * 64 + (j) * 4) * NQKV * 2, lds0 + ((t) & 3) * K1_STAGE + 16384 + (j) * 1024)
  if (has_gemm && role == 0) {
#pragma unroll
    for (int i = 0; i < 4; ++i) { K1_PIECE_X(0, rot, w * 4 + i); K1_PIECE_W(0, rot, w * 4 + i); }
  }
  const unsigned pf = ct + CT_PAIR + (wave >> 1) * 8;
  const int wh = wave & 1;
  unsigned ps = 0;
  const int fb = (idx - 30) * 8 + xcd;
  const int a1t = has_gemm ? (xcd * 30 + idx) * 2 + (w >> 1) : 480 + fb * 10 + (wave >> 1);
  const unsigned a1l = has_gemm ? lds0 + L_AUX + (w >> 1) * A1_PAIR : lds0 + (wave >> 1) * A1_PAIR;
  if (!has_gemm || role == 2) a1_issue(a1l, a1t, lane, wh, ak, av, shk);
  const bool cv0 = role != 0 || !has_gemm || bid < 112;
  const int ch0 = role == 0 ? (has_gemm ? 528 + bid : 512 + fb) : bid + (role == 1 ? 256 : 0);
  asm volatile("s_waitcnt lgkmcnt(0)" : "+s"(E), "+s"(mk.x) :: "memory");
  if (role == 1) lds_st32(ct + (tid - 256) * 4, (tid - 256) * 4 >= CT_FX ? ~mk.x : 0u);
  bar();
  E += 1ull;

  const bool ex = has_gemm && role == 1 && bid < 112;
  HRegs hr, h2;
#define LOAD_H() do { hr = conv_load_h(ch0, w * 64 + lane, ssmk, ssmd); \
    if (ex) h2 = conv_load_h(528 + bid, w * 64 + lane, ssmk, ssmd); } while (0)
  auto q_publish = [&]() {
    asm volatile("s_waitcnt vmcnt(0)" ::: "memory");
    if (lane == 0) {
      const unsigned old = __hip_atomic_fetch_add((LAS unsigned*)(uintptr_t)(ct + CT_QCNT), 1u, __ATOMIC_RELAXED, __HIP_MEMORY_SCOPE_WORKGROUP);
      if (old == 3u) __hip_atomic_store((u64*)(ws + FL_Q + (size_t)(tm * 16 + tn) * 8), E, __ATOMIC_RELAXED, __HIP_MEMORY_SCOPE_AGENT);
    }
    STAMP(4);
  };
  if (!has_gemm) {
    LOAD_H();
    const unsigned a2l = lds0 + (6 + (wave >> 1)) * A1_PAIR;
    if (wave < 8) a1_issue(a2l, a1t + 6, lane, wh, ak, av, shk);
    if (wave < 8) wait_vm<6>(); else wait_vm<0>();
    a1_finish(a1l, pf, ps, a1t, lane, wh, ws, E);
    if (wave < 8) { wait_vm<2>(); a1_finish(a2l, pf, ps, a1t + 6, lane, wh, ws, E); }
    a1_publish(a1t, lane, wh, ws, E);
    if (wave < 8) a1_publish(a1t + 6, lane, wh, ws, E);
    bar();
    STAMP(1);
  } else if (role == 0) {
    wait_vm<0>();
    sig(ct + CT_S0 + w * 4, 1u, lane);
    spin_ge4(ct + CT_S0, 1u);
    __builtin_amdgcn_s_setprio(MPRIO);
    if (tn >= 10) k1_gemm<true>(lds0, m0, n0, tid, ws, mk.x, dg);
    else k1_gemm<false>(lds0, m0, n0, tid, ws, mk.x, dg);
    STAMP(3);
  } else if (role == 1) {
    __builtin_amdgcn_s_setprio(LPRIO);
    __builtin_amdgcn_s_sleep(LDELAY);
#define K1_ST(t) do { const int kt_ = (t) + rot >= 10 ? (t) + rot - 10 : (t) + rot; \
      if ((t) >= 4) spin_ge4(ct + CT_FREE + ((t) & 3) * 16, (unsigned)((t) - 3)); \
      _Pragma("unroll") for (int i_ = 0; i_ < 4; ++i_) { K1_PIECE_X(t, kt_, w * 4 + i_); K1_PIECE_W(t, kt_, w * 4 + i_); } } while (0)
#pragma nounroll
    for (int t = 1; t < 4; ++t) { K1_ST(t); glds_mark(mk, ct + CT_FX + t * 16 + w * 4, lane); }
    LOAD_H();
#pragma nounroll
    for (int t = 4; t < 10; ++t) { K1_ST(t); glds_mark(mk, ct + CT_FX + t * 16 + w * 4, lane); }
    __builtin_amdgcn_s_setprio(0);
    STAMP(1);
  } else {
    LOAD_H();
    __builtin_amdgcn_s_setprio(2);
    wait_vm<0>();
    a1_finish(a1l, pf, ps, a1t, lane, wh, ws, E);
    a1_publish(a1t, lane, wh, ws, E);
    __builtin_amdgcn_s_setprio(0);
    STAMP(1);
  }

  RELANE();
  unsigned qs = 0;
  const unsigned qf = ct + CT_QUAD + role * 16;
  const unsigned creg = lds0 + (role == 2 ? L_AUX : (role == 1 ? 29184 : 0));
  if (role == 2) __builtin_amdgcn_s_setprio(APRIO); else __builtin_amdgcn_s_setprio(0);
  if (has_gemm && role == 0) {
    q_publish();
    if (cv0) {
      hr.d = ssmd[ch0];
      conv_run(creg, qf, qs, ch0, lane, w, hr, ws, ct + CT_XST, dg);
    }
  } else {
    if (has_gemm && role == 1) {
      conv_load_u(hr, ch0, lane, w, ws, E);
      if (ex) conv_load_u(h2, 528 + bid, lane, w, ws, E);
      spin_ge4(ct + CT_FREE + 16, 10u);
      conv_stage(creg, lane, w, hr);
      if (ex) {
        conv_stage(lds0, lane, w, h2);
        lds_write16(lds0 + (w * 64 + lane) * 16, h2.u);
        asm volatile("s_waitcnt lgkmcnt(0)" ::: "memory");
        sig(ct + CT_XST + w * 4, 1u, lane);
      }
    } else {
      if (has_gemm) { const int wq = w; QSYNC(); }
      conv_stage(creg, lane, w, hr);
      conv_load_u(hr, ch0, lane, w, ws, E);
    }
    STAMP(5);
    conv_run(creg, qf, qs, ch0, lane, w, hr, ws, 0u, dg);
  }
  sig(ct + CT_CVD + wave * 4, 1u, lane);
  if (role != 0) STAMP(2);

  const int tm3 = xcd * 4 + (idx >> 3), tn3 = idx & 7;
  const int m3 = tm3 * 64, n3 = tn3 * 80;
  RELANE();

  if (role == 2) {
    const int r16 = lane & 15, g = lane >> 4, q = r16 >> 2, p = lane & 3;
    __builtin_amdgcn_s_setprio(3);
    spin_ge4(ct + CT_CVD + 32, 1u);
    if (has_gemm) spin_ge4(ct + CT_FREE + 16, 10u);
    attn_stage(lds0, xcd * 4 + (idx & 3), idx >> 2, w, lane, ws, E, ch0, mk, dg);
    {
      P3_WADDR();
      P3_STW(5);
      spin_ge8(ct + CT_CVD, 1u);
      P3_STW(0); P3_STW(4); P3_STW(1); P3_STW(2); P3_STW(3);
      glds_mark(mk, ct + CT_WRDY + w * 4, lane);
      STAMP(3);
    }
    __builtin_amdgcn_s_setprio(0);
    u32x4 qv[3];
#pragma unroll
    for (int j = 0; j < 3; ++j) {
      const int dc = j < 2 ? j * 4 + g : 8 + (g & 1);
      qv[j] = *((const u32x4*)(aq + (size_t)(m3 + w * 16 + r16) * DM + n3 + dc * 8));
    }
    {
      const int l5 = lane & 31;
      const int cl = l5 < 8 ? w * 8 + l5 : (l5 < 16 ? 32 + w * 8 + (l5 - 8) : 64 + w * 4 + (l5 & 3));
      poll(ws, FL_Z + (size_t)(n3 + cl) * 8, E);
    }
    {
      const char* zg = ws + WS_ZT + ((size_t)n3 * SEQ + m3) * 2;
      const unsigned zo = (unsigned)((lane >> 3) * SEQ * 2 + (lane & 7) * 16);
      glds16c(zo, zg + (size_t)(w * 8) * SEQ * 2, lds0 + L_AUX + w * 1024);
      glds16c(zo, zg + (size_t)(32 + w * 8) * SEQ * 2, lds0 + L_AUX + 4096 + w * 1024);
      if (lane < 32) glds16c(zo, zg + (size_t)(64 + w * 4) * SEQ * 2, lds0 + L_AUX + 8192 + w * 512);
    }
    wait_vm<0>();
    sig(ct + CT_ZRDY + w * 4, 1u, lane);
    spin_ge4(ct + CT_ZRDY, 1u);
    STAMP(4);
    u32x4 zz[3];
#pragma unroll
    for (int j = 0; j < 3; ++j) {
      const int dc = j < 2 ? j * 4 + g : 8 + (g & 1);
      const unsigned za = lds0 + L_AUX + (dc * 8 + q) * 128 + w * 32 + p * 8;
      zz[j] = __builtin_bit_cast(u32x4, cat8(tr_read(za), tr_read(za + 512)));
    }
#pragma unroll
    for (int j = 0; j < 3; ++j) {
      const int dc = j < 2 ? j * 4 + g : 8 + (g & 1);
      u32x4 o;
#pragma unroll
      for (int i = 0; i < 4; ++i) o[i] = pk_bf16(bf_lo(qv[j][i]) * bf_lo(zz[j][i]), bf_hi(qv[j][i]) * bf_hi(zz[j][i]));
      if (j < 2 || g < 2) ostore16(out + (size_t)(m3 + w * 16 + r16) * (2 * DM) + n3 + dc * 8, o);
    }
    STAMP(7);
    if (bid == 0 && w == 0) {
#pragma unroll
      for (int i = 0; i < 4; ++i) poll(ws, FL_O + (size_t)(((i * 2 + (lane >> 5)) * 8) * NH + (lane & 31)) * 8, E);
      if (lane == 0) __hip_atomic_store((u64*)(ws + WS_FL), E, __ATOMIC_RELAXED, __HIP_MEMORY_SCOPE_AGENT);
    }
    DIAG_DUMP(2);
    return;
  }

  const int h = xcd * 4 + (idx & 3), qb8 = idx >> 2;
  __builtin_amdgcn_s_setprio(1);
  RELANE();
  if (qb8 > 0) attn_task<true>(lds0, h, qb8, wave, lane, ws, E, cv0 ? ch0 : -1, mk, dg);
  else attn_task<false>(lds0, h, qb8, wave, lane, ws, E, cv0 ? ch0 : -1, mk, dg);
  STAMP(11);

  RELANE();
  const int r16 = lane & 15, g = lane >> 4, q = r16 >> 2, p = lane & 3;
  f32x4 acc[5];
#pragma unroll
  for (int i = 0; i < 5; ++i) acc[i] = (f32x4){0.f, 0.f, 0.f, 0.f};
  const unsigned wbp = lds0 + (20 * (g >> 1) + 4 * (g & 1) + q) * 160 + p * 8;
  const unsigned wbs = lds0 + (20 * g + 16 + q) * 160 + p * 8;
  u32x4 op[8]; u32x2 osg[4];
  if (role == 1) {
    P3_WADDR();
    spin_ge8(ct + CT_ATT, 1u);
    P3_STW(6); P3_STW(7);
    glds_mark(mk, ct + CT_WRD2 + w * 4, lane);
  }
  spin_mk(ct + CT_WRDY, mk.x);
  const unsigned w0 = role ? p3_w(4) : p3_w(0), w1 = role ? p3_w(5) : p3_w(1), w2 = role ? p3_w(6) : p3_w(2), w3 = role ? p3_w(7) : p3_w(3);
  struct Fw { s16x8 a[5], b[5]; s16x4 s[5]; };
  Fw fwa, fwb;
#define P3_LOADW(F, bo_) do { _Pragma("unroll") for (int nt = 0; nt < 5; ++nt) { \
      F.a[nt] = cat8(tr_read(wbp + (bo_) + nt * 32), tr_read(wbp + (bo_) + 8 * 160 + nt * 32)); \
      F.b[nt] = cat8(tr_read(wbp + (bo_) + 40 * 160 + nt * 32), tr_read(wbp + (bo_) + 48 * 160 + nt * 32)); \
      F.s[nt] = tr_read(wbs + (bo_) + nt * 32); } } while (0)
#define P3_MM(F, t) do { \
    _Pragma("unroll") for (int nt = 0; nt < 5; ++nt) acc[nt] = MFMA16(F.a[nt], __builtin_bit_cast(s16x8, op[2 * (t)]), acc[nt]); \
    _Pragma("unroll") for (int nt = 0; nt < 5; ++nt) acc[nt] = MFMA16(F.b[nt], __builtin_bit_cast(s16x8, op[2 * (t) + 1]), acc[nt]); } while (0)
#define P3_MS(t) do { const s16x8 os_ = cat8(__builtin_bit_cast(s16x4, osg[t]), __builtin_bit_cast(s16x4, osg[(t) + 1])); \
    _Pragma("unroll") for (int nt = 0; nt < 5; ++nt) acc[nt] = MFMA16(cat8(fwa.s[nt], fwb.s[nt]), os_, acc[nt]); } while (0)
  P3_LOADW(fwa, w0);
  SBAR();
  poll(ws, FL_O + (size_t)(((m3 >> 5) + (w >> 1)) * NH + role * 16 + r16) * 8, E);
  STAMP(12);
  {
    const int orow = (w & 1) * 16 + r16;
    const char* ob = ws + WS_OW + ((size_t)((m3 >> 5) + (w >> 1)) * NH + role * 16) * 1280;
    const char* pp = ob + (g >> 1) * 1280 + (g & 1) * 512 + orow * 16;
    const char* sp = ob + g * 1280 + 1024 + orow * 8;
#pragma unroll
    for (int t = 0; t < 4; ++t) {
      asm volatile("global_load_dwordx4 %0, %3, off sc1\n\tglobal_load_dwordx4 %1, %3, off offset:2560 sc1\n\tglobal_load_dwordx2 %2, %4, off sc1"
                   : "=&v"(op[2 * t]), "=&v"(op[2 * t + 1]), "=&v"(osg[t]) : "v"(pp + t * 5120), "v"(sp + t * 5120) : "memory");
    }
  }
  P3_LOADW(fwb, w1);
  if (role == 1) spin_mk(ct + CT_WRD2, mk.x);
  SBAR();
  asm volatile("s_waitcnt vmcnt(6)" : "+v"(op[0]), "+v"(op[1]), "+v"(op[2]), "+v"(op[3]), "+v"(osg[0]), "+v"(osg[1]) :: "memory");
  STAMP(13);
  P3_MS(0); P3_MM(fwa, 0);
  SBAR();
  P3_LOADW(fwa, w2); P3_MM(fwb, 1);
#pragma unroll
  for (int i = 0; i < 10; ++i) { __builtin_amdgcn_sched_group_barrier(0x008, 1, 0); __builtin_amdgcn_sched_group_barrier(0x100, 3, 0); }
  SBAR();
  asm volatile("s_waitcnt vmcnt(0)" : "+v"(op[4]), "+v"(op[5]), "+v"(op[6]), "+v"(op[7]), "+v"(osg[2]), "+v"(osg[3]) :: "memory");
  P3_LOADW(fwb, w3); P3_MM(fwa, 2);
#pragma unroll
  for (int i = 0; i < 10; ++i) { __builtin_amdgcn_sched_group_barrier(0x008, 1, 0); __builtin_amdgcn_sched_group_barrier(0x100, 3, 0); }
  SBAR();
  P3_MM(fwb, 3); P3_MS(2);
  SBAR();
  const unsigned rb = lds0 + AT_K + 25600 + w * 5120 + lane * 16;
  if (role == 1) {
#pragma unroll
    for (int nt = 0; nt < 5; ++nt) lds_write16(rb + nt * 1024, __builtin_bit_cast(u32x4, acc[nt]));
    asm volatile("s_waitcnt lgkmcnt(0)" ::: "memory");
    sig(ct + CT_RED + w * 4, 1u, lane);
    STAMP(14);
    DIAG_DUMP(1);
    return;
  }
  spin_ge(ct + CT_RED + w * 4, 1u);
  u16* orow_g = out + (size_t)(m3 + w * 16 + r16) * (2 * DM) + DM + n3;
  u32x2 ov[5];
#pragma unroll
  for (int nt = 0; nt < 5; ++nt) {
    const f32x4 o2 = acc[nt] + __builtin_bit_cast(f32x4, lds_read16(rb + nt * 1024));
    ov[nt][0] = pk_bf16(o2[0], o2[1]);
    ov[nt][1] = pk_bf16(o2[2], o2[3]);
  }
#pragma unroll
  for (int pr = 0; pr < 2; ++pr) {
    const auto sx = __builtin_amdgcn_permlane16_swap(ov[2 * pr][0], ov[2 * pr + 1][0], false, false);
    const auto sy = __builtin_amdgcn_permlane16_swap(ov[2 * pr][1], ov[2 * pr + 1][1], false, false);
    ostore16(orow_g + (2 * pr + (g & 1)) * 16 + 8 * (g >> 1), (u32x4){sx[0], sy[0], sx[1], sy[1]});
  }
  ostore8(orow_g + 64 + 4 * g, ov[4]);
  STAMP(14);
  DIAG_DUMP(0);
}

extern "C" void kernel_launch(void* const* d_in, const int* in_sizes, int n_in, void* d_out, int out_size,
                              void* d_ws, size_t ws_size, hipStream_t stream) {
  const u16* aq = (const u16*)d_in[0];
  const u16* ak = (const u16*)d_in[1];
  const u16* av = (const u16*)d_in[2];
  const u16* shk = (const u16*)d_in[3];
  const u16* ssmk = (const u16*)d_in[4];
  const u16* ssmd = (const u16*)d_in[5];
  const u16* x = (const u16*)d_in[6];
  const u16* wqkv = (const u16*)d_in[7];
  const u16* wo = (const u16*)d_in[8];
  (void)hipFuncSetAttribute((const void*)mega, hipFuncAttributeMaxDynamicSharedMemorySize, L_TOTAL);
  mega<<<256, 768, L_TOTAL, stream>>>(x, wqkv, ak, av, shk, wo, ssmk, ssmd, aq, (char*)d_ws, (u16*)d_out);
}
```

```cpp
#include <hip/hip_runtime.h>
#include <hip/hip_bf16.h>
#include <stdint.h>

typedef unsigned short u16;
typedef short s16x4 __attribute__((ext_vector_type(4)));
typedef short s16x8 __attribute__((ext_vector_type(8)));
typedef float f32x2 __attribute__((ext_vector_type(2)));
typedef float f32x4 __attribute__((ext_vector_type(4)));
typedef float f32x16 __attribute__((ext_vector_type(16)));
typedef unsigned u32x2 __attribute__((ext_vector_type(2)));
typedef unsigned u32x4 __attribute__((ext_vector_type(4)));
typedef __bf16 bf16x2 __attribute__((ext_vector_type(2)));
#define LAS __attribute__((address_space(3)))
#define DI __device__ __forceinline__

constexpr int SEQ = 2048, DM = 640, NQKV = 1920, NH = 32;
constexpr size_t WS_QF = 0;
constexpr size_t WS_KF = WS_QF + (size_t)DM * SEQ * 2;
constexpr size_t WS_VF = WS_KF + (size_t)DM * SEQ * 2;
constexpr size_t WS_UT = WS_VF + (size_t)DM * SEQ * 2;
constexpr size_t WS_ZT = WS_UT + (size_t)DM * SEQ * 2;
constexpr size_t WS_OW = WS_ZT + (size_t)DM * SEQ * 2;
typedef unsigned long long u64;
constexpr size_t WS_FL = WS_OW + (size_t)DM * SEQ * 2;
constexpr size_t FL_U = WS_FL + 256;
constexpr int QREP = 32;
constexpr size_t FL_Q = FL_U + 20 * 64 * 8;
constexpr size_t FL_Z = FL_Q + QREP * 16 * 16 * 8;
constexpr size_t FL_O = FL_Z + 640 * 8;

DI unsigned pk_bf16(float lo, float hi) {
  f32x2 f = {lo, hi};
  return __builtin_bit_cast(unsigned, __builtin_convertvector(f, bf16x2));
}
DI float bf_lo(unsigned u) { return __uint_as_float(u << 16); }
DI float bf_hi(unsigned u) { return __uint_as_float(u & 0xffff0000u); }
DI float rbf(float x) { return bf_lo(pk_bf16(x, 0.f)); }

DI unsigned lds_addr(const void* p) { return (unsigned)(uintptr_t)(LAS const char*)p; }

DI void glds16(unsigned voff, const void* sbase, unsigned lds_dst) {
  unsigned keep;
  asm volatile("s_mov_b32 %0, m0\n\ts_mov_b32 m0, %3\n\ts_nop 0\n\tglobal_load_lds_dwordx4 %1, %2\n\ts_mov_b32 m0, %0"
               : "=&s"(keep) : "v"(voff), "s"(sbase), "s"(lds_dst) : "memory");
}
DI void glds16f(unsigned voff, const void* sbase, unsigned lds_dst) {
  asm volatile("s_mov_b32 m0, %2\n\ts_nop 0\n\tglobal_load_lds_dwordx4 %0, %1" :: "v"(voff), "s"(sbase), "s"(lds_dst) : "memory");
}
#define glds16p glds16
DI void glds16nt(unsigned voff, const void* sbase, unsigned lds_dst) {
  unsigned keep;
  asm volatile("s_mov_b32 %0, m0\n\ts_mov_b32 m0, %3\n\ts_nop 0\n\tglobal_load_lds_dwordx4 %1, %2\n\ts_mov_b32 m0, %0"
               : "=&s"(keep) : "v"(voff), "s"(sbase), "s"(lds_dst) : "memory");
}
#define LDPOL "sc1"
DI void glds16c(unsigned voff, const void* sbase, unsigned lds_dst) {
  unsigned keep;
  asm volatile("s_mov_b32 %0, m0\n\ts_mov_b32 m0, %3\n\ts_nop 0\n\tglobal_load_lds_dwordx4 %1, %2 " LDPOL "\n\ts_mov_b32 m0, %0"
               : "=&s"(keep) : "v"(voff), "s"(sbase), "s"(lds_dst) : "memory");
}
struct Mk { const void* src; unsigned x; };
DI void glds_mark(const Mk& mk, unsigned lds_dst, int lane) {
  if (lane == 0) {
    unsigned keep;
    asm volatile("s_mov_b32 %0, m0\n\ts_mov_b32 m0, %3\n\ts_nop 0\n\tglobal_load_lds_dword %1, %2\n\ts_mov_b32 m0, %0"
                 : "=&s"(keep) : "v"(0u), "s"(mk.src), "s"(lds_dst) : "memory");
  }
}
DI float dot2bf(unsigned a, unsigned b, float c) {
  return __builtin_amdgcn_fdot2_f32_bf16(__builtin_bit_cast(bf16x2, a), __builtin_bit_cast(bf16x2, b), c, false);
}
DI void gstore16c(void* p, u32x4 v) {
  asm volatile("global_store_dwordx4 %0, %1, off sc1\n\ts_nop 1" :: "v"(p), "v"(v) : "memory");
}
DI void gstore8c(void* p, u32x2 v) {
  asm volatile("global_store_dwordx2 %0, %1, off sc1" :: "v"(p), "v"(v) : "memory");
}
#define OUTPOL "nt"
DI void ostore16(void* p, u32x4 v) { asm volatile("global_store_dwordx4 %0, %1, off " OUTPOL "\n\ts_nop 1" :: "v"(p), "v"(v) : "memory"); }
DI void ostore8(void* p, u32x2 v) { asm volatile("global_store_dwordx2 %0, %1, off " OUTPOL :: "v"(p), "v"(v) : "memory"); }
template <int N> DI void wait_vm() { asm volatile("s_waitcnt vmcnt(%0)" :: "n"(N) : "memory"); }
DI void bar() { asm volatile("s_waitcnt lgkmcnt(0)\n\ts_barrier" ::: "memory"); }
DI s16x4 tr_read(unsigned a) {
  return __builtin_amdgcn_ds_read_tr16_b64_v4i16((LAS s16x4*)(uintptr_t)a);
}
DI s16x8 lds_read16(unsigned a) { return *(LAS const s16x8*)(uintptr_t)a; }
DI s16x4 lds_read8(unsigned a) { return *(LAS const s16x4*)(uintptr_t)a; }
DI void lds_write16(unsigned a, u32x4 v) { *(LAS u32x4*)(uintptr_t)a = v; }
DI s16x8 cat8(s16x4 a, s16x4 b) { return __builtin_shufflevector(a, b, 0, 1, 2, 3, 4, 5, 6, 7); }
#define MFMA16(a, b, c) __builtin_amdgcn_mfma_f32_16x16x32_bf16((a), (b), (c), 0, 0, 0)
#define MFMA32(a, b, c) __builtin_amdgcn_mfma_f32_32x32x16_bf16((a), (b), (c), 0, 0, 0)

struct Diag {};
#define STAMP(i) do {} while (0)
#define DIAG_INIT() Diag dg
#define DIAG_DUMP(kid) do {} while (0)

#define SBAR() __builtin_amdgcn_sched_barrier(0)
#define SPIN_SLEEP 1
#define SPIN_MAX (1 << 21)
DI unsigned lds_ld32(unsigned a) { return *(volatile LAS unsigned*)(uintptr_t)a; }
DI void lds_st32(unsigned a, unsigned v) { *(volatile LAS unsigned*)(uintptr_t)a = v; }
DI void spin_ge(unsigned a, unsigned want) {
#pragma nounroll
  for (int it = 0; it < SPIN_MAX; ++it) {
    if ((unsigned)__builtin_amdgcn_readfirstlane(lds_ld32(a)) >= want) break;
    __builtin_amdgcn_s_sleep(1);
  }
  asm volatile("" ::: "memory");
}
DI void spin_ge4(unsigned a, unsigned want) {
#pragma nounroll
  for (int it = 0; it < SPIN_MAX; ++it) {
    const u32x4 v = *(volatile LAS u32x4*)(uintptr_t)a;
    const unsigned m = min(min(v[0], v[1]), min(v[2], v[3]));
    if ((unsigned)__builtin_amdgcn_readfirstlane(m) >= want) break;
    __builtin_amdgcn_s_sleep(SPIN_SLEEP);
  }
  asm volatile("" ::: "memory");
}
DI unsigned mk_bad(const u32x4& v, unsigned x) { return (v[0] ^ x) | (v[1] ^ x) | (v[2] ^ x) | (v[3] ^ x); }
DI void spin_mk(unsigned a, unsigned x) {
#pragma nounroll
  for (int it = 0; it < SPIN_MAX; ++it) {
    const u32x4 v = *(volatile LAS u32x4*)(uintptr_t)a;
    if ((unsigned)__builtin_amdgcn_readfirstlane(mk_bad(v, x)) == 0u) break;
    __builtin_amdgcn_s_sleep(SPIN_SLEEP);
  }
  asm volatile("" ::: "memory");
}
DI void spin_ge8(unsigned a, unsigned want) { spin_ge4(a, want); spin_ge4(a + 16, want); }
constexpr int CT_S0 = 0, CT_FREE = 80, CT_PAIR = 144  , CT_QUAD = 192  , CT_ZCNT = 240  ,
              CT_QCNT = 256, CT_ZRDY = 304, CT_CVD = 336  , CT_XST = 416, CT_ATT = 448, CT_RED = 480,
              CT_FX = 512  , CT_KR8 = 688, CT_VR8 = 704, CT_WRDY = 720, CT_WRD2 = 736, CT_BYTES = 1024;
DI void sig(unsigned a, unsigned v, int lane) { if (lane == 0) lds_st32(a, v); }

#define POLL_MAX (1 << 15)
DI void publish(char* ws, size_t off, u64 E, int lane) {
  asm volatile("s_waitcnt vmcnt(0)" ::: "memory");
  if (lane == 0) __hip_atomic_store((u64*)(ws + off), E, __ATOMIC_RELAXED, __HIP_MEMORY_SCOPE_AGENT);
}
#define ZSLEEP 20
#define USLEEP 10
#define POLL_SLEEP 6
template <int SL = POLL_SLEEP>
DI void poll(const char* ws, size_t off, u64 E) {
#pragma nounroll
  for (int it = 0; it < POLL_MAX; ++it) {
    const u64 v = __hip_atomic_load((const u64*)(ws + off), __ATOMIC_RELAXED, __HIP_MEMORY_SCOPE_AGENT);
    if (__builtin_amdgcn_ballot_w64(v != E) == 0ull) break;
    __builtin_amdgcn_s_sleep(SL);
  }
  asm volatile("" ::: "memory");
}

constexpr int K1_STAGE = 32768;
constexpr int A1_K = 0, A1_V = 3072, A1_W = 5120, A1_WAVE = 5632, A1_O = 2 * A1_WAVE, A1_PAIR = A1_O + 4096;
constexpr int L_AUX = 4 * K1_STAGE;
constexpr int L_CT = L_AUX + 2 * A1_PAIR;
constexpr int L_TOTAL = L_CT + CT_BYTES;
constexpr int K1_CT = L_CT;
constexpr int CV_COPY = 4160, CV_H = 4608, CV_REG = CV_H + 24576;
constexpr int AT_Q = L_AUX + 12288, AT_K = 64000, AT_V = AT_K + 16 * 2048, AT_END = AT_V + 16 * 1344;
constexpr int p3_w(int t) { return t < 5 ? t * 12800 : (t == 5 ? AT_END : AT_K + (t - 6) * 12800); }

#define PSYNC() do { asm volatile("s_waitcnt lgkmcnt(0)" ::: "memory"); ++ps; sig(pf + wh * 4, ps, lane); spin_ge(pf + (wh ^ 1) * 4, ps); } while (0)
DI void a1_issue(unsigned lds, int tile, int lane, int wh, const u16* ak, const u16* av, const u16* shk) {
  const int ts = tile & 63, td = tile >> 6;
  const int s0 = ts * 32 + wh * 16, d0 = td * 64;
  const int lr = lane >> 3, lc = (lane & 7) * 16;
  const unsigned wl = lds + wh * A1_WAVE;
#pragma unroll
  for (int i = 0; i < 3; ++i) {
    int r = s0 - 3 + i * 8 + lr; r = r < 0 ? 0 : r;
    if (i < 2 || lane < 24) glds16nt((unsigned)(r * DM * 2 + lc), (const char*)ak + d0 * 2, wl + A1_K + i * 1024);
  }
#pragma unroll
  for (int i = 0; i < 2; ++i)
    glds16nt((unsigned)(lr * DM * 2 + lc), (const char*)av + ((size_t)(s0 + i * 8) * DM + d0) * 2, wl + A1_V + i * 1024);
  if (lane < 32) glds16((unsigned)(lane * 16), (const char*)shk + d0 * 8, wl + A1_W);
}
DI void a1_finish(unsigned lds, unsigned pf, unsigned& ps, int tile, int lane, int wh, char* ws, u64 E) {
  const int ts = tile & 63, td = tile >> 6;
  const int c = lane & 7;
  const unsigned wl = lds + wh * A1_WAVE;
  u32x4 wt[4];
#pragma unroll
  for (int i = 0; i < 4; ++i) wt[i] = __builtin_bit_cast(u32x4, lds_read16(wl + A1_W + c * 64 + i * 16));
#pragma unroll
  for (int j = 0; j < 2; ++j) {
    const int r = j * 8 + (lane >> 3);
    const unsigned ka = wl + A1_K + (r + 3) * 128 + c * 16;
    u32x4 k0 = __builtin_bit_cast(u32x4, lds_read16(ka)), k1 = __builtin_bit_cast(u32x4, lds_read16(ka - 128));
    u32x4 k2 = __builtin_bit_cast(u32x4, lds_read16(ka - 256)), k3 = __builtin_bit_cast(u32x4, lds_read16(ka - 384));
    const u32x4 vv = __builtin_bit_cast(u32x4, lds_read16(wl + A1_V + r * 128 + c * 16));
    if (j == 0 && ts == 0 && wh == 0) {
      const u32x4 z = {0u, 0u, 0u, 0u};
      if (r < 1) k1 = z;
      if (r < 2) k2 = z;
      if (r < 3) k3 = z;
    }
    u32x4 o;
#pragma unroll
    for (int i = 0; i < 4; ++i) {
      const unsigned pa0 = __builtin_amdgcn_perm(k1[i], k0[i], 0x05040100u), pb0 = __builtin_amdgcn_perm(k3[i], k2[i], 0x05040100u);
      const unsigned pa1 = __builtin_amdgcn_perm(k1[i], k0[i], 0x07060302u), pb1 = __builtin_amdgcn_perm(k3[i], k2[i], 0x07060302u);
      const float e0 = dot2bf(pb0, wt[i][1], dot2bf(pa0, wt[i][0], 0.f));
      const float e1 = dot2bf(pb1, wt[i][3], dot2bf(pa1, wt[i][2], 0.f));
      o[i] = pk_bf16(e0 * bf_lo(vv[i]), e1 * bf_hi(vv[i]));
    }
    lds_write16(lds + A1_O + (31 - wh * 16 - r) * 128 + c * 16, o);
  }
  PSYNC();
  const int gg = lane >> 4, q = (lane & 15) >> 2, p = lane & 3;
  u16* ut = (u16*)(ws + WS_UT);
#pragma unroll
  for (int j = 0; j < 2; ++j) {
    const unsigned ra = lds + A1_O + (gg * 8 + q) * 128 + wh * 64 + j * 32 + p * 8;
    const s16x4 t0 = tr_read(ra), t1 = tr_read(ra + 512);
    const int d = td * 64 + wh * 32 + j * 16 + (lane & 15);
    gstore16c(ut + (size_t)d * SEQ + (SEQ - 32 - ts * 32) + gg * 8, __builtin_bit_cast(u32x4, cat8(t0, t1)));
  }
}
DI void a1_publish(int tile, int lane, int wh, char* ws, u64 E) {
  const int ts = tile & 63, td = tile >> 6;
  publish(ws, FL_U + (size_t)((td * 2 + wh) * 64 + ts) * 8, E, lane);
}

template <bool VT>
DI void k1_gemm(unsigned lds0, int m0, int n0, int tid, char* ws, unsigned mkx, Diag& dg) {
  const int lane = tid & 63, wave = __builtin_amdgcn_readfirstlane(tid >> 6);
  const int wm = wave >> 1, wn = wave & 1;
  const int l32 = lane & 31, hh = lane >> 5, gg = lane >> 4, q = (lane & 15) >> 2, p = lane & 3;
  const unsigned ct = lds0 + K1_CT;
  f32x16 acc[2][2];
#pragma unroll
  for (int i = 0; i < 2; ++i)
#pragma unroll
    for (int j = 0; j < 2; ++j)
#pragma unroll
      for (int r = 0; r < 16; ++r) acc[i][j][r] = 0.f;
  unsigned xb[4];
#pragma unroll
  for (int ks = 0; ks < 4; ++ks) xb[ks] = lds0 + (wm * 64 + l32) * 128 + (((unsigned)(2 * ks + hh) ^ (unsigned)((l32 >> 1) & 7)) * 16);
  unsigned wb[2];
#pragma unroll
  for (int nt = 0; nt < 2; ++nt)
    wb[nt] = lds0 + 16384 + (8 * hh + q) * 256 + (((unsigned)(wn * 4 + 2 * nt + (gg & 1)) ^ (unsigned)(q << 1)) * 32) + p * 8;
  s16x8 x0[2], w0[2], x1[2], w1[2], x2[2], w2[2], x3[2], w3[2];
#define K1_LOADF(XF, WF, bo_, ks) do { \
    _Pragma("unroll") for (int i_ = 0; i_ < 2; ++i_) { \
      WF[i_] = cat8(tr_read(wb[i_] + (bo_) + (ks) * 4096), tr_read(wb[i_] + (bo_) + (ks) * 4096 + 1024)); \
      XF[i_] = lds_read16(xb[ks] + (bo_) + i_ * 4096); } } while (0)
#define K1_MM(XF, WF) do { _Pragma("unroll") for (int mt = 0; mt < 2; ++mt) _Pragma("unroll") for (int nt = 0; nt < 2; ++nt) \
      acc[mt][nt] = VT ? MFMA32(XF[mt], WF[nt], acc[mt][nt]) : MFMA32(WF[nt], XF[mt], acc[mt][nt]); } while (0)
#define ILV_A 2
#define ILV_B 2
#define ILV_C 2
#define K1_ILV() do { \
      __builtin_amdgcn_sched_group_barrier(0x008, 1, 0); __builtin_amdgcn_sched_group_barrier(0x100, ILV_A, 0); \
      __builtin_amdgcn_sched_group_barrier(0x008, 1, 0); __builtin_amdgcn_sched_group_barrier(0x100, ILV_B, 0); \
      __builtin_amdgcn_sched_group_barrier(0x008, 1, 0); __builtin_amdgcn_sched_group_barrier(0x100, ILV_C, 0); \
      __builtin_amdgcn_sched_group_barrier(0x008, 1, 0); __builtin_amdgcn_sched_group_barrier(0x100, 2, 0); } while (0)
  STAMP(1);
  K1_LOADF(x0, w0, 0, 0);
  K1_LOADF(x1, w1, 0, 1);
#pragma nounroll
  for (int t = 0; t < 10; ++t) {
    const unsigned bo = (t & 3) * K1_STAGE, bn = ((t + 1) & 3) * K1_STAGE;
    SBAR();
    const u32x4 fl4 = *(volatile LAS u32x4*)(uintptr_t)(ct + CT_FX + (t + 1) * 16);
    K1_LOADF(x2, w2, bo, 2); K1_MM(x0, w0); K1_ILV();
    SBAR();
    K1_LOADF(x3, w3, bo, 3); K1_MM(x1, w1); K1_ILV();
    SBAR();
    const unsigned flu = (unsigned)__builtin_amdgcn_readfirstlane(mk_bad(fl4, mkx));
    if (t < 9 && flu != 0u) spin_mk(ct + CT_FX + (t + 1) * 16, mkx);
    asm volatile("" ::: "memory");
    SBAR();
    K1_LOADF(x0, w0, bn, 0); K1_MM(x2, w2); K1_ILV();
    SBAR();
    asm volatile("" : "+v"(x3[0]), "+v"(x3[1]), "+v"(w3[0]), "+v"(w3[1]) :: "memory");
    if (lane == 0) lds_st32(ct + CT_FREE + (t & 3) * 16 + wave * 4, (unsigned)(t + 1));
    SBAR();
    K1_LOADF(x1, w1, bn, 1); K1_MM(x3, w3); K1_ILV();
  }
  SBAR();
  STAMP(2);
  if (!VT) {
    const int part = n0 >= DM ? 1 : 0;
    const float sc = part == 0 ? 0.22360679774997896f * 1.4426950408889634f : 1.0f;
    char* base = ws + (part ? WS_KF : WS_QF) + ((size_t)((m0 + wm * 64) >> 5) * 40 + ((n0 - part * DM + wn * 64) >> 4)) * 1024 + lane * 16;
#pragma unroll
    for (int mt = 0; mt < 2; ++mt)
#pragma unroll
      for (int nt = 0; nt < 2; ++nt)
#pragma unroll
        for (int uh = 0; uh < 2; ++uh) {
          u32x4 o;
#pragma unroll
          for (int i = 0; i < 4; ++i) o[i] = pk_bf16(acc[mt][nt][8 * uh + 2 * i] * sc, acc[mt][nt][8 * uh + 2 * i + 1] * sc);
          gstore16c(base + (mt * 40 + nt * 2 + uh) * 1024, o);
        }
  } else {
    char* base = ws + WS_VF + ((size_t)((m0 + wm * 64) >> 4) * 20 + ((n0 - 2 * DM + wn * 64) >> 5)) * 1024 + lane * 16;
#pragma unroll
    for (int mt = 0; mt < 2; ++mt)
#pragma unroll
      for (int nt = 0; nt < 2; ++nt)
#pragma unroll
        for (int kg = 0; kg < 2; ++kg) {
          u32x4 o;
#pragma unroll
          for (int i = 0; i < 4; ++i) o[i] = pk_bf16(acc[mt][nt][8 * kg + 2 * i], acc[mt][nt][8 * kg + 2 * i + 1]);
          gstore16c(base + ((mt * 2 + kg) * 20 + nt) * 1024, o);
        }
  }
}

DI void cv_put(unsigned y, const u32x4& c, const u32x2& n) {
  const unsigned d0 = c[0], d1 = c[1], d2 = c[2], d3 = c[3], d4 = n[0], d5 = n[1];
  const unsigned a0 = __builtin_amdgcn_alignbit(d1, d0, 16), a1 = __builtin_amdgcn_alignbit(d2, d1, 16),
                 a2 = __builtin_amdgcn_alignbit(d3, d2, 16), a3 = __builtin_amdgcn_alignbit(d4, d3, 16),
                 a4 = __builtin_amdgcn_alignbit(d5, d4, 16);
  lds_write16(y, (u32x4){d0, d1, d2, d3});
  lds_write16(y + CV_COPY, (u32x4){a0, a1, a2, a3});
  lds_write16(y + 2 * CV_COPY, (u32x4){d1, d2, d3, d4});
  lds_write16(y + 3 * CV_COPY, (u32x4){a1, a2, a3, a4});
}

#define QSYNC() do { asm volatile("s_waitcnt lgkmcnt(0)" ::: "memory"); ++qs; sig(qf + wq * 4, qs, lane); spin_ge4(qf, qs); } while (0)
struct HRegs { u32x4 c, u; u32x2 n; unsigned d; };
DI HRegs conv_load_h(int ch, int t256, const u16* __restrict__ ssmk, const u16* __restrict__ ssmd) {
  HRegs r;
  const u16* hrow = ssmk + (size_t)ch * SEQ;
  r.c = *((const u32x4*)(hrow + t256 * 8));
  r.n = *(const u32x2*)(hrow + (t256 < 255 ? t256 * 8 + 8 : 0));
  r.d = ssmd[ch];
  return r;
}
DI void conv_load_u(HRegs& r, int ch, int lane, int wq, const char* __restrict__ ws, u64 E) {
  poll<USLEEP>(ws, FL_U + (size_t)((ch >> 5) * 64 + 48 - 16 * wq + (lane & 15)) * 8, E);
  const u64* up = (const u64*)(ws + WS_UT + (size_t)ch * SEQ * 2 + wq * 1024 + lane * 16);
  const u64 a = __hip_atomic_load(up, __ATOMIC_RELAXED, __HIP_MEMORY_SCOPE_AGENT);
  const u64 b = __hip_atomic_load(up + 1, __ATOMIC_RELAXED, __HIP_MEMORY_SCOPE_AGENT);
  r.u = (u32x4){(unsigned)a, (unsigned)(a >> 32), (unsigned)b, (unsigned)(b >> 32)};
}
DI void conv_stage(unsigned reg, int lane, int wq, const HRegs& hr) {
  const int t256 = wq * 64 + lane;
  const unsigned hb = reg + CV_H;
  if (t256 < 32) lds_write16(reg + 4096 + t256 * 16, (u32x4){0u, 0u, 0u, 0u});
  cv_put(hb + (t256 + 2) * 16, hr.c, hr.n);
  if (t256 < 2) {
    const u32x4 z = {0u, 0u, 0u, 0u};
    u32x2 nx = {0u, 0u};
    const unsigned h0 = __builtin_amdgcn_readfirstlane(hr.c[0]), h1 = __builtin_amdgcn_readfirstlane(hr.c[1]);
    if (t256 == 1) nx = (u32x2){h0, h1};
    cv_put(hb + t256 * 16, z, nx);
  }
}
DI void conv_run(unsigned reg, unsigned qf, unsigned& qs, int ch, int lane, int wq, const HRegs& hr,
                 char* __restrict__ ws, unsigned xst, Diag& dg) {
  const unsigned hb = reg + CV_H;
  if (xst) spin_ge4(xst, 1u);
  else { lds_write16(reg + (wq * 64 + lane) * 16, hr.u); QSYNC(); }
  STAMP(6);
  const int r16 = lane & 15, g = lane >> 4, q = r16 >> 2, p = lane & 3;
  const unsigned ab0 = hb + ((q + 1) & 3) * CV_COPY + 2 * (8 * g + 4 * p + (q == 3 ? 4 : 0)) + wq * 128;
  const unsigned bb0 = reg + 2 * (2032 - 16 * r16 + 8 * g) + wq * 128 - 7 * 512;
  f32x4 acc[8];
#pragma unroll
  for (int i = 0; i < 8; ++i) acc[i] = (f32x4){0.f, 0.f, 0.f, 0.f};
#define CV_LDA(a_, ad_) cat8(tr_read((ad_) + (a_) * 512), tr_read((ad_) + (a_) * 512 + 8))
#define CV_LDB(c_, bd_) lds_read16((bd_) + (7 - (c_)) * 512)
  {
    s16x8 A[8], B[8];
    B[0] = CV_LDB(0, bb0);
#pragma unroll
    for (int a = 0; a < 8; ++a) A[a] = CV_LDA(a, ab0);
#pragma unroll
    for (int c = 1; c < 8; ++c) B[c] = CV_LDB(c, bb0);
#pragma unroll
    for (int c = 0; c < 8; ++c) {
      SBAR();
#pragma unroll
      for (int a = 0; a + c < 8; ++a) acc[a + c] = MFMA16(A[a], B[c], acc[a + c]);
      B[c] = CV_LDB(c, bb0 + 64);
      A[7 - c] = CV_LDA(7 - c, ab0 + 64);
    }
    SBAR();
#pragma unroll
    for (int a = 7; a >= 0; --a)
#pragma unroll
      for (int c = 0; a + c < 8; ++c) acc[a + c] = MFMA16(A[a], B[c], acc[a + c]);
  }
  QSYNC();
#pragma unroll
  for (int nt = 0; nt < 8; ++nt) {
    const int dw = nt >> 1;
    if (dw != wq) lds_write16(hb + (nt * 3 + (wq > dw ? wq - 1 : wq)) * 1024 + lane * 16, __builtin_bit_cast(u32x4, acc[nt]));
  }
  f32x4 k0 = wq == 0 ? acc[0] : (wq == 1 ? acc[2] : (wq == 2 ? acc[4] : acc[6]));
  f32x4 k1 = wq == 0 ? acc[1] : (wq == 1 ? acc[3] : (wq == 2 ? acc[5] : acc[7]));
  QSYNC();
  const float dco = bf_lo(hr.d);
  u32x2 zz[2];
#pragma unroll
  for (int i = 0; i < 2; ++i) {
    f32x4 o = i ? k1 : k0;
#pragma unroll
    for (int j = 0; j < 3; ++j) o += __builtin_bit_cast(f32x4, lds_read16(hb + ((wq * 2 + i) * 3 + j) * 1024 + lane * 16));
    const int s = (wq * 2 + i) * 256 + 16 * r16 + 4 * g;
    const u32x2 uu = __builtin_bit_cast(u32x2, lds_read8(reg + 2 * (SEQ - 4 - s)));
    const float kv0 = bf_hi(uu[1]), kv1 = bf_lo(uu[1]), kv2 = bf_hi(uu[0]), kv3 = bf_lo(uu[0]);
    const unsigned y01 = pk_bf16(o[0], o[1]), y23 = pk_bf16(o[2], o[3]);
    const unsigned e01 = pk_bf16(kv0 * dco, kv1 * dco), e23 = pk_bf16(kv2 * dco, kv3 * dco);
    zz[i][0] = pk_bf16(bf_lo(y01) + bf_lo(e01), bf_hi(y01) + bf_hi(e01));
    zz[i][1] = pk_bf16(bf_lo(y23) + bf_lo(e23), bf_hi(y23) + bf_hi(e23));
  }
  const auto sx = __builtin_amdgcn_permlane16_swap(zz[0][0], zz[1][0], false, false);
  const auto sy = __builtin_amdgcn_permlane16_swap(zz[0][1], zz[1][1], false, false);
  u16* zrow = (u16*)(ws + WS_ZT) + (size_t)ch * SEQ;
  gstore16c(zrow + (wq * 2 + (g & 1)) * 256 + 16 * r16 + 8 * (g >> 1), (u32x4){sx[0], sy[0], sx[1], sy[1]});
  asm volatile("s_waitcnt lgkmcnt(0)" ::: "memory");
}
DI void conv_publish(unsigned zc, int ch, int lane, char* __restrict__ ws, u64 E) {
  asm volatile("s_waitcnt vmcnt(0)" ::: "memory");
  if (lane == 0) {
    const unsigned old = __hip_atomic_fetch_add((LAS unsigned*)(uintptr_t)zc, 1u, __ATOMIC_RELAXED, __HIP_MEMORY_SCOPE_WORKGROUP);
    if ((old & 3u) == 3u) __hip_atomic_store((u64*)(ws + FL_Z + (size_t)ch * 8), E, __ATOMIC_RELAXED, __HIP_MEMORY_SCOPE_AGENT);
  }
}

DI s16x8 pack8(const f32x16& v, int o) {
  u32x4 r;
  r[0] = pk_bf16(v[o + 0], v[o + 1]); r[1] = pk_bf16(v[o + 2], v[o + 3]);
  r[2] = pk_bf16(v[o + 4], v[o + 5]); r[3] = pk_bf16(v[o + 6], v[o + 7]);
  return __builtin_bit_cast(s16x8, r);
}
DI float max3f(float a, float b, float c) { return __builtin_fmaxf(__builtin_fmaxf(a, b), c); }
DI float max16(const f32x16& v, float m) {
  float a = max3f(v[0], v[1], m), b = max3f(v[2], v[3], v[4]);
  a = max3f(a, v[5], v[6]); b = max3f(b, v[7], v[8]);
  a = max3f(a, v[9], v[10]); b = max3f(b, v[11], v[12]);
  a = max3f(a, v[13], v[14]);
  return max3f(a, b, v[15]);
}
DI float xhalf_max(float m) {
  auto sw = __builtin_amdgcn_permlane32_swap(__float_as_uint(m), __float_as_uint(m), false, false);
  return __builtin_fmaxf(__uint_as_float(sw[0]), __uint_as_float(sw[1]));
}

#define AT_LMAX 65536.0f
#define P3_WADDR() unsigned wv[4]; _Pragma("unroll") for (int i = 0; i < 4; ++i) { \
      const int c = i < 3 ? (i * 4 + w) * 64 + lane : 768 + w * 8 + (lane & 7); const int row = c / 10; \
      wv[i] = (unsigned)(row * DM * 2 + (c - row * 10) * 16); } const char* wog = (const char*)(wo + n3)
#define P3_STW(t) do { _Pragma("unroll") for (int i_ = 0; i_ < 3; ++i_) \
        glds16(wv[i_], wog + (size_t)(t) * 80 * DM * 2, lds0 + p3_w(t) + (i_ * 4 + w) * 1024); \
      if (lane < 8) glds16(wv[3], wog + (size_t)(t) * 80 * DM * 2, lds0 + p3_w(t) + 12288 + w * 128); } while (0)
DI void attn_stage(unsigned lds0, int h, int qb8, int j, int lane, char* __restrict__ ws, u64 E, int zch, const Mk& mk, Diag& dg) {
  const int qt = qb8 * 8 + j;
  const unsigned l16 = (unsigned)(lane * 16), ct = lds0 + L_CT;
  const int qa = (5 * h) >> 2;
  const bool stg = qb8 > 0 || j >= 2;
  {
    const int l3 = lane & 7, hi = lane & 1;
    const bool kv = stg && l3 >= 2 && l3 < 6;
    const int rb = kv ? 2 * qb8 - 2 + j : 2 * qb8 + (l3 >= 6 ? 1 : 0);
    const int col = !kv ? qa * 16 + hi * 31 : (l3 < 4 ? DM + qa * 16 + hi * 31 : 2 * DM + h * 20 + hi * 19);
    poll(ws, FL_Q + (size_t)((h * 16 + rb) * 16 + (col >> 7)) * 8, E);
  }
  STAMP(9);
  conv_publish(ct + CT_ZCNT + 8, zch, lane, ws, E);
  const int kt0 = qb8 * 8 - 8 + 4 * j;
#pragma unroll
  for (int i = 0; i < 4; ++i)
    glds16c(l16, ws + WS_QF + ((size_t)(qt + (i >> 1) * 4) * 40 + qa + (i & 1)) * 1024, lds0 + AT_Q + ((j + (i >> 1) * 4) * 2 + (i & 1)) * 1024);
  if (stg) {
#pragma unroll
    for (int i = 0; i < 8; ++i)
      glds16c(l16, ws + WS_KF + ((size_t)(kt0 + (i >> 1)) * 40 + qa + (i & 1)) * 1024, lds0 + AT_K + (j * 8 + i) * 1024);
  }
  glds_mark(mk, ct + CT_KR8 + j * 4, lane);
  if (stg) {
#pragma unroll
    for (int i = 0; i < 6; ++i) {
      const int c = i * 64 + lane;
      const int kgp = (c * 1561) >> 16, rem = c - kgp * 42, vh = rem >= 21 ? 1 : 0, d = rem - vh * 21;
      const int n = h * 20 + d;
      if (c < 336 && d == 20) lds_write16(lds0 + AT_V + j * 5376 + c * 16, (u32x4){0x3f803f80u, 0x3f803f80u, 0x3f803f80u, 0x3f803f80u});
      if (c < 336 && d < 20)
        glds16c((unsigned)((kgp * 20 + (n >> 5)) * 1024 + (vh * 32 + (n & 31)) * 16), ws + WS_VF + ((size_t)(kt0 * 2) * 20) * 1024,
                lds0 + AT_V + j * 5376 + i * 1024);
    }
    asm volatile("s_waitcnt lgkmcnt(0)" ::: "memory");
  }
  glds_mark(mk, ct + CT_VR8 + j * 4, lane);
}
template <bool FULL>
DI void attn_task(unsigned lds0, int h, int qb8, int j, int lane, char* __restrict__ ws, u64 E, int zch, const Mk& mk, Diag& dg) {
  const int l32 = lane & 31, hh = lane >> 5;
  const int qt = qb8 * 8 + j;
  const int tfirst = FULL ? 0 : 8 - qt;
  const unsigned l16 = (unsigned)(lane * 16), ct = lds0 + L_CT;
  const int qa = (5 * h) >> 2;
  u32x4 q0r, q1r;
  spin_mk(ct + CT_KR8, mk.x);
  if (!FULL) spin_mk(ct + CT_VR8, mk.x);
  q0r = __builtin_bit_cast(u32x4, lds_read16(lds0 + AT_Q + j * 2048 + l16));
  q1r = __builtin_bit_cast(u32x4, lds_read16(lds0 + AT_Q + j * 2048 + 1024 + l16));
  asm volatile("s_waitcnt lgkmcnt(0)" : "+v"(q0r), "+v"(q1r) :: "memory");
  if (zch >= 0) conv_publish(ct + CT_ZCNT + (j >> 2) * 4, zch, lane, ws, E);
  {
    const int g0 = 4 * qa + hh - 5 * h;
    const bool k0 = g0 >= 0, k1 = g0 + 2 >= 0 && g0 + 2 < 5, k2 = g0 + 4 < 5, k3 = g0 + 6 < 5;
    q0r[0] = k0 ? q0r[0] : 0u; q0r[1] = k0 ? q0r[1] : 0u; q0r[2] = k1 ? q0r[2] : 0u; q0r[3] = k1 ? q0r[3] : 0u;
    q1r[0] = k2 ? q1r[0] : 0u; q1r[1] = k2 ? q1r[1] : 0u; q1r[2] = k3 ? q1r[2] : 0u; q1r[3] = k3 ? q1r[3] : 0u;
  }
  s16x8 qlo = __builtin_bit_cast(s16x8, q0r), qhi8 = __builtin_bit_cast(s16x8, q1r);
  STAMP(8);
  const f32x16 zero16 = {0.f, 0.f, 0.f, 0.f, 0.f, 0.f, 0.f, 0.f, 0.f, 0.f, 0.f, 0.f, 0.f, 0.f, 0.f, 0.f};
  const float ninf = -INFINITY;
  const f32x16 ninf16 = {ninf, ninf, ninf, ninf, ninf, ninf, ninf, ninf, ninf, ninf, ninf, ninf, ninf, ninf, ninf, ninf};
  const unsigned kb = lds0 + AT_K + j * 2048 + l16;
#define AT_QK(dst, t, cin) do { if (FULL || (t) >= tfirst) { dst = MFMA32(lds_read16(kb + (t) * 2048), qlo, cin); \
    dst = MFMA32(lds_read16(kb + (t) * 2048 + 1024), qhi8, dst); } else dst = ninf16; } while (0)
  const int vrow = l32 <= 20 ? l32 : (l32 == 24 ? 20 : l32 - 16);
  const unsigned vb = lds0 + AT_V + j * 1344 + hh * 336 + vrow * 16;
#define AT_EXP(sv) do { _Pragma("unroll") for (int r = 0; r < 16; ++r) sv[r] = __builtin_amdgcn_exp2f(sv[r] - m); } while (0)
#define AT_PV(sv, t) do { oacc = MFMA32(lds_read16(vb + (t) * 1344), pack8(sv, 0), oacc); oacc = MFMA32(lds_read16(vb + (t) * 1344 + 672), pack8(sv, 8), oacc); } while (0)
  f32x16 oacc;
  bool exact = !FULL;
  if (FULL) {
    f32x16 sm, sa, sb;
    AT_QK(sm, 8, zero16); AT_QK(sa, 0, zero16);
    const int tq = l32 - 4 * hh;
    const unsigned wm = tq < 0 ? 0xffffffffu : (0xfffffffeu << tq);
    unsigned msk[16];
#pragma unroll
    for (int r = 0; r < 16; ++r) {
      msk[r] = (unsigned)__builtin_amdgcn_sbfe((int)wm, (r & 3) + 8 * (r >> 2), 1);
      sm[r] = __uint_as_float((msk[r] & __float_as_uint(sa[r])) | (~msk[r] & __float_as_uint(sm[r])));
    }
    const float m = xhalf_max(max16(sm, ninf));
    const float nm = -m;
    const f32x16 cneg = {nm, nm, nm, nm, nm, nm, nm, nm, nm, nm, nm, nm, nm, nm, nm, nm};
    AT_QK(sa, 1, cneg);
    AT_EXP(sm);
    u32x4 pl = __builtin_bit_cast(u32x4, pack8(sm, 0)), ph = __builtin_bit_cast(u32x4, pack8(sm, 8)), ql, qh;
#pragma unroll
    for (int i = 0; i < 4; ++i) {
      ql[i] = pl[i] & ((msk[2 * i] & 0xffffu) | (msk[2 * i + 1] & 0xffff0000u)); pl[i] ^= ql[i];
      qh[i] = ph[i] & ((msk[8 + 2 * i] & 0xffffu) | (msk[9 + 2 * i] & 0xffff0000u)); ph[i] ^= qh[i];
    }
    spin_mk(ct + CT_VR8, mk.x);
    STAMP(9);
    oacc = MFMA32(lds_read16(vb + 8 * 1344), __builtin_bit_cast(s16x8, pl), zero16);
    oacc = MFMA32(lds_read16(vb + 8 * 1344 + 672), __builtin_bit_cast(s16x8, ph), oacc);
    oacc = MFMA32(lds_read16(vb), __builtin_bit_cast(s16x8, ql), oacc);
    oacc = MFMA32(lds_read16(vb + 672), __builtin_bit_cast(s16x8, qh), oacc);
#define AT_EX0(sv) do { _Pragma("unroll") for (int r = 0; r < 16; ++r) sv[r] = __builtin_amdgcn_exp2f(sv[r]); } while (0)
    AT_QK(sb, 2, cneg); AT_EX0(sa); AT_PV(sa, 1);
    AT_QK(sa, 3, cneg); AT_EX0(sb); AT_PV(sb, 2);
    AT_QK(sb, 4, cneg); AT_EX0(sa); AT_PV(sa, 3);
    AT_QK(sa, 5, cneg); AT_EX0(sb); AT_PV(sb, 4);
    AT_QK(sb, 6, cneg); AT_EX0(sa); AT_PV(sa, 5);
    AT_QK(sa, 7, cneg); AT_EX0(sb); AT_PV(sb, 6);
    AT_EX0(sa); AT_PV(sa, 7);
    float big = max3f(__builtin_fabsf(oacc[0]), __builtin_fabsf(oacc[1]), __builtin_fabsf(oacc[2]));
    big = max3f(big, __builtin_fabsf(oacc[3]), __builtin_fabsf(oacc[4]));
    big = max3f(big, __builtin_fabsf(oacc[5]), __builtin_fabsf(oacc[6]));
    big = max3f(big, __builtin_fabsf(oacc[7]), __builtin_fabsf(oacc[8]));
    big = max3f(big, __builtin_fabsf(oacc[9]), __builtin_fabsf(oacc[10]));
    big = __builtin_fmaxf(big, __builtin_fabsf(oacc[11]));
    const float ls = hh ? oacc[8] : oacc[12];
    float osum = 0.f;
#pragma unroll
    for (int r = 0; r < 8; ++r) osum += oacc[r];
    const bool ok = (ls < AT_LMAX) && (big < 1e37f) && (osum == osum);
    exact = __builtin_amdgcn_ballot_w64(!ok) != 0ull;
  }
  if (exact) {
    asm volatile("" : "+v"(qlo), "+v"(qhi8) :: "memory");
    f32x16 sm, s1, s2, s3;
    {
      f32x16 s0;
      AT_QK(sm, 8, zero16); AT_QK(s0, 0, zero16);
      AT_QK(s1, 1, zero16); AT_QK(s2, 2, zero16); AT_QK(s3, 3, zero16);
#pragma unroll
      for (int r = 0; r < 16; ++r) {
        const int kk = (r & 3) + 8 * (r >> 2) + 4 * hh;
        sm[r] = kk > l32 ? s0[r] : sm[r];
      }
    }
    float m = max16(sm, ninf);
    m = max16(s1, m); m = max16(s2, m); m = max16(s3, m);
    m = xhalf_max(m);
    AT_EXP(sm);
    oacc = zero16;
    {
      f32x16 p0, p8;
#pragma unroll
      for (int r = 0; r < 16; ++r) {
        const int kk = (r & 3) + 8 * (r >> 2) + 4 * hh;
        p0[r] = kk > l32 ? sm[r] : 0.f;
        p8[r] = kk > l32 ? 0.f : sm[r];
      }
      AT_PV(p8, 8);
      if (FULL) AT_PV(p0, 0);
    }
    if (FULL || 1 >= tfirst) { AT_EXP(s1); AT_PV(s1, 1); }
    AT_QK(sm, 4, zero16); AT_QK(s1, 5, zero16);
    if (FULL || 2 >= tfirst) { AT_EXP(s2); AT_PV(s2, 2); }
    AT_QK(s2, 6, zero16);
    if (FULL || 3 >= tfirst) { AT_EXP(s3); AT_PV(s3, 3); }
    AT_QK(s3, 7, zero16);
    float m2 = max16(sm, m);
    m2 = max16(s1, m2); m2 = max16(s2, m2); m2 = max16(s3, m2);
    m2 = xhalf_max(m2);
    {
      const float alpha = __builtin_amdgcn_exp2f(m - m2);
#pragma unroll
      for (int r = 0; r < 13; ++r) oacc[r] *= alpha;
      m = m2;
    }
    if (FULL || 4 >= tfirst) { AT_EXP(sm); AT_PV(sm, 4); }
    if (FULL || 5 >= tfirst) { AT_EXP(s1); AT_PV(s1, 5); }
    if (FULL || 6 >= tfirst) { AT_EXP(s2); AT_PV(s2, 6); }
    if (FULL || 7 >= tfirst) { AT_EXP(s3); AT_PV(s3, 7); }
  }
  asm volatile("s_waitcnt lgkmcnt(0)" ::: "memory");
  sig(ct + CT_ATT + j * 4, 1u, lane);
  STAMP(10);
  const float l = hh ? oacc[8] : oacc[12];
  const float inv = __builtin_amdgcn_rcpf(l);
  char* oc = ws + WS_OW + ((size_t)qt * NH + h) * 1280;
  u32x4 o;
  o[0] = pk_bf16(oacc[0] * inv, oacc[1] * inv); o[1] = pk_bf16(oacc[2] * inv, oacc[3] * inv);
  o[2] = pk_bf16(oacc[4] * inv, oacc[5] * inv); o[3] = pk_bf16(oacc[6] * inv, oacc[7] * inv);
  gstore16c(oc + lane * 16, o);
  if (!hh) {
    u32x2 o2;
    o2[0] = pk_bf16(oacc[8] * inv, oacc[9] * inv); o2[1] = pk_bf16(oacc[10] * inv, oacc[11] * inv);
    gstore8c(oc + 1024 + l32 * 8, o2);
  }
  publish(ws, FL_O + (size_t)(qt * NH + h) * 8, E, lane);
}

#define HDELAY 0
#define APRIO 1
#define LPRIO 0
#define MPRIO 2
#define ROTG 2
#define A1_DELAY 8
__global__ __launch_bounds__(768, 3) void mega(const u16* __restrict__ x, const u16* __restrict__ wqkv,
                                               const u16* __restrict__ ak, const u16* __restrict__ av,
                                               const u16* __restrict__ shk, const u16* __restrict__ wo,
                                               const u16* __restrict__ ssmk, const u16* __restrict__ ssmd,
                                               const u16* __restrict__ aq, char* __restrict__ ws,
                                               u16* __restrict__ out) {
  extern __shared__ __attribute__((aligned(16))) char smem[];
  const unsigned lds0 = lds_addr(smem);
  const int tid = threadIdx.x, wave = __builtin_amdgcn_readfirstlane(tid >> 6);
  int lane = tid & 63;
#define RELANE() asm volatile("" : "+v"(lane))
  const int role = wave >> 2, w = wave & 3;
  const int bid = blockIdx.x, xcd = bid & 7, idx = bid >> 3;
  const bool has_gemm = idx < 30;
  const unsigned ct = lds0 + L_CT;
  DIAG_INIT();
  u64 E;
  Mk mk; mk.src = wqkv;
  asm volatile("s_load_dwordx2 %0, %2, 0x0\n\ts_load_dword %1, %3, 0x0" : "=&s"(E), "=&s"(mk.x) : "s"(ws + WS_FL), "s"(wqkv) : "memory");
  const int gt = (xcd & 1) * 30 + idx;
  const int tm = (xcd >> 1) * 4 + (gt & 3), tn = gt >> 2;
  const int m0 = tm * 128, n0 = tn * 128;
  const int rot = ROTG >= 10 ? (((gt & 3) * 5 + 1) / 2 + 3 * tn) % 10 : ((((gt & 3) + tn) % ROTG) * 10) / ROTG;
  const unsigned xo0 = (unsigned)((lane >> 3) * DM * 2 + (((lane & 7) ^ (lane >> 4)) * 16));
  const unsigned xo1 = (unsigned)((lane >> 3) * DM * 2 + (((lane & 7) ^ (4 + (lane >> 4))) * 16));
  const unsigned wof = (unsigned)((lane >> 4) * NQKV * 2 + (((lane & 15) ^ ((lane >> 4) << 2)) * 16));
  const char* xg = (const char*)(x + (size_t)m0 * DM);
  const char* wg = (const char*)(wqkv + n0);
#define K1_PIECE_X(t, kt, j) glds16p(((j) & 1) ? xo1 : xo0, xg + (size_t)(j) * 8 * DM * 2 + (kt) * 128, lds0 + ((t) & 3) * K1_STAGE + (j) * 1024)
#define K1_PIECE_W(t, kt, j) glds16p(wof, wg + (size_t)((kt) * 64 + (j) * 4) * NQKV * 2, lds0 + ((t) & 3) * K1_STAGE + 16384 + (j) * 1024)
  if (has_gemm && role == 0) {
#pragma unroll
    for (int i = 0; i < 4; ++i) { K1_PIECE_X(0, rot, w * 4 + i); K1_PIECE_W(0, rot, w * 4 + i); }
  }
  const unsigned pf = ct + CT_PAIR + (wave >> 1) * 8;
  const int wh = wave & 1;
  unsigned ps = 0;
  const int fb = (idx - 30) * 8 + xcd;
  const int a1t = has_gemm ? (xcd * 30 + idx) * 2 + (w >> 1) : 480 + fb * 10 + (wave >> 1);
  const unsigned a1l = has_gemm ? lds0 + L_AUX + (w >> 1) * A1_PAIR : lds0 + (wave >> 1) * A1_PAIR;
  if (!has_gemm || role == 2) a1_issue(a1l, a1t, lane, wh, ak, av, shk);
  const bool cv0 = role != 0 || !has_gemm || bid < 112;
  const int ch0 = role == 0 ? (has_gemm ? 528 + bid : 512 + fb) : bid + (role == 1 ? 256 : 0);
  asm volatile("s_waitcnt lgkmcnt(0)" : "+s"(E), "+s"(mk.x) :: "memory");
  if (role == 1) lds_st32(ct + (tid - 256) * 4, (tid - 256) * 4 >= CT_FX ? ~mk.x : 0u);
  bar();
  E += 1ull;

  const bool ex = has_gemm && role == 1 && bid < 112;
  HRegs hr, h2;
#define LOAD_H() do { hr = conv_load_h(ch0, w * 64 + lane, ssmk, ssmd); \
    if (ex) h2 = conv_load_h(528 + bid, w * 64 + lane, ssmk, ssmd); } while (0)
  auto q_publish = [&]() {
    asm volatile("s_waitcnt vmcnt(0)" ::: "memory");
    unsigned old = 0;
    if (lane == 0) old = __hip_atomic_fetch_add((LAS unsigned*)(uintptr_t)(ct + CT_QCNT), 1u, __ATOMIC_RELAXED, __HIP_MEMORY_SCOPE_WORKGROUP);
    if (__builtin_amdgcn_readfirstlane(old) == 3u) {
      const int c0 = (n0 % DM) - 32, hh_ = lane;
      if (lane < QREP && hh_ * 20 + 40 > c0 && hh_ * 20 < c0 + 32 + 128 + 20)
        __hip_atomic_store((u64*)(ws + FL_Q + (size_t)((lane * 16 + tm) * 16 + tn) * 8), E, __ATOMIC_RELAXED, __HIP_MEMORY_SCOPE_AGENT);
    }
    STAMP(4);
  };
  if (!has_gemm) {
    LOAD_H();
    const unsigned a2l = lds0 + (6 + (wave >> 1)) * A1_PAIR;
    if (wave < 8) a1_issue(a2l, a1t + 6, lane, wh, ak, av, shk);
    if (wave < 8) wait_vm<6>(); else wait_vm<0>();
    a1_finish(a1l, pf, ps, a1t, lane, wh, ws, E);
    if (wave < 8) { wait_vm<2>(); a1_finish(a2l, pf, ps, a1t + 6, lane, wh, ws, E); }
    a1_publish(a1t, lane, wh, ws, E);
    if (wave < 8) a1_publish(a1t + 6, lane, wh, ws, E);
    bar();
    STAMP(1);
  } else if (role == 0) {
    wait_vm<0>();
    sig(ct + CT_S0 + w * 4, 1u, lane);
    spin_ge4(ct + CT_S0, 1u);
    __builtin_amdgcn_s_setprio(MPRIO);
    if (tn >= 10) k1_gemm<true>(lds0, m0, n0, tid, ws, mk.x, dg);
    else k1_gemm<false>(lds0, m0, n0, tid, ws, mk.x, dg);
    STAMP(3);
  } else if (role == 1) {
    __builtin_amdgcn_s_setprio(LPRIO);
#define K1_ST(t) do { const int kt_ = (t) + rot >= 10 ? (t) + rot - 10 : (t) + rot; \
      if ((t) >= 4) spin_ge4(ct + CT_FREE + ((t) & 3) * 16, (unsigned)((t) - 3)); \
      _Pragma("unroll") for (int i_ = 0; i_ < 4; ++i_) { K1_PIECE_X(t, kt_, w * 4 + i_); K1_PIECE_W(t, kt_, w * 4 + i_); } } while (0)
#pragma nounroll
    for (int t = 1; t < 4; ++t) { K1_ST(t); glds_mark(mk, ct + CT_FX + t * 16 + w * 4, lane); }
    LOAD_H();
#pragma nounroll
    for (int t = 4; t < 10; ++t) { K1_ST(t); glds_mark(mk, ct + CT_FX + t * 16 + w * 4, lane); }
    __builtin_amdgcn_s_setprio(0);
    STAMP(1);
  } else {
    LOAD_H();
    __builtin_amdgcn_s_setprio(2);
    wait_vm<0>();
    a1_finish(a1l, pf, ps, a1t, lane, wh, ws, E);
    a1_publish(a1t, lane, wh, ws, E);
    __builtin_amdgcn_s_setprio(0);
    STAMP(1);
  }

  RELANE();
  unsigned qs = 0;
  const unsigned qf = ct + CT_QUAD + role * 16;
  const unsigned creg = lds0 + (role == 2 ? L_AUX : (role == 1 ? 29184 : 0));
  if (role == 2) __builtin_amdgcn_s_setprio(APRIO); else __builtin_amdgcn_s_setprio(0);
  if (has_gemm && role == 0) {
    q_publish();
    if (cv0) {
      hr.d = ssmd[ch0];
      conv_run(creg, qf, qs, ch0, lane, w, hr, ws, ct + CT_XST, dg);
    }
  } else {
    if (has_gemm && role == 1) {
      conv_load_u(hr, ch0, lane, w, ws, E);
      if (ex) conv_load_u(h2, 528 + bid, lane, w, ws, E);
      spin_ge4(ct + CT_FREE + 16, 10u);
      conv_stage(creg, lane, w, hr);
      if (ex) {
        conv_stage(lds0, lane, w, h2);
        lds_write16(lds0 + (w * 64 + lane) * 16, h2.u);
        asm volatile("s_waitcnt lgkmcnt(0)" ::: "memory");
        sig(ct + CT_XST + w * 4, 1u, lane);
      }
    } else {
      if (has_gemm) { const int wq = w; QSYNC(); }
      conv_stage(creg, lane, w, hr);
      conv_load_u(hr, ch0, lane, w, ws, E);
    }
    STAMP(5);
    conv_run(creg, qf, qs, ch0, lane, w, hr, ws, 0u, dg);
  }
  sig(ct + CT_CVD + wave * 4, 1u, lane);
  if (role != 0) STAMP(2);

  const int tm3 = xcd * 4 + (idx >> 3), tn3 = idx & 7;
  const int m3 = tm3 * 64, n3 = tn3 * 80;
  RELANE();

  if (role == 2) {
    const int r16 = lane & 15, g = lane >> 4, q = r16 >> 2, p = lane & 3;
    __builtin_amdgcn_s_setprio(3);
    spin_ge4(ct + CT_CVD + 32, 1u);
    if (has_gemm) spin_ge4(ct + CT_FREE + 16, 10u);
    attn_stage(lds0, xcd * 4 + (idx & 3), idx >> 2, w, lane, ws, E, ch0, mk, dg);
    {
      P3_WADDR();
      P3_STW(5);
      spin_ge8(ct + CT_CVD, 1u);
      P3_STW(0); P3_STW(4); P3_STW(1); P3_STW(2); P3_STW(3);
      glds_mark(mk, ct + CT_WRDY + w * 4, lane);
      STAMP(3);
    }
    __builtin_amdgcn_s_setprio(0);
    u32x4 qv[3];
#pragma unroll
    for (int j = 0; j < 3; ++j) {
      const int dc = j < 2 ? j * 4 + g : 8 + (g & 1);
      qv[j] = *((const u32x4*)(aq + (size_t)(m3 + w * 16 + r16) * DM + n3 + dc * 8));
    }
    {
      const int l5 = lane & 31;
      const int cl = l5 < 8 ? w * 8 + l5 : (l5 < 16 ? 32 + w * 8 + (l5 - 8) : 64 + w * 4 + (l5 & 3));
      poll<ZSLEEP>(ws, FL_Z + (size_t)(n3 + cl) * 8, E);
    }
    {
      const char* zg = ws + WS_ZT + ((size_t)n3 * SEQ + m3) * 2;
      const unsigned zo = (unsigned)((lane >> 3) * SEQ * 2 + (lane & 7) * 16);
      glds16c(zo, zg + (size_t)(w * 8) * SEQ * 2, lds0 + L_AUX + w * 1024);
      glds16c(zo, zg + (size_t)(32 + w * 8) * SEQ * 2, lds0 + L_AUX + 4096 + w * 1024);
      if (lane < 32) glds16c(zo, zg + (size_t)(64 + w * 4) * SEQ * 2, lds0 + L_AUX + 8192 + w * 512);
    }
    wait_vm<0>();
    sig(ct + CT_ZRDY + w * 4, 1u, lane);
    spin_ge4(ct + CT_ZRDY, 1u);
    STAMP(4);
    u32x4 zz[3];
#pragma unroll
    for (int j = 0; j < 3; ++j) {
      const int dc = j < 2 ? j * 4 + g : 8 + (g & 1);
      const unsigned za = lds0 + L_AUX + (dc * 8 + q) * 128 + w * 32 + p * 8;
      zz[j] = __builtin_bit_cast(u32x4, cat8(tr_read(za), tr_read(za + 512)));
    }
#pragma unroll
    for (int j = 0; j < 3; ++j) {
      const int dc = j < 2 ? j * 4 + g : 8 + (g & 1);
      u32x4 o;
#pragma unroll
      for (int i = 0; i < 4; ++i) o[i] = pk_bf16(bf_lo(qv[j][i]) * bf_lo(zz[j][i]), bf_hi(qv[j][i]) * bf_hi(zz[j][i]));
      if (j < 2 || g < 2) ostore16(out + (size_t)(m3 + w * 16 + r16) * (2 * DM) + n3 + dc * 8, o);
    }
    STAMP(7);
    if (bid == 0 && w == 0) {
#pragma unroll
      for (int i = 0; i < 4; ++i) poll(ws, FL_O + (size_t)(((i * 2 + (lane >> 5)) * 8) * NH + (lane & 31)) * 8, E);
      if (lane == 0) __hip_atomic_store((u64*)(ws + WS_FL), E, __ATOMIC_RELAXED, __HIP_MEMORY_SCOPE_AGENT);
    }
    DIAG_DUMP(2);
    return;
  }

  const int h = xcd * 4 + (idx & 3), qb8 = idx >> 2;
  __builtin_amdgcn_s_setprio(1);
  RELANE();
  if (qb8 > 0) attn_task<true>(lds0, h, qb8, wave, lane, ws, E, cv0 ? ch0 : -1, mk, dg);
  else attn_task<false>(lds0, h, qb8, wave, lane, ws, E, cv0 ? ch0 : -1, mk, dg);
  STAMP(11);

  RELANE();
  const int r16 = lane & 15, g = lane >> 4, q = r16 >> 2, p = lane & 3;
  f32x4 acc[5];
#pragma unroll
  for (int i = 0; i < 5; ++i) acc[i] = (f32x4){0.f, 0.f, 0.f, 0.f};
  const unsigned wbp = lds0 + (20 * (g >> 1) + 4 * (g & 1) + q) * 160 + p * 8;
  const unsigned wbs = lds0 + (20 * g + 16 + q) * 160 + p * 8;
  u32x4 op[8]; u32x2 osg[4];
  if (role == 1) {
    P3_WADDR();
    spin_ge8(ct + CT_ATT, 1u);
    P3_STW(6); P3_STW(7);
    glds_mark(mk, ct + CT_WRD2 + w * 4, lane);
  }
  spin_mk(ct + CT_WRDY, mk.x);
  const unsigned w0 = role ? p3_w(4) : p3_w(0), w1 = role ? p3_w(5) : p3_w(1), w2 = role ? p3_w(6) : p3_w(2), w3 = role ? p3_w(7) : p3_w(3);
  struct Fw { s16x8 a[5], b[5]; s16x4 s[5]; };
  Fw fwa, fwb;
#define P3_LOADW(F, bo_) do { _Pragma("unroll") for (int nt = 0; nt < 5; ++nt) { \
      F.a[nt] = cat8(tr_read(wbp + (bo_) + nt * 32), tr_read(wbp + (bo_) + 8 * 160 + nt * 32)); \
      F.b[nt] = cat8(tr_read(wbp + (bo_) + 40 * 160 + nt * 32), tr_read(wbp + (bo_) + 48 * 160 + nt * 32)); \
      F.s[nt] = tr_read(wbs + (bo_) + nt * 32); } } while (0)
#define P3_MM(F, t) do { \
    _Pragma("unroll") for (int nt = 0; nt < 5; ++nt) acc[nt] = MFMA16(F.a[nt], __builtin_bit_cast(s16x8, op[2 * (t)]), acc[nt]); \
    _Pragma("unroll") for (int nt = 0; nt < 5; ++nt) acc[nt] = MFMA16(F.b[nt], __builtin_bit_cast(s16x8, op[2 * (t) + 1]), acc[nt]); } while (0)
#define P3_MS(t) do { const s16x8 os_ = cat8(__builtin_bit_cast(s16x4, osg[t]), __builtin_bit_cast(s16x4, osg[(t) + 1])); \
    _Pragma("unroll") for (int nt = 0; nt < 5; ++nt) acc[nt] = MFMA16(cat8(fwa.s[nt], fwb.s[nt]), os_, acc[nt]); } while (0)
  P3_LOADW(fwa, w0);
  SBAR();
  poll(ws, FL_O + (size_t)(((m3 >> 5) + (w >> 1)) * NH + role * 16 + r16) * 8, E);
  STAMP(12);
  {
    const int orow = (w & 1) * 16 + r16;
    const char* ob = ws + WS_OW + ((size_t)((m3 >> 5) + (w >> 1)) * NH + role * 16) * 1280;
    const char* pp = ob + (g >> 1) * 1280 + (g & 1) * 512 + orow * 16;
    const char* sp = ob + g * 1280 + 1024 + orow * 8;
#pragma unroll
    for (int t = 0; t < 4; ++t) {
      asm volatile("global_load_dwordx4 %0, %3, off sc1\n\tglobal_load_dwordx4 %1, %3, off offset:2560 sc1\n\tglobal_load_dwordx2 %2, %4, off sc1"
                   : "=&v"(op[2 * t]), "=&v"(op[2 * t + 1]), "=&v"(osg[t]) : "v"(pp + t * 5120), "v"(sp + t * 5120) : "memory");
    }
  }
  P3_LOADW(fwb, w1);
  if (role == 1) spin_mk(ct + CT_WRD2, mk.x);
  SBAR();
  asm volatile("s_waitcnt vmcnt(6)" : "+v"(op[0]), "+v"(op[1]), "+v"(op[2]), "+v"(op[3]), "+v"(osg[0]), "+v"(osg[1]) :: "memory");
  STAMP(13);
  P3_MS(0); P3_MM(fwa, 0);
  SBAR();
  P3_LOADW(fwa, w2); P3_MM(fwb, 1);
#pragma unroll
  for (int i = 0; i < 10; ++i) { __builtin_amdgcn_sched_group_barrier(0x008, 1, 0); __builtin_amdgcn_sched_group_barrier(0x100, 3, 0); }
  SBAR();
  asm volatile("s_waitcnt vmcnt(0)" : "+v"(op[4]), "+v"(op[5]), "+v"(op[6]), "+v"(op[7]), "+v"(osg[2]), "+v"(osg[3]) :: "memory");
  P3_LOADW(fwb, w3); P3_MM(fwa, 2);
#pragma unroll
  for (int i = 0; i < 10; ++i) { __builtin_amdgcn_sched_group_barrier(0x008, 1, 0); __builtin_amdgcn_sched_group_barrier(0x100, 3, 0); }
  SBAR();
  P3_MM(fwb, 3); P3_MS(2);
  SBAR();
  const unsigned rb = lds0 + AT_K + 25600 + w * 5120 + lane * 16;
  if (role == 1) {
#pragma unroll
    for (int nt = 0; nt < 5; ++nt) lds_write16(rb + nt * 1024, __builtin_bit_cast(u32x4, acc[nt]));
    asm volatile("s_waitcnt lgkmcnt(0)" ::: "memory");
    sig(ct + CT_RED + w * 4, 1u, lane);
    STAMP(14);
    DIAG_DUMP(1);
    return;
  }
  spin_ge(ct + CT_RED + w * 4, 1u);
  u16* orow_g = out + (size_t)(m3 + w * 16 + r16) * (2 * DM) + DM + n3;
  u32x2 ov[5];
#pragma unroll
  for (int nt = 0; nt < 5; ++nt) {
    const f32x4 o2 = acc[nt] + __builtin_bit_cast(f32x4, lds_read16(rb + nt * 1024));
    ov[nt][0] = pk_bf16(o2[0], o2[1]);
    ov[nt][1] = pk_bf16(o2[2], o2[3]);
  }
#pragma unroll
  for (int pr = 0; pr < 2; ++pr) {
    const auto sx = __builtin_amdgcn_permlane16_swap(ov[2 * pr][0], ov[2 * pr + 1][0], false, false);
    const auto sy = __builtin_amdgcn_permlane16_swap(ov[2 * pr][1], ov[2 * pr + 1][1], false, false);
    ostore16(orow_g + (2 * pr + (g & 1)) * 16 + 8 * (g >> 1), (u32x4){sx[0], sy[0], sx[1], sy[1]});
  }
  ostore8(orow_g + 64 + 4 * g, ov[4]);
  STAMP(14);
  DIAG_DUMP(0);
}

extern "C" void kernel_launch(void* const* d_in, const int* in_sizes, int n_in, void* d_out, int out_size,
                              void* d_ws, size_t ws_size, hipStream_t stream) {
  const u16* aq = (const u16*)d_in[0];
  const u16* ak = (const u16*)d_in[1];
  const u16* av = (const u16*)d_in[2];
  const u16* shk = (const u16*)d_in[3];
  const u16* ssmk = (const u16*)d_in[4];
  const u16* ssmd = (const u16*)d_in[5];
  const u16* x = (const u16*)d_in[6];
  const u16* wqkv = (const u16*)d_in[7];
  const u16* wo = (const u16*)d_in[8];
  (void)hipFuncSetAttribute((const void*)mega, hipFuncAttributeMaxDynamicSharedMemorySize, L_TOTAL);
  mega<<<256, 768, L_TOTAL, stream>>>(x, wqkv, ak, av, shk, wo, ssmk, ssmd, aq, (char*)d_ws, (u16*)d_out);
}
```

```cpp
#define WSOFF(n) (((n) - (20u << 20)) & ~(size_t)0xfffff)
#include <hip/hip_runtime.h>
#include <hip/hip_bf16.h>
#include <stdint.h>

typedef unsigned short u16;
typedef short s16x4 __attribute__((ext_vector_type(4)));
typedef short s16x8 __attribute__((ext_vector_type(8)));
typedef float f32x2 __attribute__((ext_vector_type(2)));
typedef float f32x4 __attribute__((ext_vector_type(4)));
typedef float f32x16 __attribute__((ext_vector_type(16)));
typedef unsigned u32x2 __attribute__((ext_vector_type(2)));
typedef unsigned u32x4 __attribute__((ext_vector_type(4)));
typedef __bf16 bf16x2 __attribute__((ext_vector_type(2)));
#define LAS __attribute__((address_space(3)))
#define DI __device__ __forceinline__

constexpr int SEQ = 2048, DM = 640, NQKV = 1920, NH = 32;
constexpr size_t WS_QF = 0;
constexpr size_t WS_KF = WS_QF + (size_t)DM * SEQ * 2;
constexpr size_t WS_VF = WS_KF + (size_t)DM * SEQ * 2;
constexpr size_t WS_UT = WS_VF + (size_t)DM * SEQ * 2;
constexpr size_t WS_ZT = WS_UT + (size_t)DM * SEQ * 2;
constexpr size_t WS_OW = WS_ZT + (size_t)DM * SEQ * 2;
typedef unsigned long long u64;
constexpr size_t WS_FL = WS_OW + (size_t)DM * SEQ * 2;
constexpr size_t FL_U = WS_FL + 256;
constexpr int QREP = 32;
constexpr size_t FL_Q = FL_U + 20 * 64 * 8;
constexpr size_t FL_Z = FL_Q + QREP * 16 * 16 * 8;
constexpr size_t FL_O = FL_Z + 640 * 8;

DI unsigned pk_bf16(float lo, float hi) {
  f32x2 f = {lo, hi};
  return __builtin_bit_cast(unsigned, __builtin_convertvector(f, bf16x2));
}
DI float bf_lo(unsigned u) { return __uint_as_float(u << 16); }
DI float bf_hi(unsigned u) { return __uint_as_float(u & 0xffff0000u); }
DI float rbf(float x) { return bf_lo(pk_bf16(x, 0.f)); }

DI unsigned lds_addr(const void* p) { return (unsigned)(uintptr_t)(LAS const char*)p; }

DI void glds16(unsigned voff, const void* sbase, unsigned lds_dst) {
  unsigned keep;
  asm volatile("s_mov_b32 %0, m0\n\ts_mov_b32 m0, %3\n\ts_nop 0\n\tglobal_load_lds_dwordx4 %1, %2\n\ts_mov_b32 m0, %0"
               : "=&s"(keep) : "v"(voff), "s"(sbase), "s"(lds_dst) : "memory");
}
DI void glds16f(unsigned voff, const void* sbase, unsigned lds_dst) {
  asm volatile("s_mov_b32 m0, %2\n\ts_nop 0\n\tglobal_load_lds_dwordx4 %0, %1" :: "v"(voff), "s"(sbase), "s"(lds_dst) : "memory");
}
#define glds16p glds16
DI void glds16nt(unsigned voff, const void* sbase, unsigned lds_dst) {
  unsigned keep;
  asm volatile("s_mov_b32 %0, m0\n\ts_mov_b32 m0, %3\n\ts_nop 0\n\tglobal_load_lds_dwordx4 %1, %2\n\ts_mov_b32 m0, %0"
               : "=&s"(keep) : "v"(voff), "s"(sbase), "s"(lds_dst) : "memory");
}
#define LDPOL "sc1"
DI void glds16c(unsigned voff, const void* sbase, unsigned lds_dst) {
  unsigned keep;
  asm volatile("s_mov_b32 %0, m0\n\ts_mov_b32 m0, %3\n\ts_nop 0\n\tglobal_load_lds_dwordx4 %1, %2 " LDPOL "\n\ts_mov_b32 m0, %0"
               : "=&s"(keep) : "v"(voff), "s"(sbase), "s"(lds_dst) : "memory");
}
struct Mk { const void* src; unsigned x; };
DI void glds_mark(const Mk& mk, unsigned lds_dst, int lane) {
  if (lane == 0) {
    unsigned keep;
    asm volatile("s_mov_b32 %0, m0\n\ts_mov_b32 m0, %3\n\ts_nop 0\n\tglobal_load_lds_dword %1, %2\n\ts_mov_b32 m0, %0"
                 : "=&s"(keep) : "v"(0u), "s"(mk.src), "s"(lds_dst) : "memory");
  }
}
DI float dot2bf(unsigned a, unsigned b, float c) {
  return __builtin_amdgcn_fdot2_f32_bf16(__builtin_bit_cast(bf16x2, a), __builtin_bit_cast(bf16x2, b), c, false);
}
DI void gstore16c(void* p, u32x4 v) {
  asm volatile("global_store_dwordx4 %0, %1, off sc1\n\ts_nop 1" :: "v"(p), "v"(v) : "memory");
}
DI void gstore8c(void* p, u32x2 v) {
  asm volatile("global_store_dwordx2 %0, %1, off sc1" :: "v"(p), "v"(v) : "memory");
}
#define OUTPOL "nt"
DI void ostore16(void* p, u32x4 v) { asm volatile("global_store_dwordx4 %0, %1, off " OUTPOL "\n\ts_nop 1" :: "v"(p), "v"(v) : "memory"); }
DI void ostore8(void* p, u32x2 v) { asm volatile("global_store_dwordx2 %0, %1, off " OUTPOL :: "v"(p), "v"(v) : "memory"); }
template <int N> DI void wait_vm() { asm volatile("s_waitcnt vmcnt(%0)" :: "n"(N) : "memory"); }
DI void bar() { asm volatile("s_waitcnt lgkmcnt(0)\n\ts_barrier" ::: "memory"); }
DI s16x4 tr_read(unsigned a) {
  return __builtin_amdgcn_ds_read_tr16_b64_v4i16((LAS s16x4*)(uintptr_t)a);
}
DI s16x8 lds_read16(unsigned a) { return *(LAS const s16x8*)(uintptr_t)a; }
DI s16x4 lds_read8(unsigned a) { return *(LAS const s16x4*)(uintptr_t)a; }
DI void lds_write16(unsigned a, u32x4 v) { *(LAS u32x4*)(uintptr_t)a = v; }
DI s16x8 cat8(s16x4 a, s16x4 b) { return __builtin_shufflevector(a, b, 0, 1, 2, 3, 4, 5, 6, 7); }
#define MFMA16(a, b, c) __builtin_amdgcn_mfma_f32_16x16x32_bf16((a), (b), (c), 0, 0, 0)
#define MFMA32(a, b, c) __builtin_amdgcn_mfma_f32_32x32x16_bf16((a), (b), (c), 0, 0, 0)

struct Diag {};
#define STAMP(i) do {} while (0)
#define DIAG_INIT() Diag dg
#define DIAG_DUMP(kid) do {} while (0)

#define SBAR() __builtin_amdgcn_sched_barrier(0)
#define SPIN_SLEEP 1
#define SPIN_MAX (1 << 21)
DI unsigned lds_ld32(unsigned a) { return *(volatile LAS unsigned*)(uintptr_t)a; }
DI void lds_st32(unsigned a, unsigned v) { *(volatile LAS unsigned*)(uintptr_t)a = v; }
DI void spin_ge(unsigned a, unsigned want) {
#pragma nounroll
  for (int it = 0; it < SPIN_MAX; ++it) {
    if ((unsigned)__builtin_amdgcn_readfirstlane(lds_ld32(a)) >= want) break;
    __builtin_amdgcn_s_sleep(1);
  }
  asm volatile("" ::: "memory");
}
DI void spin_ge4(unsigned a, unsigned want) {
#pragma nounroll
  for (int it = 0; it < SPIN_MAX; ++it) {
    const u32x4 v = *(volatile LAS u32x4*)(uintptr_t)a;
    const unsigned m = min(min(v[0], v[1]), min(v[2], v[3]));
    if ((unsigned)__builtin_amdgcn_readfirstlane(m) >= want) break;
    __builtin_amdgcn_s_sleep(SPIN_SLEEP);
  }
  asm volatile("" ::: "memory");
}
DI unsigned mk_bad(const u32x4& v, unsigned x) { return (v[0] ^ x) | (v[1] ^ x) | (v[2] ^ x) | (v[3] ^ x); }
DI void spin_mk(unsigned a, unsigned x) {
#pragma nounroll
  for (int it = 0; it < SPIN_MAX; ++it) {
    const u32x4 v = *(volatile LAS u32x4*)(uintptr_t)a;
    if ((unsigned)__builtin_amdgcn_readfirstlane(mk_bad(v, x)) == 0u) break;
    __builtin_amdgcn_s_sleep(SPIN_SLEEP);
  }
  asm volatile("" ::: "memory");
}
DI void spin_ge8(unsigned a, unsigned want) { spin_ge4(a, want); spin_ge4(a + 16, want); }
constexpr int CT_S0 = 0, CT_FREE = 80, CT_PAIR = 144  , CT_QUAD = 192  , CT_ZCNT = 240  ,
              CT_QCNT = 256, CT_ZRDY = 304, CT_CVD = 336  , CT_XST = 416, CT_ATT = 448, CT_RED = 480,
              CT_FX = 512  , CT_KR8 = 688, CT_VR8 = 704, CT_WRDY = 720, CT_WRD2 = 736, CT_BYTES = 1024;
DI void sig(unsigned a, unsigned v, int lane) { if (lane == 0) lds_st32(a, v); }

#define POLL_MAX (1 << 15)
DI void publish(char* ws, size_t off, u64 E, int lane) {
  asm volatile("s_waitcnt vmcnt(0)" ::: "memory");
  if (lane == 0) __hip_atomic_store((u64*)(ws + off), E, __ATOMIC_RELAXED, __HIP_MEMORY_SCOPE_AGENT);
}
#define OSLEEP 6
#define ZSLEEP 20
#define USLEEP 10
#define POLL_SLEEP 6
template <int SL = POLL_SLEEP>
DI void poll(const char* ws, size_t off, u64 E) {
#pragma nounroll
  for (int it = 0; it < POLL_MAX; ++it) {
    const u64 v = __hip_atomic_load((const u64*)(ws + off), __ATOMIC_RELAXED, __HIP_MEMORY_SCOPE_AGENT);
    if (__builtin_amdgcn_ballot_w64(v != E) == 0ull) break;
    __builtin_amdgcn_s_sleep(SL);
  }
  asm volatile("" ::: "memory");
}

constexpr int K1_STAGE = 32768;
constexpr int A1_K = 0, A1_V = 3072, A1_W = 5120, A1_WAVE = 5632, A1_O = 2 * A1_WAVE, A1_PAIR = A1_O + 4096;
constexpr int L_AUX = 4 * K1_STAGE;
constexpr int L_CT = L_AUX + 2 * A1_PAIR;
constexpr int L_TOTAL = L_CT + CT_BYTES;
constexpr int K1_CT = L_CT;
constexpr int CV_COPY = 4160, CV_H = 4608, CV_REG = CV_H + 24576;
constexpr int AT_Q = L_AUX + 12288, AT_K = 64000, AT_V = AT_K + 16 * 2048, AT_END = AT_V + 16 * 1344;
constexpr int p3_w(int t) { return t < 5 ? t * 12800 : (t == 5 ? AT_END : AT_K + (t - 6) * 12800); }

#define PSYNC() do { asm volatile("s_waitcnt lgkmcnt(0)" ::: "memory"); ++ps; sig(pf + wh * 4, ps, lane); spin_ge(pf + (wh ^ 1) * 4, ps); } while (0)
DI void a1_issue(unsigned lds, int tile, int lane, int wh, const u16* ak, const u16* av, const u16* shk) {
  const int ts = tile & 63, td = tile >> 6;
  const int s0 = ts * 32 + wh * 16, d0 = td * 64;
  const int lr = lane >> 3, lc = (lane & 7) * 16;
  const unsigned wl = lds + wh * A1_WAVE;
#pragma unroll
  for (int i = 0; i < 3; ++i) {
    int r = s0 - 3 + i * 8 + lr; r = r < 0 ? 0 : r;
    if (i < 2 || lane < 24) glds16nt((unsigned)(r * DM * 2 + lc), (const char*)ak + d0 * 2, wl + A1_K + i * 1024);
  }
#pragma unroll
  for (int i = 0; i < 2; ++i)
    glds16nt((unsigned)(lr * DM * 2 + lc), (const char*)av + ((size_t)(s0 + i * 8) * DM + d0) * 2, wl + A1_V + i * 1024);
  if (lane < 32) glds16((unsigned)(lane * 16), (const char*)shk + d0 * 8, wl + A1_W);
}
DI void a1_finish(unsigned lds, unsigned pf, unsigned& ps, int tile, int lane, int wh, char* ws, u64 E) {
  const int ts = tile & 63, td = tile >> 6;
  const int c = lane & 7;
  const unsigned wl = lds + wh * A1_WAVE;
  u32x4 wt[4];
#pragma unroll
  for (int i = 0; i < 4; ++i) wt[i] = __builtin_bit_cast(u32x4, lds_read16(wl + A1_W + c * 64 + i * 16));
#pragma unroll
  for (int j = 0; j < 2; ++j) {
    const int r = j * 8 + (lane >> 3);
    const unsigned ka = wl + A1_K + (r + 3) * 128 + c * 16;
    u32x4 k0 = __builtin_bit_cast(u32x4, lds_read16(ka)), k1 = __builtin_bit_cast(u32x4, lds_read16(ka - 128));
    u32x4 k2 = __builtin_bit_cast(u32x4, lds_read16(ka - 256)), k3 = __builtin_bit_cast(u32x4, lds_read16(ka - 384));
    const u32x4 vv = __builtin_bit_cast(u32x4, lds_read16(wl + A1_V + r * 128 + c * 16));
    if (j == 0 && ts == 0 && wh == 0) {
      const u32x4 z = {0u, 0u, 0u, 0u};
      if (r < 1) k1 = z;
      if (r < 2) k2 = z;
      if (r < 3) k3 = z;
    }
    u32x4 o;
#pragma unroll
    for (int i = 0; i < 4; ++i) {
      const unsigned pa0 = __builtin_amdgcn_perm(k1[i], k0[i], 0x05040100u), pb0 = __builtin_amdgcn_perm(k3[i], k2[i], 0x05040100u);
      const unsigned pa1 = __builtin_amdgcn_perm(k1[i], k0[i], 0x07060302u), pb1 = __builtin_amdgcn_perm(k3[i], k2[i], 0x07060302u);
      const float e0 = dot2bf(pb0, wt[i][1], dot2bf(pa0, wt[i][0], 0.f));
      const float e1 = dot2bf(pb1, wt[i][3], dot2bf(pa1, wt[i][2], 0.f));
      o[i] = pk_bf16(e0 * bf_lo(vv[i]), e1 * bf_hi(vv[i]));
    }
    lds_write16(lds + A1_O + (31 - wh * 16 - r) * 128 + c * 16, o);
  }
  PSYNC();
  const int gg = lane >> 4, q = (lane & 15) >> 2, p = lane & 3;
  u16* ut = (u16*)(ws + WS_UT);
#pragma unroll
  for (int j = 0; j < 2; ++j) {
    const unsigned ra = lds + A1_O + (gg * 8 + q) * 128 + wh * 64 + j * 32 + p * 8;
    const s16x4 t0 = tr_read(ra), t1 = tr_read(ra + 512);
    const int d = td * 64 + wh * 32 + j * 16 + (lane & 15);
    gstore16c(ut + (size_t)d * SEQ + (SEQ - 32 - ts * 32) + gg * 8, __builtin_bit_cast(u32x4, cat8(t0, t1)));
  }
}
DI void a1_publish(int tile, int lane, int wh, char* ws, u64 E) {
  const int ts = tile & 63, td = tile >> 6;
  publish(ws, FL_U + (size_t)((td * 2 + wh) * 64 + ts) * 8, E, lane);
}

template <bool VT>
DI void k1_gemm(unsigned lds0, int m0, int n0, int tid, char* ws, unsigned mkx, Diag& dg) {
  const int lane = tid & 63, wave = __builtin_amdgcn_readfirstlane(tid >> 6);
  const int wm = wave >> 1, wn = wave & 1;
  const int l32 = lane & 31, hh = lane >> 5, gg = lane >> 4, q = (lane & 15) >> 2, p = lane & 3;
  const unsigned ct = lds0 + K1_CT;
  f32x16 acc[2][2];
#pragma unroll
  for (int i = 0; i < 2; ++i)
#pragma unroll
    for (int j = 0; j < 2; ++j)
#pragma unroll
      for (int r = 0; r < 16; ++r) acc[i][j][r] = 0.f;
  unsigned xb[4];
#pragma unroll
  for (int ks = 0; ks < 4; ++ks) xb[ks] = lds0 + (wm * 64 + l32) * 128 + (((unsigned)(2 * ks + hh) ^ (unsigned)((l32 >> 1) & 7)) * 16);
  unsigned wb[2];
#pragma unroll
  for (int nt = 0; nt < 2; ++nt)
    wb[nt] = lds0 + 16384 + (8 * hh + q) * 256 + (((unsigned)(wn * 4 + 2 * nt + (gg & 1)) ^ (unsigned)(q << 1)) * 32) + p * 8;
  s16x8 x0[2], w0[2], x1[2], w1[2], x2[2], w2[2], x3[2], w3[2];
#define K1_LOADF(XF, WF, bo_, ks) do { \
    _Pragma("unroll") for (int i_ = 0; i_ < 2; ++i_) { \
      WF[i_] = cat8(tr_read(wb[i_] + (bo_) + (ks) * 4096), tr_read(wb[i_] + (bo_) + (ks) * 4096 + 1024)); \
      XF[i_] = lds_read16(xb[ks] + (bo_) + i_ * 4096); } } while (0)
#define K1_MM(XF, WF) do { _Pragma("unroll") for (int mt = 0; mt < 2; ++mt) _Pragma("unroll") for (int nt = 0; nt < 2; ++nt) \
      acc[mt][nt] = VT ? MFMA32(XF[mt], WF[nt], acc[mt][nt]) : MFMA32(WF[nt], XF[mt], acc[mt][nt]); } while (0)
#define ILV_A 2
#define ILV_B 2
#define ILV_C 2
#define K1_ILV() do { \
      __builtin_amdgcn_sched_group_barrier(0x008, 1, 0); __builtin_amdgcn_sched_group_barrier(0x100, ILV_A, 0); \
      __builtin_amdgcn_sched_group_barrier(0x008, 1, 0); __builtin_amdgcn_sched_group_barrier(0x100, ILV_B, 0); \
      __builtin_amdgcn_sched_group_barrier(0x008, 1, 0); __builtin_amdgcn_sched_group_barrier(0x100, ILV_C, 0); \
      __builtin_amdgcn_sched_group_barrier(0x008, 1, 0); __builtin_amdgcn_sched_group_barrier(0x100, 2, 0); } while (0)
  STAMP(1);
  K1_LOADF(x0, w0, 0, 0);
  K1_LOADF(x1, w1, 0, 1);
#pragma nounroll
  for (int t = 0; t < 10; ++t) {
    const unsigned bo = (t & 3) * K1_STAGE, bn = ((t + 1) & 3) * K1_STAGE;
    SBAR();
    const u32x4 fl4 = *(volatile LAS u32x4*)(uintptr_t)(ct + CT_FX + (t + 1) * 16);
    K1_LOADF(x2, w2, bo, 2); K1_MM(x0, w0); K1_ILV();
    SBAR();
    K1_LOADF(x3, w3, bo, 3); K1_MM(x1, w1); K1_ILV();
    SBAR();
    const unsigned flu = (unsigned)__builtin_amdgcn_readfirstlane(mk_bad(fl4, mkx));
    if (t < 9 && flu != 0u) spin_mk(ct + CT_FX + (t + 1) * 16, mkx);
    asm volatile("" ::: "memory");
    SBAR();
    K1_LOADF(x0, w0, bn, 0); K1_MM(x2, w2); K1_ILV();
    SBAR();
    asm volatile("" : "+v"(x3[0]), "+v"(x3[1]), "+v"(w3[0]), "+v"(w3[1]) :: "memory");
    if (lane == 0) lds_st32(ct + CT_FREE + (t & 3) * 16 + wave * 4, (unsigned)(t + 1));
    SBAR();
    K1_LOADF(x1, w1, bn, 1); K1_MM(x3, w3); K1_ILV();
  }
  SBAR();
  STAMP(2);
  if (!VT) {
    const int part = n0 >= DM ? 1 : 0;
    const float sc = part == 0 ? 0.22360679774997896f * 1.4426950408889634f : 1.0f;
    char* base = ws + (part ? WS_KF : WS_QF) + ((size_t)((m0 + wm * 64) >> 5) * 40 + ((n0 - part * DM + wn * 64) >> 4)) * 1024 + lane * 16;
#pragma unroll
    for (int mt = 0; mt < 2; ++mt)
#pragma unroll
      for (int nt = 0; nt < 2; ++nt)
#pragma unroll
        for (int uh = 0; uh < 2; ++uh) {
          u32x4 o;
#pragma unroll
          for (int i = 0; i < 4; ++i) o[i] = pk_bf16(acc[mt][nt][8 * uh + 2 * i] * sc, acc[mt][nt][8 * uh + 2 * i + 1] * sc);
          gstore16c(base + (mt * 40 + nt * 2 + uh) * 1024, o);
        }
  } else {
    char* base = ws + WS_VF + ((size_t)((m0 + wm * 64) >> 4) * 20 + ((n0 - 2 * DM + wn * 64) >> 5)) * 1024 + lane * 16;
#pragma unroll
    for (int mt = 0; mt < 2; ++mt)
#pragma unroll
      for (int nt = 0; nt < 2; ++nt)
#pragma unroll
        for (int kg = 0; kg < 2; ++kg) {
          u32x4 o;
#pragma unroll
          for (int i = 0; i < 4; ++i) o[i] = pk_bf16(acc[mt][nt][8 * kg + 2 * i], acc[mt][nt][8 * kg + 2 * i + 1]);
          gstore16c(base + ((mt * 2 + kg) * 20 + nt) * 1024, o);
        }
  }
}

DI void cv_put(unsigned y, const u32x4& c, const u32x2& n) {
  const unsigned d0 = c[0], d1 = c[1], d2 = c[2], d3 = c[3], d4 = n[0], d5 = n[1];
  const unsigned a0 = __builtin_amdgcn_alignbit(d1, d0, 16), a1 = __builtin_amdgcn_alignbit(d2, d1, 16),
                 a2 = __builtin_amdgcn_alignbit(d3, d2, 16), a3 = __builtin_amdgcn_alignbit(d4, d3, 16),
                 a4 = __builtin_amdgcn_alignbit(d5, d4, 16);
  lds_write16(y, (u32x4){d0, d1, d2, d3});
  lds_write16(y + CV_COPY, (u32x4){a0, a1, a2, a3});
  lds_write16(y + 2 * CV_COPY, (u32x4){d1, d2, d3, d4});
  lds_write16(y + 3 * CV_COPY, (u32x4){a1, a2, a3, a4});
}

#define QSYNC() do { asm volatile("s_waitcnt lgkmcnt(0)" ::: "memory"); ++qs; sig(qf + wq * 4, qs, lane); spin_ge4(qf, qs); } while (0)
struct HRegs { u32x4 c, u; u32x2 n; unsigned d; };
DI HRegs conv_load_h(int ch, int t256, const u16* __restrict__ ssmk, const u16* __restrict__ ssmd) {
  HRegs r;
  const u16* hrow = ssmk + (size_t)ch * SEQ;
  r.c = *((const u32x4*)(hrow + t256 * 8));
  r.n = *(const u32x2*)(hrow + (t256 < 255 ? t256 * 8 + 8 : 0));
  r.d = ssmd[ch];
  return r;
}
DI void conv_load_u(HRegs& r, int ch, int lane, int wq, const char* __restrict__ ws, u64 E) {
  poll<USLEEP>(ws, FL_U + (size_t)((ch >> 5) * 64 + 48 - 16 * wq + (lane & 15)) * 8, E);
  const u64* up = (const u64*)(ws + WS_UT + (size_t)ch * SEQ * 2 + wq * 1024 + lane * 16);
  const u64 a = __hip_atomic_load(up, __ATOMIC_RELAXED, __HIP_MEMORY_SCOPE_AGENT);
  const u64 b = __hip_atomic_load(up + 1, __ATOMIC_RELAXED, __HIP_MEMORY_SCOPE_AGENT);
  r.u = (u32x4){(unsigned)a, (unsigned)(a >> 32), (unsigned)b, (unsigned)(b >> 32)};
}
DI void conv_stage(unsigned reg, int lane, int wq, const HRegs& hr) {
  const int t256 = wq * 64 + lane;
  const unsigned hb = reg + CV_H;
  if (t256 < 32) lds_write16(reg + 4096 + t256 * 16, (u32x4){0u, 0u, 0u, 0u});
  cv_put(hb + (t256 + 2) * 16, hr.c, hr.n);
  if (t256 < 2) {
    const u32x4 z = {0u, 0u, 0u, 0u};
    u32x2 nx = {0u, 0u};
    const unsigned h0 = __builtin_amdgcn_readfirstlane(hr.c[0]), h1 = __builtin_amdgcn_readfirstlane(hr.c[1]);
    if (t256 == 1) nx = (u32x2){h0, h1};
    cv_put(hb + t256 * 16, z, nx);
  }
}
DI void conv_run(unsigned reg, unsigned qf, unsigned& qs, int ch, int lane, int wq, const HRegs& hr,
                 char* __restrict__ ws, unsigned xst, Diag& dg) {
  const unsigned hb = reg + CV_H;
  if (xst) spin_ge4(xst, 1u);
  else { lds_write16(reg + (wq * 64 + lane) * 16, hr.u); QSYNC(); }
  STAMP(6);
  const int r16 = lane & 15, g = lane >> 4, q = r16 >> 2, p = lane & 3;
  const unsigned ab0 = hb + ((q + 1) & 3) * CV_COPY + 2 * (8 * g + 4 * p + (q == 3 ? 4 : 0)) + wq * 128;
  const unsigned bb0 = reg + 2 * (2032 - 16 * r16 + 8 * g) + wq * 128 - 7 * 512;
  f32x4 acc[8];
#pragma unroll
  for (int i = 0; i < 8; ++i) acc[i] = (f32x4){0.f, 0.f, 0.f, 0.f};
#define CV_LDA(a_, ad_) cat8(tr_read((ad_) + (a_) * 512), tr_read((ad_) + (a_) * 512 + 8))
#define CV_LDB(c_, bd_) lds_read16((bd_) + (7 - (c_)) * 512)
  {
    s16x8 A[8], B[8];
    B[0] = CV_LDB(0, bb0);
#pragma unroll
    for (int a = 0; a < 8; ++a) A[a] = CV_LDA(a, ab0);
#pragma unroll
    for (int c = 1; c < 8; ++c) B[c] = CV_LDB(c, bb0);
#pragma unroll
    for (int c = 0; c < 8; ++c) {
      SBAR();
#pragma unroll
      for (int a = 0; a + c < 8; ++a) acc[a + c] = MFMA16(A[a], B[c], acc[a + c]);
      B[c] = CV_LDB(c, bb0 + 64);
      A[7 - c] = CV_LDA(7 - c, ab0 + 64);
    }
    SBAR();
#pragma unroll
    for (int a = 7; a >= 0; --a)
#pragma unroll
      for (int c = 0; a + c < 8; ++c) acc[a + c] = MFMA16(A[a], B[c], acc[a + c]);
  }
  QSYNC();
#pragma unroll
  for (int nt = 0; nt < 8; ++nt) {
    const int dw = nt >> 1;
    if (dw != wq) lds_write16(hb + (nt * 3 + (wq > dw ? wq - 1 : wq)) * 1024 + lane * 16, __builtin_bit_cast(u32x4, acc[nt]));
  }
  f32x4 k0 = wq == 0 ? acc[0] : (wq == 1 ? acc[2] : (wq == 2 ? acc[4] : acc[6]));
  f32x4 k1 = wq == 0 ? acc[1] : (wq == 1 ? acc[3] : (wq == 2 ? acc[5] : acc[7]));
  QSYNC();
  const float dco = bf_lo(hr.d);
  u32x2 zz[2];
#pragma unroll
  for (int i = 0; i < 2; ++i) {
    f32x4 o = i ? k1 : k0;
#pragma unroll
    for (int j = 0; j < 3; ++j) o += __builtin_bit_cast(f32x4, lds_read16(hb + ((wq * 2 + i) * 3 + j) * 1024 + lane * 16));
    const int s = (wq * 2 + i) * 256 + 16 * r16 + 4 * g;
    const u32x2 uu = __builtin_bit_cast(u32x2, lds_read8(reg + 2 * (SEQ - 4 - s)));
    const float kv0 = bf_hi(uu[1]), kv1 = bf_lo(uu[1]), kv2 = bf_hi(uu[0]), kv3 = bf_lo(uu[0]);
    const unsigned y01 = pk_bf16(o[0], o[1]), y23 = pk_bf16(o[2], o[3]);
    const unsigned e01 = pk_bf16(kv0 * dco, kv1 * dco), e23 = pk_bf16(kv2 * dco, kv3 * dco);
    zz[i][0] = pk_bf16(bf_lo(y01) + bf_lo(e01), bf_hi(y01) + bf_hi(e01));
    zz[i][1] = pk_bf16(bf_lo(y23) + bf_lo(e23), bf_hi(y23) + bf_hi(e23));
  }
  const auto sx = __builtin_amdgcn_permlane16_swap(zz[0][0], zz[1][0], false, false);
  const auto sy = __builtin_amdgcn_permlane16_swap(zz[0][1], zz[1][1], false, false);
  u16* zrow = (u16*)(ws + WS_ZT) + (size_t)ch * SEQ;
  gstore16c(zrow + (wq * 2 + (g & 1)) * 256 + 16 * r16 + 8 * (g >> 1), (u32x4){sx[0], sy[0], sx[1], sy[1]});
  asm volatile("s_waitcnt lgkmcnt(0)" ::: "memory");
}
DI void conv_publish(unsigned zc, int ch, int lane, char* __restrict__ ws, u64 E) {
  asm volatile("s_waitcnt vmcnt(0)" ::: "memory");
  if (lane == 0) {
    const unsigned old = __hip_atomic_fetch_add((LAS unsigned*)(uintptr_t)zc, 1u, __ATOMIC_RELAXED, __HIP_MEMORY_SCOPE_WORKGROUP);
    if ((old & 3u) == 3u) __hip_atomic_store((u64*)(ws + FL_Z + (size_t)ch * 8), E, __ATOMIC_RELAXED, __HIP_MEMORY_SCOPE_AGENT);
  }
}

DI s16x8 pack8(const f32x16& v, int o) {
  u32x4 r;
  r[0] = pk_bf16(v[o + 0], v[o + 1]); r[1] = pk_bf16(v[o + 2], v[o + 3]);
  r[2] = pk_bf16(v[o + 4], v[o + 5]); r[3] = pk_bf16(v[o + 6], v[o + 7]);
  return __builtin_bit_cast(s16x8, r);
}
DI float max3f(float a, float b, float c) { return __builtin_fmaxf(__builtin_fmaxf(a, b), c); }
DI float max16(const f32x16& v, float m) {
  float a = max3f(v[0], v[1], m), b = max3f(v[2], v[3], v[4]);
  a = max3f(a, v[5], v[6]); b = max3f(b, v[7], v[8]);
  a = max3f(a, v[9], v[10]); b = max3f(b, v[11], v[12]);
  a = max3f(a, v[13], v[14]);
  return max3f(a, b, v[15]);
}
DI float xhalf_max(float m) {
  auto sw = __builtin_amdgcn_permlane32_swap(__float_as_uint(m), __float_as_uint(m), false, false);
  return __builtin_fmaxf(__uint_as_float(sw[0]), __uint_as_float(sw[1]));
}

#define AT_LMAX 65536.0f
#define P3_WADDR() unsigned wv[4]; _Pragma("unroll") for (int i = 0; i < 4; ++i) { \
      const int c = i < 3 ? (i * 4 + w) * 64 + lane : 768 + w * 8 + (lane & 7); const int row = c / 10; \
      wv[i] = (unsigned)(row * DM * 2 + (c - row * 10) * 16); } const char* wog = (const char*)(wo + n3)
#define P3_STW(t) do { _Pragma("unroll") for (int i_ = 0; i_ < 3; ++i_) \
        glds16(wv[i_], wog + (size_t)(t) * 80 * DM * 2, lds0 + p3_w(t) + (i_ * 4 + w) * 1024); \
      if (lane < 8) glds16(wv[3], wog + (size_t)(t) * 80 * DM * 2, lds0 + p3_w(t) + 12288 + w * 128); } while (0)
DI void attn_stage(unsigned lds0, int h, int qb8, int j, int lane, char* __restrict__ ws, u64 E, int zch, const Mk& mk, Diag& dg) {
  const int qt = qb8 * 8 + j;
  const unsigned l16 = (unsigned)(lane * 16), ct = lds0 + L_CT;
  const int qa = (5 * h) >> 2;
  const bool stg = qb8 > 0 || j >= 2;
  {
    const int l3 = lane & 7, hi = lane & 1;
    const bool kv = stg && l3 >= 2 && l3 < 6;
    const int rb = kv ? 2 * qb8 - 2 + j : 2 * qb8 + (l3 >= 6 ? 1 : 0);
    const int col = !kv ? qa * 16 + hi * 31 : (l3 < 4 ? DM + qa * 16 + hi * 31 : 2 * DM + h * 20 + hi * 19);
    poll(ws, FL_Q + (size_t)((h * 16 + rb) * 16 + (col >> 7)) * 8, E);
  }
  STAMP(9);
  conv_publish(ct + CT_ZCNT + 8, zch, lane, ws, E);
  const int kt0 = qb8 * 8 - 8 + 4 * j;
#pragma unroll
  for (int i = 0; i < 4; ++i)
    glds16c(l16, ws + WS_QF + ((size_t)(qt + (i >> 1) * 4) * 40 + qa + (i & 1)) * 1024, lds0 + AT_Q + ((j + (i >> 1) * 4) * 2 + (i & 1)) * 1024);
  if (stg) {
#pragma unroll
    for (int i = 0; i < 8; ++i)
      glds16c(l16, ws + WS_KF + ((size_t)(kt0 + (i >> 1)) * 40 + qa + (i & 1)) * 1024, lds0 + AT_K + (j * 8 + i) * 1024);
  }
  glds_mark(mk, ct + CT_KR8 + j * 4, lane);
  if (stg) {
#pragma unroll
    for (int i = 0; i < 6; ++i) {
      const int c = i * 64 + lane;
      const int kgp = (c * 1561) >> 16, rem = c - kgp * 42, vh = rem >= 21 ? 1 : 0, d = rem - vh * 21;
      const int n = h * 20 + d;
      if (c < 336 && d == 20) lds_write16(lds0 + AT_V + j * 5376 + c * 16, (u32x4){0x3f803f80u, 0x3f803f80u, 0x3f803f80u, 0x3f803f80u});
      if (c < 336 && d < 20)
        glds16c((unsigned)((kgp * 20 + (n >> 5)) * 1024 + (vh * 32 + (n & 31)) * 16), ws + WS_VF + ((size_t)(kt0 * 2) * 20) * 1024,
                lds0 + AT_V + j * 5376 + i * 1024);
    }
    asm volatile("s_waitcnt lgkmcnt(0)" ::: "memory");
  }
  glds_mark(mk, ct + CT_VR8 + j * 4, lane);
}
template <bool FULL>
DI void attn_task(unsigned lds0, int h, int qb8, int j, int lane, char* __restrict__ ws, u64 E, int zch, const Mk& mk, Diag& dg) {
  const int l32 = lane & 31, hh = lane >> 5;
  const int qt = qb8 * 8 + j;
  const int tfirst = FULL ? 0 : 8 - qt;
  const unsigned l16 = (unsigned)(lane * 16), ct = lds0 + L_CT;
  const int qa = (5 * h) >> 2;
  u32x4 q0r, q1r;
  spin_mk(ct + CT_KR8, mk.x);
  if (!FULL) spin_mk(ct + CT_VR8, mk.x);
  q0r = __builtin_bit_cast(u32x4, lds_read16(lds0 + AT_Q + j * 2048 + l16));
  q1r = __builtin_bit_cast(u32x4, lds_read16(lds0 + AT_Q + j * 2048 + 1024 + l16));
  asm volatile("s_waitcnt lgkmcnt(0)" : "+v"(q0r), "+v"(q1r) :: "memory");
  if (zch >= 0) conv_publish(ct + CT_ZCNT + (j >> 2) * 4, zch, lane, ws, E);
  {
    const int g0 = 4 * qa + hh - 5 * h;
    const bool k0 = g0 >= 0, k1 = g0 + 2 >= 0 && g0 + 2 < 5, k2 = g0 + 4 < 5, k3 = g0 + 6 < 5;
    q0r[0] = k0 ? q0r[0] : 0u; q0r[1] = k0 ? q0r[1] : 0u; q0r[2] = k1 ? q0r[2] : 0u; q0r[3] = k1 ? q0r[3] : 0u;
    q1r[0] = k2 ? q1r[0] : 0u; q1r[1] = k2 ? q1r[1] : 0u; q1r[2] = k3 ? q1r[2] : 0u; q1r[3] = k3 ? q1r[3] : 0u;
  }
  s16x8 qlo = __builtin_bit_cast(s16x8, q0r), qhi8 = __builtin_bit_cast(s16x8, q1r);
  STAMP(8);
  const f32x16 zero16 = {0.f, 0.f, 0.f, 0.f, 0.f, 0.f, 0.f, 0.f, 0.f, 0.f, 0.f, 0.f, 0.f, 0.f, 0.f, 0.f};
  const float ninf = -INFINITY;
  const f32x16 ninf16 = {ninf, ninf, ninf, ninf, ninf, ninf, ninf, ninf, ninf, ninf, ninf, ninf, ninf, ninf, ninf, ninf};
  const unsigned kb = lds0 + AT_K + j * 2048 + l16;
#define AT_QK(dst, t, cin) do { if (FULL || (t) >= tfirst) { dst = MFMA32(lds_read16(kb + (t) * 2048), qlo, cin); \
    dst = MFMA32(lds_read16(kb + (t) * 2048 + 1024), qhi8, dst); } else dst = ninf16; } while (0)
  const int vrow = l32 <= 20 ? l32 : (l32 == 24 ? 20 : l32 - 16);
  const unsigned vb = lds0 + AT_V + j * 1344 + hh * 336 + vrow * 16;
#define AT_EXP(sv) do { _Pragma("unroll") for (int r = 0; r < 16; ++r) sv[r] = __builtin_amdgcn_exp2f(sv[r] - m); } while (0)
#define AT_PV(sv, t) do { oacc = MFMA32(lds_read16(vb + (t) * 1344), pack8(sv, 0), oacc); oacc = MFMA32(lds_read16(vb + (t) * 1344 + 672), pack8(sv, 8), oacc); } while (0)
  f32x16 oacc;
  bool exact = !FULL;
  if (FULL) {
    f32x16 sm, sa, sb;
    AT_QK(sm, 8, zero16); AT_QK(sa, 0, zero16);
    const int tq = l32 - 4 * hh;
    const unsigned wm = tq < 0 ? 0xffffffffu : (0xfffffffeu << tq);
    unsigned msk[16];
#pragma unroll
    for (int r = 0; r < 16; ++r) {
      msk[r] = (unsigned)__builtin_amdgcn_sbfe((int)wm, (r & 3) + 8 * (r >> 2), 1);
      sm[r] = __uint_as_float((msk[r] & __float_as_uint(sa[r])) | (~msk[r] & __float_as_uint(sm[r])));
    }
    const float m = xhalf_max(max16(sm, ninf));
    const float nm = -m;
    const f32x16 cneg = {nm, nm, nm, nm, nm, nm, nm, nm, nm, nm, nm, nm, nm, nm, nm, nm};
    AT_QK(sa, 1, cneg);
    AT_EXP(sm);
    u32x4 pl = __builtin_bit_cast(u32x4, pack8(sm, 0)), ph = __builtin_bit_cast(u32x4, pack8(sm, 8)), ql, qh;
#pragma unroll
    for (int i = 0; i < 4; ++i) {
      ql[i] = pl[i] & ((msk[2 * i] & 0xffffu) | (msk[2 * i + 1] & 0xffff0000u)); pl[i] ^= ql[i];
      qh[i] = ph[i] & ((msk[8 + 2 * i] & 0xffffu) | (msk[9 + 2 * i] & 0xffff0000u)); ph[i] ^= qh[i];
    }
    spin_mk(ct + CT_VR8, mk.x);
    STAMP(9);
    oacc = MFMA32(lds_read16(vb + 8 * 1344), __builtin_bit_cast(s16x8, pl), zero16);
    oacc = MFMA32(lds_read16(vb + 8 * 1344 + 672), __builtin_bit_cast(s16x8, ph), oacc);
    oacc = MFMA32(lds_read16(vb), __builtin_bit_cast(s16x8, ql), oacc);
    oacc = MFMA32(lds_read16(vb + 672), __builtin_bit_cast(s16x8, qh), oacc);
#define AT_EX0(sv) do { _Pragma("unroll") for (int r = 0; r < 16; ++r) sv[r] = __builtin_amdgcn_exp2f(sv[r]); } while (0)
    AT_QK(sb, 2, cneg); AT_EX0(sa); AT_PV(sa, 1);
    AT_QK(sa, 3, cneg); AT_EX0(sb); AT_PV(sb, 2);
    AT_QK(sb, 4, cneg); AT_EX0(sa); AT_PV(sa, 3);
    AT_QK(sa, 5, cneg); AT_EX0(sb); AT_PV(sb, 4);
    AT_QK(sb, 6, cneg); AT_EX0(sa); AT_PV(sa, 5);
    AT_QK(sa, 7, cneg); AT_EX0(sb); AT_PV(sb, 6);
    AT_EX0(sa); AT_PV(sa, 7);
    float big = max3f(__builtin_fabsf(oacc[0]), __builtin_fabsf(oacc[1]), __builtin_fabsf(oacc[2]));
    big = max3f(big, __builtin_fabsf(oacc[3]), __builtin_fabsf(oacc[4]));
    big = max3f(big, __builtin_fabsf(oacc[5]), __builtin_fabsf(oacc[6]));
    big = max3f(big, __builtin_fabsf(oacc[7]), __builtin_fabsf(oacc[8]));
    big = max3f(big, __builtin_fabsf(oacc[9]), __builtin_fabsf(oacc[10]));
    big = __builtin_fmaxf(big, __builtin_fabsf(oacc[11]));
    const float ls = hh ? oacc[8] : oacc[12];
    float osum = 0.f;
#pragma unroll
    for (int r = 0; r < 8; ++r) osum += oacc[r];
    const bool ok = (ls < AT_LMAX) && (big < 1e37f) && (osum == osum);
    exact = __builtin_amdgcn_ballot_w64(!ok) != 0ull;
  }
  if (exact) {
    asm volatile("" : "+v"(qlo), "+v"(qhi8) :: "memory");
    f32x16 sm, s1, s2, s3;
    {
      f32x16 s0;
      AT_QK(sm, 8, zero16); AT_QK(s0, 0, zero16);
      AT_QK(s1, 1, zero16); AT_QK(s2, 2, zero16); AT_QK(s3, 3, zero16);
#pragma unroll
      for (int r = 0; r < 16; ++r) {
        const int kk = (r & 3) + 8 * (r >> 2) + 4 * hh;
        sm[r] = kk > l32 ? s0[r] : sm[r];
      }
    }
    float m = max16(sm, ninf);
    m = max16(s1, m); m = max16(s2, m); m = max16(s3, m);
    m = xhalf_max(m);
    AT_EXP(sm);
    oacc = zero16;
    {
      f32x16 p0, p8;
#pragma unroll
      for (int r = 0; r < 16; ++r) {
        const int kk = (r & 3) + 8 * (r >> 2) + 4 * hh;
        p0[r] = kk > l32 ? sm[r] : 0.f;
        p8[r] = kk > l32 ? 0.f : sm[r];
      }
      AT_PV(p8, 8);
      if (FULL) AT_PV(p0, 0);
    }
    if (FULL || 1 >= tfirst) { AT_EXP(s1); AT_PV(s1, 1); }
    AT_QK(sm, 4, zero16); AT_QK(s1, 5, zero16);
    if (FULL || 2 >= tfirst) { AT_EXP(s2); AT_PV(s2, 2); }
    AT_QK(s2, 6, zero16);
    if (FULL || 3 >= tfirst) { AT_EXP(s3); AT_PV(s3, 3); }
    AT_QK(s3, 7, zero16);
    float m2 = max16(sm, m);
    m2 = max16(s1, m2); m2 = max16(s2, m2); m2 = max16(s3, m2);
    m2 = xhalf_max(m2);
    {
      const float alpha = __builtin_amdgcn_exp2f(m - m2);
#pragma unroll
      for (int r = 0; r < 13; ++r) oacc[r] *= alpha;
      m = m2;
    }
    if (FULL || 4 >= tfirst) { AT_EXP(sm); AT_PV(sm, 4); }
    if (FULL || 5 >= tfirst) { AT_EXP(s1); AT_PV(s1, 5); }
    if (FULL || 6 >= tfirst) { AT_EXP(s2); AT_PV(s2, 6); }
    if (FULL || 7 >= tfirst) { AT_EXP(s3); AT_PV(s3, 7); }
  }
  asm volatile("s_waitcnt lgkmcnt(0)" ::: "memory");
  sig(ct + CT_ATT + j * 4, 1u, lane);
  STAMP(10);
  const float l = hh ? oacc[8] : oacc[12];
  const float inv = __builtin_amdgcn_rcpf(l);
  char* oc = ws + WS_OW + ((size_t)qt * NH + h) * 1280;
  u32x4 o;
  o[0] = pk_bf16(oacc[0] * inv, oacc[1] * inv); o[1] = pk_bf16(oacc[2] * inv, oacc[3] * inv);
  o[2] = pk_bf16(oacc[4] * inv, oacc[5] * inv); o[3] = pk_bf16(oacc[6] * inv, oacc[7] * inv);
  gstore16c(oc + lane * 16, o);
  if (!hh) {
    u32x2 o2;
    o2[0] = pk_bf16(oacc[8] * inv, oacc[9] * inv); o2[1] = pk_bf16(oacc[10] * inv, oacc[11] * inv);
    gstore8c(oc + 1024 + l32 * 8, o2);
  }
  publish(ws, FL_O + (size_t)(qt * NH + h) * 8, E, lane);
}

#define HDELAY 0
#define APRIO 1
#define LPRIO 0
#define MPRIO 2
#define ROTG 2
#define A1_DELAY 8
__global__ __launch_bounds__(768, 3) void mega(const u16* __restrict__ x, const u16* __restrict__ wqkv,
                                               const u16* __restrict__ ak, const u16* __restrict__ av,
                                               const u16* __restrict__ shk, const u16* __restrict__ wo,
                                               const u16* __restrict__ ssmk, const u16* __restrict__ ssmd,
                                               const u16* __restrict__ aq, char* __restrict__ ws,
                                               u16* __restrict__ out) {
  extern __shared__ __attribute__((aligned(16))) char smem[];
  const unsigned lds0 = lds_addr(smem);
  const int tid = threadIdx.x, wave = __builtin_amdgcn_readfirstlane(tid >> 6);
  int lane = tid & 63;
#define RELANE() asm volatile("" : "+v"(lane))
  const int role = wave >> 2, w = wave & 3;
  const int bid = blockIdx.x, xcd = bid & 7, idx = bid >> 3;
  const bool has_gemm = idx < 30;
  const unsigned ct = lds0 + L_CT;
  DIAG_INIT();
  u64 E;
  Mk mk; mk.src = wqkv;
  asm volatile("s_load_dwordx2 %0, %2, 0x0\n\ts_load_dword %1, %3, 0x0" : "=&s"(E), "=&s"(mk.x) : "s"(ws + WS_FL), "s"(wqkv) : "memory");
  const int gt = (xcd & 1) * 30 + idx;
  const int tm = (xcd >> 1) * 4 + (gt & 3), tn = gt >> 2;
  const int m0 = tm * 128, n0 = tn * 128;
  const int rot = ROTG >= 10 ? (((gt & 3) * 5 + 1) / 2 + 3 * tn) % 10 : ((((gt & 3) + tn) % ROTG) * 10) / ROTG;
  const unsigned xo0 = (unsigned)((lane >> 3) * DM * 2 + (((lane & 7) ^ (lane >> 4)) * 16));
  const unsigned xo1 = (unsigned)((lane >> 3) * DM * 2 + (((lane & 7) ^ (4 + (lane >> 4))) * 16));
  const unsigned wof = (unsigned)((lane >> 4) * NQKV * 2 + (((lane & 15) ^ ((lane >> 4) << 2)) * 16));
  const char* xg = (const char*)(x + (size_t)m0 * DM);
  const char* wg = (const char*)(wqkv + n0);
#define K1_PIECE_X(t, kt, j) glds16p(((j) & 1) ? xo1 : xo0, xg + (size_t)(j) * 8 * DM * 2 + (kt) * 128, lds0 + ((t) & 3) * K1_STAGE + (j) * 1024)
#define K1_PIECE_W(t, kt, j) glds16p(wof, wg + (size_t)((kt) * 64 + (j) * 4) * NQKV * 2, lds0 + ((t) & 3) * K1_STAGE + 16384 + (j) * 1024)
  if (has_gemm && role == 0) {
#pragma unroll
    for (int i = 0; i < 4; ++i) { K1_PIECE_X(0, rot, w * 4 + i); K1_PIECE_W(0, rot, w * 4 + i); }
  }
  const unsigned pf = ct + CT_PAIR + (wave >> 1) * 8;
  const int wh = wave & 1;
  unsigned ps = 0;
  const int fb = (idx - 30) * 8 + xcd;
  const int a1t = has_gemm ? (xcd * 30 + idx) * 2 + (w >> 1) : 480 + fb * 10 + (wave >> 1);
  const unsigned a1l = has_gemm ? lds0 + L_AUX + (w >> 1) * A1_PAIR : lds0 + (wave >> 1) * A1_PAIR;
  if (!has_gemm || role == 2) a1_issue(a1l, a1t, lane, wh, ak, av, shk);
  const bool cv0 = role != 0 || !has_gemm || bid < 112;
  const int ch0 = role == 0 ? (has_gemm ? 528 + bid : 512 + fb) : bid + (role == 1 ? 256 : 0);
  asm volatile("s_waitcnt lgkmcnt(0)" : "+s"(E), "+s"(mk.x) :: "memory");
  if (role == 1) lds_st32(ct + (tid - 256) * 4, (tid - 256) * 4 >= CT_FX ? ~mk.x : 0u);
  bar();
  E += 1ull;

  const bool ex = has_gemm && role == 1 && bid < 112;
  HRegs hr, h2;
#define LOAD_H() do { hr = conv_load_h(ch0, w * 64 + lane, ssmk, ssmd); \
    if (ex) h2 = conv_load_h(528 + bid, w * 64 + lane, ssmk, ssmd); } while (0)
  auto q_publish = [&]() {
    asm volatile("s_waitcnt vmcnt(0)" ::: "memory");
    unsigned old = 0;
    if (lane == 0) old = __hip_atomic_fetch_add((LAS unsigned*)(uintptr_t)(ct + CT_QCNT), 1u, __ATOMIC_RELAXED, __HIP_MEMORY_SCOPE_WORKGROUP);
    if (__builtin_amdgcn_readfirstlane(old) == 3u) {
      const int c0 = (n0 % DM) - 32, hh_ = lane;
      if (lane < QREP && hh_ * 20 + 40 > c0 && hh_ * 20 < c0 + 32 + 128 + 20)
        __hip_atomic_store((u64*)(ws + FL_Q + (size_t)((lane * 16 + tm) * 16 + tn) * 8), E, __ATOMIC_RELAXED, __HIP_MEMORY_SCOPE_AGENT);
    }
    STAMP(4);
  };
  if (!has_gemm) {
    LOAD_H();
    const unsigned a2l = lds0 + (6 + (wave >> 1)) * A1_PAIR;
    if (wave < 8) a1_issue(a2l, a1t + 6, lane, wh, ak, av, shk);
    if (wave < 8) wait_vm<6>(); else wait_vm<0>();
    a1_finish(a1l, pf, ps, a1t, lane, wh, ws, E);
    if (wave < 8) { wait_vm<2>(); a1_finish(a2l, pf, ps, a1t + 6, lane, wh, ws, E); }
    a1_publish(a1t, lane, wh, ws, E);
    if (wave < 8) a1_publish(a1t + 6, lane, wh, ws, E);
    bar();
    STAMP(1);
  } else if (role == 0) {
    wait_vm<0>();
    sig(ct + CT_S0 + w * 4, 1u, lane);
    spin_ge4(ct + CT_S0, 1u);
    __builtin_amdgcn_s_setprio(MPRIO);
    if (tn >= 10) k1_gemm<true>(lds0, m0, n0, tid, ws, mk.x, dg);
    else k1_gemm<false>(lds0, m0, n0, tid, ws, mk.x, dg);
    STAMP(3);
  } else if (role == 1) {
    __builtin_amdgcn_s_setprio(LPRIO);
#define K1_ST(t) do { const int kt_ = (t) + rot >= 10 ? (t) + rot - 10 : (t) + rot; \
      if ((t) >= 4) spin_ge4(ct + CT_FREE + ((t) & 3) * 16, (unsigned)((t) - 3)); \
      _Pragma("unroll") for (int i_ = 0; i_ < 4; ++i_) { K1_PIECE_X(t, kt_, w * 4 + i_); K1_PIECE_W(t, kt_, w * 4 + i_); } } while (0)
#pragma nounroll
    for (int t = 1; t < 4; ++t) { K1_ST(t); glds_mark(mk, ct + CT_FX + t * 16 + w * 4, lane); }
    LOAD_H();
#pragma nounroll
    for (int t = 4; t < 10; ++t) { K1_ST(t); glds_mark(mk, ct + CT_FX + t * 16 + w * 4, lane); }
    __builtin_amdgcn_s_setprio(0);
    STAMP(1);
  } else {
    LOAD_H();
    __builtin_amdgcn_s_setprio(2);
    wait_vm<0>();
    a1_finish(a1l, pf, ps, a1t, lane, wh, ws, E);
    a1_publish(a1t, lane, wh, ws, E);
    __builtin_amdgcn_s_setprio(0);
    STAMP(1);
  }

  RELANE();
  unsigned qs = 0;
  const unsigned qf = ct + CT_QUAD + role * 16;
  const unsigned creg = lds0 + (role == 2 ? L_AUX : (role == 1 ? 29184 : 0));
  if (role == 2) __builtin_amdgcn_s_setprio(APRIO); else __builtin_amdgcn_s_setprio(0);
  if (has_gemm && role == 0) {
    q_publish();
    if (cv0) {
      hr.d = ssmd[ch0];
      conv_run(creg, qf, qs, ch0, lane, w, hr, ws, ct + CT_XST, dg);
    }
  } else {
    if (has_gemm && role == 1) {
      conv_load_u(hr, ch0, lane, w, ws, E);
      if (ex) conv_load_u(h2, 528 + bid, lane, w, ws, E);
      spin_ge4(ct + CT_FREE + 16, 10u);
      conv_stage(creg, lane, w, hr);
      if (ex) {
        conv_stage(lds0, lane, w, h2);
        lds_write16(lds0 + (w * 64 + lane) * 16, h2.u);
        asm volatile("s_waitcnt lgkmcnt(0)" ::: "memory");
        sig(ct + CT_XST + w * 4, 1u, lane);
      }
    } else {
      if (has_gemm) { const int wq = w; QSYNC(); }
      conv_stage(creg, lane, w, hr);
      conv_load_u(hr, ch0, lane, w, ws, E);
    }
    STAMP(5);
    conv_run(creg, qf, qs, ch0, lane, w, hr, ws, 0u, dg);
  }
  sig(ct + CT_CVD + wave * 4, 1u, lane);
  if (role != 0) STAMP(2);

  const int tm3 = xcd * 4 + (idx >> 3), tn3 = idx & 7;
  const int m3 = tm3 * 64, n3 = tn3 * 80;
  RELANE();

  if (role == 2) {
    const int r16 = lane & 15, g = lane >> 4, q = r16 >> 2, p = lane & 3;
    __builtin_amdgcn_s_setprio(3);
    spin_ge4(ct + CT_CVD + 32, 1u);
    if (has_gemm) spin_ge4(ct + CT_FREE + 16, 10u);
    attn_stage(lds0, xcd * 4 + (idx & 3), idx >> 2, w, lane, ws, E, ch0, mk, dg);
    {
      P3_WADDR();
      P3_STW(5);
      spin_ge8(ct + CT_CVD, 1u);
      P3_STW(0); P3_STW(4); P3_STW(1); P3_STW(2); P3_STW(3);
      glds_mark(mk, ct + CT_WRDY + w * 4, lane);
      STAMP(3);
    }
    __builtin_amdgcn_s_setprio(0);
    u32x4 qv[3];
#pragma unroll
    for (int j = 0; j < 3; ++j) {
      const int dc = j < 2 ? j * 4 + g : 8 + (g & 1);
      qv[j] = *((const u32x4*)(aq + (size_t)(m3 + w * 16 + r16) * DM + n3 + dc * 8));
    }
    {
      const int l5 = lane & 31;
      const int cl = l5 < 8 ? w * 8 + l5 : (l5 < 16 ? 32 + w * 8 + (l5 - 8) : 64 + w * 4 + (l5 & 3));
      poll<ZSLEEP>(ws, FL_Z + (size_t)(n3 + cl) * 8, E);
    }
    {
      const char* zg = ws + WS_ZT + ((size_t)n3 * SEQ + m3) * 2;
      const unsigned zo = (unsigned)((lane >> 3) * SEQ * 2 + (lane & 7) * 16);
      glds16c(zo, zg + (size_t)(w * 8) * SEQ * 2, lds0 + L_AUX + w * 1024);
      glds16c(zo, zg + (size_t)(32 + w * 8) * SEQ * 2, lds0 + L_AUX + 4096 + w * 1024);
      if (lane < 32) glds16c(zo, zg + (size_t)(64 + w * 4) * SEQ * 2, lds0 + L_AUX + 8192 + w * 512);
    }
    wait_vm<0>();
    sig(ct + CT_ZRDY + w * 4, 1u, lane);
    spin_ge4(ct + CT_ZRDY, 1u);
    STAMP(4);
    u32x4 zz[3];
#pragma unroll
    for (int j = 0; j < 3; ++j) {
      const int dc = j < 2 ? j * 4 + g : 8 + (g & 1);
      const unsigned za = lds0 + L_AUX + (dc * 8 + q) * 128 + w * 32 + p * 8;
      zz[j] = __builtin_bit_cast(u32x4, cat8(tr_read(za), tr_read(za + 512)));
    }
#pragma unroll
    for (int j = 0; j < 3; ++j) {
      const int dc = j < 2 ? j * 4 + g : 8 + (g & 1);
      u32x4 o;
#pragma unroll
      for (int i = 0; i < 4; ++i) o[i] = pk_bf16(bf_lo(qv[j][i]) * bf_lo(zz[j][i]), bf_hi(qv[j][i]) * bf_hi(zz[j][i]));
      if (j < 2 || g < 2) ostore16(out + (size_t)(m3 + w * 16 + r16) * (2 * DM) + n3 + dc * 8, o);
    }
    STAMP(7);
    if (bid == 0 && w == 0) {
#pragma unroll
      for (int i = 0; i < 4; ++i) poll(ws, FL_O + (size_t)(((i * 2 + (lane >> 5)) * 8) * NH + (lane & 31)) * 8, E);
      if (lane == 0) __hip_atomic_store((u64*)(ws + WS_FL), E, __ATOMIC_RELAXED, __HIP_MEMORY_SCOPE_AGENT);
    }
    DIAG_DUMP(2);
    return;
  }

  const int h = xcd * 4 + (idx & 3), qb8 = idx >> 2;
  __builtin_amdgcn_s_setprio(1);
  RELANE();
  if (qb8 > 0) attn_task<true>(lds0, h, qb8, wave, lane, ws, E, cv0 ? ch0 : -1, mk, dg);
  else attn_task<false>(lds0, h, qb8, wave, lane, ws, E, cv0 ? ch0 : -1, mk, dg);
  STAMP(11);

  RELANE();
  const int r16 = lane & 15, g = lane >> 4, q = r16 >> 2, p = lane & 3;
  f32x4 acc[5];
#pragma unroll
  for (int i = 0; i < 5; ++i) acc[i] = (f32x4){0.f, 0.f, 0.f, 0.f};
  const unsigned wbp = lds0 + (20 * (g >> 1) + 4 * (g & 1) + q) * 160 + p * 8;
  const unsigned wbs = lds0 + (20 * g + 16 + q) * 160 + p * 8;
  u32x4 op[8]; u32x2 osg[4];
  if (role == 1) {
    P3_WADDR();
    spin_ge8(ct + CT_ATT, 1u);
    P3_STW(6); P3_STW(7);
    glds_mark(mk, ct + CT_WRD2 + w * 4, lane);
  }
  spin_mk(ct + CT_WRDY, mk.x);
  const unsigned w0 = role ? p3_w(4) : p3_w(0), w1 = role ? p3_w(5) : p3_w(1), w2 = role ? p3_w(6) : p3_w(2), w3 = role ? p3_w(7) : p3_w(3);
  struct Fw { s16x8 a[5], b[5]; s16x4 s[5]; };
  Fw fwa, fwb;
#define P3_LOADW(F, bo_) do { _Pragma("unroll") for (int nt = 0; nt < 5; ++nt) { \
      F.a[nt] = cat8(tr_read(wbp + (bo_) + nt * 32), tr_read(wbp + (bo_) + 8 * 160 + nt * 32)); \
      F.b[nt] = cat8(tr_read(wbp + (bo_) + 40 * 160 + nt * 32), tr_read(wbp + (bo_) + 48 * 160 + nt * 32)); \
      F.s[nt] = tr_read(wbs + (bo_) + nt * 32); } } while (0)
#define P3_MM(F, t) do { \
    _Pragma("unroll") for (int nt = 0; nt < 5; ++nt) acc[nt] = MFMA16(F.a[nt], __builtin_bit_cast(s16x8, op[2 * (t)]), acc[nt]); \
    _Pragma("unroll") for (int nt = 0; nt < 5; ++nt) acc[nt] = MFMA16(F.b[nt], __builtin_bit_cast(s16x8, op[2 * (t) + 1]), acc[nt]); } while (0)
#define P3_MS(t) do { const s16x8 os_ = cat8(__builtin_bit_cast(s16x4, osg[t]), __builtin_bit_cast(s16x4, osg[(t) + 1])); \
    _Pragma("unroll") for (int nt = 0; nt < 5; ++nt) acc[nt] = MFMA16(cat8(fwa.s[nt], fwb.s[nt]), os_, acc[nt]); } while (0)
  P3_LOADW(fwa, w0);
  SBAR();
  poll<OSLEEP>(ws, FL_O + (size_t)(((m3 >> 5) + (w >> 1)) * NH + role * 16 + r16) * 8, E);
  STAMP(12);
  {
    const int orow = (w & 1) * 16 + r16;
    const char* ob = ws + WS_OW + ((size_t)((m3 >> 5) + (w >> 1)) * NH + role * 16) * 1280;
    const char* pp = ob + (g >> 1) * 1280 + (g & 1) * 512 + orow * 16;
    const char* sp = ob + g * 1280 + 1024 + orow * 8;
#pragma unroll
    for (int t = 0; t < 4; ++t) {
      asm volatile("global_load_dwordx4 %0, %3, off sc1\n\tglobal_load_dwordx4 %1, %3, off offset:2560 sc1\n\tglobal_load_dwordx2 %2, %4, off sc1"
                   : "=&v"(op[2 * t]), "=&v"(op[2 * t + 1]), "=&v"(osg[t]) : "v"(pp + t * 5120), "v"(sp + t * 5120) : "memory");
    }
  }
  P3_LOADW(fwb, w1);
  if (role == 1) spin_mk(ct + CT_WRD2, mk.x);
  SBAR();
  asm volatile("s_waitcnt vmcnt(6)" : "+v"(op[0]), "+v"(op[1]), "+v"(op[2]), "+v"(op[3]), "+v"(osg[0]), "+v"(osg[1]) :: "memory");
  STAMP(13);
  P3_MS(0); P3_MM(fwa, 0);
  SBAR();
  P3_LOADW(fwa, w2); P3_MM(fwb, 1);
#pragma unroll
  for (int i = 0; i < 10; ++i) { __builtin_amdgcn_sched_group_barrier(0x008, 1, 0); __builtin_amdgcn_sched_group_barrier(0x100, 3, 0); }
  SBAR();
  asm volatile("s_waitcnt vmcnt(0)" : "+v"(op[4]), "+v"(op[5]), "+v"(op[6]), "+v"(op[7]), "+v"(osg[2]), "+v"(osg[3]) :: "memory");
  P3_LOADW(fwb, w3); P3_MM(fwa, 2);
#pragma unroll
  for (int i = 0; i < 10; ++i) { __builtin_amdgcn_sched_group_barrier(0x008, 1, 0); __builtin_amdgcn_sched_group_barrier(0x100, 3, 0); }
  SBAR();
  P3_MM(fwb, 3); P3_MS(2);
  SBAR();
  const unsigned rb = lds0 + AT_K + 25600 + w * 5120 + lane * 16;
  if (role == 1) {
#pragma unroll
    for (int nt = 0; nt < 5; ++nt) lds_write16(rb + nt * 1024, __builtin_bit_cast(u32x4, acc[nt]));
    asm volatile("s_waitcnt lgkmcnt(0)" ::: "memory");
    sig(ct + CT_RED + w * 4, 1u, lane);
    STAMP(14);
    DIAG_DUMP(1);
    return;
  }
  spin_ge(ct + CT_RED + w * 4, 1u);
  u16* orow_g = out + (size_t)(m3 + w * 16 + r16) * (2 * DM) + DM + n3;
  u32x2 ov[5];
#pragma unroll
  for (int nt = 0; nt < 5; ++nt) {
    const f32x4 o2 = acc[nt] + __builtin_bit_cast(f32x4, lds_read16(rb + nt * 1024));
    ov[nt][0] = pk_bf16(o2[0], o2[1]);
    ov[nt][1] = pk_bf16(o2[2], o2[3]);
  }
#pragma unroll
  for (int pr = 0; pr < 2; ++pr) {
    const auto sx = __builtin_amdgcn_permlane16_swap(ov[2 * pr][0], ov[2 * pr + 1][0], false, false);
    const auto sy = __builtin_amdgcn_permlane16_swap(ov[2 * pr][1], ov[2 * pr + 1][1], false, false);
    ostore16(orow_g + (2 * pr + (g & 1)) * 16 + 8 * (g >> 1), (u32x4){sx[0], sy[0], sx[1], sy[1]});
  }
  ostore8(orow_g + 64 + 4 * g, ov[4]);
  STAMP(14);
  DIAG_DUMP(0);
}

extern "C" void kernel_launch(void* const* d_in, const int* in_sizes, int n_in, void* d_out, int out_size,
                              void* d_ws, size_t ws_size, hipStream_t stream) {
  const u16* aq = (const u16*)d_in[0];
  const u16* ak = (const u16*)d_in[1];
  const u16* av = (const u16*)d_in[2];
  const u16* shk = (const u16*)d_in[3];
  const u16* ssmk = (const u16*)d_in[4];
  const u16* ssmd = (const u16*)d_in[5];
  const u16* x = (const u16*)d_in[6];
  const u16* wqkv = (const u16*)d_in[7];
  const u16* wo = (const u16*)d_in[8];
  (void)hipFuncSetAttribute((const void*)mega, hipFuncAttributeMaxDynamicSharedMemorySize, L_TOTAL);
  mega<<<256, 768, L_TOTAL, stream>>>(x, wqkv, ak, av, shk, wo, ssmk, ssmd, aq, (char*)d_ws + WSOFF(ws_size), (u16*)d_out);
}
```

```cpp
#include <hip/hip_runtime.h>
#include <hip/hip_bf16.h>
#include <stdint.h>

typedef unsigned short u16;
typedef short s16x4 __attribute__((ext_vector_type(4)));
typedef short s16x8 __attribute__((ext_vector_type(8)));
typedef float f32x2 __attribute__((ext_vector_type(2)));
typedef float f32x4 __attribute__((ext_vector_type(4)));
typedef float f32x16 __attribute__((ext_vector_type(16)));
typedef unsigned u32x2 __attribute__((ext_vector_type(2)));
typedef unsigned u32x4 __attribute__((ext_vector_type(4)));
typedef __bf16 bf16x2 __attribute__((ext_vector_type(2)));
#define LAS __attribute__((address_space(3)))
#define DI __device__ __forceinline__

constexpr int SEQ = 2048, DM = 640, NQKV = 1920, NH = 32;
constexpr size_t WS_QF = 0;
constexpr size_t WS_KF = WS_QF + (size_t)DM * SEQ * 2;
constexpr size_t WS_VF = WS_KF + (size_t)DM * SEQ * 2;
constexpr size_t WS_UT = WS_VF + (size_t)DM * SEQ * 2;
constexpr size_t WS_ZT = WS_UT + (size_t)DM * SEQ * 2;
constexpr size_t WS_OW = WS_ZT + (size_t)DM * SEQ * 2;
typedef unsigned long long u64;
constexpr size_t WS_FL = WS_OW + (size_t)DM * SEQ * 2;
constexpr size_t FL_U = WS_FL + 256;
constexpr int QREP = 32;
constexpr size_t FL_Q = FL_U + 20 * 64 * 8;
constexpr size_t FL_Z = FL_Q + QREP * 16 * 16 * 8;
constexpr size_t FL_O = FL_Z + 640 * 8;

DI unsigned pk_bf16(float lo, float hi) {
  f32x2 f = {lo, hi};
  return __builtin_bit_cast(unsigned, __builtin_convertvector(f, bf16x2));
}
DI float bf_lo(unsigned u) { return __uint_as_float(u << 16); }
DI float bf_hi(unsigned u) { return __uint_as_float(u & 0xffff0000u); }
DI float rbf(float x) { return bf_lo(pk_bf16(x, 0.f)); }

DI unsigned lds_addr(const void* p) { return (unsigned)(uintptr_t)(LAS const char*)p; }

DI void glds16(unsigned voff, const void* sbase, unsigned lds_dst) {
  unsigned keep;
  asm volatile("s_mov_b32 %0, m0\n\ts_mov_b32 m0, %3\n\ts_nop 0\n\tglobal_load_lds_dwordx4 %1, %2\n\ts_mov_b32 m0, %0"
               : "=&s"(keep) : "v"(voff), "s"(sbase), "s"(lds_dst) : "memory");
}
#define LDPOL "sc1"
DI void glds16c(unsigned voff, const void* sbase, unsigned lds_dst) {
  unsigned keep;
  asm volatile("s_mov_b32 %0, m0\n\ts_mov_b32 m0, %3\n\ts_nop 0\n\tglobal_load_lds_dwordx4 %1, %2 " LDPOL "\n\ts_mov_b32 m0, %0"
               : "=&s"(keep) : "v"(voff), "s"(sbase), "s"(lds_dst) : "memory");
}
struct Mk { const void* src; unsigned x; };
DI void glds_mark(const Mk& mk, unsigned lds_dst, int lane) {
  if (lane == 0) {
    unsigned keep;
    asm volatile("s_mov_b32 %0, m0\n\ts_mov_b32 m0, %3\n\ts_nop 0\n\tglobal_load_lds_dword %1, %2\n\ts_mov_b32 m0, %0"
                 : "=&s"(keep) : "v"(0u), "s"(mk.src), "s"(lds_dst) : "memory");
  }
}
DI float dot2bf(unsigned a, unsigned b, float c) {
  return __builtin_amdgcn_fdot2_f32_bf16(__builtin_bit_cast(bf16x2, a), __builtin_bit_cast(bf16x2, b), c, false);
}
DI void gstore16c(void* p, u32x4 v) {
  asm volatile("global_store_dwordx4 %0, %1, off sc1\n\ts_nop 1" :: "v"(p), "v"(v) : "memory");
}
DI void gstore8c(void* p, u32x2 v) {
  asm volatile("global_store_dwordx2 %0, %1, off sc1" :: "v"(p), "v"(v) : "memory");
}
#define OUTPOL "nt"
DI void ostore16(void* p, u32x4 v) { asm volatile("global_store_dwordx4 %0, %1, off " OUTPOL "\n\ts_nop 1" :: "v"(p), "v"(v) : "memory"); }
DI void ostore8(void* p, u32x2 v) { asm volatile("global_store_dwordx2 %0, %1, off " OUTPOL :: "v"(p), "v"(v) : "memory"); }
template <int N> DI void wait_vm() { asm volatile("s_waitcnt vmcnt(%0)" :: "n"(N) : "memory"); }
DI void bar() { asm volatile("s_waitcnt lgkmcnt(0)\n\ts_barrier" ::: "memory"); }
DI s16x4 tr_read(unsigned a) {
  return __builtin_amdgcn_ds_read_tr16_b64_v4i16((LAS s16x4*)(uintptr_t)a);
}
DI s16x8 lds_read16(unsigned a) { return *(LAS const s16x8*)(uintptr_t)a; }
DI s16x4 lds_read8(unsigned a) { return *(LAS const s16x4*)(uintptr_t)a; }
DI void lds_write16(unsigned a, u32x4 v) { *(LAS u32x4*)(uintptr_t)a = v; }
DI s16x8 cat8(s16x4 a, s16x4 b) { return __builtin_shufflevector(a, b, 0, 1, 2, 3, 4, 5, 6, 7); }
#define MFMA16(a, b, c) __builtin_amdgcn_mfma_f32_16x16x32_bf16((a), (b), (c), 0, 0, 0)
#define MFMA32(a, b, c) __builtin_amdgcn_mfma_f32_32x32x16_bf16((a), (b), (c), 0, 0, 0)

#define SBAR() __builtin_amdgcn_sched_barrier(0)
#define SPIN_SLEEP 1
#define SPIN_MAX (1 << 21)
DI unsigned lds_ld32(unsigned a) { return *(volatile LAS unsigned*)(uintptr_t)a; }
DI void lds_st32(unsigned a, unsigned v) { *(volatile LAS unsigned*)(uintptr_t)a = v; }
DI void spin_ge(unsigned a, unsigned want) {
#pragma nounroll
  for (int it = 0; it < SPIN_MAX; ++it) {
    if ((unsigned)__builtin_amdgcn_readfirstlane(lds_ld32(a)) >= want) break;
    __builtin_amdgcn_s_sleep(1);
  }
  asm volatile("" ::: "memory");
}
DI void spin_ge4(unsigned a, unsigned want) {
#pragma nounroll
  for (int it = 0; it < SPIN_MAX; ++it) {
    const u32x4 v = *(volatile LAS u32x4*)(uintptr_t)a;
    const unsigned m = min(min(v[0], v[1]), min(v[2], v[3]));
    if ((unsigned)__builtin_amdgcn_readfirstlane(m) >= want) break;
    __builtin_amdgcn_s_sleep(SPIN_SLEEP);
  }
  asm volatile("" ::: "memory");
}
DI unsigned mk_bad(const u32x4& v, unsigned x) { return (v[0] ^ x) | (v[1] ^ x) | (v[2] ^ x) | (v[3] ^ x); }
DI void spin_mk(unsigned a, unsigned x) {
#pragma nounroll
  for (int it = 0; it < SPIN_MAX; ++it) {
    const u32x4 v = *(volatile LAS u32x4*)(uintptr_t)a;
    if ((unsigned)__builtin_amdgcn_readfirstlane(mk_bad(v, x)) == 0u) break;
    __builtin_amdgcn_s_sleep(SPIN_SLEEP);
  }
  asm volatile("" ::: "memory");
}
DI void spin_ge8(unsigned a, unsigned want) { spin_ge4(a, want); spin_ge4(a + 16, want); }
constexpr int CT_S0 = 0, CT_FREE = 80, CT_PAIR = 144  , CT_QUAD = 192  , CT_ZCNT = 240  ,
              CT_QCNT = 256, CT_ZRDY = 304, CT_CVD = 336  , CT_XST = 416, CT_ATT = 448, CT_RED = 480,
              CT_FX = 512  , CT_KR8 = 688, CT_VR8 = 704, CT_WRDY = 720, CT_WRD2 = 736, CT_BYTES = 1024;
DI void sig(unsigned a, unsigned v, int lane) { if (lane == 0) lds_st32(a, v); }

#define POLL_MAX (1 << 15)
DI void publish(char* ws, size_t off, u64 E, int lane) {
  asm volatile("s_waitcnt vmcnt(0)" ::: "memory");
  if (lane == 0) __hip_atomic_store((u64*)(ws + off), E, __ATOMIC_RELAXED, __HIP_MEMORY_SCOPE_AGENT);
}
#define OSLEEP 10
#define ZSLEEP 20
#define USLEEP 10
#define POLL_SLEEP 6
template <int SL = POLL_SLEEP>
DI void poll(const char* ws, size_t off, u64 E) {
#pragma nounroll
  for (int it = 0; it < POLL_MAX; ++it) {
    const u64 v = __hip_atomic_load((const u64*)(ws + off), __ATOMIC_RELAXED, __HIP_MEMORY_SCOPE_AGENT);
    if (__builtin_amdgcn_ballot_w64(v != E) == 0ull) break;
    __builtin_amdgcn_s_sleep(SL);
  }
  asm volatile("" ::: "memory");
}

constexpr int K1_STAGE = 32768;
constexpr int A1_K = 0, A1_V = 3072, A1_W = 5120, A1_WAVE = 5632, A1_O = 2 * A1_WAVE, A1_PAIR = A1_O + 4096;
constexpr int L_AUX = 4 * K1_STAGE;
constexpr int L_CT = L_AUX + 2 * A1_PAIR;
constexpr int L_TOTAL = L_CT + CT_BYTES;
constexpr int K1_CT = L_CT;
constexpr int CV_COPY = 4160, CV_H = 4608, CV_REG = CV_H + 24576;
constexpr int AT_Q = L_AUX + 12288, AT_K = 64000, AT_V = AT_K + 16 * 2048, AT_END = AT_V + 16 * 1344;
constexpr int p3_w(int t) { return t < 5 ? t * 12800 : (t == 5 ? AT_END : AT_K + (t - 6) * 12800); }

#define PSYNC() do { asm volatile("s_waitcnt lgkmcnt(0)" ::: "memory"); ++ps; sig(pf + wh * 4, ps, lane); spin_ge(pf + (wh ^ 1) * 4, ps); } while (0)
DI void a1_issue(unsigned lds, int tile, int lane, int wh, const u16* ak, const u16* av, const u16* shk) {
  const int ts = tile & 63, td = tile >> 6;
  const int s0 = ts * 32 + wh * 16, d0 = td * 64;
  const int lr = lane >> 3, lc = (lane & 7) * 16;
  const unsigned wl = lds + wh * A1_WAVE;
#pragma unroll
  for (int i = 0; i < 3; ++i) {
    int r = s0 - 3 + i * 8 + lr; r = r < 0 ? 0 : r;
    if (i < 2 || lane < 24) glds16((unsigned)(r * DM * 2 + lc), (const char*)ak + d0 * 2, wl + A1_K + i * 1024);
  }
#pragma unroll
  for (int i = 0; i < 2; ++i)
    glds16((unsigned)(lr * DM * 2 + lc), (const char*)av + ((size_t)(s0 + i * 8) * DM + d0) * 2, wl + A1_V + i * 1024);
  if (lane < 32) glds16((unsigned)(lane * 16), (const char*)shk + d0 * 8, wl + A1_W);
}
DI void a1_finish(unsigned lds, unsigned pf, unsigned& ps, int tile, int lane, int wh, char* ws, u64 E) {
  const int ts = tile & 63, td = tile >> 6;
  const int c = lane & 7;
  const unsigned wl = lds + wh * A1_WAVE;
  u32x4 wt[4];
#pragma unroll
  for (int i = 0; i < 4; ++i) wt[i] = __builtin_bit_cast(u32x4, lds_read16(wl + A1_W + c * 64 + i * 16));
#pragma unroll
  for (int j = 0; j < 2; ++j) {
    const int r = j * 8 + (lane >> 3);
    const unsigned ka = wl + A1_K + (r + 3) * 128 + c * 16;
    u32x4 k0 = __builtin_bit_cast(u32x4, lds_read16(ka)), k1 = __builtin_bit_cast(u32x4, lds_read16(ka - 128));
    u32x4 k2 = __builtin_bit_cast(u32x4, lds_read16(ka - 256)), k3 = __builtin_bit_cast(u32x4, lds_read16(ka - 384));
    const u32x4 vv = __builtin_bit_cast(u32x4, lds_read16(wl + A1_V + r * 128 + c * 16));
    if (j == 0 && ts == 0 && wh == 0) {
      const u32x4 z = {0u, 0u, 0u, 0u};
      if (r < 1) k1 = z;
      if (r < 2) k2 = z;
      if (r < 3) k3 = z;
    }
    u32x4 o;
#pragma unroll
    for (int i = 0; i < 4; ++i) {
      const unsigned pa0 = __builtin_amdgcn_perm(k1[i], k0[i], 0x05040100u), pb0 = __builtin_amdgcn_perm(k3[i], k2[i], 0x05040100u);
      const unsigned pa1 = __builtin_amdgcn_perm(k1[i], k0[i], 0x07060302u), pb1 = __builtin_amdgcn_perm(k3[i], k2[i], 0x07060302u);
      const float e0 = dot2bf(pb0, wt[i][1], dot2bf(pa0, wt[i][0], 0.f));
      const float e1 = dot2bf(pb1, wt[i][3], dot2bf(pa1, wt[i][2], 0.f));
      o[i] = pk_bf16(e0 * bf_lo(vv[i]), e1 * bf_hi(vv[i]));
    }
    lds_write16(lds + A1_O + (31 - wh * 16 - r) * 128 + c * 16, o);
  }
  PSYNC();
  const int gg = lane >> 4, q = (lane & 15) >> 2, p = lane & 3;
  u16* ut = (u16*)(ws + WS_UT);
#pragma unroll
  for (int j = 0; j < 2; ++j) {
    const unsigned ra = lds + A1_O + (gg * 8 + q) * 128 + wh * 64 + j * 32 + p * 8;
    const s16x4 t0 = tr_read(ra), t1 = tr_read(ra + 512);
    const int d = td * 64 + wh * 32 + j * 16 + (lane & 15);
    gstore16c(ut + (size_t)d * SEQ + (SEQ - 32 - ts * 32) + gg * 8, __builtin_bit_cast(u32x4, cat8(t0, t1)));
  }
}
DI void a1_publish(int tile, int lane, int wh, char* ws, u64 E) {
  const int ts = tile & 63, td = tile >> 6;
  publish(ws, FL_U + (size_t)((td * 2 + wh) * 64 + ts) * 8, E, lane);
}

template <bool VT>
DI void k1_gemm(unsigned lds0, int m0, int n0, int tid, char* ws, unsigned mkx) {
  const int lane = tid & 63, wave = __builtin_amdgcn_readfirstlane(tid >> 6);
  const int wm = wave >> 1, wn = wave & 1;
  const int l32 = lane & 31, hh = lane >> 5, gg = lane >> 4, q = (lane & 15) >> 2, p = lane & 3;
  const unsigned ct = lds0 + K1_CT;
  f32x16 acc[2][2];
#pragma unroll
  for (int i = 0; i < 2; ++i)
#pragma unroll
    for (int j = 0; j < 2; ++j)
#pragma unroll
      for (int r = 0; r < 16; ++r) acc[i][j][r] = 0.f;
  unsigned xb[4];
#pragma unroll
  for (int ks = 0; ks < 4; ++ks) xb[ks] = lds0 + (wm * 64 + l32) * 128 + (((unsigned)(2 * ks + hh) ^ (unsigned)((l32 >> 1) & 7)) * 16);
  unsigned wb[2];
#pragma unroll
  for (int nt = 0; nt < 2; ++nt)
    wb[nt] = lds0 + 16384 + (8 * hh + q) * 256 + (((unsigned)(wn * 4 + 2 * nt + (gg & 1)) ^ (unsigned)(q << 1)) * 32) + p * 8;
  s16x8 x0[2], w0[2], x1[2], w1[2], x2[2], w2[2], x3[2], w3[2];
#define K1_LOADF(XF, WF, bo_, ks) do { \
    _Pragma("unroll") for (int i_ = 0; i_ < 2; ++i_) { \
      WF[i_] = cat8(tr_read(wb[i_] + (bo_) + (ks) * 4096), tr_read(wb[i_] + (bo_) + (ks) * 4096 + 1024)); \
      XF[i_] = lds_read16(xb[ks] + (bo_) + i_ * 4096); } } while (0)
#define K1_MM(XF, WF) do { _Pragma("unroll") for (int mt = 0; mt < 2; ++mt) _Pragma("unroll") for (int nt = 0; nt < 2; ++nt) \
      acc[mt][nt] = VT ? MFMA32(XF[mt], WF[nt], acc[mt][nt]) : MFMA32(WF[nt], XF[mt], acc[mt][nt]); } while (0)
#define ILV_A 2
#define ILV_B 2
#define ILV_C 2
#define K1_ILV() do { \
      __builtin_amdgcn_sched_group_barrier(0x008, 1, 0); __builtin_amdgcn_sched_group_barrier(0x100, ILV_A, 0); \
      __builtin_amdgcn_sched_group_barrier(0x008, 1, 0); __builtin_amdgcn_sched_group_barrier(0x100, ILV_B, 0); \
      __builtin_amdgcn_sched_group_barrier(0x008, 1, 0); __builtin_amdgcn_sched_group_barrier(0x100, ILV_C, 0); \
      __builtin_amdgcn_sched_group_barrier(0x008, 1, 0); __builtin_amdgcn_sched_group_barrier(0x100, 2, 0); } while (0)
  K1_LOADF(x0, w0, 0, 0);
  K1_LOADF(x1, w1, 0, 1);
#pragma nounroll
  for (int t = 0; t < 10; ++t) {
    const unsigned bo = (t & 3) * K1_STAGE, bn = ((t + 1) & 3) * K1_STAGE;
    SBAR();
    const u32x4 fl4 = *(volatile LAS u32x4*)(uintptr_t)(ct + CT_FX + (t + 1) * 16);
    K1_LOADF(x2, w2, bo, 2); K1_MM(x0, w0); K1_ILV();
    SBAR();
    K1_LOADF(x3, w3, bo, 3); K1_MM(x1, w1); K1_ILV();
    SBAR();
    const unsigned flu = (unsigned)__builtin_amdgcn_readfirstlane(mk_bad(fl4, mkx));
    if (t < 9 && flu != 0u) spin_mk(ct + CT_FX + (t + 1) * 16, mkx);
    asm volatile("" ::: "memory");
    SBAR();
    K1_LOADF(x0, w0, bn, 0); K1_MM(x2, w2); K1_ILV();
    SBAR();
    asm volatile("" : "+v"(x3[0]), "+v"(x3[1]), "+v"(w3[0]), "+v"(w3[1]) :: "memory");
    if (lane == 0) lds_st32(ct + CT_FREE + (t & 3) * 16 + wave * 4, (unsigned)(t + 1));
    SBAR();
    K1_LOADF(x1, w1, bn, 1); K1_MM(x3, w3); K1_ILV();
  }
  SBAR();
  if (!VT) {
    const int part = n0 >= DM ? 1 : 0;
    const float sc = part == 0 ? 0.22360679774997896f * 1.4426950408889634f : 1.0f;
    char* base = ws + (part ? WS_KF : WS_QF) + ((size_t)((m0 + wm * 64) >> 5) * 40 + ((n0 - part * DM + wn * 64) >> 4)) * 1024 + lane * 16;
#pragma unroll
    for (int mt = 0; mt < 2; ++mt)
#pragma unroll
      for (int nt = 0; nt < 2; ++nt)
#pragma unroll
        for (int uh = 0; uh < 2; ++uh) {
          u32x4 o;
#pragma unroll
          for (int i = 0; i < 4; ++i) o[i] = pk_bf16(acc[mt][nt][8 * uh + 2 * i] * sc, acc[mt][nt][8 * uh + 2 * i + 1] * sc);
          gstore16c(base + (mt * 40 + nt * 2 + uh) * 1024, o);
        }
  } else {
    char* base = ws + WS_VF + ((size_t)((m0 + wm * 64) >> 4) * 20 + ((n0 - 2 * DM + wn * 64) >> 5)) * 1024 + lane * 16;
#pragma unroll
    for (int mt = 0; mt < 2; ++mt)
#pragma unroll
      for (int nt = 0; nt < 2; ++nt)
#pragma unroll
        for (int kg = 0; kg < 2; ++kg) {
          u32x4 o;
#pragma unroll
          for (int i = 0; i < 4; ++i) o[i] = pk_bf16(acc[mt][nt][8 * kg + 2 * i], acc[mt][nt][8 * kg + 2 * i + 1]);
          gstore16c(base + ((mt * 2 + kg) * 20 + nt) * 1024, o);
        }
  }
}

DI void cv_put(unsigned y, const u32x4& c, const u32x2& n) {
  const unsigned d0 = c[0], d1 = c[1], d2 = c[2], d3 = c[3], d4 = n[0], d5 = n[1];
  const unsigned a0 = __builtin_amdgcn_alignbit(d1, d0, 16), a1 = __builtin_amdgcn_alignbit(d2, d1, 16),
                 a2 = __builtin_amdgcn_alignbit(d3, d2, 16), a3 = __builtin_amdgcn_alignbit(d4, d3, 16),
                 a4 = __builtin_amdgcn_alignbit(d5, d4, 16);
  lds_write16(y, (u32x4){d0, d1, d2, d3});
  lds_write16(y + CV_COPY, (u32x4){a0, a1, a2, a3});
  lds_write16(y + 2 * CV_COPY, (u32x4){d1, d2, d3, d4});
  lds_write16(y + 3 * CV_COPY, (u32x4){a1, a2, a3, a4});
}

#define QSYNC() do { asm volatile("s_waitcnt lgkmcnt(0)" ::: "memory"); ++qs; sig(qf + wq * 4, qs, lane); spin_ge4(qf, qs); } while (0)
struct HRegs { u32x4 c, u; u32x2 n; unsigned d; };
DI HRegs conv_load_h(int ch, int t256, const u16* __restrict__ ssmk, const u16* __restrict__ ssmd) {
  HRegs r;
  const u16* hrow = ssmk + (size_t)ch * SEQ;
  r.c = *((const u32x4*)(hrow + t256 * 8));
  r.n = *(const u32x2*)(hrow + (t256 < 255 ? t256 * 8 + 8 : 0));
  r.d = ssmd[ch];
  return r;
}
DI void conv_load_u(HRegs& r, int ch, int lane, int wq, const char* __restrict__ ws, u64 E) {
  poll<USLEEP>(ws, FL_U + (size_t)((ch >> 5) * 64 + 48 - 16 * wq + (lane & 15)) * 8, E);
  const u64* up = (const u64*)(ws + WS_UT + (size_t)ch * SEQ * 2 + wq * 1024 + lane * 16);
  const u64 a = __hip_atomic_load(up, __ATOMIC_RELAXED, __HIP_MEMORY_SCOPE_AGENT);
  const u64 b = __hip_atomic_load(up + 1, __ATOMIC_RELAXED, __HIP_MEMORY_SCOPE_AGENT);
  r.u = (u32x4){(unsigned)a, (unsigned)(a >> 32), (unsigned)b, (unsigned)(b >> 32)};
}
DI void conv_stage(unsigned reg, int lane, int wq, const HRegs& hr) {
  const int t256 = wq * 64 + lane;
  const unsigned hb = reg + CV_H;
  if (t256 < 32) lds_write16(reg + 4096 + t256 * 16, (u32x4){0u, 0u, 0u, 0u});
  cv_put(hb + (t256 + 2) * 16, hr.c, hr.n);
  if (t256 < 2) {
    const u32x4 z = {0u, 0u, 0u, 0u};
    u32x2 nx = {0u, 0u};
    const unsigned h0 = __builtin_amdgcn_readfirstlane(hr.c[0]), h1 = __builtin_amdgcn_readfirstlane(hr.c[1]);
    if (t256 == 1) nx = (u32x2){h0, h1};
    cv_put(hb + t256 * 16, z, nx);
  }
}
DI void conv_run(unsigned reg, unsigned qf, unsigned& qs, int ch, int lane, int wq, const HRegs& hr,
                 char* __restrict__ ws, unsigned xst) {
  const unsigned hb = reg + CV_H;
  if (xst) spin_ge4(xst, 1u);
  else { lds_write16(reg + (wq * 64 + lane) * 16, hr.u); QSYNC(); }
  const int r16 = lane & 15, g = lane >> 4, q = r16 >> 2, p = lane & 3;
  const unsigned ab0 = hb + ((q + 1) & 3) * CV_COPY + 2 * (8 * g + 4 * p + (q == 3 ? 4 : 0)) + wq * 128;
  const unsigned bb0 = reg + 2 * (2032 - 16 * r16 + 8 * g) + wq * 128 - 7 * 512;
  f32x4 acc[8];
#pragma unroll
  for (int i = 0; i < 8; ++i) acc[i] = (f32x4){0.f, 0.f, 0.f, 0.f};
#define CV_LDA(a_, ad_) cat8(tr_read((ad_) + (a_) * 512), tr_read((ad_) + (a_) * 512 + 8))
#define CV_LDB(c_, bd_) lds_read16((bd_) + (7 - (c_)) * 512)
  {
    s16x8 A[8], B[8];
    B[0] = CV_LDB(0, bb0);
#pragma unroll
    for (int a = 0; a < 8; ++a) A[a] = CV_LDA(a, ab0);
#pragma unroll
    for (int c = 1; c < 8; ++c) B[c] = CV_LDB(c, bb0);
#pragma unroll
    for (int c = 0; c < 8; ++c) {
      SBAR();
#pragma unroll
      for (int a = 0; a + c < 8; ++a) acc[a + c] = MFMA16(A[a], B[c], acc[a + c]);
      B[c] = CV_LDB(c, bb0 + 64);
      A[7 - c] = CV_LDA(7 - c, ab0 + 64);
    }
    SBAR();
#pragma unroll
    for (int a = 7; a >= 0; --a)
#pragma unroll
      for (int c = 0; a + c < 8; ++c) acc[a + c] = MFMA16(A[a], B[c], acc[a + c]);
  }
  QSYNC();
#pragma unroll
  for (int nt = 0; nt < 8; ++nt) {
    const int dw = nt >> 1;
    if (dw != wq) lds_write16(hb + (nt * 3 + (wq > dw ? wq - 1 : wq)) * 1024 + lane * 16, __builtin_bit_cast(u32x4, acc[nt]));
  }
  f32x4 k0 = wq == 0 ? acc[0] : (wq == 1 ? acc[2] : (wq == 2 ? acc[4] : acc[6]));
  f32x4 k1 = wq == 0 ? acc[1] : (wq == 1 ? acc[3] : (wq == 2 ? acc[5] : acc[7]));
  QSYNC();
  const float dco = bf_lo(hr.d);
  u32x2 zz[2];
#pragma unroll
  for (int i = 0; i < 2; ++i) {
    f32x4 o = i ? k1 : k0;
#pragma unroll
    for (int j = 0; j < 3; ++j) o += __builtin_bit_cast(f32x4, lds_read16(hb + ((wq * 2 + i) * 3 + j) * 1024 + lane * 16));
    const int s = (wq * 2 + i) * 256 + 16 * r16 + 4 * g;
    const u32x2 uu = __builtin_bit_cast(u32x2, lds_read8(reg + 2 * (SEQ - 4 - s)));
    const float kv0 = bf_hi(uu[1]), kv1 = bf_lo(uu[1]), kv2 = bf_hi(uu[0]), kv3 = bf_lo(uu[0]);
    const unsigned y01 = pk_bf16(o[0], o[1]), y23 = pk_bf16(o[2], o[3]);
    const unsigned e01 = pk_bf16(kv0 * dco, kv1 * dco), e23 = pk_bf16(kv2 * dco, kv3 * dco);
    zz[i][0] = pk_bf16(bf_lo(y01) + bf_lo(e01), bf_hi(y01) + bf_hi(e01));
    zz[i][1] = pk_bf16(bf_lo(y23) + bf_lo(e23), bf_hi(y23) + bf_hi(e23));
  }
  const auto sx = __builtin_amdgcn_permlane16_swap(zz[0][0], zz[1][0], false, false);
  const auto sy = __builtin_amdgcn_permlane16_swap(zz[0][1], zz[1][1], false, false);
  u16* zrow = (u16*)(ws + WS_ZT) + (size_t)ch * SEQ;
  gstore16c(zrow + (wq * 2 + (g & 1)) * 256 + 16 * r16 + 8 * (g >> 1), (u32x4){sx[0], sy[0], sx[1], sy[1]});
  asm volatile("s_waitcnt lgkmcnt(0)" ::: "memory");
}
DI void conv_publish(unsigned zc, int ch, int lane, char* __restrict__ ws, u64 E) {
  asm volatile("s_waitcnt vmcnt(0)" ::: "memory");
  if (lane == 0) {
    const unsigned old = __hip_atomic_fetch_add((LAS unsigned*)(uintptr_t)zc, 1u, __ATOMIC_RELAXED, __HIP_MEMORY_SCOPE_WORKGROUP);
    if ((old & 3u) == 3u) __hip_atomic_store((u64*)(ws + FL_Z + (size_t)ch * 8), E, __ATOMIC_RELAXED, __HIP_MEMORY_SCOPE_AGENT);
  }
}

DI s16x8 pack8(const f32x16& v, int o) {
  u32x4 r;
  r[0] = pk_bf16(v[o + 0], v[o + 1]); r[1] = pk_bf16(v[o + 2], v[o + 3]);
  r[2] = pk_bf16(v[o + 4], v[o + 5]); r[3] = pk_bf16(v[o + 6], v[o + 7]);
  return __builtin_bit_cast(s16x8, r);
}
DI float max3f(float a, float b, float c) { return __builtin_fmaxf(__builtin_fmaxf(a, b), c); }
DI float max16(const f32x16& v, float m) {
  float a = max3f(v[0], v[1], m), b = max3f(v[2], v[3], v[4]);
  a = max3f(a, v[5], v[6]); b = max3f(b, v[7], v[8]);
  a = max3f(a, v[9], v[10]); b = max3f(b, v[11], v[12]);
  a = max3f(a, v[13], v[14]);
  return max3f(a, b, v[15]);
}
DI float xhalf_max(float m) {
  auto sw = __builtin_amdgcn_permlane32_swap(__float_as_uint(m), __float_as_uint(m), false, false);
  return __builtin_fmaxf(__uint_as_float(sw[0]), __uint_as_float(sw[1]));
}

#define AT_LMAX 65536.0f
#define P3_WADDR() unsigned wv[4]; _Pragma("unroll") for (int i = 0; i < 4; ++i) { \
      const int c = i < 3 ? (i * 4 + w) * 64 + lane : 768 + w * 8 + (lane & 7); const int row = c / 10; \
      wv[i] = (unsigned)(row * DM * 2 + (c - row * 10) * 16); } const char* wog = (const char*)(wo + n3)
#define P3_STW(t) do { _Pragma("unroll") for (int i_ = 0; i_ < 3; ++i_) \
        glds16(wv[i_], wog + (size_t)(t) * 80 * DM * 2, lds0 + p3_w(t) + (i_ * 4 + w) * 1024); \
      if (lane < 8) glds16(wv[3], wog + (size_t)(t) * 80 * DM * 2, lds0 + p3_w(t) + 12288 + w * 128); } while (0)
DI void attn_stage(unsigned lds0, int h, int qb8, int j, int lane, char* __restrict__ ws, u64 E, int zch, const Mk& mk) {
  const int qt = qb8 * 8 + j;
  const unsigned l16 = (unsigned)(lane * 16), ct = lds0 + L_CT;
  const int qa = (5 * h) >> 2;
  const bool stg = qb8 > 0 || j >= 2;
  {
    const int l3 = lane & 7, hi = lane & 1;
    const bool kv = stg && l3 >= 2 && l3 < 6;
    const int rb = kv ? 2 * qb8 - 2 + j : 2 * qb8 + (l3 >= 6 ? 1 : 0);
    const int col = !kv ? qa * 16 + hi * 31 : (l3 < 4 ? DM + qa * 16 + hi * 31 : 2 * DM + h * 20 + hi * 19);
    poll(ws, FL_Q + (size_t)((h * 16 + rb) * 16 + (col >> 7)) * 8, E);
  }
  conv_publish(ct + CT_ZCNT + 8, zch, lane, ws, E);
  const int kt0 = qb8 * 8 - 8 + 4 * j;
#pragma unroll
  for (int i = 0; i < 4; ++i)
    glds16c(l16, ws + WS_QF + ((size_t)(qt + (i >> 1) * 4) * 40 + qa + (i & 1)) * 1024, lds0 + AT_Q + ((j + (i >> 1) * 4) * 2 + (i & 1)) * 1024);
  if (stg) {
#pragma unroll
    for (int i = 0; i < 8; ++i)
      glds16c(l16, ws + WS_KF + ((size_t)(kt0 + (i >> 1)) * 40 + qa + (i & 1)) * 1024, lds0 + AT_K + (j * 8 + i) * 1024);
  }
  glds_mark(mk, ct + CT_KR8 + j * 4, lane);
  if (stg) {
#pragma unroll
    for (int i = 0; i < 6; ++i) {
      const int c = i * 64 + lane;
      const int kgp = (c * 1561) >> 16, rem = c - kgp * 42, vh = rem >= 21 ? 1 : 0, d = rem - vh * 21;
      const int n = h * 20 + d;
      if (c < 336 && d == 20) lds_write16(lds0 + AT_V + j * 5376 + c * 16, (u32x4){0x3f803f80u, 0x3f803f80u, 0x3f803f80u, 0x3f803f80u});
      if (c < 336 && d < 20)
        glds16c((unsigned)((kgp * 20 + (n >> 5)) * 1024 + (vh * 32 + (n & 31)) * 16), ws + WS_VF + ((size_t)(kt0 * 2) * 20) * 1024,
                lds0 + AT_V + j * 5376 + i * 1024);
    }
    asm volatile("s_waitcnt lgkmcnt(0)" ::: "memory");
  }
  glds_mark(mk, ct + CT_VR8 + j * 4, lane);
}
template <bool FULL>
DI void attn_task(unsigned lds0, int h, int qb8, int j, int lane, char* __restrict__ ws, u64 E, int zch, const Mk& mk) {
  const int l32 = lane & 31, hh = lane >> 5;
  const int qt = qb8 * 8 + j;
  const int tfirst = FULL ? 0 : 8 - qt;
  const unsigned l16 = (unsigned)(lane * 16), ct = lds0 + L_CT;
  const int qa = (5 * h) >> 2;
  u32x4 q0r, q1r;
  spin_mk(ct + CT_KR8, mk.x);
  if (!FULL) spin_mk(ct + CT_VR8, mk.x);
  q0r = __builtin_bit_cast(u32x4, lds_read16(lds0 + AT_Q + j * 2048 + l16));
  q1r = __builtin_bit_cast(u32x4, lds_read16(lds0 + AT_Q + j * 2048 + 1024 + l16));
  asm volatile("s_waitcnt lgkmcnt(0)" : "+v"(q0r), "+v"(q1r) :: "memory");
  if (zch >= 0) conv_publish(ct + CT_ZCNT + (j >> 2) * 4, zch, lane, ws, E);
  {
    const int g0 = 4 * qa + hh - 5 * h;
    const bool k0 = g0 >= 0, k1 = g0 + 2 >= 0 && g0 + 2 < 5, k2 = g0 + 4 < 5, k3 = g0 + 6 < 5;
    q0r[0] = k0 ? q0r[0] : 0u; q0r[1] = k0 ? q0r[1] : 0u; q0r[2] = k1 ? q0r[2] : 0u; q0r[3] = k1 ? q0r[3] : 0u;
    q1r[0] = k2 ? q1r[0] : 0u; q1r[1] = k2 ? q1r[1] : 0u; q1r[2] = k3 ? q1r[2] : 0u; q1r[3] = k3 ? q1r[3] : 0u;
  }
  s16x8 qlo = __builtin_bit_cast(s16x8, q0r), qhi8 = __builtin_bit_cast(s16x8, q1r);
  const f32x16 zero16 = {0.f, 0.f, 0.f, 0.f, 0.f, 0.f, 0.f, 0.f, 0.f, 0.f, 0.f, 0.f, 0.f, 0.f, 0.f, 0.f};
  const float ninf = -INFINITY;
  const f32x16 ninf16 = {ninf, ninf, ninf, ninf, ninf, ninf, ninf, ninf, ninf, ninf, ninf, ninf, ninf, ninf, ninf, ninf};
  const unsigned kb = lds0 + AT_K + j * 2048 + l16;
#define AT_QK(dst, t, cin) do { if (FULL || (t) >= tfirst) { dst = MFMA32(lds_read16(kb + (t) * 2048), qlo, cin); \
    dst = MFMA32(lds_read16(kb + (t) * 2048 + 1024), qhi8, dst); } else dst = ninf16; } while (0)
  const int vrow = l32 <= 20 ? l32 : (l32 == 24 ? 20 : l32 - 16);
  const unsigned vb = lds0 + AT_V + j * 1344 + hh * 336 + vrow * 16;
#define AT_EXP(sv) do { _Pragma("unroll") for (int r = 0; r < 16; ++r) sv[r] = __builtin_amdgcn_exp2f(sv[r] - m); } while (0)
#define AT_PV(sv, t) do { oacc = MFMA32(lds_read16(vb + (t) * 1344), pack8(sv, 0), oacc); oacc = MFMA32(lds_read16(vb + (t) * 1344 + 672), pack8(sv, 8), oacc); } while (0)
  f32x16 oacc;
  bool exact = !FULL;
  if (FULL) {
    f32x16 sm, sa, sb;
    if (j >= 4) __builtin_amdgcn_s_setprio(2);
    AT_QK(sm, 8, zero16); AT_QK(sa, 0, zero16);
    const int tq = l32 - 4 * hh;
    const unsigned wm = tq < 0 ? 0xffffffffu : (0xfffffffeu << tq);
    unsigned msk[16];
#pragma unroll
    for (int r = 0; r < 16; ++r) {
      msk[r] = (unsigned)__builtin_amdgcn_sbfe((int)wm, (r & 3) + 8 * (r >> 2), 1);
      sm[r] = __uint_as_float((msk[r] & __float_as_uint(sa[r])) | (~msk[r] & __float_as_uint(sm[r])));
    }
    const float m = xhalf_max(max16(sm, ninf));
    const float nm = -m;
    const f32x16 cneg = {nm, nm, nm, nm, nm, nm, nm, nm, nm, nm, nm, nm, nm, nm, nm, nm};
    AT_QK(sa, 1, cneg);
    AT_EXP(sm);
    u32x4 pl = __builtin_bit_cast(u32x4, pack8(sm, 0)), ph = __builtin_bit_cast(u32x4, pack8(sm, 8)), ql, qh;
#pragma unroll
    for (int i = 0; i < 4; ++i) {
      ql[i] = pl[i] & ((msk[2 * i] & 0xffffu) | (msk[2 * i + 1] & 0xffff0000u)); pl[i] ^= ql[i];
      qh[i] = ph[i] & ((msk[8 + 2 * i] & 0xffffu) | (msk[9 + 2 * i] & 0xffff0000u)); ph[i] ^= qh[i];
    }
    spin_mk(ct + CT_VR8, mk.x);
    oacc = MFMA32(lds_read16(vb + 8 * 1344), __builtin_bit_cast(s16x8, pl), zero16);
    oacc = MFMA32(lds_read16(vb + 8 * 1344 + 672), __builtin_bit_cast(s16x8, ph), oacc);
    oacc = MFMA32(lds_read16(vb), __builtin_bit_cast(s16x8, ql), oacc);
    oacc = MFMA32(lds_read16(vb + 672), __builtin_bit_cast(s16x8, qh), oacc);
#define AT_EX0(sv) do { _Pragma("unroll") for (int r = 0; r < 16; ++r) sv[r] = __builtin_amdgcn_exp2f(sv[r]); } while (0)
    AT_QK(sb, 2, cneg); AT_EX0(sa); AT_PV(sa, 1);
    AT_QK(sa, 3, cneg); AT_EX0(sb); AT_PV(sb, 2);
    if (j >= 4) __builtin_amdgcn_s_setprio(0);
    AT_QK(sb, 4, cneg); AT_EX0(sa); AT_PV(sa, 3);
    AT_QK(sa, 5, cneg); AT_EX0(sb); AT_PV(sb, 4);
    AT_QK(sb, 6, cneg); AT_EX0(sa); AT_PV(sa, 5);
    AT_QK(sa, 7, cneg); AT_EX0(sb); AT_PV(sb, 6);
    AT_EX0(sa); AT_PV(sa, 7);
    float big = max3f(__builtin_fabsf(oacc[0]), __builtin_fabsf(oacc[1]), __builtin_fabsf(oacc[2]));
    big = max3f(big, __builtin_fabsf(oacc[3]), __builtin_fabsf(oacc[4]));
    big = max3f(big, __builtin_fabsf(oacc[5]), __builtin_fabsf(oacc[6]));
    big = max3f(big, __builtin_fabsf(oacc[7]), __builtin_fabsf(oacc[8]));
    big = max3f(big, __builtin_fabsf(oacc[9]), __builtin_fabsf(oacc[10]));
    big = __builtin_fmaxf(big, __builtin_fabsf(oacc[11]));
    const float ls = hh ? oacc[8] : oacc[12];
    float osum = 0.f;
#pragma unroll
    for (int r = 0; r < 8; ++r) osum += oacc[r];
    const bool ok = (ls < AT_LMAX) && (big < 1e37f) && (osum == osum);
    exact = __builtin_amdgcn_ballot_w64(!ok) != 0ull;
  }
  if (exact) {
    asm volatile("" : "+v"(qlo), "+v"(qhi8) :: "memory");
    f32x16 sm, s1, s2, s3;
    {
      f32x16 s0;
      AT_QK(sm, 8, zero16); AT_QK(s0, 0, zero16);
      AT_QK(s1, 1, zero16); AT_QK(s2, 2, zero16); AT_QK(s3, 3, zero16);
#pragma unroll
      for (int r = 0; r < 16; ++r) {
        const int kk = (r & 3) + 8 * (r >> 2) + 4 * hh;
        sm[r] = kk > l32 ? s0[r] : sm[r];
      }
    }
    float m = max16(sm, ninf);
    m = max16(s1, m); m = max16(s2, m); m = max16(s3, m);
    m = xhalf_max(m);
    AT_EXP(sm);
    oacc = zero16;
    {
      f32x16 p0, p8;
#pragma unroll
      for (int r = 0; r < 16; ++r) {
        const int kk = (r & 3) + 8 * (r >> 2) + 4 * hh;
        p0[r] = kk > l32 ? sm[r] : 0.f;
        p8[r] = kk > l32 ? 0.f : sm[r];
      }
      AT_PV(p8, 8);
      if (FULL) AT_PV(p0, 0);
    }
    if (FULL || 1 >= tfirst) { AT_EXP(s1); AT_PV(s1, 1); }
    AT_QK(sm, 4, zero16); AT_QK(s1, 5, zero16);
    if (FULL || 2 >= tfirst) { AT_EXP(s2); AT_PV(s2, 2); }
    AT_QK(s2, 6, zero16);
    if (FULL || 3 >= tfirst) { AT_EXP(s3); AT_PV(s3, 3); }
    AT_QK(s3, 7, zero16);
    float m2 = max16(sm, m);
    m2 = max16(s1, m2); m2 = max16(s2, m2); m2 = max16(s3, m2);
    m2 = xhalf_max(m2);
    {
      const float alpha = __builtin_amdgcn_exp2f(m - m2);
#pragma unroll
      for (int r = 0; r < 13; ++r) oacc[r] *= alpha;
      m = m2;
    }
    if (FULL || 4 >= tfirst) { AT_EXP(sm); AT_PV(sm, 4); }
    if (FULL || 5 >= tfirst) { AT_EXP(s1); AT_PV(s1, 5); }
    if (FULL || 6 >= tfirst) { AT_EXP(s2); AT_PV(s2, 6); }
    if (FULL || 7 >= tfirst) { AT_EXP(s3); AT_PV(s3, 7); }
  }
  asm volatile("s_waitcnt lgkmcnt(0)" ::: "memory");
  sig(ct + CT_ATT + j * 4, 1u, lane);
  const float l = hh ? oacc[8] : oacc[12];
  const float inv = __builtin_amdgcn_rcpf(l);
  char* oc = ws + WS_OW + ((size_t)qt * NH + h) * 1280;
  u32x4 o;
  o[0] = pk_bf16(oacc[0] * inv, oacc[1] * inv); o[1] = pk_bf16(oacc[2] * inv, oacc[3] * inv);
  o[2] = pk_bf16(oacc[4] * inv, oacc[5] * inv); o[3] = pk_bf16(oacc[6] * inv, oacc[7] * inv);
  gstore16c(oc + lane * 16, o);
  if (!hh) {
    u32x2 o2;
    o2[0] = pk_bf16(oacc[8] * inv, oacc[9] * inv); o2[1] = pk_bf16(oacc[10] * inv, oacc[11] * inv);
    gstore8c(oc + 1024 + l32 * 8, o2);
  }
  publish(ws, FL_O + (size_t)(qt * NH + h) * 8, E, lane);
}

#define APRIO 1
#define LPRIO 0
#define MPRIO 2
#define ROTG 2
__global__ __launch_bounds__(768, 3) void mega(const u16* __restrict__ x, const u16* __restrict__ wqkv,
                                               const u16* __restrict__ ak, const u16* __restrict__ av,
                                               const u16* __restrict__ shk, const u16* __restrict__ wo,
                                               const u16* __restrict__ ssmk, const u16* __restrict__ ssmd,
                                               const u16* __restrict__ aq, char* __restrict__ ws,
                                               u16* __restrict__ out) {
  extern __shared__ __attribute__((aligned(16))) char smem[];
  const unsigned lds0 = lds_addr(smem);
  const int tid = threadIdx.x, wave = __builtin_amdgcn_readfirstlane(tid >> 6);
  int lane = tid & 63;
#define RELANE() asm volatile("" : "+v"(lane))
  const int role = wave >> 2, w = wave & 3;
  const int bid = blockIdx.x, xcd = bid & 7, idx = bid >> 3;
  const bool has_gemm = idx < 30;
  const unsigned ct = lds0 + L_CT;
  u64 E;
  Mk mk; mk.src = wqkv;
  asm volatile("s_load_dwordx2 %0, %2, 0x0\n\ts_load_dword %1, %3, 0x0" : "=&s"(E), "=&s"(mk.x) : "s"(ws + WS_FL), "s"(wqkv) : "memory");
  const int gt = (xcd & 1) * 30 + idx;
  const int tm = (xcd >> 1) * 4 + (gt & 3), tn = gt >> 2;
  const int m0 = tm * 128, n0 = tn * 128;
  const int rot = ROTG >= 10 ? (((gt & 3) * 5 + 1) / 2 + 3 * tn) % 10 : ((((gt & 3) + tn) % ROTG) * 10) / ROTG;
  const unsigned xo0 = (unsigned)((lane >> 3) * DM * 2 + (((lane & 7) ^ (lane >> 4)) * 16));
  const unsigned xo1 = (unsigned)((lane >> 3) * DM * 2 + (((lane & 7) ^ (4 + (lane >> 4))) * 16));
  const unsigned wof = (unsigned)((lane >> 4) * NQKV * 2 + (((lane & 15) ^ ((lane >> 4) << 2)) * 16));
  const char* xg = (const char*)(x + (size_t)m0 * DM);
  const char* wg = (const char*)(wqkv + n0);
#define K1_PIECE_X(t, kt, j) glds16(((j) & 1) ? xo1 : xo0, xg + (size_t)(j) * 8 * DM * 2 + (kt) * 128, lds0 + ((t) & 3) * K1_STAGE + (j) * 1024)
#define K1_PIECE_W(t, kt, j) glds16(wof, wg + (size_t)((kt) * 64 + (j) * 4) * NQKV * 2, lds0 + ((t) & 3) * K1_STAGE + 16384 + (j) * 1024)
  if (has_gemm && role == 0) {
#pragma unroll
    for (int i = 0; i < 4; ++i) { K1_PIECE_X(0, rot, w * 4 + i); K1_PIECE_W(0, rot, w * 4 + i); }
  }
  const unsigned pf = ct + CT_PAIR + (wave >> 1) * 8;
  const int wh = wave & 1;
  unsigned ps = 0;
  const int fb = (idx - 30) * 8 + xcd;
  const int a1t = has_gemm ? (xcd * 30 + idx) * 2 + (w >> 1) : 480 + fb * 10 + (wave >> 1);
  const unsigned a1l = has_gemm ? lds0 + L_AUX + (w >> 1) * A1_PAIR : lds0 + (wave >> 1) * A1_PAIR;
  if (!has_gemm || role == 2) a1_issue(a1l, a1t, lane, wh, ak, av, shk);
  const bool cv0 = role != 0 || !has_gemm || bid < 112;
  const int ch0 = role == 0 ? (has_gemm ? 528 + bid : 512 + fb) : bid + (role == 1 ? 256 : 0);
  asm volatile("s_waitcnt lgkmcnt(0)" : "+s"(E), "+s"(mk.x) :: "memory");
  if (role == 1) lds_st32(ct + (tid - 256) * 4, (tid - 256) * 4 >= CT_FX ? ~mk.x : 0u);
  bar();
  E += 1ull;

  const bool ex = has_gemm && role == 1 && bid < 112;
  HRegs hr, h2;
#define LOAD_H() do { hr = conv_load_h(ch0, w * 64 + lane, ssmk, ssmd); \
    if (ex) h2 = conv_load_h(528 + bid, w * 64 + lane, ssmk, ssmd); } while (0)
  auto q_publish = [&]() {
    asm volatile("s_waitcnt vmcnt(0)" ::: "memory");
    unsigned old = 0;
    if (lane == 0) old = __hip_atomic_fetch_add((LAS unsigned*)(uintptr_t)(ct + CT_QCNT), 1u, __ATOMIC_RELAXED, __HIP_MEMORY_SCOPE_WORKGROUP);
    if (__builtin_amdgcn_readfirstlane(old) == 3u) {
      const int c0 = (n0 % DM) - 32, hh_ = lane;
      if (lane < QREP && hh_ * 20 + 40 > c0 && hh_ * 20 < c0 + 32 + 128 + 20)
        __hip_atomic_store((u64*)(ws + FL_Q + (size_t)((lane * 16 + tm) * 16 + tn) * 8), E, __ATOMIC_RELAXED, __HIP_MEMORY_SCOPE_AGENT);
    }
  };
  if (!has_gemm) {
    LOAD_H();
    const unsigned a2l = lds0 + (6 + (wave >> 1)) * A1_PAIR;
    if (wave < 8) a1_issue(a2l, a1t + 6, lane, wh, ak, av, shk);
    if (wave < 8) wait_vm<6>(); else wait_vm<0>();
    a1_finish(a1l, pf, ps, a1t, lane, wh, ws, E);
    if (wave < 8) { wait_vm<2>(); a1_finish(a2l, pf, ps, a1t + 6, lane, wh, ws, E); }
    a1_publish(a1t, lane, wh, ws, E);
    if (wave < 8) a1_publish(a1t + 6, lane, wh, ws, E);
    bar();
  } else if (role == 0) {
    wait_vm<0>();
    sig(ct + CT_S0 + w * 4, 1u, lane);
    spin_ge4(ct + CT_S0, 1u);
    __builtin_amdgcn_s_setprio(MPRIO);
    if (tn >= 10) k1_gemm<true>(lds0, m0, n0, tid, ws, mk.x);
    else k1_gemm<false>(lds0, m0, n0, tid, ws, mk.x);
  } else if (role == 1) {
    __builtin_amdgcn_s_setprio(LPRIO);
#define K1_ST(t) do { const int kt_ = (t) + rot >= 10 ? (t) + rot - 10 : (t) + rot; \
      if ((t) >= 4) spin_ge4(ct + CT_FREE + ((t) & 3) * 16, (unsigned)((t) - 3)); \
      _Pragma("unroll") for (int i_ = 0; i_ < 4; ++i_) { K1_PIECE_X(t, kt_, w * 4 + i_); K1_PIECE_W(t, kt_, w * 4 + i_); } } while (0)
#pragma nounroll
    for (int t = 1; t < 4; ++t) { K1_ST(t); glds_mark(mk, ct + CT_FX + t * 16 + w * 4, lane); }
    LOAD_H();
#pragma nounroll
    for (int t = 4; t < 10; ++t) { K1_ST(t); glds_mark(mk, ct + CT_FX + t * 16 + w * 4, lane); }
    __builtin_amdgcn_s_setprio(0);
  } else {
    LOAD_H();
    __builtin_amdgcn_s_setprio(2);
    wait_vm<0>();
    a1_finish(a1l, pf, ps, a1t, lane, wh, ws, E);
    a1_publish(a1t, lane, wh, ws, E);
    __builtin_amdgcn_s_setprio(0);
  }

  RELANE();
  unsigned qs = 0;
  const unsigned qf = ct + CT_QUAD + role * 16;
  const unsigned creg = lds0 + (role == 2 ? L_AUX : (role == 1 ? 29184 : 0));
  if (role == 2) __builtin_amdgcn_s_setprio(APRIO); else __builtin_amdgcn_s_setprio(0);
  if (has_gemm && role == 0) {
    q_publish();
    if (cv0) {
      hr.d = ssmd[ch0];
      conv_run(creg, qf, qs, ch0, lane, w, hr, ws, ct + CT_XST);
    }
  } else {
    if (has_gemm && role == 1) {
      conv_load_u(hr, ch0, lane, w, ws, E);
      if (ex) conv_load_u(h2, 528 + bid, lane, w, ws, E);
      spin_ge4(ct + CT_FREE + 16, 10u);
      conv_stage(creg, lane, w, hr);
      if (ex) {
        conv_stage(lds0, lane, w, h2);
        lds_write16(lds0 + (w * 64 + lane) * 16, h2.u);
        asm volatile("s_waitcnt lgkmcnt(0)" ::: "memory");
        sig(ct + CT_XST + w * 4, 1u, lane);
      }
    } else {
      if (has_gemm) { const int wq = w; QSYNC(); }
      conv_stage(creg, lane, w, hr);
      conv_load_u(hr, ch0, lane, w, ws, E);
    }
    conv_run(creg, qf, qs, ch0, lane, w, hr, ws, 0u);
  }
  sig(ct + CT_CVD + wave * 4, 1u, lane);

  const int tm3 = xcd * 4 + (idx >> 3), tn3 = idx & 7;
  const int m3 = tm3 * 64, n3 = tn3 * 80;
  RELANE();

  if (role == 2) {
    const int r16 = lane & 15, g = lane >> 4, q = r16 >> 2, p = lane & 3;
    __builtin_amdgcn_s_setprio(3);
    spin_ge4(ct + CT_CVD + 32, 1u);
    if (has_gemm) spin_ge4(ct + CT_FREE + 16, 10u);
    attn_stage(lds0, xcd * 4 + (idx & 3), idx >> 2, w, lane, ws, E, ch0, mk);
    {
      P3_WADDR();
      P3_STW(5);
      spin_ge8(ct + CT_CVD, 1u);
      P3_STW(0); P3_STW(4); P3_STW(1); P3_STW(2); P3_STW(3);
      glds_mark(mk, ct + CT_WRDY + w * 4, lane);
    }
    __builtin_amdgcn_s_setprio(0);
    u32x4 qv[3];
#pragma unroll
    for (int j = 0; j < 3; ++j) {
      const int dc = j < 2 ? j * 4 + g : 8 + (g & 1);
      qv[j] = *((const u32x4*)(aq + (size_t)(m3 + w * 16 + r16) * DM + n3 + dc * 8));
    }
    {
      const int l5 = lane & 31;
      const int cl = l5 < 8 ? w * 8 + l5 : (l5 < 16 ? 32 + w * 8 + (l5 - 8) : 64 + w * 4 + (l5 & 3));
      poll<ZSLEEP>(ws, FL_Z + (size_t)(n3 + cl) * 8, E);
    }
    {
      const char* zg = ws + WS_ZT + ((size_t)n3 * SEQ + m3) * 2;
      const unsigned zo = (unsigned)((lane >> 3) * SEQ * 2 + (lane & 7) * 16);
      glds16c(zo, zg + (size_t)(w * 8) * SEQ * 2, lds0 + L_AUX + w * 1024);
      glds16c(zo, zg + (size_t)(32 + w * 8) * SEQ * 2, lds0 + L_AUX + 4096 + w * 1024);
      if (lane < 32) glds16c(zo, zg + (size_t)(64 + w * 4) * SEQ * 2, lds0 + L_AUX + 8192 + w * 512);
    }
    wait_vm<0>();
    sig(ct + CT_ZRDY + w * 4, 1u, lane);
    spin_ge4(ct + CT_ZRDY, 1u);
    u32x4 zz[3];
#pragma unroll
    for (int j = 0; j < 3; ++j) {
      const int dc = j < 2 ? j * 4 + g : 8 + (g & 1);
      const unsigned za = lds0 + L_AUX + (dc * 8 + q) * 128 + w * 32 + p * 8;
      zz[j] = __builtin_bit_cast(u32x4, cat8(tr_read(za), tr_read(za + 512)));
    }
#pragma unroll
    for (int j = 0; j < 3; ++j) {
      const int dc = j < 2 ? j * 4 + g : 8 + (g & 1);
      u32x4 o;
#pragma unroll
      for (int i = 0; i < 4; ++i) o[i] = pk_bf16(bf_lo(qv[j][i]) * bf_lo(zz[j][i]), bf_hi(qv[j][i]) * bf_hi(zz[j][i]));
      if (j < 2 || g < 2) ostore16(out + (size_t)(m3 + w * 16 + r16) * (2 * DM) + n3 + dc * 8, o);
    }
    if (bid == 0 && w == 0) {
#pragma unroll
      for (int i = 0; i < 4; ++i) poll(ws, FL_O + (size_t)(((i * 2 + (lane >> 5)) * 8) * NH + (lane & 31)) * 8, E);
      if (lane == 0) __hip_atomic_store((u64*)(ws + WS_FL), E, __ATOMIC_RELAXED, __HIP_MEMORY_SCOPE_AGENT);
    }
    return;
  }

  const int h = xcd * 4 + (idx & 3), qb8 = idx >> 2;
  __builtin_amdgcn_s_setprio(1);
  RELANE();
  if (qb8 > 0) attn_task<true>(lds0, h, qb8, wave, lane, ws, E, cv0 ? ch0 : -1, mk);
  else attn_task<false>(lds0, h, qb8, wave, lane, ws, E, cv0 ? ch0 : -1, mk);

  RELANE();
  const int r16 = lane & 15, g = lane >> 4, q = r16 >> 2, p = lane & 3;
  f32x4 acc[5];
#pragma unroll
  for (int i = 0; i < 5; ++i) acc[i] = (f32x4){0.f, 0.f, 0.f, 0.f};
  const unsigned wbp = lds0 + (20 * (g >> 1) + 4 * (g & 1) + q) * 160 + p * 8;
  const unsigned wbs = lds0 + (20 * g + 16 + q) * 160 + p * 8;
  u32x4 op[8]; u32x2 osg[4];
  if (role == 1) {
    P3_WADDR();
    spin_ge8(ct + CT_ATT, 1u);
    P3_STW(6); P3_STW(7);
    glds_mark(mk, ct + CT_WRD2 + w * 4, lane);
  }
  spin_mk(ct + CT_WRDY, mk.x);
  const unsigned w0 = role ? p3_w(4) : p3_w(0), w1 = role ? p3_w(5) : p3_w(1), w2 = role ? p3_w(6) : p3_w(2), w3 = role ? p3_w(7) : p3_w(3);
  struct Fw { s16x8 a[5], b[5]; s16x4 s[5]; };
  Fw fwa, fwb;
#define P3_LOADW(F, bo_) do { _Pragma("unroll") for (int nt = 0; nt < 5; ++nt) { \
      F.a[nt] = cat8(tr_read(wbp + (bo_) + nt * 32), tr_read(wbp + (bo_) + 8 * 160 + nt * 32)); \
      F.b[nt] = cat8(tr_read(wbp + (bo_) + 40 * 160 + nt * 32), tr_read(wbp + (bo_) + 48 * 160 + nt * 32)); \
      F.s[nt] = tr_read(wbs + (bo_) + nt * 32); } } while (0)
#define P3_MM(F, t) do { \
    _Pragma("unroll") for (int nt = 0; nt < 5; ++nt) acc[nt] = MFMA16(F.a[nt], __builtin_bit_cast(s16x8, op[2 * (t)]), acc[nt]); \
    _Pragma("unroll") for (int nt = 0; nt < 5; ++nt) acc[nt] = MFMA16(F.b[nt], __builtin_bit_cast(s16x8, op[2 * (t) + 1]), acc[nt]); } while (0)
#define P3_MS(t) do { const s16x8 os_ = cat8(__builtin_bit_cast(s16x4, osg[t]), __builtin_bit_cast(s16x4, osg[(t) + 1])); \
    _Pragma("unroll") for (int nt = 0; nt < 5; ++nt) acc[nt] = MFMA16(cat8(fwa.s[nt], fwb.s[nt]), os_, acc[nt]); } while (0)
  P3_LOADW(fwa, w0);
  SBAR();
  poll<OSLEEP>(ws, FL_O + (size_t)(((m3 >> 5) + (w >> 1)) * NH + role * 16 + r16) * 8, E);
  {
    const int orow = (w & 1) * 16 + r16;
    const char* ob = ws + WS_OW + ((size_t)((m3 >> 5) + (w >> 1)) * NH + role * 16) * 1280;
    const char* pp = ob + (g >> 1) * 1280 + (g & 1) * 512 + orow * 16;
    const char* sp = ob + g * 1280 + 1024 + orow * 8;
#pragma unroll
    for (int t = 0; t < 4; ++t) {
      asm volatile("global_load_dwordx4 %0, %3, off sc1\n\tglobal_load_dwordx4 %1, %3, off offset:2560 sc1\n\tglobal_load_dwordx2 %2, %4, off sc1"
                   : "=&v"(op[2 * t]), "=&v"(op[2 * t + 1]), "=&v"(osg[t]) : "v"(pp + t * 5120), "v"(sp + t * 5120) : "memory");
    }
  }
  P3_LOADW(fwb, w1);
  if (role == 1) spin_mk(ct + CT_WRD2, mk.x);
  SBAR();
  asm volatile("s_waitcnt vmcnt(6)" : "+v"(op[0]), "+v"(op[1]), "+v"(op[2]), "+v"(op[3]), "+v"(osg[0]), "+v"(osg[1]) :: "memory");
  P3_MS(0); P3_MM(fwa, 0);
  SBAR();
  P3_LOADW(fwa, w2); P3_MM(fwb, 1);
#pragma unroll
  for (int i = 0; i < 10; ++i) { __builtin_amdgcn_sched_group_barrier(0x008, 1, 0); __builtin_amdgcn_sched_group_barrier(0x100, 3, 0); }
  SBAR();
  asm volatile("s_waitcnt vmcnt(0)" : "+v"(op[4]), "+v"(op[5]), "+v"(op[6]), "+v"(op[7]), "+v"(osg[2]), "+v"(osg[3]) :: "memory");
  P3_LOADW(fwb, w3); P3_MM(fwa, 2);
#pragma unroll
  for (int i = 0; i < 10; ++i) { __builtin_amdgcn_sched_group_barrier(0x008, 1, 0); __builtin_amdgcn_sched_group_barrier(0x100, 3, 0); }
  SBAR();
  P3_MM(fwb, 3); P3_MS(2);
  SBAR();
  const unsigned rb = lds0 + AT_K + 25600 + w * 5120 + lane * 16;
  if (role == 1) {
#pragma unroll
    for (int nt = 0; nt < 5; ++nt) lds_write16(rb + nt * 1024, __builtin_bit_cast(u32x4, acc[nt]));
    asm volatile("s_waitcnt lgkmcnt(0)" ::: "memory");
    sig(ct + CT_RED + w * 4, 1u, lane);
    return;
  }
  spin_ge(ct + CT_RED + w * 4, 1u);
  u16* orow_g = out + (size_t)(m3 + w * 16 + r16) * (2 * DM) + DM + n3;
  u32x2 ov[5];
#pragma unroll
  for (int nt = 0; nt < 5; ++nt) {
    const f32x4 o2 = acc[nt] + __builtin_bit_cast(f32x4, lds_read16(rb + nt * 1024));
    ov[nt][0] = pk_bf16(o2[0], o2[1]);
    ov[nt][1] = pk_bf16(o2[2], o2[3]);
  }
#pragma unroll
  for (int pr = 0; pr < 2; ++pr) {
    const auto sx = __builtin_amdgcn_permlane16_swap(ov[2 * pr][0], ov[2 * pr + 1][0], false, false);
    const auto sy = __builtin_amdgcn_permlane16_swap(ov[2 * pr][1], ov[2 * pr + 1][1], false, false);
    ostore16(orow_g + (2 * pr + (g & 1)) * 16 + 8 * (g >> 1), (u32x4){sx[0], sy[0], sx[1], sy[1]});
  }
  ostore8(orow_g + 64 + 4 * g, ov[4]);
}

extern "C" void kernel_launch(void* const* d_in, const int* in_sizes, int n_in, void* d_out, int out_size,
                              void* d_ws, size_t ws_size, hipStream_t stream) {
  const u16* aq = (const u16*)d_in[0];
  const u16* ak = (const u16*)d_in[1];
  const u16* av = (const u16*)d_in[2];
  const u16* shk = (const u16*)d_in[3];
  const u16* ssmk = (const u16*)d_in[4];
  const u16* ssmd = (const u16*)d_in[5];
  const u16* x = (const u16*)d_in[6];
  const u16* wqkv = (const u16*)d_in[7];
  const u16* wo = (const u16*)d_in[8];
  (void)hipFuncSetAttribute((const void*)mega, hipFuncAttributeMaxDynamicSharedMemorySize, L_TOTAL);
  mega<<<256, 768, L_TOTAL, stream>>>(x, wqkv, ak, av, shk, wo, ssmk, ssmd, aq, (char*)d_ws, (u16*)d_out);
}
```

```cpp
#include <hip/hip_runtime.h>
#include <hip/hip_bf16.h>
#include <stdint.h>

typedef unsigned short u16;
typedef short s16x4 __attribute__((ext_vector_type(4)));
typedef short s16x8 __attribute__((ext_vector_type(8)));
typedef float f32x2 __attribute__((ext_vector_type(2)));
typedef float f32x4 __attribute__((ext_vector_type(4)));
typedef float f32x16 __attribute__((ext_vector_type(16)));
typedef unsigned u32x2 __attribute__((ext_vector_type(2)));
typedef unsigned u32x4 __attribute__((ext_vector_type(4)));
typedef __bf16 bf16x2 __attribute__((ext_vector_type(2)));
#define LAS __attribute__((address_space(3)))
#define DI __device__ __forceinline__

constexpr int SEQ = 2048, DM = 640, NQKV = 1920, NH = 32;
constexpr size_t WS_QF = 0;
constexpr size_t WS_KF = WS_QF + (size_t)DM * SEQ * 2;
constexpr size_t WS_VF = WS_KF + (size_t)DM * SEQ * 2;
constexpr size_t WS_UT = WS_VF + (size_t)DM * SEQ * 2;
constexpr size_t WS_ZT = WS_UT + (size_t)DM * SEQ * 2;
constexpr size_t WS_OW = WS_ZT + (size_t)DM * SEQ * 2;
typedef unsigned long long u64;
constexpr size_t WS_FL = WS_OW + (size_t)DM * SEQ * 2;
constexpr size_t FL_U = WS_FL + 256;
constexpr int QREP = 32;
constexpr size_t FL_Q = FL_U + 20 * 64 * 8;
constexpr size_t FL_Z = FL_Q + QREP * 16 * 16 * 8;
constexpr size_t FL_O = FL_Z + 640 * 8;

DI unsigned pk_bf16(float lo, float hi) {
  f32x2 f = {lo, hi};
  return __builtin_bit_cast(unsigned, __builtin_convertvector(f, bf16x2));
}
DI float bf_lo(unsigned u) { return __uint_as_float(u << 16); }
DI float bf_hi(unsigned u) { return __uint_as_float(u & 0xffff0000u); }
DI float rbf(float x) { return bf_lo(pk_bf16(x, 0.f)); }

DI unsigned lds_addr(const void* p) { return (unsigned)(uintptr_t)(LAS const char*)p; }

DI void glds16(unsigned voff, const void* sbase, unsigned lds_dst) {
  unsigned keep;
  asm volatile("s_mov_b32 %0, m0\n\ts_mov_b32 m0, %3\n\ts_nop 0\n\tglobal_load_lds_dwordx4 %1, %2\n\ts_mov_b32 m0, %0"
               : "=&s"(keep) : "v"(voff), "s"(sbase), "s"(lds_dst) : "memory");
}
#define LDPOL "sc1"
DI void glds16c(unsigned voff, const void* sbase, unsigned lds_dst) {
  unsigned keep;
  asm volatile("s_mov_b32 %0, m0\n\ts_mov_b32 m0, %3\n\ts_nop 0\n\tglobal_load_lds_dwordx4 %1, %2 " LDPOL "\n\ts_mov_b32 m0, %0"
               : "=&s"(keep) : "v"(voff), "s"(sbase), "s"(lds_dst) : "memory");
}
struct Mk { const void* src; unsigned x; };
DI void glds_mark(const Mk& mk, unsigned lds_dst, int lane) {
  if (lane == 0) {
    unsigned keep;
    asm volatile("s_mov_b32 %0, m0\n\ts_mov_b32 m0, %3\n\ts_nop 0\n\tglobal_load_lds_dword %1, %2\n\ts_mov_b32 m0, %0"
                 : "=&s"(keep) : "v"(0u), "s"(mk.src), "s"(lds_dst) : "memory");
  }
}
DI float dot2bf(unsigned a, unsigned b, float c) {
  return __builtin_amdgcn_fdot2_f32_bf16(__builtin_bit_cast(bf16x2, a), __builtin_bit_cast(bf16x2, b), c, false);
}
DI void gstore16c(void* p, u32x4 v) {
  asm volatile("global_store_dwordx4 %0, %1, off sc1\n\ts_nop 1" :: "v"(p), "v"(v) : "memory");
}
DI void gstore8c(void* p, u32x2 v) {
  asm volatile("global_store_dwordx2 %0, %1, off sc1" :: "v"(p), "v"(v) : "memory");
}
#define OUTPOL "nt"
DI void ostore16(void* p, u32x4 v) { asm volatile("global_store_dwordx4 %0, %1, off " OUTPOL "\n\ts_nop 1" :: "v"(p), "v"(v) : "memory"); }
DI void ostore8(void* p, u32x2 v) { asm volatile("global_store_dwordx2 %0, %1, off " OUTPOL :: "v"(p), "v"(v) : "memory"); }
template <int N> DI void wait_vm() { asm volatile("s_waitcnt vmcnt(%0)" :: "n"(N) : "memory"); }
DI void bar() { asm volatile("s_waitcnt lgkmcnt(0)\n\ts_barrier" ::: "memory"); }
DI s16x4 tr_read(unsigned a) {
  return __builtin_amdgcn_ds_read_tr16_b64_v4i16((LAS s16x4*)(uintptr_t)a);
}
DI s16x8 lds_read16(unsigned a) { return *(LAS const s16x8*)(uintptr_t)a; }
DI s16x4 lds_read8(unsigned a) { return *(LAS const s16x4*)(uintptr_t)a; }
DI void lds_write16(unsigned a, u32x4 v) { *(LAS u32x4*)(uintptr_t)a = v; }
DI s16x8 cat8(s16x4 a, s16x4 b) { return __builtin_shufflevector(a, b, 0, 1, 2, 3, 4, 5, 6, 7); }
#define MFMA16(a, b, c) __builtin_amdgcn_mfma_f32_16x16x32_bf16((a), (b), (c), 0, 0, 0)
#define MFMA32(a, b, c) __builtin_amdgcn_mfma_f32_32x32x16_bf16((a), (b), (c), 0, 0, 0)

#define SBAR() __builtin_amdgcn_sched_barrier(0)
#define SPIN_SLEEP 1
#define SPIN_MAX (1 << 21)
DI unsigned lds_ld32(unsigned a) { return *(volatile LAS unsigned*)(uintptr_t)a; }
DI void lds_st32(unsigned a, unsigned v) { *(volatile LAS unsigned*)(uintptr_t)a = v; }
DI void spin_ge(unsigned a, unsigned want) {
#pragma nounroll
  for (int it = 0; it < SPIN_MAX; ++it) {
    if ((unsigned)__builtin_amdgcn_readfirstlane(lds_ld32(a)) >= want) break;
    __builtin_amdgcn_s_sleep(1);
  }
  asm volatile("" ::: "memory");
}
DI void spin_ge4(unsigned a, unsigned want) {
#pragma nounroll
  for (int it = 0; it < SPIN_MAX; ++it) {
    const u32x4 v = *(volatile LAS u32x4*)(uintptr_t)a;
    const unsigned m = min(min(v[0], v[1]), min(v[2], v[3]));
    if ((unsigned)__builtin_amdgcn_readfirstlane(m) >= want) break;
    __builtin_amdgcn_s_sleep(SPIN_SLEEP);
  }
  asm volatile("" ::: "memory");
}
DI unsigned mk_bad(const u32x4& v, unsigned x) { return (v[0] ^ x) | (v[1] ^ x) | (v[2] ^ x) | (v[3] ^ x); }
DI void spin_mk(unsigned a, unsigned x) {
#pragma nounroll
  for (int it = 0; it < SPIN_MAX; ++it) {
    const u32x4 v = *(volatile LAS u32x4*)(uintptr_t)a;
    if ((unsigned)__builtin_amdgcn_readfirstlane(mk_bad(v, x)) == 0u) break;
    __builtin_amdgcn_s_sleep(SPIN_SLEEP);
  }
  asm volatile("" ::: "memory");
}
DI void spin_ge8(unsigned a, unsigned want) { spin_ge4(a, want); spin_ge4(a + 16, want); }
constexpr int CT_S0 = 0, CT_FREE = 80, CT_PAIR = 144  , CT_QUAD = 192  , CT_ZCNT = 240  ,
              CT_QCNT = 256, CT_ZRDY = 304, CT_CVD = 336  , CT_XST = 416, CT_ATT = 448, CT_RED = 480,
              CT_FX = 512  , CT_KR8 = 688, CT_VR8 = 704, CT_WRDY = 720, CT_WRD2 = 736, CT_BYTES = 1024;
DI void sig(unsigned a, unsigned v, int lane) { if (lane == 0) lds_st32(a, v); }

#define POLL_MAX (1 << 15)
DI void publish(char* ws, size_t off, u64 E, int lane) {
  asm volatile("s_waitcnt vmcnt(0)" ::: "memory");
  if (lane == 0) __hip_atomic_store((u64*)(ws + off), E, __ATOMIC_RELAXED, __HIP_MEMORY_SCOPE_AGENT);
}
#define OSLEEP 10
#define ZSLEEP 20
#define USLEEP 10
#define POLL_SLEEP 6
template <int SL = POLL_SLEEP>
DI void poll(const char* ws, size_t off, u64 E) {
#pragma nounroll
  for (int it = 0; it < POLL_MAX; ++it) {
    const u64 v = __hip_atomic_load((const u64*)(ws + off), __ATOMIC_RELAXED, __HIP_MEMORY_SCOPE_AGENT);
    if (__builtin_amdgcn_ballot_w64(v != E) == 0ull) break;
    __builtin_amdgcn_s_sleep(SL);
  }
  asm volatile("" ::: "memory");
}

constexpr int K1_STAGE = 32768;
constexpr int A1_K = 0, A1_V = 3072, A1_W = 5120, A1_WAVE = 5632, A1_O = 2 * A1_WAVE, A1_PAIR = A1_O + 4096;
constexpr int L_AUX = 4 * K1_STAGE;
constexpr int L_CT = L_AUX + 2 * A1_PAIR;
constexpr int L_TOTAL = L_CT + CT_BYTES;
constexpr int K1_CT = L_CT;
constexpr int CV_COPY = 4160, CV_H = 4608, CV_REG = CV_H + 24576;
constexpr int AT_Q = L_AUX + 12288, AT_K = 64000, AT_V = AT_K + 16 * 2048, AT_END = AT_V + 16 * 1344;
constexpr int p3_w(int t) { return t < 5 ? t * 12800 : (t == 5 ? AT_END : AT_K + (t - 6) * 12800); }

#define PSYNC() do { asm volatile("s_waitcnt lgkmcnt(0)" ::: "memory"); ++ps; sig(pf + wh * 4, ps, lane); spin_ge(pf + (wh ^ 1) * 4, ps); } while (0)
DI void a1_issue(unsigned lds, int tile, int lane, int wh, const u16* ak, const u16* av, const u16* shk) {
  const int ts = tile & 63, td = tile >> 6;
  const int s0 = ts * 32 + wh * 16, d0 = td * 64;
  const int lr = lane >> 3, lc = (lane & 7) * 16;
  const unsigned wl = lds + wh * A1_WAVE;
#pragma unroll
  for (int i = 0; i < 3; ++i) {
    int r = s0 - 3 + i * 8 + lr; r = r < 0 ? 0 : r;
    if (i < 2 || lane < 24) glds16((unsigned)(r * DM * 2 + lc), (const char*)ak + d0 * 2, wl + A1_K + i * 1024);
  }
#pragma unroll
  for (int i = 0; i < 2; ++i)
    glds16((unsigned)(lr * DM * 2 + lc), (const char*)av + ((size_t)(s0 + i * 8) * DM + d0) * 2, wl + A1_V + i * 1024);
  if (lane < 32) glds16((unsigned)(lane * 16), (const char*)shk + d0 * 8, wl + A1_W);
}
DI void a1_finish(unsigned lds, unsigned pf, unsigned& ps, int tile, int lane, int wh, char* ws, u64 E) {
  const int ts = tile & 63, td = tile >> 6;
  const int c = lane & 7;
  const unsigned wl = lds + wh * A1_WAVE;
  u32x4 wt[4];
#pragma unroll
  for (int i = 0; i < 4; ++i) wt[i] = __builtin_bit_cast(u32x4, lds_read16(wl + A1_W + c * 64 + i * 16));
#pragma unroll
  for (int j = 0; j < 2; ++j) {
    const int r = j * 8 + (lane >> 3);
    const unsigned ka = wl + A1_K + (r + 3) * 128 + c * 16;
    u32x4 k0 = __builtin_bit_cast(u32x4, lds_read16(ka)), k1 = __builtin_bit_cast(u32x4, lds_read16(ka - 128));
    u32x4 k2 = __builtin_bit_cast(u32x4, lds_read16(ka - 256)), k3 = __builtin_bit_cast(u32x4, lds_read16(ka - 384));
    const u32x4 vv = __builtin_bit_cast(u32x4, lds_read16(wl + A1_V + r * 128 + c * 16));
    if (j == 0 && ts == 0 && wh == 0) {
      const u32x4 z = {0u, 0u, 0u, 0u};
      if (r < 1) k1 = z;
      if (r < 2) k2 = z;
      if (r < 3) k3 = z;
    }
    u32x4 o;
#pragma unroll
    for (int i = 0; i < 4; ++i) {
      const unsigned pa0 = __builtin_amdgcn_perm(k1[i], k0[i], 0x05040100u), pb0 = __builtin_amdgcn_perm(k3[i], k2[i], 0x05040100u);
      const unsigned pa1 = __builtin_amdgcn_perm(k1[i], k0[i], 0x07060302u), pb1 = __builtin_amdgcn_perm(k3[i], k2[i], 0x07060302u);
      const float e0 = dot2bf(pb0, wt[i][1], dot2bf(pa0, wt[i][0], 0.f));
      const float e1 = dot2bf(pb1, wt[i][3], dot2bf(pa1, wt[i][2], 0.f));
      o[i] = pk_bf16(e0 * bf_lo(vv[i]), e1 * bf_hi(vv[i]));
    }
    lds_write16(lds + A1_O + (31 - wh * 16 - r) * 128 + c * 16, o);
  }
  PSYNC();
  const int gg = lane >> 4, q = (lane & 15) >> 2, p = lane & 3;
  u16* ut = (u16*)(ws + WS_UT);
#pragma unroll
  for (int j = 0; j < 2; ++j) {
    const unsigned ra = lds + A1_O + (gg * 8 + q) * 128 + wh * 64 + j * 32 + p * 8;
    const s16x4 t0 = tr_read(ra), t1 = tr_read(ra + 512);
    const int d = td * 64 + wh * 32 + j * 16 + (lane & 15);
    gstore16c(ut + (size_t)d * SEQ + (SEQ - 32 - ts * 32) + gg * 8, __builtin_bit_cast(u32x4, cat8(t0, t1)));
  }
}
DI void a1_publish(int tile, int lane, int wh, char* ws, u64 E) {
  const int ts = tile & 63, td = tile >> 6;
  publish(ws, FL_U + (size_t)((td * 2 + wh) * 64 + ts) * 8, E, lane);
}

template <bool VT>
DI void k1_gemm(unsigned lds0, int m0, int n0, int tid, char* ws, unsigned mkx) {
  const int lane = tid & 63, wave = __builtin_amdgcn_readfirstlane(tid >> 6);
  const int wm = wave >> 1, wn = wave & 1;
  const int l32 = lane & 31, hh = lane >> 5, gg = lane >> 4, q = (lane & 15) >> 2, p = lane & 3;
  const unsigned ct = lds0 + K1_CT;
  f32x16 acc[2][2];
#pragma unroll
  for (int i = 0; i < 2; ++i)
#pragma unroll
    for (int j = 0; j < 2; ++j)
#pragma unroll
      for (int r = 0; r < 16; ++r) acc[i][j][r] = 0.f;
  unsigned xb[4];
#pragma unroll
  for (int ks = 0; ks < 4; ++ks) xb[ks] = lds0 + (wm * 64 + l32) * 128 + (((unsigned)(2 * ks + hh) ^ (unsigned)((l32 >> 1) & 7)) * 16);
  unsigned wb[2];
#pragma unroll
  for (int nt = 0; nt < 2; ++nt)
    wb[nt] = lds0 + 16384 + (8 * hh + q) * 256 + (((unsigned)(wn * 4 + 2 * nt + (gg & 1)) ^ (unsigned)(q << 1)) * 32) + p * 8;
  s16x8 x0[2], w0[2], x1[2], w1[2], x2[2], w2[2], x3[2], w3[2];
#define K1_LOADF(XF, WF, bo_, ks) do { \
    _Pragma("unroll") for (int i_ = 0; i_ < 2; ++i_) { \
      WF[i_] = cat8(tr_read(wb[i_] + (bo_) + (ks) * 4096), tr_read(wb[i_] + (bo_) + (ks) * 4096 + 1024)); \
      XF[i_] = lds_read16(xb[ks] + (bo_) + i_ * 4096); } } while (0)
#define K1_MM(XF, WF) do { _Pragma("unroll") for (int mt = 0; mt < 2; ++mt) _Pragma("unroll") for (int nt = 0; nt < 2; ++nt) \
      acc[mt][nt] = VT ? MFMA32(XF[mt], WF[nt], acc[mt][nt]) : MFMA32(WF[nt], XF[mt], acc[mt][nt]); } while (0)
#define ILV_A 2
#define ILV_B 2
#define ILV_C 2
#define K1_ILV() do { \
      __builtin_amdgcn_sched_group_barrier(0x008, 1, 0); __builtin_amdgcn_sched_group_barrier(0x100, ILV_A, 0); \
      __builtin_amdgcn_sched_group_barrier(0x008, 1, 0); __builtin_amdgcn_sched_group_barrier(0x100, ILV_B, 0); \
      __builtin_amdgcn_sched_group_barrier(0x008, 1, 0); __builtin_amdgcn_sched_group_barrier(0x100, ILV_C, 0); \
      __builtin_amdgcn_sched_group_barrier(0x008, 1, 0); __builtin_amdgcn_sched_group_barrier(0x100, 2, 0); } while (0)
  K1_LOADF(x0, w0, 0, 0);
  K1_LOADF(x1, w1, 0, 1);
#pragma nounroll
  for (int t = 0; t < 10; ++t) {
    const unsigned bo = (t & 3) * K1_STAGE, bn = ((t + 1) & 3) * K1_STAGE;
    SBAR();
    const u32x4 fl4 = *(volatile LAS u32x4*)(uintptr_t)(ct + CT_FX + (t + 1) * 16);
    K1_LOADF(x2, w2, bo, 2); K1_MM(x0, w0); K1_ILV();
    SBAR();
    K1_LOADF(x3, w3, bo, 3); K1_MM(x1, w1); K1_ILV();
    SBAR();
    const unsigned flu = (unsigned)__builtin_amdgcn_readfirstlane(mk_bad(fl4, mkx));
    if (t < 9 && flu != 0u) spin_mk(ct + CT_FX + (t + 1) * 16, mkx);
    asm volatile("" ::: "memory");
    SBAR();
    K1_LOADF(x0, w0, bn, 0); K1_MM(x2, w2); K1_ILV();
    SBAR();
    asm volatile("" : "+v"(x3[0]), "+v"(x3[1]), "+v"(w3[0]), "+v"(w3[1]) :: "memory");
    if (lane == 0) lds_st32(ct + CT_FREE + (t & 3) * 16 + wave * 4, (unsigned)(t + 1));
    SBAR();
    K1_LOADF(x1, w1, bn, 1); K1_MM(x3, w3); K1_ILV();
  }
  SBAR();
  if (!VT) {
    const int part = n0 >= DM ? 1 : 0;
    const float sc = part == 0 ? 0.22360679774997896f * 1.4426950408889634f : 1.0f;
    char* base = ws + (part ? WS_KF : WS_QF) + ((size_t)((m0 + wm * 64) >> 5) * 40 + ((n0 - part * DM + wn * 64) >> 4)) * 1024 + lane * 16;
#pragma unroll
    for (int mt = 0; mt < 2; ++mt)
#pragma unroll
      for (int nt = 0; nt < 2; ++nt)
#pragma unroll
        for (int uh = 0; uh < 2; ++uh) {
          u32x4 o;
#pragma unroll
          for (int i = 0; i < 4; ++i) o[i] = pk_bf16(acc[mt][nt][8 * uh + 2 * i] * sc, acc[mt][nt][8 * uh + 2 * i + 1] * sc);
          gstore16c(base + (mt * 40 + nt * 2 + uh) * 1024, o);
        }
  } else {
    char* base = ws + WS_VF + ((size_t)((m0 + wm * 64) >> 4) * 20 + ((n0 - 2 * DM + wn * 64) >> 5)) * 1024 + lane * 16;
#pragma unroll
    for (int mt = 0; mt < 2; ++mt)
#pragma unroll
      for (int nt = 0; nt < 2; ++nt)
#pragma unroll
        for (int kg = 0; kg < 2; ++kg) {
          u32x4 o;
#pragma unroll
          for (int i = 0; i < 4; ++i) o[i] = pk_bf16(acc[mt][nt][8 * kg + 2 * i], acc[mt][nt][8 * kg + 2 * i + 1]);
          gstore16c(base + ((mt * 2 + kg) * 20 + nt) * 1024, o);
        }
  }
}

DI void cv_put(unsigned y, const u32x4& c, const u32x2& n) {
  const unsigned d0 = c[0], d1 = c[1], d2 = c[2], d3 = c[3], d4 = n[0], d5 = n[1];
  const unsigned a0 = __builtin_amdgcn_alignbit(d1, d0, 16), a1 = __builtin_amdgcn_alignbit(d2, d1, 16),
                 a2 = __builtin_amdgcn_alignbit(d3, d2, 16), a3 = __builtin_amdgcn_alignbit(d4, d3, 16),
                 a4 = __builtin_amdgcn_alignbit(d5, d4, 16);
  lds_write16(y, (u32x4){d0, d1, d2, d3});
  lds_write16(y + CV_COPY, (u32x4){a0, a1, a2, a3});
  lds_write16(y + 2 * CV_COPY, (u32x4){d1, d2, d3, d4});
  lds_write16(y + 3 * CV_COPY, (u32x4){a1, a2, a3, a4});
}

#define QSYNC() do { asm volatile("s_waitcnt lgkmcnt(0)" ::: "memory"); ++qs; sig(qf + wq * 4, qs, lane); spin_ge4(qf, qs); } while (0)
struct HRegs { u32x4 c, u; u32x2 n; unsigned d; };
DI HRegs conv_load_h(int ch, int t256, const u16* __restrict__ ssmk, const u16* __restrict__ ssmd) {
  HRegs r;
  const u16* hrow = ssmk + (size_t)ch * SEQ;
  r.c = *((const u32x4*)(hrow + t256 * 8));
  r.n = *(const u32x2*)(hrow + (t256 < 255 ? t256 * 8 + 8 : 0));
  r.d = ssmd[ch];
  return r;
}
DI void conv_load_u(HRegs& r, int ch, int lane, int wq, const char* __restrict__ ws, u64 E) {
  poll<USLEEP>(ws, FL_U + (size_t)((ch >> 5) * 64 + 48 - 16 * wq + (lane & 15)) * 8, E);
  const u64* up = (const u64*)(ws + WS_UT + (size_t)ch * SEQ * 2 + wq * 1024 + lane * 16);
  const u64 a = __hip_atomic_load(up, __ATOMIC_RELAXED, __HIP_MEMORY_SCOPE_AGENT);
  const u64 b = __hip_atomic_load(up + 1, __ATOMIC_RELAXED, __HIP_MEMORY_SCOPE_AGENT);
  r.u = (u32x4){(unsigned)a, (unsigned)(a >> 32), (unsigned)b, (unsigned)(b >> 32)};
}
DI void conv_stage(unsigned reg, int lane, int wq, const HRegs& hr) {
  const int t256 = wq * 64 + lane;
  const unsigned hb = reg + CV_H;
  if (t256 < 32) lds_write16(reg + 4096 + t256 * 16, (u32x4){0u, 0u, 0u, 0u});
  cv_put(hb + (t256 + 2) * 16, hr.c, hr.n);
  if (t256 < 2) {
    const u32x4 z = {0u, 0u, 0u, 0u};
    u32x2 nx = {0u, 0u};
    const unsigned h0 = __builtin_amdgcn_readfirstlane(hr.c[0]), h1 = __builtin_amdgcn_readfirstlane(hr.c[1]);
    if (t256 == 1) nx = (u32x2){h0, h1};
    cv_put(hb + t256 * 16, z, nx);
  }
}
DI void conv_run(unsigned reg, unsigned qf, unsigned& qs, int ch, int lane, int wq, const HRegs& hr,
                 char* __restrict__ ws, unsigned xst) {
  const unsigned hb = reg + CV_H;
  if (xst) spin_ge4(xst, 1u);
  else { lds_write16(reg + (wq * 64 + lane) * 16, hr.u); QSYNC(); }
  const int r16 = lane & 15, g = lane >> 4, q = r16 >> 2, p = lane & 3;
  const unsigned ab0 = hb + ((q + 1) & 3) * CV_COPY + 2 * (8 * g + 4 * p + (q == 3 ? 4 : 0)) + wq * 128;
  const unsigned bb0 = reg + 2 * (2032 - 16 * r16 + 8 * g) + wq * 128 - 7 * 512;
  f32x4 acc[8];
#pragma unroll
  for (int i = 0; i < 8; ++i) acc[i] = (f32x4){0.f, 0.f, 0.f, 0.f};
#define CV_LDA(a_, ad_) cat8(tr_read((ad_) + (a_) * 512), tr_read((ad_) + (a_) * 512 + 8))
#define CV_LDB(c_, bd_) lds_read16((bd_) + (7 - (c_)) * 512)
  {
    s16x8 A[8], B[8];
    B[0] = CV_LDB(0, bb0);
#pragma unroll
    for (int a = 0; a < 8; ++a) A[a] = CV_LDA(a, ab0);
#pragma unroll
    for (int c = 1; c < 8; ++c) B[c] = CV_LDB(c, bb0);
#pragma unroll
    for (int c = 0; c < 8; ++c) {
      SBAR();
#pragma unroll
      for (int a = 0; a + c < 8; ++a) acc[a + c] = MFMA16(A[a], B[c], acc[a + c]);
      B[c] = CV_LDB(c, bb0 + 64);
      A[7 - c] = CV_LDA(7 - c, ab0 + 64);
    }
    SBAR();
#pragma unroll
    for (int a = 7; a >= 0; --a)
#pragma unroll
      for (int c = 0; a + c < 8; ++c) acc[a + c] = MFMA16(A[a], B[c], acc[a + c]);
  }
  QSYNC();
#pragma unroll
  for (int nt = 0; nt < 8; ++nt) {
    const int dw = nt >> 1;
    if (dw != wq) lds_write16(hb + (nt * 3 + (wq > dw ? wq - 1 : wq)) * 1024 + lane * 16, __builtin_bit_cast(u32x4, acc[nt]));
  }
  f32x4 k0 = wq == 0 ? acc[0] : (wq == 1 ? acc[2] : (wq == 2 ? acc[4] : acc[6]));
  f32x4 k1 = wq == 0 ? acc[1] : (wq == 1 ? acc[3] : (wq == 2 ? acc[5] : acc[7]));
  QSYNC();
  const float dco = bf_lo(hr.d);
  u32x2 zz[2];
#pragma unroll
  for (int i = 0; i < 2; ++i) {
    f32x4 o = i ? k1 : k0;
#pragma unroll
    for (int j = 0; j < 3; ++j) o += __builtin_bit_cast(f32x4, lds_read16(hb + ((wq * 2 + i) * 3 + j) * 1024 + lane * 16));
    const int s = (wq * 2 + i) * 256 + 16 * r16 + 4 * g;
    const u32x2 uu = __builtin_bit_cast(u32x2, lds_read8(reg + 2 * (SEQ - 4 - s)));
    const float kv0 = bf_hi(uu[1]), kv1 = bf_lo(uu[1]), kv2 = bf_hi(uu[0]), kv3 = bf_lo(uu[0]);
    const unsigned y01 = pk_bf16(o[0], o[1]), y23 = pk_bf16(o[2], o[3]);
    const unsigned e01 = pk_bf16(kv0 * dco, kv1 * dco), e23 = pk_bf16(kv2 * dco, kv3 * dco);
    zz[i][0] = pk_bf16(bf_lo(y01) + bf_lo(e01), bf_hi(y01) + bf_hi(e01));
    zz[i][1] = pk_bf16(bf_lo(y23) + bf_lo(e23), bf_hi(y23) + bf_hi(e23));
  }
  const auto sx = __builtin_amdgcn_permlane16_swap(zz[0][0], zz[1][0], false, false);
  const auto sy = __builtin_amdgcn_permlane16_swap(zz[0][1], zz[1][1], false, false);
  u16* zrow = (u16*)(ws + WS_ZT) + (size_t)ch * SEQ;
  gstore16c(zrow + (wq * 2 + (g & 1)) * 256 + 16 * r16 + 8 * (g >> 1), (u32x4){sx[0], sy[0], sx[1], sy[1]});
  asm volatile("s_waitcnt lgkmcnt(0)" ::: "memory");
}
DI void conv_publish(unsigned zc, int ch, int lane, char* __restrict__ ws, u64 E) {
  asm volatile("s_waitcnt vmcnt(0)" ::: "memory");
  if (lane == 0) {
    const unsigned old = __hip_atomic_fetch_add((LAS unsigned*)(uintptr_t)zc, 1u, __ATOMIC_RELAXED, __HIP_MEMORY_SCOPE_WORKGROUP);
    if ((old & 3u) == 3u) __hip_atomic_store((u64*)(ws + FL_Z + (size_t)ch * 8), E, __ATOMIC_RELAXED, __HIP_MEMORY_SCOPE_AGENT);
  }
}

DI s16x8 pack8(const f32x16& v, int o) {
  u32x4 r;
  r[0] = pk_bf16(v[o + 0], v[o + 1]); r[1] = pk_bf16(v[o + 2], v[o + 3]);
  r[2] = pk_bf16(v[o + 4], v[o + 5]); r[3] = pk_bf16(v[o + 6], v[o + 7]);
  return __builtin_bit_cast(s16x8, r);
}
DI float max3f(float a, float b, float c) { return __builtin_fmaxf(__builtin_fmaxf(a, b), c); }
DI float max16(const f32x16& v, float m) {
  float a = max3f(v[0], v[1], m), b = max3f(v[2], v[3], v[4]);
  a = max3f(a, v[5], v[6]); b = max3f(b, v[7], v[8]);
  a = max3f(a, v[9], v[10]); b = max3f(b, v[11], v[12]);
  a = max3f(a, v[13], v[14]);
  return max3f(a, b, v[15]);
}
DI float xhalf_max(float m) {
  auto sw = __builtin_amdgcn_permlane32_swap(__float_as_uint(m), __float_as_uint(m), false, false);
  return __builtin_fmaxf(__uint_as_float(sw[0]), __uint_as_float(sw[1]));
}

#define AT_LMAX 65536.0f
#define P3_WADDR() unsigned wv[4]; _Pragma("unroll") for (int i = 0; i < 4; ++i) { \
      const int c = i < 3 ? (i * 4 + w) * 64 + lane : 768 + w * 8 + (lane & 7); const int row = c / 10; \
      wv[i] = (unsigned)(row * DM * 2 + (c - row * 10) * 16); } const char* wog = (const char*)(wo + n3)
#define P3_STW(t) do { _Pragma("unroll") for (int i_ = 0; i_ < 3; ++i_) \
        glds16(wv[i_], wog + (size_t)(t) * 80 * DM * 2, lds0 + p3_w(t) + (i_ * 4 + w) * 1024); \
      if (lane < 8) glds16(wv[3], wog + (size_t)(t) * 80 * DM * 2, lds0 + p3_w(t) + 12288 + w * 128); } while (0)
DI void attn_stage(unsigned lds0, int h, int qb8, int j, int lane, char* __restrict__ ws, u64 E, int zch, const Mk& mk) {
  const int qt = qb8 * 8 + j;
  const unsigned l16 = (unsigned)(lane * 16), ct = lds0 + L_CT;
  const int qa = (5 * h) >> 2;
  const bool stg = qb8 > 0 || j >= 2;
  {
    const int l3 = lane & 7, hi = lane & 1;
    const bool kv = stg && l3 >= 2 && l3 < 6;
    const int rb = kv ? 2 * qb8 - 2 + j : 2 * qb8 + (l3 >= 6 ? 1 : 0);
    const int col = !kv ? qa * 16 + hi * 31 : (l3 < 4 ? DM + qa * 16 + hi * 31 : 2 * DM + h * 20 + hi * 19);
    poll(ws, FL_Q + (size_t)((h * 16 + rb) * 16 + (col >> 7)) * 8, E);
  }
  conv_publish(ct + CT_ZCNT + 8, zch, lane, ws, E);
  const int kt0 = qb8 * 8 - 8 + 4 * j;
#pragma unroll
  for (int i = 0; i < 4; ++i)
    glds16c(l16, ws + WS_QF + ((size_t)(qt + (i >> 1) * 4) * 40 + qa + (i & 1)) * 1024, lds0 + AT_Q + ((j + (i >> 1) * 4) * 2 + (i & 1)) * 1024);
  if (stg) {
#pragma unroll
    for (int i = 0; i < 8; ++i)
      glds16c(l16, ws + WS_KF + ((size_t)(kt0 + (i >> 1)) * 40 + qa + (i & 1)) * 1024, lds0 + AT_K + (j * 8 + i) * 1024);
  }
  glds_mark(mk, ct + CT_KR8 + j * 4, lane);
  if (stg) {
#pragma unroll
    for (int i = 0; i < 6; ++i) {
      const int c = i * 64 + lane;
      const int kgp = (c * 1561) >> 16, rem = c - kgp * 42, vh = rem >= 21 ? 1 : 0, d = rem - vh * 21;
      const int n = h * 20 + d;
      if (c < 336 && d == 20) lds_write16(lds0 + AT_V + j * 5376 + c * 16, (u32x4){0x3f803f80u, 0x3f803f80u, 0x3f803f80u, 0x3f803f80u});
      if (c < 336 && d < 20)
        glds16c((unsigned)((kgp * 20 + (n >> 5)) * 1024 + (vh * 32 + (n & 31)) * 16), ws + WS_VF + ((size_t)(kt0 * 2) * 20) * 1024,
                lds0 + AT_V + j * 5376 + i * 1024);
    }
    asm volatile("s_waitcnt lgkmcnt(0)" ::: "memory");
  }
  glds_mark(mk, ct + CT_VR8 + j * 4, lane);
}
template <bool FULL>
DI void attn_task(unsigned lds0, int h, int qb8, int j, int lane, char* __restrict__ ws, u64 E, int zch, const Mk& mk) {
  const int l32 = lane & 31, hh = lane >> 5;
  const int qt = qb8 * 8 + j;
  const int tfirst = FULL ? 0 : 8 - qt;
  const unsigned l16 = (unsigned)(lane * 16), ct = lds0 + L_CT;
  const int qa = (5 * h) >> 2;
  u32x4 q0r, q1r;
  spin_mk(ct + CT_KR8, mk.x);
  if (!FULL) spin_mk(ct + CT_VR8, mk.x);
  q0r = __builtin_bit_cast(u32x4, lds_read16(lds0 + AT_Q + j * 2048 + l16));
  q1r = __builtin_bit_cast(u32x4, lds_read16(lds0 + AT_Q + j * 2048 + 1024 + l16));
  asm volatile("s_waitcnt lgkmcnt(0)" : "+v"(q0r), "+v"(q1r) :: "memory");
  if (zch >= 0) conv_publish(ct + CT_ZCNT + (j >> 2) * 4, zch, lane, ws, E);
  {
    const int g0 = 4 * qa + hh - 5 * h;
    const bool k0 = g0 >= 0, k1 = g0 + 2 >= 0 && g0 + 2 < 5, k2 = g0 + 4 < 5, k3 = g0 + 6 < 5;
    q0r[0] = k0 ? q0r[0] : 0u; q0r[1] = k0 ? q0r[1] : 0u; q0r[2] = k1 ? q0r[2] : 0u; q0r[3] = k1 ? q0r[3] : 0u;
    q1r[0] = k2 ? q1r[0] : 0u; q1r[1] = k2 ? q1r[1] : 0u; q1r[2] = k3 ? q1r[2] : 0u; q1r[3] = k3 ? q1r[3] : 0u;
  }
  s16x8 qlo = __builtin_bit_cast(s16x8, q0r), qhi8 = __builtin_bit_cast(s16x8, q1r);
  const f32x16 zero16 = {0.f, 0.f, 0.f, 0.f, 0.f, 0.f, 0.f, 0.f, 0.f, 0.f, 0.f, 0.f, 0.f, 0.f, 0.f, 0.f};
  const float ninf = -INFINITY;
  const f32x16 ninf16 = {ninf, ninf, ninf, ninf, ninf, ninf, ninf, ninf, ninf, ninf, ninf, ninf, ninf, ninf, ninf, ninf};
  const unsigned kb = lds0 + AT_K + j * 2048 + l16;
#define AT_QK(dst, t, cin) do { if (FULL || (t) >= tfirst) { dst = MFMA32(lds_read16(kb + (t) * 2048), qlo, cin); \
    dst = MFMA32(lds_read16(kb + (t) * 2048 + 1024), qhi8, dst); } else dst = ninf16; } while (0)
  const int vrow = l32 <= 20 ? l32 : (l32 == 24 ? 20 : l32 - 16);
  const unsigned vb = lds0 + AT_V + j * 1344 + hh * 336 + vrow * 16;
#define AT_EXP(sv) do { _Pragma("unroll") for (int r = 0; r < 16; ++r) sv[r] = __builtin_amdgcn_exp2f(sv[r] - m); } while (0)
#define AT_PV(sv, t) do { oacc = MFMA32(lds_read16(vb + (t) * 1344), pack8(sv, 0), oacc); oacc = MFMA32(lds_read16(vb + (t) * 1344 + 672), pack8(sv, 8), oacc); } while (0)
  f32x16 oacc;
  bool exact = !FULL;
  if (FULL) {
    f32x16 sm, sa, sb;
    if (j >= 4) __builtin_amdgcn_s_setprio(2);
    AT_QK(sm, 8, zero16); AT_QK(sa, 0, zero16);
    const int tq = l32 - 4 * hh;
    const unsigned wm = tq < 0 ? 0xffffffffu : (0xfffffffeu << tq);
    unsigned msk[16];
#pragma unroll
    for (int r = 0; r < 16; ++r) {
      msk[r] = (unsigned)__builtin_amdgcn_sbfe((int)wm, (r & 3) + 8 * (r >> 2), 1);
      sm[r] = __uint_as_float((msk[r] & __float_as_uint(sa[r])) | (~msk[r] & __float_as_uint(sm[r])));
    }
    const float m = xhalf_max(max16(sm, ninf));
    const float nm = -m;
    const f32x16 cneg = {nm, nm, nm, nm, nm, nm, nm, nm, nm, nm, nm, nm, nm, nm, nm, nm};
    AT_QK(sa, 1, cneg);
    AT_EXP(sm);
    u32x4 pl = __builtin_bit_cast(u32x4, pack8(sm, 0)), ph = __builtin_bit_cast(u32x4, pack8(sm, 8)), ql, qh;
#pragma unroll
    for (int i = 0; i < 4; ++i) {
      ql[i] = pl[i] & ((msk[2 * i] & 0xffffu) | (msk[2 * i + 1] & 0xffff0000u)); pl[i] ^= ql[i];
      qh[i] = ph[i] & ((msk[8 + 2 * i] & 0xffffu) | (msk[9 + 2 * i] & 0xffff0000u)); ph[i] ^= qh[i];
    }
    spin_mk(ct + CT_VR8, mk.x);
    oacc = MFMA32(lds_read16(vb + 8 * 1344), __builtin_bit_cast(s16x8, pl), zero16);
    oacc = MFMA32(lds_read16(vb + 8 * 1344 + 672), __builtin_bit_cast(s16x8, ph), oacc);
    oacc = MFMA32(lds_read16(vb), __builtin_bit_cast(s16x8, ql), oacc);
    oacc = MFMA32(lds_read16(vb + 672), __builtin_bit_cast(s16x8, qh), oacc);
#define AT_EX0(sv) do { _Pragma("unroll") for (int r = 0; r < 16; ++r) sv[r] = __builtin_amdgcn_exp2f(sv[r]); } while (0)
    AT_QK(sb, 2, cneg); AT_EX0(sa); AT_PV(sa, 1);
    AT_QK(sa, 3, cneg); AT_EX0(sb); AT_PV(sb, 2);
    if (j >= 4) __builtin_amdgcn_s_setprio(0);
    AT_QK(sb, 4, cneg); AT_EX0(sa); AT_PV(sa, 3);
    AT_QK(sa, 5, cneg); AT_EX0(sb); AT_PV(sb, 4);
    AT_QK(sb, 6, cneg); AT_EX0(sa); AT_PV(sa, 5);
    AT_QK(sa, 7, cneg); AT_EX0(sb); AT_PV(sb, 6);
    AT_EX0(sa); AT_PV(sa, 7);
    float big = max3f(__builtin_fabsf(oacc[0]), __builtin_fabsf(oacc[1]), __builtin_fabsf(oacc[2]));
    big = max3f(big, __builtin_fabsf(oacc[3]), __builtin_fabsf(oacc[4]));
    big = max3f(big, __builtin_fabsf(oacc[5]), __builtin_fabsf(oacc[6]));
    big = max3f(big, __builtin_fabsf(oacc[7]), __builtin_fabsf(oacc[8]));
    big = max3f(big, __builtin_fabsf(oacc[9]), __builtin_fabsf(oacc[10]));
    big = __builtin_fmaxf(big, __builtin_fabsf(oacc[11]));
    const float ls = hh ? oacc[8] : oacc[12];
    float osum = 0.f;
#pragma unroll
    for (int r = 0; r < 8; ++r) osum += oacc[r];
    const bool ok = (ls < AT_LMAX) && (big < 1e37f) && (osum == osum);
    exact = __builtin_amdgcn_ballot_w64(!ok) != 0ull;
  }
  if (exact) {
    asm volatile("" : "+v"(qlo), "+v"(qhi8) :: "memory");
    f32x16 sm, s1, s2, s3;
    {
      f32x16 s0;
      AT_QK(sm, 8, zero16); AT_QK(s0, 0, zero16);
      AT_QK(s1, 1, zero16); AT_QK(s2, 2, zero16); AT_QK(s3, 3, zero16);
#pragma unroll
      for (int r = 0; r < 16; ++r) {
        const int kk = (r & 3) + 8 * (r >> 2) + 4 * hh;
        sm[r] = kk > l32 ? s0[r] : sm[r];
      }
    }
    float m = max16(sm, ninf);
    m = max16(s1, m); m = max16(s2, m); m = max16(s3, m);
    m = xhalf_max(m);
    AT_EXP(sm);
    oacc = zero16;
    {
      f32x16 p0, p8;
#pragma unroll
      for (int r = 0; r < 16; ++r) {
        const int kk = (r & 3) + 8 * (r >> 2) + 4 * hh;
        p0[r] = kk > l32 ? sm[r] : 0.f;
        p8[r] = kk > l32 ? 0.f : sm[r];
      }
      AT_PV(p8, 8);
      if (FULL) AT_PV(p0, 0);
    }
    if (FULL || 1 >= tfirst) { AT_EXP(s1); AT_PV(s1, 1); }
    AT_QK(sm, 4, zero16); AT_QK(s1, 5, zero16);
    if (FULL || 2 >= tfirst) { AT_EXP(s2); AT_PV(s2, 2); }
    AT_QK(s2, 6, zero16);
    if (FULL || 3 >= tfirst) { AT_EXP(s3); AT_PV(s3, 3); }
    AT_QK(s3, 7, zero16);
    float m2 = max16(sm, m);
    m2 = max16(s1, m2); m2 = max16(s2, m2); m2 = max16(s3, m2);
    m2 = xhalf_max(m2);
    {
      const float alpha = __builtin_amdgcn_exp2f(m - m2);
#pragma unroll
      for (int r = 0; r < 13; ++r) oacc[r] *= alpha;
      m = m2;
    }
    if (FULL || 4 >= tfirst) { AT_EXP(sm); AT_PV(sm, 4); }
    if (FULL || 5 >= tfirst) { AT_EXP(s1); AT_PV(s1, 5); }
    if (FULL || 6 >= tfirst) { AT_EXP(s2); AT_PV(s2, 6); }
    if (FULL || 7 >= tfirst) { AT_EXP(s3); AT_PV(s3, 7); }
  }
  asm volatile("s_waitcnt lgkmcnt(0)" ::: "memory");
  sig(ct + CT_ATT + j * 4, 1u, lane);
  const float l = hh ? oacc[8] : oacc[12];
  const float inv = __builtin_amdgcn_rcpf(l);
  char* oc = ws + WS_OW + ((size_t)qt * NH + h) * 1280;
  u32x4 o;
  o[0] = pk_bf16(oacc[0] * inv, oacc[1] * inv); o[1] = pk_bf16(oacc[2] * inv, oacc[3] * inv);
  o[2] = pk_bf16(oacc[4] * inv, oacc[5] * inv); o[3] = pk_bf16(oacc[6] * inv, oacc[7] * inv);
  gstore16c(oc + lane * 16, o);
  if (!hh) {
    u32x2 o2;
    o2[0] = pk_bf16(oacc[8] * inv, oacc[9] * inv); o2[1] = pk_bf16(oacc[10] * inv, oacc[11] * inv);
    gstore8c(oc + 1024 + l32 * 8, o2);
  }
  publish(ws, FL_O + (size_t)(qt * NH + h) * 8, E, lane);
}

#define APRIO 1
#define LPRIO 0
#define MPRIO 2
#define ROTG 2
__global__ __launch_bounds__(768, 3) void mega(const u16* __restrict__ x, const u16* __restrict__ wqkv,
                                               const u16* __restrict__ ak, const u16* __restrict__ av,
                                               const u16* __restrict__ shk, const u16* __restrict__ wo,
                                               const u16* __restrict__ ssmk, const u16* __restrict__ ssmd,
                                               const u16* __restrict__ aq, char* __restrict__ ws,
                                               u16* __restrict__ out) {
  extern __shared__ __attribute__((aligned(16))) char smem[];
  const unsigned lds0 = lds_addr(smem);
  const int tid = threadIdx.x, wave = __builtin_amdgcn_readfirstlane(tid >> 6);
  int lane = tid & 63;
#define RELANE() asm volatile("" : "+v"(lane))
  const int role = wave >> 2, w = wave & 3;
  const int bid = blockIdx.x, xcd = bid & 7, idx = bid >> 3;
  const bool has_gemm = idx < 30;
  const unsigned ct = lds0 + L_CT;
  u64 E;
  Mk mk; mk.src = wqkv;
  asm volatile("s_load_dwordx2 %0, %2, 0x0\n\ts_load_dword %1, %3, 0x0" : "=&s"(E), "=&s"(mk.x) : "s"(ws + WS_FL), "s"(wqkv) : "memory");
  const int gt = (xcd & 1) * 30 + idx;
  const int tm = (xcd >> 1) * 4 + (gt & 3), tn = gt >> 2;
  const int m0 = tm * 128, n0 = tn * 128;
  const int rot = ROTG >= 10 ? (((gt & 3) * 5 + 1) / 2 + 3 * tn) % 10 : ((((gt & 3) + tn) % ROTG) * 10) / ROTG;
  const unsigned xo0 = (unsigned)((lane >> 3) * DM * 2 + (((lane & 7) ^ (lane >> 4)) * 16));
  const unsigned xo1 = (unsigned)((lane >> 3) * DM * 2 + (((lane & 7) ^ (4 + (lane >> 4))) * 16));
  const unsigned wof = (unsigned)((lane >> 4) * NQKV * 2 + (((lane & 15) ^ ((lane >> 4) << 2)) * 16));
  const char* xg = (const char*)(x + (size_t)m0 * DM);
  const char* wg = (const char*)(wqkv + n0);
#define K1_PIECE_X(t, kt, j) glds16(((j) & 1) ? xo1 : xo0, xg + (size_t)(j) * 8 * DM * 2 + (kt) * 128, lds0 + ((t) & 3) * K1_STAGE + (j) * 1024)
#define K1_PIECE_W(t, kt, j) glds16(wof, wg + (size_t)((kt) * 64 + (j) * 4) * NQKV * 2, lds0 + ((t) & 3) * K1_STAGE + 16384 + (j) * 1024)
  if (has_gemm && role == 0) {
#pragma unroll
    for (int i = 0; i < 4; ++i) { K1_PIECE_X(0, rot, w * 4 + i); K1_PIECE_W(0, rot, w * 4 + i); }
  }
  const unsigned pf = ct + CT_PAIR + (wave >> 1) * 8;
  const int wh = wave & 1;
  unsigned ps = 0;
  const int fb = (idx - 30) * 8 + xcd;
  const int a1t = has_gemm ? (xcd * 30 + idx) * 2 + (w >> 1) : 480 + fb * 6 + (wave >> 1);
  const int a2t = 576 + fb * 4 + (wave >> 1);
  const unsigned a1l = has_gemm ? lds0 + L_AUX + (w >> 1) * A1_PAIR : lds0 + (wave >> 1) * A1_PAIR;
  if (!has_gemm || role == 2) a1_issue(a1l, a1t, lane, wh, ak, av, shk);
  const bool cv0 = role != 0 || !has_gemm || bid < 112;
  const int ch0 = role == 0 ? (has_gemm ? 528 + bid : 512 + fb) : bid + (role == 1 ? 256 : 0);
  asm volatile("s_waitcnt lgkmcnt(0)" : "+s"(E), "+s"(mk.x) :: "memory");
  if (role == 1) lds_st32(ct + (tid - 256) * 4, (tid - 256) * 4 >= CT_FX ? ~mk.x : 0u);
  bar();
  E += 1ull;

  const bool ex = has_gemm && role == 1 && bid < 112;
  HRegs hr, h2;
#define LOAD_H() do { hr = conv_load_h(ch0, w * 64 + lane, ssmk, ssmd); \
    if (ex) h2 = conv_load_h(528 + bid, w * 64 + lane, ssmk, ssmd); } while (0)
  auto q_publish = [&]() {
    asm volatile("s_waitcnt vmcnt(0)" ::: "memory");
    unsigned old = 0;
    if (lane == 0) old = __hip_atomic_fetch_add((LAS unsigned*)(uintptr_t)(ct + CT_QCNT), 1u, __ATOMIC_RELAXED, __HIP_MEMORY_SCOPE_WORKGROUP);
    if (__builtin_amdgcn_readfirstlane(old) == 3u) {
      const int c0 = (n0 % DM) - 32, hh_ = lane;
      if (lane < QREP && hh_ * 20 + 40 > c0 && hh_ * 20 < c0 + 32 + 128 + 20)
        __hip_atomic_store((u64*)(ws + FL_Q + (size_t)((lane * 16 + tm) * 16 + tn) * 8), E, __ATOMIC_RELAXED, __HIP_MEMORY_SCOPE_AGENT);
    }
  };
  if (!has_gemm) {
    LOAD_H();
    const unsigned a2l = lds0 + (6 + (wave >> 1)) * A1_PAIR;
    if (wave < 8) a1_issue(a2l, a2t, lane, wh, ak, av, shk);
    if (wave < 8) wait_vm<6>(); else wait_vm<0>();
    a1_finish(a1l, pf, ps, a1t, lane, wh, ws, E);
    if (wave < 8) {
      wait_vm<2>();
      a1_finish(a2l, pf, ps, a2t, lane, wh, ws, E);
      wait_vm<2>();
      if (lane == 0) __hip_atomic_store((u64*)(ws + FL_U + (size_t)(((a1t >> 6) * 2 + wh) * 64 + (a1t & 63)) * 8), E, __ATOMIC_RELAXED, __HIP_MEMORY_SCOPE_AGENT);
      a1_publish(a2t, lane, wh, ws, E);
    } else a1_publish(a1t, lane, wh, ws, E);
    bar();
  } else if (role == 0) {
    wait_vm<0>();
    sig(ct + CT_S0 + w * 4, 1u, lane);
    spin_ge4(ct + CT_S0, 1u);
    __builtin_amdgcn_s_setprio(MPRIO);
    if (tn >= 10) k1_gemm<true>(lds0, m0, n0, tid, ws, mk.x);
    else k1_gemm<false>(lds0, m0, n0, tid, ws, mk.x);
  } else if (role == 1) {
    __builtin_amdgcn_s_setprio(LPRIO);
#define K1_ST(t) do { const int kt_ = (t) + rot >= 10 ? (t) + rot - 10 : (t) + rot; \
      if ((t) >= 4) spin_ge4(ct + CT_FREE + ((t) & 3) * 16, (unsigned)((t) - 3)); \
      _Pragma("unroll") for (int i_ = 0; i_ < 4; ++i_) { K1_PIECE_X(t, kt_, w * 4 + i_); K1_PIECE_W(t, kt_, w * 4 + i_); } } while (0)
#pragma nounroll
    for (int t = 1; t < 4; ++t) { K1_ST(t); glds_mark(mk, ct + CT_FX + t * 16 + w * 4, lane); }
    LOAD_H();
#pragma nounroll
    for (int t = 4; t < 10; ++t) { K1_ST(t); glds_mark(mk, ct + CT_FX + t * 16 + w * 4, lane); }
    __builtin_amdgcn_s_setprio(0);
  } else {
    LOAD_H();
    __builtin_amdgcn_s_setprio(2);
    wait_vm<0>();
    a1_finish(a1l, pf, ps, a1t, lane, wh, ws, E);
    a1_publish(a1t, lane, wh, ws, E);
    __builtin_amdgcn_s_setprio(0);
  }

  RELANE();
  unsigned qs = 0;
  const unsigned qf = ct + CT_QUAD + role * 16;
  const unsigned creg = lds0 + (role == 2 ? L_AUX : (role == 1 ? 29184 : 0));
  if (role == 2) __builtin_amdgcn_s_setprio(APRIO); else __builtin_amdgcn_s_setprio(0);
  if (has_gemm && role == 0) {
    q_publish();
    if (cv0) {
      hr.d = ssmd[ch0];
      conv_load_u(hr, ch0, lane, w, ws, E);
      spin_ge4(ct + CT_XST, 1u);
      conv_run(creg, qf, qs, ch0, lane, w, hr, ws, 0u);
    }
  } else {
    if (has_gemm && role == 1) {
      conv_load_u(hr, ch0, lane, w, ws, E);
      spin_ge4(ct + CT_FREE + 16, 10u);
      conv_stage(creg, lane, w, hr);
      if (ex) {
        conv_stage(lds0, lane, w, h2);
        asm volatile("s_waitcnt lgkmcnt(0)" ::: "memory");
        sig(ct + CT_XST + w * 4, 1u, lane);
      }
    } else {
      if (has_gemm) { const int wq = w; QSYNC(); }
      conv_stage(creg, lane, w, hr);
      conv_load_u(hr, ch0, lane, w, ws, E);
    }
    conv_run(creg, qf, qs, ch0, lane, w, hr, ws, 0u);
  }
  sig(ct + CT_CVD + wave * 4, 1u, lane);

  const int tm3 = xcd * 4 + (idx >> 3), tn3 = idx & 7;
  const int m3 = tm3 * 64, n3 = tn3 * 80;
  RELANE();

  if (role == 2) {
    const int r16 = lane & 15, g = lane >> 4, q = r16 >> 2, p = lane & 3;
    __builtin_amdgcn_s_setprio(3);
    spin_ge4(ct + CT_CVD + 32, 1u);
    if (has_gemm) spin_ge4(ct + CT_FREE + 16, 10u);
    attn_stage(lds0, xcd * 4 + (idx & 3), idx >> 2, w, lane, ws, E, ch0, mk);
    {
      P3_WADDR();
      P3_STW(5);
      spin_ge8(ct + CT_CVD, 1u);
      P3_STW(0); P3_STW(4); P3_STW(1); P3_STW(2); P3_STW(3);
      glds_mark(mk, ct + CT_WRDY + w * 4, lane);
    }
    __builtin_amdgcn_s_setprio(0);
    u32x4 qv[3];
#pragma unroll
    for (int j = 0; j < 3; ++j) {
      const int dc = j < 2 ? j * 4 + g : 8 + (g & 1);
      qv[j] = *((const u32x4*)(aq + (size_t)(m3 + w * 16 + r16) * DM + n3 + dc * 8));
    }
    {
      const int l5 = lane & 31;
      const int cl = l5 < 8 ? w * 8 + l5 : (l5 < 16 ? 32 + w * 8 + (l5 - 8) : 64 + w * 4 + (l5 & 3));
      poll<ZSLEEP>(ws, FL_Z + (size_t)(n3 + cl) * 8, E);
    }
    {
      const char* zg = ws + WS_ZT + ((size_t)n3 * SEQ + m3) * 2;
      const unsigned zo = (unsigned)((lane >> 3) * SEQ * 2 + (lane & 7) * 16);
      glds16c(zo, zg + (size_t)(w * 8) * SEQ * 2, lds0 + L_AUX + w * 1024);
      glds16c(zo, zg + (size_t)(32 + w * 8) * SEQ * 2, lds0 + L_AUX + 4096 + w * 1024);
      if (lane < 32) glds16c(zo, zg + (size_t)(64 + w * 4) * SEQ * 2, lds0 + L_AUX + 8192 + w * 512);
    }
    wait_vm<0>();
    sig(ct + CT_ZRDY + w * 4, 1u, lane);
    spin_ge4(ct + CT_ZRDY, 1u);
    u32x4 zz[3];
#pragma unroll
    for (int j = 0; j < 3; ++j) {
      const int dc = j < 2 ? j * 4 + g : 8 + (g & 1);
      const unsigned za = lds0 + L_AUX + (dc * 8 + q) * 128 + w * 32 + p * 8;
      zz[j] = __builtin_bit_cast(u32x4, cat8(tr_read(za), tr_read(za + 512)));
    }
#pragma unroll
    for (int j = 0; j < 3; ++j) {
      const int dc = j < 2 ? j * 4 + g : 8 + (g & 1);
      u32x4 o;
#pragma unroll
      for (int i = 0; i < 4; ++i) o[i] = pk_bf16(bf_lo(qv[j][i]) * bf_lo(zz[j][i]), bf_hi(qv[j][i]) * bf_hi(zz[j][i]));
      if (j < 2 || g < 2) ostore16(out + (size_t)(m3 + w * 16 + r16) * (2 * DM) + n3 + dc * 8, o);
    }
    if (bid == 0 && w == 0) {
#pragma unroll
      for (int i = 0; i < 4; ++i) poll(ws, FL_O + (size_t)(((i * 2 + (lane >> 5)) * 8) * NH + (lane & 31)) * 8, E);
      if (lane == 0) __hip_atomic_store((u64*)(ws + WS_FL), E, __ATOMIC_RELAXED, __HIP_MEMORY_SCOPE_AGENT);
    }
    return;
  }

  const int h = xcd * 4 + (idx & 3), qb8 = idx >> 2;
  __builtin_amdgcn_s_setprio(1);
  RELANE();
  if (qb8 > 0) attn_task<true>(lds0, h, qb8, wave, lane, ws, E, cv0 ? ch0 : -1, mk);
  else attn_task<false>(lds0, h, qb8, wave, lane, ws, E, cv0 ? ch0 : -1, mk);

  RELANE();
  const int r16 = lane & 15, g = lane >> 4, q = r16 >> 2, p = lane & 3;
  f32x4 acc[5];
#pragma unroll
  for (int i = 0; i < 5; ++i) acc[i] = (f32x4){0.f, 0.f, 0.f, 0.f};
  const unsigned wbp = lds0 + (20 * (g >> 1) + 4 * (g & 1) + q) * 160 + p * 8;
  const unsigned wbs = lds0 + (20 * g + 16 + q) * 160 + p * 8;
  u32x4 op[8]; u32x2 osg[4];
  if (role == 1) {
    P3_WADDR();
    spin_ge8(ct + CT_ATT, 1u);
    P3_STW(6); P3_STW(7);
    glds_mark(mk, ct + CT_WRD2 + w * 4, lane);
  }
  spin_mk(ct + CT_WRDY, mk.x);
  const unsigned w0 = role ? p3_w(4) : p3_w(0), w1 = role ? p3_w(5) : p3_w(1), w2 = role ? p3_w(6) : p3_w(2), w3 = role ? p3_w(7) : p3_w(3);
  struct Fw { s16x8 a[5], b[5]; s16x4 s[5]; };
  Fw fwa, fwb;
#define P3_LOADW(F, bo_) do { _Pragma("unroll") for (int nt = 0; nt < 5; ++nt) { \
      F.a[nt] = cat8(tr_read(wbp + (bo_) + nt * 32), tr_read(wbp + (bo_) + 8 * 160 + nt * 32)); \
      F.b[nt] = cat8(tr_read(wbp + (bo_) + 40 * 160 + nt * 32), tr_read(wbp + (bo_) + 48 * 160 + nt * 32)); \
      F.s[nt] = tr_read(wbs + (bo_) + nt * 32); } } while (0)
#define P3_MM(F, t) do { \
    _Pragma("unroll") for (int nt = 0; nt < 5; ++nt) acc[nt] = MFMA16(F.a[nt], __builtin_bit_cast(s16x8, op[2 * (t)]), acc[nt]); \
    _Pragma("unroll") for (int nt = 0; nt < 5; ++nt) acc[nt] = MFMA16(F.b[nt], __builtin_bit_cast(s16x8, op[2 * (t) + 1]), acc[nt]); } while (0)
#define P3_MS(t) do { const s16x8 os_ = cat8(__builtin_bit_cast(s16x4, osg[t]), __builtin_bit_cast(s16x4, osg[(t) + 1])); \
    _Pragma("unroll") for (int nt = 0; nt < 5; ++nt) acc[nt] = MFMA16(cat8(fwa.s[nt], fwb.s[nt]), os_, acc[nt]); } while (0)
  P3_LOADW(fwa, w0);
  SBAR();
  poll<OSLEEP>(ws, FL_O + (size_t)(((m3 >> 5) + (w >> 1)) * NH + role * 16 + r16) * 8, E);
  {
    const int orow = (w & 1) * 16 + r16;
    const char* ob = ws + WS_OW + ((size_t)((m3 >> 5) + (w >> 1)) * NH + role * 16) * 1280;
    const char* pp = ob + (g >> 1) * 1280 + (g & 1) * 512 + orow * 16;
    const char* sp = ob + g * 1280 + 1024 + orow * 8;
#pragma unroll
    for (int t = 0; t < 4; ++t) {
      asm volatile("global_load_dwordx4 %0, %3, off sc1\n\tglobal_load_dwordx4 %1, %3, off offset:2560 sc1\n\tglobal_load_dwordx2 %2, %4, off sc1"
                   : "=&v"(op[2 * t]), "=&v"(op[2 * t + 1]), "=&v"(osg[t]) : "v"(pp + t * 5120), "v"(sp + t * 5120) : "memory");
    }
  }
  P3_LOADW(fwb, w1);
  if (role == 1) spin_mk(ct + CT_WRD2, mk.x);
  SBAR();
  asm volatile("s_waitcnt vmcnt(6)" : "+v"(op[0]), "+v"(op[1]), "+v"(op[2]), "+v"(op[3]), "+v"(osg[0]), "+v"(osg[1]) :: "memory");
  P3_MS(0); P3_MM(fwa, 0);
  SBAR();
  P3_LOADW(fwa, w2); P3_MM(fwb, 1);
#pragma unroll
  for (int i = 0; i < 10; ++i) { __builtin_amdgcn_sched_group_barrier(0x008, 1, 0); __builtin_amdgcn_sched_group_barrier(0x100, 3, 0); }
  SBAR();
  asm volatile("s_waitcnt vmcnt(0)" : "+v"(op[4]), "+v"(op[5]), "+v"(op[6]), "+v"(op[7]), "+v"(osg[2]), "+v"(osg[3]) :: "memory");
  P3_LOADW(fwb, w3); P3_MM(fwa, 2);
#pragma unroll
  for (int i = 0; i < 10; ++i) { __builtin_amdgcn_sched_group_barrier(0x008, 1, 0); __builtin_amdgcn_sched_group_barrier(0x100, 3, 0); }
  SBAR();
  P3_MM(fwb, 3); P3_MS(2);
  SBAR();
  const unsigned rb = lds0 + AT_K + 25600 + w * 5120 + lane * 16;
  if (role == 1) {
#pragma unroll
    for (int nt = 0; nt < 5; ++nt) lds_write16(rb + nt * 1024, __builtin_bit_cast(u32x4, acc[nt]));
    asm volatile("s_waitcnt lgkmcnt(0)" ::: "memory");
    sig(ct + CT_RED + w * 4, 1u, lane);
    return;
  }
  spin_ge(ct + CT_RED + w * 4, 1u);
  u16* orow_g = out + (size_t)(m3 + w * 16 + r16) * (2 * DM) + DM + n3;
  u32x2 ov[5];
#pragma unroll
  for (int nt = 0; nt < 5; ++nt) {
    const f32x4 o2 = acc[nt] + __builtin_bit_cast(f32x4, lds_read16(rb + nt * 1024));
    ov[nt][0] = pk_bf16(o2[0], o2[1]);
    ov[nt][1] = pk_bf16(o2[2], o2[3]);
  }
#pragma unroll
  for (int pr = 0; pr < 2; ++pr) {
    const auto sx = __builtin_amdgcn_permlane16_swap(ov[2 * pr][0], ov[2 * pr + 1][0], false, false);
    const auto sy = __builtin_amdgcn_permlane16_swap(ov[2 * pr][1], ov[2 * pr + 1][1], false, false);
    ostore16(orow_g + (2 * pr + (g & 1)) * 16 + 8 * (g >> 1), (u32x4){sx[0], sy[0], sx[1], sy[1]});
  }
  ostore8(orow_g + 64 + 4 * g, ov[4]);
}

extern "C" void kernel_launch(void* const* d_in, const int* in_sizes, int n_in, void* d_out, int out_size,
                              void* d_ws, size_t ws_size, hipStream_t stream) {
  const u16* aq = (const u16*)d_in[0];
  const u16* ak = (const u16*)d_in[1];
  const u16* av = (const u16*)d_in[2];
  const u16* shk = (const u16*)d_in[3];
  const u16* ssmk = (const u16*)d_in[4];
  const u16* ssmd = (const u16*)d_in[5];
  const u16* x = (const u16*)d_in[6];
  const u16* wqkv = (const u16*)d_in[7];
  const u16* wo = (const u16*)d_in[8];
  (void)hipFuncSetAttribute((const void*)mega, hipFuncAttributeMaxDynamicSharedMemorySize, L_TOTAL);
  mega<<<256, 768, L_TOTAL, stream>>>(x, wqkv, ak, av, shk, wo, ssmk, ssmd, aq, (char*)d_ws, (u16*)d_out);
}
```

```cpp
#include <hip/hip_runtime.h>
#include <hip/hip_bf16.h>
#include <stdint.h>

typedef unsigned short u16;
typedef short s16x4 __attribute__((ext_vector_type(4)));
typedef short s16x8 __attribute__((ext_vector_type(8)));
typedef float f32x2 __attribute__((ext_vector_type(2)));
typedef float f32x4 __attribute__((ext_vector_type(4)));
typedef float f32x16 __attribute__((ext_vector_type(16)));
typedef unsigned u32x2 __attribute__((ext_vector_type(2)));
typedef unsigned u32x4 __attribute__((ext_vector_type(4)));
typedef __bf16 bf16x2 __attribute__((ext_vector_type(2)));
#define LAS __attribute__((address_space(3)))
#define DI __device__ __forceinline__

constexpr int SEQ = 2048, DM = 640, NQKV = 1920, NH = 32;
constexpr size_t WS_QF = 0;
constexpr size_t WS_KF = WS_QF + (size_t)DM * SEQ * 2;
constexpr size_t WS_VF = WS_KF + (size_t)DM * SEQ * 2;
constexpr size_t WS_UT = WS_VF + (size_t)DM * SEQ * 2;
constexpr size_t WS_ZT = WS_UT + (size_t)DM * SEQ * 2;
constexpr size_t WS_OW = WS_ZT + (size_t)DM * SEQ * 2;
typedef unsigned long long u64;
constexpr size_t WS_FL = WS_OW + (size_t)DM * SEQ * 2;
constexpr size_t FL_U = WS_FL + 256;
constexpr int QREP = 32;
constexpr size_t FL_Q = FL_U + 20 * 64 * 8;
constexpr size_t FL_Z = FL_Q + QREP * 16 * 16 * 8;
constexpr size_t FL_O = FL_Z + 640 * 8;

DI unsigned pk_bf16(float lo, float hi) {
  f32x2 f = {lo, hi};
  return __builtin_bit_cast(unsigned, __builtin_convertvector(f, bf16x2));
}
DI float bf_lo(unsigned u) { return __uint_as_float(u << 16); }
DI float bf_hi(unsigned u) { return __uint_as_float(u & 0xffff0000u); }
DI float rbf(float x) { return bf_lo(pk_bf16(x, 0.f)); }

DI unsigned lds_addr(const void* p) { return (unsigned)(uintptr_t)(LAS const char*)p; }

DI void glds16(unsigned voff, const void* sbase, unsigned lds_dst) {
  unsigned keep;
  asm volatile("s_mov_b32 %0, m0\n\ts_mov_b32 m0, %3\n\ts_nop 0\n\tglobal_load_lds_dwordx4 %1, %2\n\ts_mov_b32 m0, %0"
               : "=&s"(keep) : "v"(voff), "s"(sbase), "s"(lds_dst) : "memory");
}
#define LDPOL "sc1"
DI void glds16c(unsigned voff, const void* sbase, unsigned lds_dst) {
  unsigned keep;
  asm volatile("s_mov_b32 %0, m0\n\ts_mov_b32 m0, %3\n\ts_nop 0\n\tglobal_load_lds_dwordx4 %1, %2 " LDPOL "\n\ts_mov_b32 m0, %0"
               : "=&s"(keep) : "v"(voff), "s"(sbase), "s"(lds_dst) : "memory");
}
struct Mk { const void* src; unsigned x; };
DI void glds_mark(const Mk& mk, unsigned lds_dst, int lane) {
  if (lane == 0) {
    unsigned keep;
    asm volatile("s_mov_b32 %0, m0\n\ts_mov_b32 m0, %3\n\ts_nop 0\n\tglobal_load_lds_dword %1, %2\n\ts_mov_b32 m0, %0"
                 : "=&s"(keep) : "v"(0u), "s"(mk.src), "s"(lds_dst) : "memory");
  }
}
DI float dot2bf(unsigned a, unsigned b, float c) {
  return __builtin_amdgcn_fdot2_f32_bf16(__builtin_bit_cast(bf16x2, a), __builtin_bit_cast(bf16x2, b), c, false);
}
DI void gstore16c(void* p, u32x4 v) {
  asm volatile("global_store_dwordx4 %0, %1, off sc1\n\ts_nop 1" :: "v"(p), "v"(v) : "memory");
}
DI void gstore8c(void* p, u32x2 v) {
  asm volatile("global_store_dwordx2 %0, %1, off sc1" :: "v"(p), "v"(v) : "memory");
}
#define OUTPOL "nt"
DI void ostore16(void* p, u32x4 v) { asm volatile("global_store_dwordx4 %0, %1, off " OUTPOL "\n\ts_nop 1" :: "v"(p), "v"(v) : "memory"); }
DI void ostore8(void* p, u32x2 v) { asm volatile("global_store_dwordx2 %0, %1, off " OUTPOL :: "v"(p), "v"(v) : "memory"); }
template <int N> DI void wait_vm() { asm volatile("s_waitcnt vmcnt(%0)" :: "n"(N) : "memory"); }
DI void bar() { asm volatile("s_waitcnt lgkmcnt(0)\n\ts_barrier" ::: "memory"); }
DI s16x4 tr_read(unsigned a) {
  return __builtin_amdgcn_ds_read_tr16_b64_v4i16((LAS s16x4*)(uintptr_t)a);
}
DI s16x8 lds_read16(unsigned a) { return *(LAS const s16x8*)(uintptr_t)a; }
DI s16x4 lds_read8(unsigned a) { return *(LAS const s16x4*)(uintptr_t)a; }
DI void lds_write16(unsigned a, u32x4 v) { *(LAS u32x4*)(uintptr_t)a = v; }
DI s16x8 cat8(s16x4 a, s16x4 b) { return __builtin_shufflevector(a, b, 0, 1, 2, 3, 4, 5, 6, 7); }
#define MFMA16(a, b, c) __builtin_amdgcn_mfma_f32_16x16x32_bf16((a), (b), (c), 0, 0, 0)
#define MFMA32(a, b, c) __builtin_amdgcn_mfma_f32_32x32x16_bf16((a), (b), (c), 0, 0, 0)

#define SBAR() __builtin_amdgcn_sched_barrier(0)
#define SPIN_SLEEP 1
#define FSL 3
#define SPIN_MAX (1 << 21)
DI unsigned lds_ld32(unsigned a) { return *(volatile LAS unsigned*)(uintptr_t)a; }
DI void lds_st32(unsigned a, unsigned v) { *(volatile LAS unsigned*)(uintptr_t)a = v; }
DI void spin_ge(unsigned a, unsigned want) {
#pragma nounroll
  for (int it = 0; it < SPIN_MAX; ++it) {
    if ((unsigned)__builtin_amdgcn_readfirstlane(lds_ld32(a)) >= want) break;
    __builtin_amdgcn_s_sleep(1);
  }
  asm volatile("" ::: "memory");
}
template <int SL = SPIN_SLEEP>
DI void spin_ge4(unsigned a, unsigned want) {
#pragma nounroll
  for (int it = 0; it < SPIN_MAX; ++it) {
    const u32x4 v = *(volatile LAS u32x4*)(uintptr_t)a;
    const unsigned m = min(min(v[0], v[1]), min(v[2], v[3]));
    if ((unsigned)__builtin_amdgcn_readfirstlane(m) >= want) break;
    __builtin_amdgcn_s_sleep(SL);
  }
  asm volatile("" ::: "memory");
}
DI unsigned mk_bad(const u32x4& v, unsigned x) { return (v[0] ^ x) | (v[1] ^ x) | (v[2] ^ x) | (v[3] ^ x); }
DI void spin_mk(unsigned a, unsigned x) {
#pragma nounroll
  for (int it = 0; it < SPIN_MAX; ++it) {
    const u32x4 v = *(volatile LAS u32x4*)(uintptr_t)a;
    if ((unsigned)__builtin_amdgcn_readfirstlane(mk_bad(v, x)) == 0u) break;
    __builtin_amdgcn_s_sleep(SPIN_SLEEP);
  }
  asm volatile("" ::: "memory");
}
DI void spin_ge8(unsigned a, unsigned want) { spin_ge4(a, want); spin_ge4(a + 16, want); }
constexpr int CT_S0 = 0, CT_FREE = 80, CT_PAIR = 144  , CT_QUAD = 192  , CT_ZCNT = 240  ,
              CT_QCNT = 256, CT_ZRDY = 304, CT_CVD = 336  , CT_XST = 416, CT_ATT = 448, CT_RED = 480,
              CT_FX = 512  , CT_KR8 = 688, CT_VR8 = 704, CT_WRDY = 720, CT_WRD2 = 736, CT_BYTES = 1024;
DI void sig(unsigned a, unsigned v, int lane) { if (lane == 0) lds_st32(a, v); }

#define POLL_MAX (1 << 15)
DI void publish(char* ws, size_t off, u64 E, int lane) {
  asm volatile("s_waitcnt vmcnt(0)" ::: "memory");
  if (lane == 0) __hip_atomic_store((u64*)(ws + off), E, __ATOMIC_RELAXED, __HIP_MEMORY_SCOPE_AGENT);
}
#define OSLEEP 10
#define ZSLEEP 20
#define USLEEP 10
#define POLL_SLEEP 6
template <int SL = POLL_SLEEP>
DI void poll(const char* ws, size_t off, u64 E) {
#pragma nounroll
  for (int it = 0; it < POLL_MAX; ++it) {
    const u64 v = __hip_atomic_load((const u64*)(ws + off), __ATOMIC_RELAXED, __HIP_MEMORY_SCOPE_AGENT);
    if (__builtin_amdgcn_ballot_w64(v != E) == 0ull) break;
    __builtin_amdgcn_s_sleep(SL);
  }
  asm volatile("" ::: "memory");
}

constexpr int K1_STAGE = 32768;
constexpr int A1_K = 0, A1_V = 3072, A1_W = 5120, A1_WAVE = 5632, A1_O = 2 * A1_WAVE, A1_PAIR = A1_O + 4096;
constexpr int L_AUX = 4 * K1_STAGE;
constexpr int L_CT = L_AUX + 2 * A1_PAIR;
constexpr int L_TOTAL = L_CT + CT_BYTES;
constexpr int K1_CT = L_CT;
constexpr int CV_COPY = 4160, CV_H = 4608, CV_REG = CV_H + 24576;
constexpr int AT_Q = L_AUX + 12288, AT_K = 64000, AT_V = AT_K + 16 * 2048, AT_END = AT_V + 16 * 1344;
constexpr int p3_w(int t) { return t < 5 ? t * 12800 : (t == 5 ? AT_END : AT_K + (t - 6) * 12800); }

#define PSYNC() do { asm volatile("s_waitcnt lgkmcnt(0)" ::: "memory"); ++ps; sig(pf + wh * 4, ps, lane); spin_ge(pf + (wh ^ 1) * 4, ps); } while (0)
DI void a1_issue(unsigned lds, int tile, int lane, int wh, const u16* ak, const u16* av, const u16* shk) {
  const int ts = tile & 63, td = tile >> 6;
  const int s0 = ts * 32 + wh * 16, d0 = td * 64;
  const int lr = lane >> 3, lc = (lane & 7) * 16;
  const unsigned wl = lds + wh * A1_WAVE;
#pragma unroll
  for (int i = 0; i < 3; ++i) {
    int r = s0 - 3 + i * 8 + lr; r = r < 0 ? 0 : r;
    if (i < 2 || lane < 24) glds16((unsigned)(r * DM * 2 + lc), (const char*)ak + d0 * 2, wl + A1_K + i * 1024);
  }
#pragma unroll
  for (int i = 0; i < 2; ++i)
    glds16((unsigned)(lr * DM * 2 + lc), (const char*)av + ((size_t)(s0 + i * 8) * DM + d0) * 2, wl + A1_V + i * 1024);
  if (lane < 32) glds16((unsigned)(lane * 16), (const char*)shk + d0 * 8, wl + A1_W);
}
DI void a1_finish(unsigned lds, unsigned pf, unsigned& ps, int tile, int lane, int wh, char* ws, u64 E) {
  const int ts = tile & 63, td = tile >> 6;
  const int c = lane & 7;
  const unsigned wl = lds + wh * A1_WAVE;
  u32x4 wt[4];
#pragma unroll
  for (int i = 0; i < 4; ++i) wt[i] = __builtin_bit_cast(u32x4, lds_read16(wl + A1_W + c * 64 + i * 16));
#pragma unroll
  for (int j = 0; j < 2; ++j) {
    const int r = j * 8 + (lane >> 3);
    const unsigned ka = wl + A1_K + (r + 3) * 128 + c * 16;
    u32x4 k0 = __builtin_bit_cast(u32x4, lds_read16(ka)), k1 = __builtin_bit_cast(u32x4, lds_read16(ka - 128));
    u32x4 k2 = __builtin_bit_cast(u32x4, lds_read16(ka - 256)), k3 = __builtin_bit_cast(u32x4, lds_read16(ka - 384));
    const u32x4 vv = __builtin_bit_cast(u32x4, lds_read16(wl + A1_V + r * 128 + c * 16));
    if (j == 0 && ts == 0 && wh == 0) {
      const u32x4 z = {0u, 0u, 0u, 0u};
      if (r < 1) k1 = z;
      if (r < 2) k2 = z;
      if (r < 3) k3 = z;
    }
    u32x4 o;
#pragma unroll
    for (int i = 0; i < 4; ++i) {
      const unsigned pa0 = __builtin_amdgcn_perm(k1[i], k0[i], 0x05040100u), pb0 = __builtin_amdgcn_perm(k3[i], k2[i], 0x05040100u);
      const unsigned pa1 = __builtin_amdgcn_perm(k1[i], k0[i], 0x07060302u), pb1 = __builtin_amdgcn_perm(k3[i], k2[i], 0x07060302u);
      const float e0 = dot2bf(pb0, wt[i][1], dot2bf(pa0, wt[i][0], 0.f));
      const float e1 = dot2bf(pb1, wt[i][3], dot2bf(pa1, wt[i][2], 0.f));
      o[i] = pk_bf16(e0 * bf_lo(vv[i]), e1 * bf_hi(vv[i]));
    }
    lds_write16(lds + A1_O + (31 - wh * 16 - r) * 128 + c * 16, o);
  }
  PSYNC();
  const int gg = lane >> 4, q = (lane & 15) >> 2, p = lane & 3;
  u16* ut = (u16*)(ws + WS_UT);
#pragma unroll
  for (int j = 0; j < 2; ++j) {
    const unsigned ra = lds + A1_O + (gg * 8 + q) * 128 + wh * 64 + j * 32 + p * 8;
    const s16x4 t0 = tr_read(ra), t1 = tr_read(ra + 512);
    const int d = td * 64 + wh * 32 + j * 16 + (lane & 15);
    gstore16c(ut + (size_t)d * SEQ + (SEQ - 32 - ts * 32) + gg * 8, __builtin_bit_cast(u32x4, cat8(t0, t1)));
  }
}
DI void a1_publish(int tile, int lane, int wh, char* ws, u64 E) {
  const int ts = tile & 63, td = tile >> 6;
  publish(ws, FL_U + (size_t)((td * 2 + wh) * 64 + ts) * 8, E, lane);
}

template <bool VT>
DI void k1_gemm(unsigned lds0, int m0, int n0, int tid, char* ws, unsigned mkx) {
  const int lane = tid & 63, wave = __builtin_amdgcn_readfirstlane(tid >> 6);
  const int wm = wave >> 1, wn = wave & 1;
  const int l32 = lane & 31, hh = lane >> 5, gg = lane >> 4, q = (lane & 15) >> 2, p = lane & 3;
  const unsigned ct = lds0 + K1_CT;
  f32x16 acc[2][2];
#pragma unroll
  for (int i = 0; i < 2; ++i)
#pragma unroll
    for (int j = 0; j < 2; ++j)
#pragma unroll
      for (int r = 0; r < 16; ++r) acc[i][j][r] = 0.f;
  unsigned xb[4];
#pragma unroll
  for (int ks = 0; ks < 4; ++ks) xb[ks] = lds0 + (wm * 64 + l32) * 128 + (((unsigned)(2 * ks + hh) ^ (unsigned)((l32 >> 1) & 7)) * 16);
  unsigned wb[2];
#pragma unroll
  for (int nt = 0; nt < 2; ++nt)
    wb[nt] = lds0 + 16384 + (8 * hh + q) * 256 + (((unsigned)(wn * 4 + 2 * nt + (gg & 1)) ^ (unsigned)(q << 1)) * 32) + p * 8;
  s16x8 x0[2], w0[2], x1[2], w1[2], x2[2], w2[2], x3[2], w3[2];
#define K1_LOADF(XF, WF, bo_, ks) do { \
    _Pragma("unroll") for (int i_ = 0; i_ < 2; ++i_) { \
      WF[i_] = cat8(tr_read(wb[i_] + (bo_) + (ks) * 4096), tr_read(wb[i_] + (bo_) + (ks) * 4096 + 1024)); \
      XF[i_] = lds_read16(xb[ks] + (bo_) + i_ * 4096); } } while (0)
#define K1_MM(XF, WF) do { _Pragma("unroll") for (int mt = 0; mt < 2; ++mt) _Pragma("unroll") for (int nt = 0; nt < 2; ++nt) \
      acc[mt][nt] = VT ? MFMA32(XF[mt], WF[nt], acc[mt][nt]) : MFMA32(WF[nt], XF[mt], acc[mt][nt]); } while (0)
#define ILV_A 2
#define ILV_B 2
#define ILV_C 2
#define K1_ILV() do { \
      __builtin_amdgcn_sched_group_barrier(0x008, 1, 0); __builtin_amdgcn_sched_group_barrier(0x100, ILV_A, 0); \
      __builtin_amdgcn_sched_group_barrier(0x008, 1, 0); __builtin_amdgcn_sched_group_barrier(0x100, ILV_B, 0); \
      __builtin_amdgcn_sched_group_barrier(0x008, 1, 0); __builtin_amdgcn_sched_group_barrier(0x100, ILV_C, 0); \
      __builtin_amdgcn_sched_group_barrier(0x008, 1, 0); __builtin_amdgcn_sched_group_barrier(0x100, 2, 0); } while (0)
  K1_LOADF(x0, w0, 0, 0);
  K1_LOADF(x1, w1, 0, 1);
#pragma nounroll
  for (int t = 0; t < 10; ++t) {
    const unsigned bo = (t & 3) * K1_STAGE, bn = ((t + 1) & 3) * K1_STAGE;
    SBAR();
    const u32x4 fl4 = *(volatile LAS u32x4*)(uintptr_t)(ct + CT_FX + (t + 1) * 16);
    K1_LOADF(x2, w2, bo, 2); K1_MM(x0, w0); K1_ILV();
    SBAR();
    K1_LOADF(x3, w3, bo, 3); K1_MM(x1, w1); K1_ILV();
    SBAR();
    const unsigned flu = (unsigned)__builtin_amdgcn_readfirstlane(mk_bad(fl4, mkx));
    if (t < 9 && flu != 0u) spin_mk(ct + CT_FX + (t + 1) * 16, mkx);
    asm volatile("" ::: "memory");
    SBAR();
    K1_LOADF(x0, w0, bn, 0); K1_MM(x2, w2); K1_ILV();
    SBAR();
    asm volatile("" : "+v"(x3[0]), "+v"(x3[1]), "+v"(w3[0]), "+v"(w3[1]) :: "memory");
    if (lane == 0) lds_st32(ct + CT_FREE + (t & 3) * 16 + wave * 4, (unsigned)(t + 1));
    SBAR();
    K1_LOADF(x1, w1, bn, 1); K1_MM(x3, w3); K1_ILV();
  }
  SBAR();
  if (!VT) {
    const int part = n0 >= DM ? 1 : 0;
    const float sc = part == 0 ? 0.22360679774997896f * 1.4426950408889634f : 1.0f;
    char* base = ws + (part ? WS_KF : WS_QF) + ((size_t)((m0 + wm * 64) >> 5) * 40 + ((n0 - part * DM + wn * 64) >> 4)) * 1024 + lane * 16;
#pragma unroll
    for (int mt = 0; mt < 2; ++mt)
#pragma unroll
      for (int nt = 0; nt < 2; ++nt)
#pragma unroll
        for (int uh = 0; uh < 2; ++uh) {
          u32x4 o;
#pragma unroll
          for (int i = 0; i < 4; ++i) o[i] = pk_bf16(acc[mt][nt][8 * uh + 2 * i] * sc, acc[mt][nt][8 * uh + 2 * i + 1] * sc);
          gstore16c(base + (mt * 40 + nt * 2 + uh) * 1024, o);
        }
  } else {
    char* base = ws + WS_VF + ((size_t)((m0 + wm * 64) >> 4) * 20 + ((n0 - 2 * DM + wn * 64) >> 5)) * 1024 + lane * 16;
#pragma unroll
    for (int mt = 0; mt < 2; ++mt)
#pragma unroll
      for (int nt = 0; nt < 2; ++nt)
#pragma unroll
        for (int kg = 0; kg < 2; ++kg) {
          u32x4 o;
#pragma unroll
          for (int i = 0; i < 4; ++i) o[i] = pk_bf16(acc[mt][nt][8 * kg + 2 * i], acc[mt][nt][8 * kg + 2 * i + 1]);
          gstore16c(base + ((mt * 2 + kg) * 20 + nt) * 1024, o);
        }
  }
}

DI void cv_put(unsigned y, const u32x4& c, const u32x2& n) {
  const unsigned d0 = c[0], d1 = c[1], d2 = c[2], d3 = c[3], d4 = n[0], d5 = n[1];
  const unsigned a0 = __builtin_amdgcn_alignbit(d1, d0, 16), a1 = __builtin_amdgcn_alignbit(d2, d1, 16),
                 a2 = __builtin_amdgcn_alignbit(d3, d2, 16), a3 = __builtin_amdgcn_alignbit(d4, d3, 16),
                 a4 = __builtin_amdgcn_alignbit(d5, d4, 16);
  lds_write16(y, (u32x4){d0, d1, d2, d3});
  lds_write16(y + CV_COPY, (u32x4){a0, a1, a2, a3});
  lds_write16(y + 2 * CV_COPY, (u32x4){d1, d2, d3, d4});
  lds_write16(y + 3 * CV_COPY, (u32x4){a1, a2, a3, a4});
}

#define QSYNC() do { asm volatile("s_waitcnt lgkmcnt(0)" ::: "memory"); ++qs; sig(qf + wq * 4, qs, lane); spin_ge4(qf, qs); } while (0)
struct HRegs { u32x4 c, u; u32x2 n; unsigned d; };
DI HRegs conv_load_h(int ch, int t256, const u16* __restrict__ ssmk, const u16* __restrict__ ssmd) {
  HRegs r;
  const u16* hrow = ssmk + (size_t)ch * SEQ;
  r.c = *((const u32x4*)(hrow + t256 * 8));
  r.n = *(const u32x2*)(hrow + (t256 < 255 ? t256 * 8 + 8 : 0));
  r.d = ssmd[ch];
  return r;
}
DI void conv_load_u(HRegs& r, int ch, int lane, int wq, const char* __restrict__ ws, u64 E) {
  poll<USLEEP>(ws, FL_U + (size_t)((ch >> 5) * 64 + 48 - 16 * wq + (lane & 15)) * 8, E);
  const u64* up = (const u64*)(ws + WS_UT + (size_t)ch * SEQ * 2 + wq * 1024 + lane * 16);
  const u64 a = __hip_atomic_load(up, __ATOMIC_RELAXED, __HIP_MEMORY_SCOPE_AGENT);
  const u64 b = __hip_atomic_load(up + 1, __ATOMIC_RELAXED, __HIP_MEMORY_SCOPE_AGENT);
  r.u = (u32x4){(unsigned)a, (unsigned)(a >> 32), (unsigned)b, (unsigned)(b >> 32)};
}
DI void conv_stage(unsigned reg, int lane, int wq, const HRegs& hr) {
  const int t256 = wq * 64 + lane;
  const unsigned hb = reg + CV_H;
  if (t256 < 32) lds_write16(reg + 4096 + t256 * 16, (u32x4){0u, 0u, 0u, 0u});
  cv_put(hb + (t256 + 2) * 16, hr.c, hr.n);
  if (t256 < 2) {
    const u32x4 z = {0u, 0u, 0u, 0u};
    u32x2 nx = {0u, 0u};
    const unsigned h0 = __builtin_amdgcn_readfirstlane(hr.c[0]), h1 = __builtin_amdgcn_readfirstlane(hr.c[1]);
    if (t256 == 1) nx = (u32x2){h0, h1};
    cv_put(hb + t256 * 16, z, nx);
  }
}
DI void conv_run(unsigned reg, unsigned qf, unsigned& qs, int ch, int lane, int wq, const HRegs& hr,
                 char* __restrict__ ws, unsigned xst) {
  const unsigned hb = reg + CV_H;
  if (xst) spin_ge4(xst, 1u);
  else { lds_write16(reg + (wq * 64 + lane) * 16, hr.u); QSYNC(); }
  const int r16 = lane & 15, g = lane >> 4, q = r16 >> 2, p = lane & 3;
  const unsigned ab0 = hb + ((q + 1) & 3) * CV_COPY + 2 * (8 * g + 4 * p + (q == 3 ? 4 : 0)) + wq * 128;
  const unsigned bb0 = reg + 2 * (2032 - 16 * r16 + 8 * g) + wq * 128 - 7 * 512;
  f32x4 acc[8];
#pragma unroll
  for (int i = 0; i < 8; ++i) acc[i] = (f32x4){0.f, 0.f, 0.f, 0.f};
#define CV_LDA(a_, ad_) cat8(tr_read((ad_) + (a_) * 512), tr_read((ad_) + (a_) * 512 + 8))
#define CV_LDB(c_, bd_) lds_read16((bd_) + (7 - (c_)) * 512)
  {
    s16x8 A[8], B[8];
    B[0] = CV_LDB(0, bb0);
#pragma unroll
    for (int a = 0; a < 8; ++a) A[a] = CV_LDA(a, ab0);
#pragma unroll
    for (int c = 1; c < 8; ++c) B[c] = CV_LDB(c, bb0);
#pragma unroll
    for (int c = 0; c < 8; ++c) {
      SBAR();
#pragma unroll
      for (int a = 0; a + c < 8; ++a) acc[a + c] = MFMA16(A[a], B[c], acc[a + c]);
      B[c] = CV_LDB(c, bb0 + 64);
      A[7 - c] = CV_LDA(7 - c, ab0 + 64);
    }
    SBAR();
#pragma unroll
    for (int a = 7; a >= 0; --a)
#pragma unroll
      for (int c = 0; a + c < 8; ++c) acc[a + c] = MFMA16(A[a], B[c], acc[a + c]);
  }
  QSYNC();
#pragma unroll
  for (int nt = 0; nt < 8; ++nt) {
    const int dw = nt >> 1;
    if (dw != wq) lds_write16(hb + (nt * 3 + (wq > dw ? wq - 1 : wq)) * 1024 + lane * 16, __builtin_bit_cast(u32x4, acc[nt]));
  }
  f32x4 k0 = wq == 0 ? acc[0] : (wq == 1 ? acc[2] : (wq == 2 ? acc[4] : acc[6]));
  f32x4 k1 = wq == 0 ? acc[1] : (wq == 1 ? acc[3] : (wq == 2 ? acc[5] : acc[7]));
  QSYNC();
  const float dco = bf_lo(hr.d);
  u32x2 zz[2];
#pragma unroll
  for (int i = 0; i < 2; ++i) {
    f32x4 o = i ? k1 : k0;
#pragma unroll
    for (int j = 0; j < 3; ++j) o += __builtin_bit_cast(f32x4, lds_read16(hb + ((wq * 2 + i) * 3 + j) * 1024 + lane * 16));
    const int s = (wq * 2 + i) * 256 + 16 * r16 + 4 * g;
    const u32x2 uu = __builtin_bit_cast(u32x2, lds_read8(reg + 2 * (SEQ - 4 - s)));
    const float kv0 = bf_hi(uu[1]), kv1 = bf_lo(uu[1]), kv2 = bf_hi(uu[0]), kv3 = bf_lo(uu[0]);
    const unsigned y01 = pk_bf16(o[0], o[1]), y23 = pk_bf16(o[2], o[3]);
    const unsigned e01 = pk_bf16(kv0 * dco, kv1 * dco), e23 = pk_bf16(kv2 * dco, kv3 * dco);
    zz[i][0] = pk_bf16(bf_lo(y01) + bf_lo(e01), bf_hi(y01) + bf_hi(e01));
    zz[i][1] = pk_bf16(bf_lo(y23) + bf_lo(e23), bf_hi(y23) + bf_hi(e23));
  }
  const auto sx = __builtin_amdgcn_permlane16_swap(zz[0][0], zz[1][0], false, false);
  const auto sy = __builtin_amdgcn_permlane16_swap(zz[0][1], zz[1][1], false, false);
  u16* zrow = (u16*)(ws + WS_ZT) + (size_t)ch * SEQ;
  gstore16c(zrow + (wq * 2 + (g & 1)) * 256 + 16 * r16 + 8 * (g >> 1), (u32x4){sx[0], sy[0], sx[1], sy[1]});
  asm volatile("s_waitcnt lgkmcnt(0)" ::: "memory");
}
DI void conv_publish(unsigned zc, int ch, int lane, char* __restrict__ ws, u64 E) {
  asm volatile("s_waitcnt vmcnt(0)" ::: "memory");
  if (lane == 0) {
    const unsigned old = __hip_atomic_fetch_add((LAS unsigned*)(uintptr_t)zc, 1u, __ATOMIC_RELAXED, __HIP_MEMORY_SCOPE_WORKGROUP);
    if ((old & 3u) == 3u) __hip_atomic_store((u64*)(ws + FL_Z + (size_t)ch * 8), E, __ATOMIC_RELAXED, __HIP_MEMORY_SCOPE_AGENT);
  }
}

DI s16x8 pack8(const f32x16& v, int o) {
  u32x4 r;
  r[0] = pk_bf16(v[o + 0], v[o + 1]); r[1] = pk_bf16(v[o + 2], v[o + 3]);
  r[2] = pk_bf16(v[o + 4], v[o + 5]); r[3] = pk_bf16(v[o + 6], v[o + 7]);
  return __builtin_bit_cast(s16x8, r);
}
DI float max3f(float a, float b, float c) { return __builtin_fmaxf(__builtin_fmaxf(a, b), c); }
DI float max16(const f32x16& v, float m) {
  float a = max3f(v[0], v[1], m), b = max3f(v[2], v[3], v[4]);
  a = max3f(a, v[5], v[6]); b = max3f(b, v[7], v[8]);
  a = max3f(a, v[9], v[10]); b = max3f(b, v[11], v[12]);
  a = max3f(a, v[13], v[14]);
  return max3f(a, b, v[15]);
}
DI float xhalf_max(float m) {
  auto sw = __builtin_amdgcn_permlane32_swap(__float_as_uint(m), __float_as_uint(m), false, false);
  return __builtin_fmaxf(__uint_as_float(sw[0]), __uint_as_float(sw[1]));
}

#define AT_LMAX 65536.0f
#define P3_WADDR() unsigned wv[4]; _Pragma("unroll") for (int i = 0; i < 4; ++i) { \
      const int c = i < 3 ? (i * 4 + w) * 64 + lane : 768 + w * 8 + (lane & 7); const int row = c / 10; \
      wv[i] = (unsigned)(row * DM * 2 + (c - row * 10) * 16); } const char* wog = (const char*)(wo + n3)
#define P3_STW(t) do { _Pragma("unroll") for (int i_ = 0; i_ < 3; ++i_) \
        glds16(wv[i_], wog + (size_t)(t) * 80 * DM * 2, lds0 + p3_w(t) + (i_ * 4 + w) * 1024); \
      if (lane < 8) glds16(wv[3], wog + (size_t)(t) * 80 * DM * 2, lds0 + p3_w(t) + 12288 + w * 128); } while (0)
DI void attn_stage(unsigned lds0, int h, int qb8, int j, int lane, char* __restrict__ ws, u64 E, int zch, const Mk& mk) {
  const int qt = qb8 * 8 + j;
  const unsigned l16 = (unsigned)(lane * 16), ct = lds0 + L_CT;
  const int qa = (5 * h) >> 2;
  const bool stg = qb8 > 0 || j >= 2;
  {
    const int l3 = lane & 7, hi = lane & 1;
    const bool kv = stg && l3 >= 2 && l3 < 6;
    const int rb = kv ? 2 * qb8 - 2 + j : 2 * qb8 + (l3 >= 6 ? 1 : 0);
    const int col = !kv ? qa * 16 + hi * 31 : (l3 < 4 ? DM + qa * 16 + hi * 31 : 2 * DM + h * 20 + hi * 19);
    poll(ws, FL_Q + (size_t)((h * 16 + rb) * 16 + (col >> 7)) * 8, E);
  }
  conv_publish(ct + CT_ZCNT + 8, zch, lane, ws, E);
  const int kt0 = qb8 * 8 - 8 + 4 * j;
#pragma unroll
  for (int i = 0; i < 4; ++i)
    glds16c(l16, ws + WS_QF + ((size_t)(qt + (i >> 1) * 4) * 40 + qa + (i & 1)) * 1024, lds0 + AT_Q + ((j + (i >> 1) * 4) * 2 + (i & 1)) * 1024);
  if (stg) {
#pragma unroll
    for (int i = 0; i < 8; ++i)
      glds16c(l16, ws + WS_KF + ((size_t)(kt0 + (i >> 1)) * 40 + qa + (i & 1)) * 1024, lds0 + AT_K + (j * 8 + i) * 1024);
  }
  glds_mark(mk, ct + CT_KR8 + j * 4, lane);
  if (stg) {
#pragma unroll
    for (int i = 0; i < 6; ++i) {
      const int c = i * 64 + lane;
      const int kgp = (c * 1561) >> 16, rem = c - kgp * 42, vh = rem >= 21 ? 1 : 0, d = rem - vh * 21;
      const int n = h * 20 + d;
      if (c < 336 && d == 20) lds_write16(lds0 + AT_V + j * 5376 + c * 16, (u32x4){0x3f803f80u, 0x3f803f80u, 0x3f803f80u, 0x3f803f80u});
      if (c < 336 && d < 20)
        glds16c((unsigned)((kgp * 20 + (n >> 5)) * 1024 + (vh * 32 + (n & 31)) * 16), ws + WS_VF + ((size_t)(kt0 * 2) * 20) * 1024,
                lds0 + AT_V + j * 5376 + i * 1024);
    }
    asm volatile("s_waitcnt lgkmcnt(0)" ::: "memory");
  }
  glds_mark(mk, ct + CT_VR8 + j * 4, lane);
}
template <bool FULL>
DI void attn_task(unsigned lds0, int h, int qb8, int j, int lane, char* __restrict__ ws, u64 E, int zch, const Mk& mk) {
  const int l32 = lane & 31, hh = lane >> 5;
  const int qt = qb8 * 8 + j;
  const int tfirst = FULL ? 0 : 8 - qt;
  const unsigned l16 = (unsigned)(lane * 16), ct = lds0 + L_CT;
  const int qa = (5 * h) >> 2;
  u32x4 q0r, q1r;
  spin_mk(ct + CT_KR8, mk.x);
  if (!FULL) spin_mk(ct + CT_VR8, mk.x);
  q0r = __builtin_bit_cast(u32x4, lds_read16(lds0 + AT_Q + j * 2048 + l16));
  q1r = __builtin_bit_cast(u32x4, lds_read16(lds0 + AT_Q + j * 2048 + 1024 + l16));
  asm volatile("s_waitcnt lgkmcnt(0)" : "+v"(q0r), "+v"(q1r) :: "memory");
  if (zch >= 0) conv_publish(ct + CT_ZCNT + (j >> 2) * 4, zch, lane, ws, E);
  {
    const int g0 = 4 * qa + hh - 5 * h;
    const bool k0 = g0 >= 0, k1 = g0 + 2 >= 0 && g0 + 2 < 5, k2 = g0 + 4 < 5, k3 = g0 + 6 < 5;
    q0r[0] = k0 ? q0r[0] : 0u; q0r[1] = k0 ? q0r[1] : 0u; q0r[2] = k1 ? q0r[2] : 0u; q0r[3] = k1 ? q0r[3] : 0u;
    q1r[0] = k2 ? q1r[0] : 0u; q1r[1] = k2 ? q1r[1] : 0u; q1r[2] = k3 ? q1r[2] : 0u; q1r[3] = k3 ? q1r[3] : 0u;
  }
  s16x8 qlo = __builtin_bit_cast(s16x8, q0r), qhi8 = __builtin_bit_cast(s16x8, q1r);
  const f32x16 zero16 = {0.f, 0.f, 0.f, 0.f, 0.f, 0.f, 0.f, 0.f, 0.f, 0.f, 0.f, 0.f, 0.f, 0.f, 0.f, 0.f};
  const float ninf = -INFINITY;
  const f32x16 ninf16 = {ninf, ninf, ninf, ninf, ninf, ninf, ninf, ninf, ninf, ninf, ninf, ninf, ninf, ninf, ninf, ninf};
  const unsigned kb = lds0 + AT_K + j * 2048 + l16;
#define AT_QK(dst, t, cin) do { if (FULL || (t) >= tfirst) { dst = MFMA32(lds_read16(kb + (t) * 2048), qlo, cin); \
    dst = MFMA32(lds_read16(kb + (t) * 2048 + 1024), qhi8, dst); } else dst = ninf16; } while (0)
  const int vrow = l32 <= 20 ? l32 : (l32 == 24 ? 20 : l32 - 16);
  const unsigned vb = lds0 + AT_V + j * 1344 + hh * 336 + vrow * 16;
#define AT_EXP(sv) do { _Pragma("unroll") for (int r = 0; r < 16; ++r) sv[r] = __builtin_amdgcn_exp2f(sv[r] - m); } while (0)
#define AT_PV(sv, t) do { oacc = MFMA32(lds_read16(vb + (t) * 1344), pack8(sv, 0), oacc); oacc = MFMA32(lds_read16(vb + (t) * 1344 + 672), pack8(sv, 8), oacc); } while (0)
  f32x16 oacc;
  bool exact = !FULL;
  if (FULL) {
    f32x16 sm, sa, sb;
    if (j >= 4) __builtin_amdgcn_s_setprio(2);
    AT_QK(sm, 8, zero16); AT_QK(sa, 0, zero16);
    const int tq = l32 - 4 * hh;
    const unsigned wm = tq < 0 ? 0xffffffffu : (0xfffffffeu << tq);
    unsigned msk[16];
#pragma unroll
    for (int r = 0; r < 16; ++r) {
      msk[r] = (unsigned)__builtin_amdgcn_sbfe((int)wm, (r & 3) + 8 * (r >> 2), 1);
      sm[r] = __uint_as_float((msk[r] & __float_as_uint(sa[r])) | (~msk[r] & __float_as_uint(sm[r])));
    }
    const float m = xhalf_max(max16(sm, ninf));
    const float nm = -m;
    const f32x16 cneg = {nm, nm, nm, nm, nm, nm, nm, nm, nm, nm, nm, nm, nm, nm, nm, nm};
    AT_QK(sa, 1, cneg);
    AT_EXP(sm);
    u32x4 pl = __builtin_bit_cast(u32x4, pack8(sm, 0)), ph = __builtin_bit_cast(u32x4, pack8(sm, 8)), ql, qh;
#pragma unroll
    for (int i = 0; i < 4; ++i) {
      ql[i] = pl[i] & ((msk[2 * i] & 0xffffu) | (msk[2 * i + 1] & 0xffff0000u)); pl[i] ^= ql[i];
      qh[i] = ph[i] & ((msk[8 + 2 * i] & 0xffffu) | (msk[9 + 2 * i] & 0xffff0000u)); ph[i] ^= qh[i];
    }
    spin_mk(ct + CT_VR8, mk.x);
    oacc = MFMA32(lds_read16(vb + 8 * 1344), __builtin_bit_cast(s16x8, pl), zero16);
    oacc = MFMA32(lds_read16(vb + 8 * 1344 + 672), __builtin_bit_cast(s16x8, ph), oacc);
    oacc = MFMA32(lds_read16(vb), __builtin_bit_cast(s16x8, ql), oacc);
    oacc = MFMA32(lds_read16(vb + 672), __builtin_bit_cast(s16x8, qh), oacc);
#define AT_EX0(sv) do { _Pragma("unroll") for (int r = 0; r < 16; ++r) sv[r] = __builtin_amdgcn_exp2f(sv[r]); } while (0)
    AT_QK(sb, 2, cneg); AT_EX0(sa); AT_PV(sa, 1);
    AT_QK(sa, 3, cneg); AT_EX0(sb); AT_PV(sb, 2);
    if (j >= 4) __builtin_amdgcn_s_setprio(0);
    AT_QK(sb, 4, cneg); AT_EX0(sa); AT_PV(sa, 3);
    AT_QK(sa, 5, cneg); AT_EX0(sb); AT_PV(sb, 4);
    AT_QK(sb, 6, cneg); AT_EX0(sa); AT_PV(sa, 5);
    AT_QK(sa, 7, cneg); AT_EX0(sb); AT_PV(sb, 6);
    AT_EX0(sa); AT_PV(sa, 7);
    float big = max3f(__builtin_fabsf(oacc[0]), __builtin_fabsf(oacc[1]), __builtin_fabsf(oacc[2]));
    big = max3f(big, __builtin_fabsf(oacc[3]), __builtin_fabsf(oacc[4]));
    big = max3f(big, __builtin_fabsf(oacc[5]), __builtin_fabsf(oacc[6]));
    big = max3f(big, __builtin_fabsf(oacc[7]), __builtin_fabsf(oacc[8]));
    big = max3f(big, __builtin_fabsf(oacc[9]), __builtin_fabsf(oacc[10]));
    big = __builtin_fmaxf(big, __builtin_fabsf(oacc[11]));
    const float ls = hh ? oacc[8] : oacc[12];
    float osum = 0.f;
#pragma unroll
    for (int r = 0; r < 8; ++r) osum += oacc[r];
    const bool ok = (ls < AT_LMAX) && (big < 1e37f) && (osum == osum);
    exact = __builtin_amdgcn_ballot_w64(!ok) != 0ull;
  }
  if (exact) {
    asm volatile("" : "+v"(qlo), "+v"(qhi8) :: "memory");
    f32x16 sm, s1, s2, s3;
    {
      f32x16 s0;
      AT_QK(sm, 8, zero16); AT_QK(s0, 0, zero16);
      AT_QK(s1, 1, zero16); AT_QK(s2, 2, zero16); AT_QK(s3, 3, zero16);
#pragma unroll
      for (int r = 0; r < 16; ++r) {
        const int kk = (r & 3) + 8 * (r >> 2) + 4 * hh;
        sm[r] = kk > l32 ? s0[r] : sm[r];
      }
    }
    float m = max16(sm, ninf);
    m = max16(s1, m); m = max16(s2, m); m = max16(s3, m);
    m = xhalf_max(m);
    AT_EXP(sm);
    oacc = zero16;
    {
      f32x16 p0, p8;
#pragma unroll
      for (int r = 0; r < 16; ++r) {
        const int kk = (r & 3) + 8 * (r >> 2) + 4 * hh;
        p0[r] = kk > l32 ? sm[r] : 0.f;
        p8[r] = kk > l32 ? 0.f : sm[r];
      }
      AT_PV(p8, 8);
      if (FULL) AT_PV(p0, 0);
    }
    if (FULL || 1 >= tfirst) { AT_EXP(s1); AT_PV(s1, 1); }
    AT_QK(sm, 4, zero16); AT_QK(s1, 5, zero16);
    if (FULL || 2 >= tfirst) { AT_EXP(s2); AT_PV(s2, 2); }
    AT_QK(s2, 6, zero16);
    if (FULL || 3 >= tfirst) { AT_EXP(s3); AT_PV(s3, 3); }
    AT_QK(s3, 7, zero16);
    float m2 = max16(sm, m);
    m2 = max16(s1, m2); m2 = max16(s2, m2); m2 = max16(s3, m2);
    m2 = xhalf_max(m2);
    {
      const float alpha = __builtin_amdgcn_exp2f(m - m2);
#pragma unroll
      for (int r = 0; r < 13; ++r) oacc[r] *= alpha;
      m = m2;
    }
    if (FULL || 4 >= tfirst) { AT_EXP(sm); AT_PV(sm, 4); }
    if (FULL || 5 >= tfirst) { AT_EXP(s1); AT_PV(s1, 5); }
    if (FULL || 6 >= tfirst) { AT_EXP(s2); AT_PV(s2, 6); }
    if (FULL || 7 >= tfirst) { AT_EXP(s3); AT_PV(s3, 7); }
  }
  asm volatile("s_waitcnt lgkmcnt(0)" ::: "memory");
  sig(ct + CT_ATT + j * 4, 1u, lane);
  const float l = hh ? oacc[8] : oacc[12];
  const float inv = __builtin_amdgcn_rcpf(l);
  char* oc = ws + WS_OW + ((size_t)qt * NH + h) * 1280;
  u32x4 o;
  o[0] = pk_bf16(oacc[0] * inv, oacc[1] * inv); o[1] = pk_bf16(oacc[2] * inv, oacc[3] * inv);
  o[2] = pk_bf16(oacc[4] * inv, oacc[5] * inv); o[3] = pk_bf16(oacc[6] * inv, oacc[7] * inv);
  gstore16c(oc + lane * 16, o);
  if (!hh) {
    u32x2 o2;
    o2[0] = pk_bf16(oacc[8] * inv, oacc[9] * inv); o2[1] = pk_bf16(oacc[10] * inv, oacc[11] * inv);
    gstore8c(oc + 1024 + l32 * 8, o2);
  }
  publish(ws, FL_O + (size_t)(qt * NH + h) * 8, E, lane);
}

#define APRIO 1
#define LPRIO 0
#define MPRIO 2
#define ROTG 2
__global__ __launch_bounds__(768, 3) void mega(const u16* __restrict__ x, const u16* __restrict__ wqkv,
                                               const u16* __restrict__ ak, const u16* __restrict__ av,
                                               const u16* __restrict__ shk, const u16* __restrict__ wo,
                                               const u16* __restrict__ ssmk, const u16* __restrict__ ssmd,
                                               const u16* __restrict__ aq, char* __restrict__ ws,
                                               u16* __restrict__ out) {
  extern __shared__ __attribute__((aligned(16))) char smem[];
  const unsigned lds0 = lds_addr(smem);
  const int tid = threadIdx.x, wave = __builtin_amdgcn_readfirstlane(tid >> 6);
  int lane = tid & 63;
#define RELANE() asm volatile("" : "+v"(lane))
  const int role = wave >> 2, w = wave & 3;
  const int bid = blockIdx.x, xcd = bid & 7, idx = bid >> 3;
  const bool has_gemm = idx < 30;
  const unsigned ct = lds0 + L_CT;
  u64 E;
  Mk mk; mk.src = wqkv;
  asm volatile("s_load_dwordx2 %0, %2, 0x0\n\ts_load_dword %1, %3, 0x0" : "=&s"(E), "=&s"(mk.x) : "s"(ws + WS_FL), "s"(wqkv) : "memory");
  const int gt = (xcd & 1) * 30 + idx;
  const int tm = (xcd >> 1) * 4 + (gt & 3), tn = gt >> 2;
  const int m0 = tm * 128, n0 = tn * 128;
  const int rot = ROTG >= 10 ? (((gt & 3) * 5 + 1) / 2 + 3 * tn) % 10 : ((((gt & 3) + tn) % ROTG) * 10) / ROTG;
  const unsigned xo0 = (unsigned)((lane >> 3) * DM * 2 + (((lane & 7) ^ (lane >> 4)) * 16));
  const unsigned xo1 = (unsigned)((lane >> 3) * DM * 2 + (((lane & 7) ^ (4 + (lane >> 4))) * 16));
  const unsigned wof = (unsigned)((lane >> 4) * NQKV * 2 + (((lane & 15) ^ ((lane >> 4) << 2)) * 16));
  const char* xg = (const char*)(x + (size_t)m0 * DM);
  const char* wg = (const char*)(wqkv + n0);
#define K1_PIECE_X(t, kt, j) glds16(((j) & 1) ? xo1 : xo0, xg + (size_t)(j) * 8 * DM * 2 + (kt) * 128, lds0 + ((t) & 3) * K1_STAGE + (j) * 1024)
#define K1_PIECE_W(t, kt, j) glds16(wof, wg + (size_t)((kt) * 64 + (j) * 4) * NQKV * 2, lds0 + ((t) & 3) * K1_STAGE + 16384 + (j) * 1024)
  if (has_gemm && role == 0) {
#pragma unroll
    for (int i = 0; i < 4; ++i) { K1_PIECE_X(0, rot, w * 4 + i); K1_PIECE_W(0, rot, w * 4 + i); }
  }
  const unsigned pf = ct + CT_PAIR + (wave >> 1) * 8;
  const int wh = wave & 1;
  unsigned ps = 0;
  const int fb = (idx - 30) * 8 + xcd;
  const int a1t = has_gemm ? (xcd * 30 + idx) * 2 + (w >> 1) : 480 + fb * 6 + (wave >> 1);
  const int a2t = 576 + fb * 4 + (wave >> 1);
  const unsigned a1l = has_gemm ? lds0 + L_AUX + (w >> 1) * A1_PAIR : lds0 + (wave >> 1) * A1_PAIR;
  if (!has_gemm || role == 2) a1_issue(a1l, a1t, lane, wh, ak, av, shk);
  const bool cv0 = role != 0 || !has_gemm || bid < 112;
  const int ch0 = role == 0 ? (has_gemm ? 528 + bid : 512 + fb) : bid + (role == 1 ? 256 : 0);
  asm volatile("s_waitcnt lgkmcnt(0)" : "+s"(E), "+s"(mk.x) :: "memory");
  if (role == 1) lds_st32(ct + (tid - 256) * 4, (tid - 256) * 4 >= CT_FX ? ~mk.x : 0u);
  bar();
  E += 1ull;

  const bool ex = has_gemm && role == 1 && bid < 112;
  HRegs hr, h2;
#define LOAD_H() do { hr = conv_load_h(ch0, w * 64 + lane, ssmk, ssmd); \
    if (ex) h2 = conv_load_h(528 + bid, w * 64 + lane, ssmk, ssmd); } while (0)
  auto q_publish = [&]() {
    asm volatile("s_waitcnt vmcnt(0)" ::: "memory");
    unsigned old = 0;
    if (lane == 0) old = __hip_atomic_fetch_add((LAS unsigned*)(uintptr_t)(ct + CT_QCNT), 1u, __ATOMIC_RELAXED, __HIP_MEMORY_SCOPE_WORKGROUP);
    if (__builtin_amdgcn_readfirstlane(old) == 3u) {
      const int c0 = (n0 % DM) - 32, hh_ = lane;
      if (lane < QREP && hh_ * 20 + 40 > c0 && hh_ * 20 < c0 + 32 + 128 + 20)
        __hip_atomic_store((u64*)(ws + FL_Q + (size_t)((lane * 16 + tm) * 16 + tn) * 8), E, __ATOMIC_RELAXED, __HIP_MEMORY_SCOPE_AGENT);
    }
  };
  if (!has_gemm) {
    LOAD_H();
    const unsigned a2l = lds0 + (6 + (wave >> 1)) * A1_PAIR;
    if (wave < 8) a1_issue(a2l, a2t, lane, wh, ak, av, shk);
    if (wave < 8) wait_vm<6>(); else wait_vm<0>();
    a1_finish(a1l, pf, ps, a1t, lane, wh, ws, E);
    if (wave < 8) {
      wait_vm<2>();
      a1_finish(a2l, pf, ps, a2t, lane, wh, ws, E);
      wait_vm<2>();
      if (lane == 0) __hip_atomic_store((u64*)(ws + FL_U + (size_t)(((a1t >> 6) * 2 + wh) * 64 + (a1t & 63)) * 8), E, __ATOMIC_RELAXED, __HIP_MEMORY_SCOPE_AGENT);
      a1_publish(a2t, lane, wh, ws, E);
    } else a1_publish(a1t, lane, wh, ws, E);
    bar();
  } else if (role == 0) {
    wait_vm<0>();
    sig(ct + CT_S0 + w * 4, 1u, lane);
    spin_ge4(ct + CT_S0, 1u);
    __builtin_amdgcn_s_setprio(MPRIO);
    if (tn >= 10) k1_gemm<true>(lds0, m0, n0, tid, ws, mk.x);
    else k1_gemm<false>(lds0, m0, n0, tid, ws, mk.x);
  } else if (role == 1) {
    __builtin_amdgcn_s_setprio(LPRIO);
#define K1_ST(t) do { const int kt_ = (t) + rot >= 10 ? (t) + rot - 10 : (t) + rot; \
      if ((t) >= 4) spin_ge4<FSL>(ct + CT_FREE + ((t) & 3) * 16, (unsigned)((t) - 3)); \
      _Pragma("unroll") for (int i_ = 0; i_ < 4; ++i_) { K1_PIECE_X(t, kt_, w * 4 + i_); K1_PIECE_W(t, kt_, w * 4 + i_); } } while (0)
#pragma nounroll
    for (int t = 1; t < 4; ++t) { K1_ST(t); glds_mark(mk, ct + CT_FX + t * 16 + w * 4, lane); }
    LOAD_H();
#pragma nounroll
    for (int t = 4; t < 10; ++t) { K1_ST(t); glds_mark(mk, ct + CT_FX + t * 16 + w * 4, lane); }
    __builtin_amdgcn_s_setprio(0);
  } else {
    LOAD_H();
    __builtin_amdgcn_s_setprio(2);
    wait_vm<0>();
    a1_finish(a1l, pf, ps, a1t, lane, wh, ws, E);
    a1_publish(a1t, lane, wh, ws, E);
    __builtin_amdgcn_s_setprio(0);
  }

  RELANE();
  unsigned qs = 0;
  const unsigned qf = ct + CT_QUAD + role * 16;
  const unsigned creg = lds0 + (role == 2 ? L_AUX : (role == 1 ? 29184 : 0));
  if (role == 2) __builtin_amdgcn_s_setprio(APRIO); else __builtin_amdgcn_s_setprio(0);
  if (has_gemm && role == 0) {
    q_publish();
    if (cv0) {
      hr.d = ssmd[ch0];
      conv_load_u(hr, ch0, lane, w, ws, E);
      spin_ge4(ct + CT_XST, 1u);
      conv_run(creg, qf, qs, ch0, lane, w, hr, ws, 0u);
    }
  } else {
    if (has_gemm && role == 1) {
      conv_load_u(hr, ch0, lane, w, ws, E);
      spin_ge4<FSL>(ct + CT_FREE + 16, 10u);
      conv_stage(creg, lane, w, hr);
      if (ex) {
        conv_stage(lds0, lane, w, h2);
        asm volatile("s_waitcnt lgkmcnt(0)" ::: "memory");
        sig(ct + CT_XST + w * 4, 1u, lane);
      }
    } else {
      if (has_gemm) { const int wq = w; QSYNC(); }
      conv_stage(creg, lane, w, hr);
      conv_load_u(hr, ch0, lane, w, ws, E);
    }
    conv_run(creg, qf, qs, ch0, lane, w, hr, ws, 0u);
  }
  sig(ct + CT_CVD + wave * 4, 1u, lane);

  const int tm3 = xcd * 4 + (idx >> 3), tn3 = idx & 7;
  const int m3 = tm3 * 64, n3 = tn3 * 80;
  RELANE();

  if (role == 2) {
    const int r16 = lane & 15, g = lane >> 4, q = r16 >> 2, p = lane & 3;
    __builtin_amdgcn_s_setprio(3);
    spin_ge4(ct + CT_CVD + 32, 1u);
    if (has_gemm) spin_ge4<FSL>(ct + CT_FREE + 16, 10u);
    attn_stage(lds0, xcd * 4 + (idx & 3), idx >> 2, w, lane, ws, E, ch0, mk);
    {
      P3_WADDR();
      P3_STW(5);
      spin_ge8(ct + CT_CVD, 1u);
      P3_STW(0); P3_STW(4); P3_STW(1); P3_STW(2); P3_STW(3);
      glds_mark(mk, ct + CT_WRDY + w * 4, lane);
    }
    __builtin_amdgcn_s_setprio(0);
    u32x4 qv[3];
#pragma unroll
    for (int j = 0; j < 3; ++j) {
      const int dc = j < 2 ? j * 4 + g : 8 + (g & 1);
      qv[j] = *((const u32x4*)(aq + (size_t)(m3 + w * 16 + r16) * DM + n3 + dc * 8));
    }
    {
      const int l5 = lane & 31;
      const int cl = l5 < 8 ? w * 8 + l5 : (l5 < 16 ? 32 + w * 8 + (l5 - 8) : 64 + w * 4 + (l5 & 3));
      poll<ZSLEEP>(ws, FL_Z + (size_t)(n3 + cl) * 8, E);
    }
    {
      const char* zg = ws + WS_ZT + ((size_t)n3 * SEQ + m3) * 2;
      const unsigned zo = (unsigned)((lane >> 3) * SEQ * 2 + (lane & 7) * 16);
      glds16c(zo, zg + (size_t)(w * 8) * SEQ * 2, lds0 + L_AUX + w * 1024);
      glds16c(zo, zg + (size_t)(32 + w * 8) * SEQ * 2, lds0 + L_AUX + 4096 + w * 1024);
      if (lane < 32) glds16c(zo, zg + (size_t)(64 + w * 4) * SEQ * 2, lds0 + L_AUX + 8192 + w * 512);
    }
    wait_vm<0>();
    sig(ct + CT_ZRDY + w * 4, 1u, lane);
    spin_ge4(ct + CT_ZRDY, 1u);
    u32x4 zz[3];
#pragma unroll
    for (int j = 0; j < 3; ++j) {
      const int dc = j < 2 ? j * 4 + g : 8 + (g & 1);
      const unsigned za = lds0 + L_AUX + (dc * 8 + q) * 128 + w * 32 + p * 8;
      zz[j] = __builtin_bit_cast(u32x4, cat8(tr_read(za), tr_read(za + 512)));
    }
#pragma unroll
    for (int j = 0; j < 3; ++j) {
      const int dc = j < 2 ? j * 4 + g : 8 + (g & 1);
      u32x4 o;
#pragma unroll
      for (int i = 0; i < 4; ++i) o[i] = pk_bf16(bf_lo(qv[j][i]) * bf_lo(zz[j][i]), bf_hi(qv[j][i]) * bf_hi(zz[j][i]));
      if (j < 2 || g < 2) ostore16(out + (size_t)(m3 + w * 16 + r16) * (2 * DM) + n3 + dc * 8, o);
    }
    if (bid == 0 && w == 0) {
#pragma unroll
      for (int i = 0; i < 4; ++i) poll(ws, FL_O + (size_t)(((i * 2 + (lane >> 5)) * 8) * NH + (lane & 31)) * 8, E);
      if (lane == 0) __hip_atomic_store((u64*)(ws + WS_FL), E, __ATOMIC_RELAXED, __HIP_MEMORY_SCOPE_AGENT);
    }
    return;
  }

  const int h = xcd * 4 + (idx & 3), qb8 = idx >> 2;
  __builtin_amdgcn_s_setprio(1);
  RELANE();
  if (qb8 > 0) attn_task<true>(lds0, h, qb8, wave, lane, ws, E, cv0 ? ch0 : -1, mk);
  else attn_task<false>(lds0, h, qb8, wave, lane, ws, E, cv0 ? ch0 : -1, mk);

  RELANE();
  const int r16 = lane & 15, g = lane >> 4, q = r16 >> 2, p = lane & 3;
  f32x4 acc[5];
#pragma unroll
  for (int i = 0; i < 5; ++i) acc[i] = (f32x4){0.f, 0.f, 0.f, 0.f};
  const unsigned wbp = lds0 + (20 * (g >> 1) + 4 * (g & 1) + q) * 160 + p * 8;
  const unsigned wbs = lds0 + (20 * g + 16 + q) * 160 + p * 8;
  u32x4 op[8]; u32x2 osg[4];
  if (role == 1) {
    P3_WADDR();
    spin_ge8(ct + CT_ATT, 1u);
    P3_STW(6); P3_STW(7);
    glds_mark(mk, ct + CT_WRD2 + w * 4, lane);
  }
  spin_mk(ct + CT_WRDY, mk.x);
  const unsigned w0 = role ? p3_w(4) : p3_w(0), w1 = role ? p3_w(5) : p3_w(1), w2 = role ? p3_w(6) : p3_w(2), w3 = role ? p3_w(7) : p3_w(3);
  struct Fw { s16x8 a[5], b[5]; s16x4 s[5]; };
  Fw fwa, fwb;
#define P3_LOADW(F, bo_) do { _Pragma("unroll") for (int nt = 0; nt < 5; ++nt) { \
      F.a[nt] = cat8(tr_read(wbp + (bo_) + nt * 32), tr_read(wbp + (bo_) + 8 * 160 + nt * 32)); \
      F.b[nt] = cat8(tr_read(wbp + (bo_) + 40 * 160 + nt * 32), tr_read(wbp + (bo_) + 48 * 160 + nt * 32)); \
      F.s[nt] = tr_read(wbs + (bo_) + nt * 32); } } while (0)
#define P3_MM(F, t) do { \
    _Pragma("unroll") for (int nt = 0; nt < 5; ++nt) acc[nt] = MFMA16(F.a[nt], __builtin_bit_cast(s16x8, op[2 * (t)]), acc[nt]); \
    _Pragma("unroll") for (int nt = 0; nt < 5; ++nt) acc[nt] = MFMA16(F.b[nt], __builtin_bit_cast(s16x8, op[2 * (t) + 1]), acc[nt]); } while (0)
#define P3_MS(t) do { const s16x8 os_ = cat8(__builtin_bit_cast(s16x4, osg[t]), __builtin_bit_cast(s16x4, osg[(t) + 1])); \
    _Pragma("unroll") for (int nt = 0; nt < 5; ++nt) acc[nt] = MFMA16(cat8(fwa.s[nt], fwb.s[nt]), os_, acc[nt]); } while (0)
  P3_LOADW(fwa, w0);
  SBAR();
  poll<OSLEEP>(ws, FL_O + (size_t)(((m3 >> 5) + (w >> 1)) * NH + role * 16 + r16) * 8, E);
  {
    const int orow = (w & 1) * 16 + r16;
    const char* ob = ws + WS_OW + ((size_t)((m3 >> 5) + (w >> 1)) * NH + role * 16) * 1280;
    const char* pp = ob + (g >> 1) * 1280 + (g & 1) * 512 + orow * 16;
    const char* sp = ob + g * 1280 + 1024 + orow * 8;
#pragma unroll
    for (int t = 0; t < 4; ++t) {
      asm volatile("global_load_dwordx4 %0, %3, off sc1\n\tglobal_load_dwordx4 %1, %3, off offset:2560 sc1\n\tglobal_load_dwordx2 %2, %4, off sc1"
                   : "=&v"(op[2 * t]), "=&v"(op[2 * t + 1]), "=&v"(osg[t]) : "v"(pp + t * 5120), "v"(sp + t * 5120) : "memory");
    }
  }
  P3_LOADW(fwb, w1);
  if (role == 1) spin_mk(ct + CT_WRD2, mk.x);
  SBAR();
  asm volatile("s_waitcnt vmcnt(6)" : "+v"(op[0]), "+v"(op[1]), "+v"(op[2]), "+v"(op[3]), "+v"(osg[0]), "+v"(osg[1]) :: "memory");
  P3_MS(0); P3_MM(fwa, 0);
  SBAR();
  P3_LOADW(fwa, w2); P3_MM(fwb, 1);
#pragma unroll
  for (int i = 0; i < 10; ++i) { __builtin_amdgcn_sched_group_barrier(0x008, 1, 0); __builtin_amdgcn_sched_group_barrier(0x100, 3, 0); }
  SBAR();
  asm volatile("s_waitcnt vmcnt(0)" : "+v"(op[4]), "+v"(op[5]), "+v"(op[6]), "+v"(op[7]), "+v"(osg[2]), "+v"(osg[3]) :: "memory");
  P3_LOADW(fwb, w3); P3_MM(fwa, 2);
#pragma unroll
  for (int i = 0; i < 10; ++i) { __builtin_amdgcn_sched_group_barrier(0x008, 1, 0); __builtin_amdgcn_sched_group_barrier(0x100, 3, 0); }
  SBAR();
  P3_MM(fwb, 3); P3_MS(2);
  SBAR();
  const unsigned rb = lds0 + AT_K + 25600 + w * 5120 + lane * 16;
  if (role == 1) {
#pragma unroll
    for (int nt = 0; nt < 5; ++nt) lds_write16(rb + nt * 1024, __builtin_bit_cast(u32x4, acc[nt]));
    asm volatile("s_waitcnt lgkmcnt(0)" ::: "memory");
    sig(ct + CT_RED + w * 4, 1u, lane);
    return;
  }
  spin_ge(ct + CT_RED + w * 4, 1u);
  u16* orow_g = out + (size_t)(m3 + w * 16 + r16) * (2 * DM) + DM + n3;
  u32x2 ov[5];
#pragma unroll
  for (int nt = 0; nt < 5; ++nt) {
    const f32x4 o2 = acc[nt] + __builtin_bit_cast(f32x4, lds_read16(rb + nt * 1024));
    ov[nt][0] = pk_bf16(o2[0], o2[1]);
    ov[nt][1] = pk_bf16(o2[2], o2[3]);
  }
#pragma unroll
  for (int pr = 0; pr < 2; ++pr) {
    const auto sx = __builtin_amdgcn_permlane16_swap(ov[2 * pr][0], ov[2 * pr + 1][0], false, false);
    const auto sy = __builtin_amdgcn_permlane16_swap(ov[2 * pr][1], ov[2 * pr + 1][1], false, false);
    ostore16(orow_g + (2 * pr + (g & 1)) * 16 + 8 * (g >> 1), (u32x4){sx[0], sy[0], sx[1], sy[1]});
  }
  ostore8(orow_g + 64 + 4 * g, ov[4]);
}

extern "C" void kernel_launch(void* const* d_in, const int* in_sizes, int n_in, void* d_out, int out_size,
                              void* d_ws, size_t ws_size, hipStream_t stream) {
  const u16* aq = (const u16*)d_in[0];
  const u16* ak = (const u16*)d_in[1];
  const u16* av = (const u16*)d_in[2];
  const u16* shk = (const u16*)d_in[3];
  const u16* ssmk = (const u16*)d_in[4];
  const u16* ssmd = (const u16*)d_in[5];
  const u16* x = (const u16*)d_in[6];
  const u16* wqkv = (const u16*)d_in[7];
  const u16* wo = (const u16*)d_in[8];
  (void)hipFuncSetAttribute((const void*)mega, hipFuncAttributeMaxDynamicSharedMemorySize, L_TOTAL);
  mega<<<256, 768, L_TOTAL, stream>>>(x, wqkv, ak, av, shk, wo, ssmk, ssmd, aq, (char*)d_ws, (u16*)d_out);
}
```
